```python
import torch

def _softmax(x, axis=-1):
    return torch.softmax(x, dim=axis)
def _gelu(x):
    return torch.nn.functional.gelu(x, approximate="tanh")
def _silu(x):
    return torch.nn.functional.silu(x)
def _sigmoid(x):
    return torch.sigmoid(x)
def _layernorm(x, axis=-1, eps=1e-5):
    m = x.mean(dim=axis, keepdim=True)
    v = x.var(dim=axis, keepdim=True, unbiased=False)
    return (x - m) / torch.sqrt(v + eps)
def _scatter_add(tgt, idx, src, axis=0):
    return tgt.clone().index_add_(axis, idx, src)
def _segment_sum(src, seg, n):
    out = torch.zeros((n,) + src.shape[1:], dtype=src.dtype)
    return out.index_add_(0, seg, src)
def _pad(x, pw):
    flat = tuple(v for pair in reversed(pw) for v in pair)
    return torch.nn.functional.pad(x, flat)

kernel_name = 'interleave_layers__linear_kernel_hedgehog__glu_gated_glu__prompt'

def setup_inputs():

    _w = (lambda a: (lambda t: t.to(torch.bfloat16) if t.is_floating_point() else t)(torch.as_tensor(a)))
    def _A_setup():
        B, S, D, H = 1, 2048, 448, 16
        Dh = D // H
        return {
            "x": _w((lambda *s: torch.randn(*s, dtype=torch.bfloat16) * 0.05)(B, S, D)),
            "wqkv": _w((lambda *s: torch.randn(*s, dtype=torch.bfloat16) * 0.4)(D, 3 * D)),
            "w_phi": _w((lambda *s: torch.randn(*s, dtype=torch.bfloat16) * 0.4)(H, Dh, Dh) * (Dh ** -0.5)),
            "wo": _w((lambda *s: torch.randn(*s, dtype=torch.bfloat16) * 0.05)(D, D)),
        }
    _A_in = _A_setup()

    def _B_setup():
        B, S, D, F = 1, 2048, 448, 1792
        return {
            "x": _w((lambda *s: torch.randn(*s, dtype=torch.bfloat16) * 0.05)(B, S, D)),
            "w_gate": _w((lambda *s: torch.randn(*s, dtype=torch.bfloat16) * 0.05)(D, F)),
            "w_up": _w((lambda *s: torch.randn(*s, dtype=torch.bfloat16) * 0.05)(D, F)),
            "w_down": _w((lambda *s: torch.randn(*s, dtype=torch.bfloat16) * 0.05)(F, D)),
        }
    _B_in = _B_setup()

    return {
        "A_x": _A_in["x"],
        "A_wqkv": _A_in["wqkv"],
        "A_w_phi": _A_in["w_phi"],
        "A_wo": _A_in["wo"],
        "B_x": _B_in["x"],
        "B_w_gate": _B_in["w_gate"],
        "B_w_up": _B_in["w_up"],
        "B_w_down": _B_in["w_down"],
    }

def reference(A_x, A_wqkv, A_w_phi, A_wo, B_x, B_w_gate, B_w_up, B_w_down):
    def _A_ref(x, wqkv, w_phi, wo):
        B, S, D = x.shape
        H = 16; Dh = D // H
        qkv = (lambda x, *d: x.permute(*d))((x @ wqkv).reshape(B, S, 3, H, Dh), 2, 0, 3, 1, 4)
        q, k, v = qkv[0], qkv[1], qkv[2]
        pq = torch.einsum("bhsd,hdf->bhsf", q, w_phi)
        pk = torch.einsum("bhsd,hdf->bhsf", k, w_phi)
        phi_q = torch.concatenate([_softmax(pq, axis=-1), _softmax(-pq, axis=-1)], axis=-1)
        phi_k = torch.concatenate([_softmax(pk, axis=-1), _softmax(-pk, axis=-1)], axis=-1)
        kv = torch.einsum("bhsf,bhsd->bhfd", phi_k, v)
        z  = torch.einsum("bhsf->bhf", phi_k)
        num = torch.einsum("bhqf,bhfd->bhqd", phi_q, kv)
        den = torch.einsum("bhqf,bhf->bhq", phi_q, z)[..., None] + 1e-6
        o = num / den
        return (lambda x, *d: x.permute(*d))(o, 0, 2, 1, 3).reshape(B, S, D) @ wo

    def _B_ref(x, w_gate, w_up, w_down):
        g = torch.einsum("bsd,df->bsf", x, w_gate)
        u = torch.einsum("bsd,df->bsf", x, w_up)
        h = _sigmoid(g) * u
        return torch.einsum("bsf,fd->bsd", h, w_down)

    x = A_x
    x = x + _A_ref(_layernorm(x, axis=-1), A_wqkv, A_w_phi, A_wo)
    x = x + _B_ref(_layernorm(x, axis=-1), B_w_gate, B_w_up, B_w_down)
    x = x + _A_ref(_layernorm(x, axis=-1), A_wqkv, A_w_phi, A_wo)
    x = x + _B_ref(_layernorm(x, axis=-1), B_w_gate, B_w_up, B_w_down)
    return x
```

```cpp
#include <hip/hip_runtime.h>
#include <hip/hip_bf16.h>
#include <stdint.h>

typedef unsigned short u16;
__device__ __forceinline__ float bf2f(u16 v) { return __uint_as_float(((unsigned)v) << 16); }
__device__ __forceinline__ u16 f2bf(float f) { __hip_bfloat16 b = __float2bfloat16(f); return *reinterpret_cast<u16*>(&b); }
__device__ __forceinline__ float rbf(float f) { return bf2f(f2bf(f)); }

constexpr int S = 2048, D = 448, H = 16, DH = 28, F = 1792;

__global__ void k_ln(const u16* __restrict__ x, u16* __restrict__ y) {
  int row = blockIdx.x, t = threadIdx.x;
  float v[7]; float s = 0.f;
  for (int i = 0; i < 7; ++i) { v[i] = bf2f(x[row * D + t + 64 * i]); s += v[i]; }
  for (int o = 32; o; o >>= 1) s += __shfl_xor(s, o);
  float m = s / D; float q = 0.f;
  for (int i = 0; i < 7; ++i) { float d = v[i] - m; q += d * d; }
  for (int o = 32; o; o >>= 1) q += __shfl_xor(q, o);
  float r = rsqrtf(q / D + 1e-5f);
  for (int i = 0; i < 7; ++i) y[row * D + t + 64 * i] = f2bf((v[i] - m) * r);
}

__global__ void k_gemm(const u16* __restrict__ A, const u16* __restrict__ B, u16* __restrict__ C, const u16* __restrict__ res, int M, int N, int K) {
  __shared__ float sA[16][33]; __shared__ float sB[32][17];
  int tx = threadIdx.x & 15, ty = threadIdx.x >> 4;
  int row = blockIdx.y * 16 + ty, col = blockIdx.x * 16 + tx;
  float acc = 0.f;
  for (int k0 = 0; k0 < K; k0 += 32) {
    for (int e = threadIdx.x; e < 512; e += 256) {
      int r = e >> 5, c = e & 31; sA[r][c] = bf2f(A[(blockIdx.y * 16 + r) * K + k0 + c]);
      int kk = e >> 4, n = e & 15; sB[kk][n] = bf2f(B[(k0 + kk) * N + blockIdx.x * 16 + n]);
    }
    __syncthreads();
    for (int k = 0; k < 32; ++k) acc += sA[ty][k] * sB[k][tx];
    __syncthreads();
  }
  float o = rbf(acc);
  if (res) o = bf2f(res[row * N + col]) + o;
  C[row * N + col] = f2bf(o);
}

__global__ __launch_bounds__(64) void k_phi(const u16* __restrict__ qkv, const u16* __restrict__ wphi, u16* __restrict__ phi  ) {
  int idx = blockIdx.x * blockDim.x + threadIdx.x;
  if (idx >= 2 * S * H) return;
  int which = idx / (S * H), r = idx % (S * H), s = r / H, h = r % H;
  float q[DH], p[DH];
#pragma unroll
  for (int d = 0; d < DH; ++d) q[d] = bf2f(qkv[s * 3 * D + which * D + h * DH + d]);
  float mx = -1e30f, mn = 1e30f;
#pragma unroll
  for (int f = 0; f < DH; ++f) {
    float a = 0.f;
#pragma unroll
    for (int d = 0; d < DH; ++d) a += q[d] * bf2f(wphi[(h * DH + d) * DH + f]);
    a = rbf(a); p[f] = a; mx = fmaxf(mx, a); mn = fminf(mn, a);
    __builtin_amdgcn_sched_barrier(0);
  }
  float sp = 0.f, sn = 0.f;
#pragma unroll
  for (int f = 0; f < DH; ++f) { sp += __expf(p[f] - mx); sn += __expf(mn - p[f]); }
  u16* o = phi + ((size_t)(which * S + s) * H + h) * 56;
#pragma unroll
  for (int f = 0; f < DH; ++f) { o[f] = f2bf(__expf(p[f] - mx) / sp); o[DH + f] = f2bf(__expf(mn - p[f]) / sn); }
}

__global__ void k_kv(const u16* __restrict__ qkv, const u16* __restrict__ phik, float* __restrict__ kv) {
  int h = blockIdx.x / 56, f = blockIdx.x % 56;
  int t = threadIdx.x;
  int d = t & 31, part = t >> 5;
  float a = 0.f;
  if (d <= DH) {
    for (int s = part; s < S; s += 8) {
      float pk = bf2f(phik[((size_t)s * H + h) * 56 + f]);
      float v = d < DH ? bf2f(qkv[s * 3 * D + 2 * D + h * DH + d]) : 1.f;
      a += pk * v;
    }
  }
  __shared__ float red[256];
  red[t] = a; __syncthreads();
  if (t < 32) { float s = 0.f; for (int i = 0; i < 8; ++i) s += red[i * 32 + t]; kv[(h * 56 + f) * 32 + t] = rbf(s); }
}

__global__ void k_o(const u16* __restrict__ phiq, const float* __restrict__ kv, u16* __restrict__ o) {
  int idx = blockIdx.x * blockDim.x + threadIdx.x;
  if (idx >= S * D) return;
  int s = idx / D, c = idx % D, h = c / DH, d = c % DH;
  float num = 0.f, den = 0.f;
  for (int f = 0; f < 56; ++f) {
    float p = bf2f(phiq[((size_t)s * H + h) * 56 + f]);
    num += p * kv[(h * 56 + f) * 32 + d]; den += p * kv[(h * 56 + f) * 32 + 28];
  }
  num = rbf(num); den = rbf(rbf(den) + 1e-6f);
  o[idx] = f2bf(num / den);
}

__global__ void k_glu(const u16* __restrict__ g, const u16* __restrict__ u, u16* __restrict__ hh, int n) {
  int i = blockIdx.x * blockDim.x + threadIdx.x;
  if (i >= n) return;
  float gg = bf2f(g[i]); float sg = rbf(1.f / (1.f + __expf(-gg)));
  hh[i] = f2bf(sg * bf2f(u[i]));
}

extern "C" void kernel_launch(void* const* d_in, const int* in_sizes, int n_in,
                              void* d_out, int out_size, void* d_ws, size_t ws_size,
                              hipStream_t stream) {
  const u16* x0 = (const u16*)d_in[0];
  const u16* wqkv = (const u16*)d_in[1];
  const u16* wphi = (const u16*)d_in[2];
  const u16* wo = (const u16*)d_in[3];
  const u16* wg = (const u16*)d_in[5];
  const u16* wu = (const u16*)d_in[6];
  const u16* wd = (const u16*)d_in[7];
  char* ws = (char*)d_ws;
  u16* ln = (u16*)ws; ws += S * D * 2;
  u16* qkv = (u16*)ws; ws += S * 3 * D * 2;
  u16* phi = (u16*)ws; ws += 2 * S * H * 56 * 2;
  float* kv = (float*)ws; ws += H * 56 * 32 * 4;
  u16* o = (u16*)ws; ws += S * D * 2;
  u16* xa = (u16*)ws; ws += S * D * 2;
  u16* xb = (u16*)ws; ws += S * D * 2;
  u16* g = (u16*)ws; ws += S * F * 2;
  u16* u = (u16*)ws; ws += S * F * 2;
  u16* hh = (u16*)ws; ws += S * F * 2;
  const u16* xin = x0;
  for (int it = 0; it < 2; ++it) {
    k_ln<<<S, 64, 0, stream>>>(xin, ln);
    k_gemm<<<dim3(3 * D / 16, S / 16), 256, 0, stream>>>(ln, wqkv, qkv, nullptr, S, 3 * D, D);
    k_phi<<<(2 * S * H + 63) / 64, 64, 0, stream>>>(qkv, wphi, phi);
    k_kv<<<H * 56, 256, 0, stream>>>(qkv, phi + (size_t)S * H * 56, kv);
    k_o<<<(S * D + 255) / 256, 256, 0, stream>>>(phi, kv, o);
    k_gemm<<<dim3(D / 16, S / 16), 256, 0, stream>>>(o, wo, xa, xin, S, D, D);
    k_ln<<<S, 64, 0, stream>>>(xa, ln);
    k_gemm<<<dim3(F / 16, S / 16), 256, 0, stream>>>(ln, wg, g, nullptr, S, F, D);
    k_gemm<<<dim3(F / 16, S / 16), 256, 0, stream>>>(ln, wu, u, nullptr, S, F, D);
    k_glu<<<(S * F + 255) / 256, 256, 0, stream>>>(g, u, hh, S * F);
    u16* xo = it == 0 ? xb : (u16*)d_out;
    k_gemm<<<dim3(D / 16, S / 16), 256, 0, stream>>>(hh, wd, xo, xa, S, D, F);
    xin = xb;
  }
}
```

```cpp
#include <hip/hip_runtime.h>
#include <stdint.h>
#include <utility>
#include <type_traits>

typedef unsigned short u16;
typedef unsigned int u32;
typedef u32 v4u __attribute__((ext_vector_type(4)));
typedef u32 v2u __attribute__((ext_vector_type(2)));
typedef float f32x2 __attribute__((ext_vector_type(2)));
typedef float f32x4 __attribute__((ext_vector_type(4)));
typedef float f32x16 __attribute__((ext_vector_type(16)));
typedef __bf16 bf16x2 __attribute__((ext_vector_type(2)));
typedef __bf16 bf16x8 __attribute__((ext_vector_type(8)));
typedef short s16x4 __attribute__((ext_vector_type(4)));
#define LAS __attribute__((address_space(3)))
#define DI __device__ __forceinline__
#define AI __attribute__((always_inline))
template <int V> using IC = std::integral_constant<int, V>;

constexpr int S = 2048, D = 448, FF = 1792;
constexpr int UNIT = 28672;

constexpr int OFF_CNT = 0;
constexpr int OFF_FLG = 65536;
constexpr int CTL_BYTES = 65536 + 65536;
constexpr int OFF_W1 = CTL_BYTES;
constexpr int OFF_W2 = OFF_W1 + 16 * 3 * UNIT;
constexpr int OFF_W3 = OFF_W2 + 8 * 2 * UNIT;
constexpr int OFF_W4 = OFF_W3 + 16 * 7 * UNIT;
constexpr int OFF_CS1 = OFF_W4 + 8 * 8 * UNIT;
constexpr int OFF_CS3 = OFF_CS1 + 14 * 16 * 384;
constexpr int OFF_WPHI = OFF_CS3 + 7 * 16 * 896;
constexpr int XB = S * D * 2;
constexpr int OFF_XIN = OFF_WPHI + 16 * 2048;
constexpr int OFF_X1 = OFF_XIN + 2 * XB;
constexpr int OFF_O = OFF_X1 + 2 * XB;
constexpr int STB = S * 16 * 8;
constexpr int OFF_STIN = OFF_O + 2 * XB;
constexpr int OFF_ST1 = OFF_STIN + 2 * STB;
constexpr int KVB = 16 * 16 * 4096;
constexpr int OFF_KVP = OFF_ST1 + 2 * STB;
constexpr int HB = S * FF * 2;
constexpr int OFF_H = OFF_KVP + 2 * KVB;
constexpr int WS_USED = OFF_H + 2 * HB;

enum { C_KV = 0, C_O = 1, C_X1 = 2, C_X2 = 4 };
constexpr int CNT_ERR = 255;

constexpr int L_SCR = 4 * UNIT;
constexpr int PHK_STRIDE = 136, V_STRIDE = 72;
constexpr int L_PHK = L_SCR;
constexpr int L_V = L_SCR + 128 * PHK_STRIDE;
constexpr int L_KVF = L_SCR + 28672;
constexpr int L_CONST = 155648;
constexpr int L_CS1 = L_CONST, L_CS3 = L_CONST + 384, L_WPHI = L_CONST + 1280, L_ARR = L_CONST + 3328;
constexpr int L_FRESH = 81920;
constexpr int LDS_BYTES = 163840;

constexpr float LOG2E = 1.4426950408889634f;
constexpr int LAG = 10;

DI u32 pk2(float a, float b) { f32x2 v = {a, b}; return __builtin_bit_cast(u32, __builtin_convertvector(v, bf16x2)); }
DI float bflo(u32 v) { return __uint_as_float(v << 16); }
DI float bfhi(u32 v) { return __uint_as_float(v & 0xffff0000u); }
DI f32x16 mfma(v4u a, v4u b, f32x16 c) {
  return __builtin_amdgcn_mfma_f32_32x32x16_bf16(__builtin_bit_cast(bf16x8, a), __builtin_bit_cast(bf16x8, b), c, 0, 0, 0);
}
DI float xsum(float v) {
  auto r = __builtin_amdgcn_permlane32_swap(__float_as_uint(v), __float_as_uint(v), false, false);
  return __uint_as_float(r[0]) + __uint_as_float(r[1]);
}
DI float xmax(float v) {
  auto r = __builtin_amdgcn_permlane32_swap(__float_as_uint(v), __float_as_uint(v), false, false);
  return fmaxf(__uint_as_float(r[0]), __uint_as_float(r[1]));
}
DI float xmin(float v) {
  auto r = __builtin_amdgcn_permlane32_swap(__float_as_uint(v), __float_as_uint(v), false, false);
  return fminf(__uint_as_float(r[0]), __uint_as_float(r[1]));
}
#define BAR() asm volatile("s_waitcnt lgkmcnt(0)\n\ts_barrier" ::: "memory")
#define RAWBAR() asm volatile("s_barrier" ::: "memory")
#define VMCNT(n) asm volatile("s_waitcnt vmcnt(" #n ")" ::: "memory")
#define SB() __builtin_amdgcn_sched_barrier(0)
#define PIN(x) asm volatile("" : "+v"(x))

typedef __amdgpu_buffer_rsrc_t rsrc_t;
DI v4u ld16(rsrc_t rs, int voff, int soff) { return __builtin_amdgcn_raw_buffer_load_b128(rs, voff, soff, 16); }
DI v2u ld8(rsrc_t rs, int voff, int soff) { return __builtin_amdgcn_raw_buffer_load_b64(rs, voff, soff, 16); }
DI u32 ld4(rsrc_t rs, int voff, int soff) { return __builtin_amdgcn_raw_buffer_load_b32(rs, voff, soff, 16); }
DI v4u ld16p(rsrc_t rs, int voff, int soff) { return __builtin_amdgcn_raw_buffer_load_b128(rs, voff, soff, 0); }
DI void st16(rsrc_t rs, int voff, int soff, v4u v) {
  __builtin_amdgcn_raw_buffer_store_b128(v, rs, voff, soff, 16);
  asm volatile("s_nop 1" ::"v"(v) : "memory");
}
DI void st8(rsrc_t rs, int voff, int soff, v2u v) { __builtin_amdgcn_raw_buffer_store_b64(v, rs, voff, soff, 16); }
DI void st4(rsrc_t rs, int voff, int soff, u32 v) { __builtin_amdgcn_raw_buffer_store_b32(v, rs, voff, soff, 16); }
DI int opq(int v) { asm volatile("" : "+s"(v)); return v; }
DI int opqv(int v) { asm volatile("" : "+v"(v)); return v; }
DI v4u lds16(LAS char* l, int off) { return *(LAS v4u*)(l + off); }
DI float dot2(u32 a, u32 b, float c) {
  return __builtin_amdgcn_fdot2_f32_bf16(__builtin_bit_cast(bf16x2, a), __builtin_bit_cast(bf16x2, b), c, false);
}
DI v4u tr_frag(LAS char* L, int base, int stride) {
  v2u lo = __builtin_bit_cast(v2u, __builtin_amdgcn_ds_read_tr16_b64_v4i16((LAS s16x4*)(L + base)));
  v2u hi = __builtin_bit_cast(v2u, __builtin_amdgcn_ds_read_tr16_b64_v4i16((LAS s16x4*)(L + base + 4 * stride)));
  return v4u{lo.x, lo.y, hi.x, hi.y};
}
DI float sum8(v4u o) {
  const u32 one = 0x3f803f80u;
  return dot2(o.x, one, dot2(o.y, one, 0.f)) + dot2(o.z, one, dot2(o.w, one, 0.f));
}

constexpr int PREP_JOBS = 112 + 112 + 64 + 16;
__global__ __launch_bounds__(256) void k_prep(const u16* __restrict__ x0, const u16* __restrict__ wqkv, const u16* __restrict__ wphi,
                                              const u16* __restrict__ wo, const u16* __restrict__ wg, const u16* __restrict__ wu,
                                              const u16* __restrict__ wd, char* __restrict__ ws) {
  __shared__ __attribute__((aligned(16))) char sm[64 * 464 + 4096];
  LAS char* L = (LAS char*)sm;
  const int b = blockIdx.x, tid = threadIdx.x, lane = tid & 63, w = __builtin_amdgcn_readfirstlane(tid >> 6);
  const int h = lane >> 5, q = (lane & 15) >> 2, c8 = ((lane >> 4) & 1) * 4 + (lane & 3);
  const int hh = c8 & 1, quad = c8 >> 1;
  if (b < 42 + 64 + 14 + 56) {
    const u16* src; int ldn;
    int kind, a0;
    if (b < 42) { kind = 0; a0 = b % 14; const int wh = b / 14; src = wqkv + (size_t)a0 * 32 * (3 * D) + (wh == 0 ? D : (wh == 1 ? 2 * D : 0)); ldn = 3 * D; }
    else if (b < 106) { kind = 1; a0 = b - 42; src = x0 + (size_t)a0 * 32 * D; ldn = D; }
    else if (b < 120) { kind = 2; a0 = b - 106; src = wo + (size_t)a0 * 32 * D; ldn = D; }
    else { kind = 3; a0 = b - 120; src = wd + (size_t)a0 * 32 * D; ldn = D; }
    constexpr int ST = 912;
    v4u t[7];
#pragma unroll
    for (int i = 0; i < 7; ++i) { const int p = tid + 256 * i, r = p / 56, c = p % 56; t[i] = *(const v4u*)(src + (size_t)r * ldn + c * 8); }
#pragma unroll
    for (int i = 0; i < 7; ++i) { const int p = tid + 256 * i, r = p / 56, c = p % 56; *(LAS v4u*)(L + r * ST + c * 16) = t[i]; }
    __syncthreads();
    if (kind == 1) {
#pragma unroll
      for (int i = 0; i < 7; ++i) {
        const int ks = w * 7 + i;
        const v4u v = *(LAS v4u*)(L + (lane & 31) * ST + ks * 32 + h * 16);
        *(v4u*)(ws + OFF_XIN + (a0 * 28 + ks) * 1024 + lane * 16) = v;
      }
      const int tk = tid & 31, cb = tid >> 5;
      float s[2] = {0.f, 0.f}, sq[2] = {0.f, 0.f};
#pragma unroll
      for (int oi = 0; oi < 7; ++oi) {
        const v4u v = *(LAS v4u*)(L + tk * ST + (cb * 7 + oi) * 16);
        s[oi >> 2] += sum8(v);
        sq[oi >> 2] += dot2(v.x, v.x, dot2(v.y, v.y, 0.f)) + dot2(v.z, v.z, dot2(v.w, v.w, 0.f));
      }
      *(f32x2*)(ws + OFF_STIN + ((a0 * 8 + cb) * 2 + 0) * 256 + tk * 8) = f32x2{s[0], sq[0]};
      *(f32x2*)(ws + OFF_STIN + ((a0 * 8 + cb) * 2 + 1) * 256 + tk * 8) = f32x2{s[1], sq[1]};
    } else if (kind == 0) {
      const int wh = b / 14;
#pragma unroll
      for (int i = 0; i < 4; ++i) {
        const int hd = w * 4 + i;
        const int cc = c8 < 7 ? c8 : 6;
        float cs = 0.f;
#pragma unroll
        for (int k2 = 0; k2 < 2; ++k2) {
          v4u o = tr_frag(L, (16 * k2 + 8 * h + q) * ST + (28 * hd + 4 * cc) * 2, ST);
          if ((lane & 31) >= 28) o = v4u{0u, 0u, 0u, 0u};
          cs += sum8(o);
          const int ks = a0 * 2 + k2;
          int pos;
          if (wh == 0) pos = ks < LAG ? ks : LAG + 2 * (ks - LAG);
          else if (wh == 1) pos = ks < 28 - LAG ? LAG + 1 + 2 * ks : 28 + ks;
          else pos = 56 + ks;
          *(v4u*)(ws + OFF_W1 + hd * 3 * UNIT + pos * 1024 + lane * 16) = o;
        }
        cs = xsum(cs);
        if (lane < 32) *(float*)(ws + OFF_CS1 + ((a0 * 16 + hd) * 3 + wh) * 128 + lane * 4) = cs;
      }
    } else {
#pragma unroll
      for (int i = 0; i < 4; ++i) {
        const int cb = w * 2 + (i >> 1), nt = i & 1;
        const int oi = nt * 4 + (quad >> 1) * 2 + hh;
        const int ro = (lane >> 4) & 1, rh = (lane >> 2) & 1;
        const bool valid = nt * 4 + ro * 2 + rh < 7;
#pragma unroll
        for (int k2 = 0; k2 < 2; ++k2) {
          v4u o = tr_frag(L, (16 * k2 + 8 * h + q) * ST + (56 * cb + 8 * (oi < 7 ? oi : 6) + 4 * (quad & 1)) * 2, ST);
          if (!valid) o = v4u{0u, 0u, 0u, 0u};
          const int ks = a0 * 2 + k2;
          int off;
          if (kind == 2) off = OFF_W2 + cb * 2 * UNIT + (((ks % 14) * 2 + ks / 14) * 2 + nt) * 1024;
          else {
            const int jp = ks / 7, s = ks % 7;
            int fr;
            if (s == 6) fr = 80 + jp * 2 + nt;
            else {
              const int o_ = 16 * s + jp, blk = o_ / 48, r = o_ % 48;
              const int c = r < 40 ? (r & 7) : ((r - 40) & 3), tp = r < 40 ? (r >> 3) : 5 + ((r - 40) >> 2);
              if (c < 4) fr = 112 + (c * 14 + blk * 7 + tp) * 2 + nt;
              else fr = ((blk * 5 + tp) * 4 + (c - 4)) * 2 + nt;
            }
            off = OFF_W4 + cb * 8 * UNIT + fr * 1024;
          }
          *(v4u*)(ws + off + lane * 16) = o;
        }
      }
    }
  } else if (b < 288) {
    const int a = b - 176, j = a / 7, kc = a % 7;
    constexpr int ST = 464;
    v4u t[7];
#pragma unroll
    for (int i = 0; i < 7; ++i) {
      const int p = tid + 256 * i, r = p / 28, c = p % 28;
      t[i] = *(const v4u*)((c < 14 ? wg : wu) + (size_t)(kc * 64 + r) * FF + 112 * j + (c < 14 ? c : c - 14) * 8);
    }
#pragma unroll
    for (int i = 0; i < 7; ++i) { const int p = tid + 256 * i, r = p / 28, c = p % 28; *(LAS v4u*)(L + r * ST + c * 16) = t[i]; }
    __syncthreads();
    LAS float* red = (LAS float*)(L + 64 * 464);
    const int ks = kc * 4 + w;
#pragma unroll
    for (int n = 0; n < 7; ++n) {
      const v4u o = tr_frag(L, (16 * w + 8 * h + q) * ST + (quad & 1) * 224 + (16 * n + 8 * hh + 4 * (quad >> 1)) * 2, ST);
      const int off = OFF_W3 + j * 7 * UNIT + (n < 2 ? (ks * 2 + n) * 1024 : n * UNIT + ks * 1024);
      *(v4u*)(ws + off + lane * 16) = o;
      const float cs = xsum(sum8(o));
      if (lane < 32) red[(w * 7 + n) * 32 + lane] = cs;
    }
    __syncthreads();
    if (tid < 224) *(float*)(ws + OFF_CS3 + (kc * 16 + j) * 896 + tid * 4) = (red[tid] + red[224 + tid]) + (red[448 + tid] + red[672 + tid]);
  } else {
    const int hd = b - 288;
    if (tid < 128) {
      const int s = tid >> 6, f = lane & 31;
      u32 o[4];
#pragma unroll
      for (int jj = 0; jj < 8; jj += 2) {
        const int d0 = 16 * s + 8 * (jj >> 2) + 4 * h + (jj & 3);
        const u32 lo = (d0 < 28 && f < 28) ? wphi[(hd * 28 + d0) * 28 + f] : 0u;
        const u32 hi = (d0 + 1 < 28 && f < 28) ? wphi[(hd * 28 + d0 + 1) * 28 + f] : 0u;
        o[jj >> 1] = lo | (hi << 16);
      }
      *(v4u*)(ws + OFF_WPHI + hd * 2048 + tid * 16) = v4u{o[0], o[1], o[2], o[3]};
    }
    if (hd == 0) *(u32*)(ws + OFF_CNT + tid * 256) = 0u;
    if (hd == 1 || hd == 2) *(u32*)(ws + OFF_FLG + ((hd - 1) * 256 + tid) * 128) = 0u;
  }
}

DI bool spin_more(char* ws, u32& spins) {
  u32* e = (u32*)(ws + OFF_CNT + CNT_ERR * 256);
  __builtin_amdgcn_s_sleep(1);
  ++spins;
  if ((spins & 255u) == 0u) {
    if (__builtin_amdgcn_readfirstlane(__hip_atomic_load(e, __ATOMIC_RELAXED, __HIP_MEMORY_SCOPE_AGENT)) != 0) return false;
    if (spins > 400000u) { __hip_atomic_store(e, 1u, __ATOMIC_RELAXED, __HIP_MEMORY_SCOPE_AGENT); return false; }
  }
  return true;
}
DI void wait_cnt(char* ws, int idx, u32 target) {
  u32* c = (u32*)(ws + OFF_CNT + idx * 256);
  u32 spins = 0;
  while ((u32)__builtin_amdgcn_readfirstlane(__hip_atomic_load(c, __ATOMIC_RELAXED, __HIP_MEMORY_SCOPE_AGENT)) < target)
    if (!spin_more(ws, spins)) break;
  __builtin_amdgcn_fence(__ATOMIC_ACQUIRE, "wavefront");
}
DI void wait_flags(char* ws, u32* fp, u32 f) {
  u32 spins = 0;
  while (!__all(f >= 16u)) {
    if (!spin_more(ws, spins)) break;
    f = __hip_atomic_load(fp, __ATOMIC_RELAXED, __HIP_MEMORY_SCOPE_AGENT);
  }
  __builtin_amdgcn_fence(__ATOMIC_ACQUIRE, "wavefront");
  asm volatile("" ::: "memory");
}
DI void signal_cnt(char* ws, int idx) {
  __hip_atomic_fetch_add((u32*)(ws + OFF_CNT + idx * 256), 1u, __ATOMIC_RELAXED, __HIP_MEMORY_SCOPE_AGENT);
}
DI void arrive_signal(LAS char* LD, int slot, u32 n, char* ws, int idx, int lane) {
  VMCNT(0);
  if (lane == 0) {
    const u32 old = __hip_atomic_fetch_add((LAS u32*)(LD + L_ARR + slot * 4), 1u, __ATOMIC_RELAXED, __HIP_MEMORY_SCOPE_WORKGROUP);
    if ((old & (n - 1)) == n - 1) signal_cnt(ws, idx);
  }
}
DI void wait_arr(LAS char* LD, int slot, u32 target) {
  while ((u32)__builtin_amdgcn_readfirstlane(__hip_atomic_load((LAS u32*)(LD + L_ARR + slot * 4), __ATOMIC_RELAXED, __HIP_MEMORY_SCOPE_WORKGROUP)) < target)
    __builtin_amdgcn_s_sleep(1);
}

#define STAMP(i)
#define USTAMP(i)

DI void dma_unit(const char* gsrc  , u32 voff  , u32 lds_dst  ) {
  u32 keep;
  asm volatile(
      "s_mov_b32 %0, m0\n\t"
      "s_mov_b32 m0, %3\n\t"
      "s_nop 0\n\t"
      "global_load_lds_dwordx4 %1, %2\n\t"
      "s_add_u32 m0, m0, 0x1000\n\t"
      "v_add_u32 %1, 0x1000, %1\n\t"
      "global_load_lds_dwordx4 %1, %2\n\t"
      "s_add_u32 m0, m0, 0x1000\n\t"
      "v_add_u32 %1, 0x1000, %1\n\t"
      "global_load_lds_dwordx4 %1, %2\n\t"
      "s_add_u32 m0, m0, 0x1000\n\t"
      "v_add_u32 %1, 0x1000, %1\n\t"
      "global_load_lds_dwordx4 %1, %2\n\t"
      "s_add_u32 m0, m0, 0x1000\n\t"
      "v_add_u32 %1, 0x1000, %1\n\t"
      "global_load_lds_dwordx4 %1, %2\n\t"
      "s_add_u32 m0, m0, 0x1000\n\t"
      "v_add_u32 %1, 0x1000, %1\n\t"
      "global_load_lds_dwordx4 %1, %2\n\t"
      "s_add_u32 m0, m0, 0x1000\n\t"
      "v_add_u32 %1, 0x1000, %1\n\t"
      "global_load_lds_dwordx4 %1, %2\n\t"
      "s_mov_b32 m0, %0"
      : "=&s"(keep), "+v"(voff)
      : "s"(gsrc), "s"(lds_dst)
      : "memory", "scc");
}

template <typename F, int... I>
DI void static_for_impl(F&& f, std::integer_sequence<int, I...>) { (f(IC<I>{}), ...); }
template <int N, typename F>
DI void static_for(F&& f) { static_for_impl(f, std::make_integer_sequence<int, N>{}); }

constexpr int PF = 6;
template <int N, typename RD, typename PRE, typename ST>
DI void pipe(RD rd, PRE pre, ST step) {
  v4u buf[PF];
  static_for<PF>([&](auto I) AI { buf[I.value] = rd(I); });
  SB();
  static_for<N>([&](auto I) AI {
    constexpr int i = I.value;
    pre(I);
    const v4u a = buf[i % PF];
    if constexpr (i + PF < N) buf[i % PF] = rd(IC<i + PF>{});
    step(I, a);
    SB();
  });
}

DI void ln_stats(const v2u (&st)[8], float& mean, float& rstd) {
  f32x2 a = __builtin_bit_cast(f32x2, st[0]), b = __builtin_bit_cast(f32x2, st[1]);
#pragma unroll
  for (int i = 2; i < 8; i += 2) { a += __builtin_bit_cast(f32x2, st[i]); b += __builtin_bit_cast(f32x2, st[i + 1]); }
  a += b;
  const float s = xsum(a[0]), q = xsum(a[1]);
  mean = s * (1.f / 448.f);
  const float var = fmaxf(q * (1.f / 448.f) - mean * mean, 0.f);
  rstd = __builtin_amdgcn_rsqf(var + 1e-5f);
}
DI v4u pack8(const f32x16& a, int s) {
  v4u r = {pk2(a[8 * s], a[8 * s + 1]), pk2(a[8 * s + 2], a[8 * s + 3]), pk2(a[8 * s + 4], a[8 * s + 5]), pk2(a[8 * s + 6], a[8 * s + 7])};
  return r;
}
DI void dual_softmax(const f32x16& p, int h, float rstd, f32x16& pp, f32x16& pn) {
  float mx = p[0], mn = p[0];
#pragma unroll
  for (int r = 1; r < 12; ++r) { mx = fmaxf(mx, p[r]); mn = fminf(mn, p[r]); }
#pragma unroll
  for (int r = 12; r < 16; ++r) { const float v = h ? p[0] : p[r]; mx = fmaxf(mx, v); mn = fminf(mn, v); }
  mx = xmax(mx); mn = xmin(mn);
  const float c = rstd * LOG2E;
  const float mxl = mx * c, mnl = mn * c;
  float sp = 0.f, sn = 0.f;
#pragma unroll
  for (int r = 0; r < 16; ++r) {
    float ep = __builtin_amdgcn_exp2f(p[r] * c - mxl);
    float en = __builtin_amdgcn_exp2f(mnl - p[r] * c);
    if (r >= 12) { ep = h ? 0.f : ep; en = h ? 0.f : en; }
    pp[r] = ep; pn[r] = en; sp += ep; sn += en;
  }
  sp = xsum(sp); sn = xsum(sn);
  const float rp = __builtin_amdgcn_rcpf(sp), rn = __builtin_amdgcn_rcpf(sn);
#pragma unroll
  for (int r = 0; r < 16; ++r) { pp[r] *= rp; pn[r] *= rn; }
}

DI void finish_tile(const f32x16& acc, v4u (&res)[2], rsrc_t rs, int xoff, int stoff, int tt, int cb, int nt, int lane, bool last, u16* out) {
  lane = opqv(lane);
  const int h = lane >> 5, tok = lane & 31;
#pragma unroll
  for (int ro = 0; ro < 2; ++ro) {
    v4u r = res[ro];
    v4u o;
    o.x = pk2(bflo(r.x) + acc[8 * ro + 0], bfhi(r.x) + acc[8 * ro + 1]);
    o.y = pk2(bflo(r.y) + acc[8 * ro + 2], bfhi(r.y) + acc[8 * ro + 3]);
    o.z = pk2(bflo(r.z) + acc[8 * ro + 4], bfhi(r.z) + acc[8 * ro + 5]);
    o.w = pk2(bflo(r.w) + acc[8 * ro + 6], bfhi(r.w) + acc[8 * ro + 7]);
    res[ro] = o;
    const int oi = nt * 4 + ro * 2 + h;
    if (oi < 7) {
      const int go = 7 * cb + oi;
      if (last) *(v4u*)(out + (size_t)(tt * 32 + tok) * D + go * 8) = o;
      else st16(rs, ((go >> 1) * 64 + tok + 32 * (go & 1)) * 16, xoff + tt * 28 * 1024, o);
    }
  }
  if (!last) {
    const bool v1 = !(nt == 1 && h == 1);
    const u32 one = 0x3f803f80u;
    float s0 = dot2(res[0].x, one, 0.f), s1 = dot2(res[1].x, one, 0.f), q0 = dot2(res[0].x, res[0].x, 0.f), q1 = dot2(res[1].x, res[1].x, 0.f);
    s0 = dot2(res[0].y, one, s0); s1 = dot2(res[1].y, one, s1); q0 = dot2(res[0].y, res[0].y, q0); q1 = dot2(res[1].y, res[1].y, q1);
    s0 = dot2(res[0].z, one, s0); s1 = dot2(res[1].z, one, s1); q0 = dot2(res[0].z, res[0].z, q0); q1 = dot2(res[1].z, res[1].z, q1);
    s0 = dot2(res[0].w, one, s0); s1 = dot2(res[1].w, one, s1); q0 = dot2(res[0].w, res[0].w, q0); q1 = dot2(res[1].w, res[1].w, q1);
    const float sum = xsum(s0 + (v1 ? s1 : 0.f));
    const float sq = xsum(q0 + (v1 ? q1 : 0.f));
    if (h == 0) st8(rs, tok * 8, stoff + ((tt * 8 + cb) * 2 + nt) * 256, v2u{__float_as_uint(sum), __float_as_uint(sq)});
  }
}

DI void kv_reduce(rsrc_t rs, LAS char* LD, int L, int j, int w, int lane) {
  const int ko = opq(OFF_KVP + L * KVB + j * 16 * 4096 + (w >> 1) * 1024 + (w & 1) * 512);
  const int lz = opqv(lane);
  const int vo = (lz >> 5) * 4096 + (lz & 31) * 16;
  v4u t[8];
#pragma unroll
  for (int i = 0; i < 8; ++i) t[i] = ld16(rs, vo, ko + i * 8192);
  float s[8] = {};
  const u32 lo1 = (u32)opq(0x00003f80), hi1 = (u32)opq(0x3f800000);
#pragma unroll
  for (int i = 0; i < 8; ++i) {
#pragma unroll
    for (int c = 0; c < 4; ++c) { s[2 * c] = dot2(t[i][c], lo1, s[2 * c]); s[2 * c + 1] = dot2(t[i][c], hi1, s[2 * c + 1]); }
  }
#pragma unroll
  for (int c = 0; c < 8; ++c) s[c] = xsum(s[c]);
  if (lz < 32) *(LAS v4u*)(LD + L_KVF + (w >> 1) * 1024 + (w & 1) * 512 + lz * 16) = v4u{pk2(s[0], s[1]), pk2(s[2], s[3]), pk2(s[4], s[5]), pk2(s[6], s[7])};
}

DI int p4_part(int a, int i) {
  return a == 0 ? i * 4096 : a == 1 ? UNIT + i * 4096 : a == 2 ? L_SCR + i * 4096 : (i < 6 ? 2 * UNIT + i * 4096 : L_SCR + UNIT);
}
DI void p4_put(const f32x16& acc, int a, int w, LAS char* LD, int l16, f32x16& keep) {
  if (w == a) { keep = acc; return; }
  const int off = opqv(p4_part(a, w - (w > a ? 1 : 0)) + opqv(l16));
#pragma unroll
  for (int qd = 0; qd < 4; ++qd) *(LAS f32x4*)(LD + off + qd * 1024) = f32x4{acc[4 * qd], acc[4 * qd + 1], acc[4 * qd + 2], acc[4 * qd + 3]};
}
DI int p4_koff(int blk, int tp, int c) {
  const int o = 48 * blk + (tp < 5 ? 8 * tp + c : 40 + 4 * (tp - 5) + c);
  return (7 * (o & 15) + (o >> 4)) * 1024;
}

constexpr int p4_lvl(int blk, int tp) { return blk == 0 ? 0 : (tp < 4 ? 1 : 2); }

__global__ __launch_bounds__(512, 2) void k_main(char* __restrict__ ws, u16* __restrict__ out) {
  extern __shared__ __attribute__((aligned(16))) char smem[];
  LAS char* LD = (LAS char*)smem;
  const int b = blockIdx.x, xcd = b & 7, bi = b >> 3;
  const int g = (xcd >> 2) * 8 + (bi >> 2), j = (xcd & 3) * 4 + (bi & 3);
  const int rb = xcd * 4 + (bi >> 3), cb = bi & 7, g2 = rb >> 1;
  const int lane = threadIdx.x & 63, w = __builtin_amdgcn_readfirstlane(threadIdx.x >> 6);
  const int h = lane >> 5, tok = lane & 31;
  const rsrc_t rs = __builtin_amdgcn_make_buffer_rsrc((void*)ws, 0, WS_USED, 0x00020000);
  const u32 lbase = (u32)(size_t)smem;
  const int l16 = lane * 16;

  if (w >= 4) {
    const int hw = w - 4;
    const u32 voff = hw * 1024 + lane * 16;
    const char* w1 = ws + OFF_W1 + j * 3 * UNIT;
    const char* w2 = ws + OFF_W2 + cb * 2 * UNIT;
    const char* w3 = ws + OFF_W3 + j * 7 * UNIT;
    const char* w4 = ws + OFF_W4 + cb * 8 * UNIT;
#define DMA(src, slot) dma_unit((src), voff, lbase + (slot) * UNIT + hw * 1024)
    v4u wr[28];
    for (int L = 0; L < 2; ++L) {
      if (L > 0) wait_arr(LD, 0, 12);
      DMA(w1, 4);
      VMCNT(0); BAR();
      DMA(w1 + UNIT, 0);
      VMCNT(0); BAR();
      BAR();
      DMA(w1 + 2 * UNIT, 1); DMA(w2, 2); DMA(w2 + UNIT, 3);
      VMCNT(14); BAR();
      if (hw < 2) {
        __builtin_amdgcn_s_setprio(2);
        f32x16 kp = {};
        const int lz = opqv(lane);
        const int g16 = (lz >> 4) & 1, q = (lz & 15) >> 2, p = lz & 3, hz = lz >> 5;
        const int ab0 = opqv(L_PHK + (8 * hz + q) * PHK_STRIDE + (g16 * 16 + 4 * p) * 2 + hw * 64);
        const int bb0 = opqv(L_V + (8 * hz + q) * V_STRIDE + (g16 * 16 + 4 * p) * 2);
        v4u ap[8], bv[8];
#pragma unroll
        for (int s = 0; s < 8; ++s) { ap[s] = tr_frag(LD, ab0 + 16 * s * PHK_STRIDE, PHK_STRIDE); bv[s] = tr_frag(LD, bb0 + 16 * s * V_STRIDE, V_STRIDE); }
        SB();
#pragma unroll
        for (int s = 0; s < 8; ++s) kp = mfma(ap[s], bv[s], kp);
        const int ko = opq(OFF_KVP + L * KVB + (j * 16 + g) * 4096 + hw * 2048);
        st16(rs, l16, ko, pack8(kp, 0));
        st16(rs, l16, ko + 1024, pack8(kp, 1));
        arrive_signal(LD, 1, 2, ws, C_KV * 32 + L * 16 + j, lane);
        __builtin_amdgcn_s_setprio(0);
      }
      DMA(w3, 0);
      BAR();
      kv_reduce(rs, LD, L, j, w, lane);
      BAR();
      DMA(w3 + UNIT, 1);
      BAR();
      DMA(w3 + 2 * UNIT, 2);
      VMCNT(7); BAR();
      const int wo4 = opq(OFF_W4 + cb * 8 * UNIT + 4 * UNIT + hw * UNIT);
#define WR(lo, hi) _Pragma("unroll") for (int i = lo; i < hi; ++i) wr[i] = ld16p(rs, l16, wo4 + i * 1024)
      wait_arr(LD, 2, 4 * L + 4);
      DMA(w3 + 3 * UNIT, 3);
      VMCNT(7); BAR();
      DMA(w3 + 4 * UNIT, 0);
      VMCNT(12); BAR();
      if (L == 0) {
        DMA(w3 + 5 * UNIT, 1); WR(0, 7);
        VMCNT(19); BAR();
        DMA(w3 + 6 * UNIT, 2); WR(7, 14);
        VMCNT(26); BAR();
        DMA(w4 + 3 * UNIT, 3); WR(14, 21);
        VMCNT(26); BAR();
        DMA(w4, 0); WR(21, 28);
        VMCNT(35); BAR();
      } else {
        DMA(w3 + 5 * UNIT, 1);
        VMCNT(12); BAR();
        DMA(w3 + 6 * UNIT, 2);
        VMCNT(12); BAR();
        DMA(w4 + 3 * UNIT, 3);
        VMCNT(12); BAR();
        DMA(w4, 0);
        VMCNT(14); BAR();
      }
      DMA(w4 + UNIT, 1);
      VMCNT(0); BAR();
      DMA(w4 + 2 * UNIT, 2);
      {
        u32* fp = (u32*)(ws + OFF_FLG + ((L * 64 + 2 * rb + (lane & 1)) * 4) * 128);
        u32 f = __hip_atomic_load(fp, __ATOMIC_RELAXED, __HIP_MEMORY_SCOPE_AGENT);
        wait_flags(ws, fp, f);
        const int ho = opq(OFF_H + L * HB + 2 * rb * 112 * 1024);
        const int hc = opq(hw);
        constexpr int HW = 8;
        v4u hx[28];
        auto ldh = [&](auto I) AI { constexpr int i = I.value, t = i >> 1; return ld16(rs, l16, ho + (i & 1) * 112 * 1024 + p4_koff(t / 7, t % 7, hc)); };
        auto lvl = [](int i) constexpr { return p4_lvl((i >> 1) / 7, (i >> 1) % 7); };
        static_for<HW>([&](auto I) AI { hx[I.value] = ldh(I); });
        f32x16 acc[4] = {};
        f32x16 keep;
        static_for<28>([&](auto I) AI {
          constexpr int i = I.value, t = i >> 1, tq = i & 1, nx = i + HW;
          if constexpr (i == 8) RAWBAR();
          if constexpr (i == 18) RAWBAR();
          acc[tq] = mfma(wr[2 * t], hx[i], acc[tq]);
          acc[2 + tq] = mfma(wr[2 * t + 1], hx[i], acc[2 + tq]);
          if constexpr (nx + 3 < 28 && lvl(nx + 3) > lvl(nx + 2)) f = __hip_atomic_load(fp + 32 * lvl(nx + 3), __ATOMIC_RELAXED, __HIP_MEMORY_SCOPE_AGENT);
          if constexpr (nx < 28) {
            if constexpr (lvl(nx) > lvl(nx - 1)) wait_flags(ws, fp + 32 * lvl(nx), f);
            hx[nx] = ldh(IC<nx>{});
          }
          SB();
        });
        p4_put(acc[0], 0, w, LD, l16, keep);
        p4_put(acc[2], 2, w, LD, l16, keep);
        p4_put(acc[1], 1, w, LD, l16, keep);
        p4_put(acc[3], 3, w, LD, l16, keep);
        BAR();
      }
    }
    return;
  }

  {
    const int t = threadIdx.x;
    if (t < 224) {
      float a[7];
#pragma unroll
      for (int kc = 0; kc < 7; ++kc) a[kc] = *(const float*)(ws + OFF_CS3 + (kc * 16 + j) * 896 + t * 4);
      *(LAS float*)(LD + L_CS3 + t * 4) = ((a[0] + a[1]) + (a[2] + a[3])) + ((a[4] + a[5]) + a[6]);
    }
    if (t < 96) {
      float a[14];
#pragma unroll
      for (int kc = 0; kc < 14; ++kc) a[kc] = *(const float*)(ws + OFF_CS1 + (kc * 16 + j) * 384 + t * 4);
      float sa = 0.f, sb = 0.f;
#pragma unroll
      for (int kc = 0; kc < 7; ++kc) { sa += a[2 * kc]; sb += a[2 * kc + 1]; }
      *(LAS float*)(LD + L_CS1 + t * 4) = sa + sb;
    } else if (t >= 128) *(LAS v4u*)(LD + L_WPHI + (t - 128) * 16) = ld16p(rs, (t - 128) * 16, OFF_WPHI + j * 2048);
    if (t == 32) *(LAS v4u*)(LD + L_ARR) = v4u{0u, 0u, 0u, 0u};
  }
  const int tt = 4 * g + w;
  const int tt2 = 2 * rb + (w & 1);
  const int kh = w >> 1;
  v4u res[2];
#pragma unroll
  for (int ro = 0; ro < 2; ++ro) {
    int oi = kh * 4 + ro * 2 + h; if (oi > 6) oi = 6;
    const int go = 7 * cb + oi;
    res[ro] = ld16p(rs, ((go >> 1) * 64 + tok + 32 * (go & 1)) * 16, OFF_XIN + tt2 * 28 * 1024);
  }
  constexpr int XW = 14;

  for (int L = 0; L < 2; ++L) {
    v4u x[28];
    v2u st[8];
    float mean, rstd;
    v4u pq[4];
    STAMP(0);
    {
      const int xo = opq(OFF_XIN + L * XB + tt * 28 * 1024);
      const int so_ = opq(OFF_STIN + L * STB + tt * 4096);
      const int sv_ = (opqv(lane) & 31) * 8 + h * 2048;
#pragma unroll
      for (int ks = 0; ks < XW; ++ks) x[ks] = ld16(rs, l16, xo + ks * 1024);
      BAR();
      f32x16 ak = {}, av = {}, p = {};
      const int sb0 = opqv(l16), sb4 = opqv(opqv(l16) + 4 * UNIT);
      const int csb = opqv(L_CS1 + 16 * (opqv(lane) >> 5)), wb = opqv(L_WPHI + opqv(l16));
      f32x4 cs[4]; v4u kf[2], wf[2];
      float nmean;
      pipe<56>([&](auto I) AI { return lds16(LD, (I.value < 28 ? sb4 : sb0) + (I.value % 28) * 1024); },
               [&](auto I) AI { if (I.value == 28 - PF || I.value == 30) RAWBAR();   },
               [&](auto I, v4u a) AI {
                 constexpr int i = I.value;
                 constexpr bool isk = i < LAG || (i < 56 - LAG && ((i - LAG) & 1) == 0);
                 constexpr int ks = i < LAG ? i : (i < 56 - LAG ? (isk ? LAG + (i - LAG) / 2 : (i - LAG) / 2) : i - 28);
                 if constexpr (isk) ak = mfma(a, x[ks], ak); else av = mfma(a, x[ks], av);
                 if constexpr (isk && ks + XW < 28) x[ks + XW] = ld16(rs, l16, xo + (ks + XW) * 1024);
                 if constexpr (!isk && ks < 8) st[ks] = ld8(rs, sv_ + ks * 256, so_);
                 if constexpr (i == 39) { ln_stats(st, mean, rstd); nmean = -mean; }
                 if constexpr (i >= 40 && i < 44) cs[i - 40] = *(LAS f32x4*)(LD + csb + 32 * (i - 40));
                 if constexpr (i == 44) wf[0] = lds16(LD, wb);
                 if constexpr (i == 45) wf[1] = lds16(LD, wb + 1024);
                 if constexpr (i >= 47 && i < 51) {
                   constexpr int qd = i - 47;
                   const float y0 = fmaf(nmean, cs[qd][0], ak[4 * qd]), y1 = fmaf(nmean, cs[qd][1], ak[4 * qd + 1]);
                   const float y2 = fmaf(nmean, cs[qd][2], ak[4 * qd + 2]), y3 = fmaf(nmean, cs[qd][3], ak[4 * qd + 3]);
                   kf[qd >> 1][2 * (qd & 1)] = pk2(y0, y1);
                   kf[qd >> 1][2 * (qd & 1) + 1] = pk2(y2, y3);
                 }
                 if constexpr (i == 49) p = mfma(wf[0], kf[0], p);
                 if constexpr (i == 51) p = mfma(wf[1], kf[1], p);
                 if constexpr (i >= 51 && i < 55) cs[i - 51] = *(LAS f32x4*)(LD + csb + 128 + 32 * (i - 51));
               });
      STAMP(1);
      f32x16 pp, pn;
      dual_softmax(p, h, rstd, pp, pn);
#pragma unroll
      for (int r = 0; r < 16; ++r) av[r] = fmaf(nmean, cs[r >> 2][r & 3], av[r]) * rstd;
      if (h) { av[12] = 1.f; av[13] = 0.f; av[14] = 0.f; av[15] = 0.f; }
      const int lz = opqv(lane);
      const int trow = w * 32 + (lz & 31);
      const int pb = opqv(L_PHK + trow * PHK_STRIDE + 8 * (lz >> 5)), vb = opqv(L_V + trow * V_STRIDE + 8 * (lz >> 5));
#pragma unroll
      for (int qd = 0; qd < 4; ++qd) {
        v2u a = {pk2(pp[4 * qd], pp[4 * qd + 1]), pk2(pp[4 * qd + 2], pp[4 * qd + 3])};
        v2u c = {pk2(pn[4 * qd], pn[4 * qd + 1]), pk2(pn[4 * qd + 2], pn[4 * qd + 3])};
        v2u v = {pk2(av[4 * qd], av[4 * qd + 1]), pk2(av[4 * qd + 2], av[4 * qd + 3])};
        *(LAS v2u*)(LD + pb + 16 * qd) = a;
        *(LAS v2u*)(LD + pb + 64 + 16 * qd) = c;
        *(LAS v2u*)(LD + vb + 16 * qd) = v;
      }
      STAMP(2);
      BAR();
      f32x16 aq = {};
      const int sb2 = opqv(opqv(l16) + 1 * UNIT);
      pipe<28>([&](auto I) AI { return lds16(LD, sb2 + I.value * 1024); }, [&](auto) AI {},
               [&](auto I, v4u a) AI {
                 constexpr int i = I.value;
                 aq = mfma(a, x[i], aq);
                 if constexpr (i >= 20 && i < 24) cs[i - 20] = *(LAS f32x4*)(LD + csb + 256 + 32 * (i - 20));
               });
#pragma unroll
      for (int r = 0; r < 16; ++r) aq[r] = fmaf(nmean, cs[r >> 2][r & 3], aq[r]);
      f32x16 p2 = {};
      p2 = mfma(wf[0], pack8(aq, 0), p2);
      p2 = mfma(wf[1], pack8(aq, 1), p2);
      dual_softmax(p2, h, rstd, pp, pn);
      pq[0] = pack8(pp, 0); pq[1] = pack8(pp, 1); pq[2] = pack8(pn, 0); pq[3] = pack8(pn, 1);
    }
    STAMP(3);
    if (w == 0) wait_cnt(ws, C_KV * 32 + L * 16 + j, 16);
    BAR();
    STAMP(4);
    kv_reduce(rs, LD, L, j, w, lane);
    BAR();
    STAMP(5);
    {
      f32x16 nm = {};
      const int kb = opqv(L_KVF + opqv(l16));
      v4u kf[4];
#pragma unroll
      for (int s = 0; s < 4; ++s) kf[s] = lds16(LD, kb + s * 1024);
#pragma unroll
      for (int s = 0; s < 4; ++s) nm = mfma(kf[s], pq[s], nm);
      auto r = __builtin_amdgcn_permlane32_swap(__float_as_uint(nm[12]), __float_as_uint(nm[12]), false, false);
      const float den = __uint_as_float(r[1]) + 1e-6f;
      const float rd = __builtin_amdgcn_rcpf(den);
      const int oo = opq(OFF_O + L * XB + tt * 28 * 1024);
      const int jo = opq(j);
#pragma unroll
      for (int qd = 0; qd < 4; ++qd) {
        v2u o = {pk2(nm[4 * qd] * rd, nm[4 * qd + 1] * rd), pk2(nm[4 * qd + 2] * rd, nm[4 * qd + 3] * rd)};
        const int k0 = 28 * jo + 8 * qd + 4 * h;
        if (qd < 3 || h == 0)
          st8(rs, ((k0 >> 4) * 64 + tok + 32 * ((k0 >> 3) & 1)) * 16 + 8 * ((k0 >> 2) & 1), oo, o);
      }
    }
    arrive_signal(LD, 0, 4, ws, C_O * 32 + L * 16 + g, lane);
    STAMP(6);
    wait_cnt(ws, C_O * 32 + L * 16 + g2, 16);
    STAMP(7);
    {
      const int xo = opq(OFF_O + L * XB + (tt2 * 28 + 14 * kh) * 1024);
#pragma unroll
      for (int t = 0; t < XW; ++t) x[t] = ld16(rs, l16, xo + t * 1024);
      f32x16 a0 = {}, a1 = {};
      const int sb3 = opqv(2 * UNIT + kh * 2048 + opqv(l16)), sb0 = opqv(3 * UNIT + kh * 2048 + opqv(l16));
      pipe<28>([&](auto I) AI { constexpr int i = I.value, t = i >> 1; return lds16(LD, (t < 7 ? sb3 : sb0) + (t % 7) * 4096 + (i & 1) * 1024); },
               [&](auto I) AI { if constexpr (I.value == 14) RAWBAR();   },
               [&](auto I, v4u a) AI {
                 constexpr int i = I.value;
                 if constexpr (i & 1) a1 = mfma(a, x[i >> 1], a1); else a0 = mfma(a, x[i >> 1], a0);
                 if constexpr (!(i & 1) && (i >> 1) + XW < 14) x[(i >> 1) + XW] = ld16(rs, l16, xo + ((i >> 1) + XW) * 1024);
               });
      const int xb = opqv(L_SCR + w * 4096 + opqv(l16)), xb2 = opqv(L_SCR + (w ^ 2) * 4096 + opqv(l16));
      f32x16 fin;
      if (kh) {
#pragma unroll
        for (int qd = 0; qd < 4; ++qd) *(LAS f32x4*)(LD + xb + qd * 1024) = f32x4{a0[4 * qd], a0[4 * qd + 1], a0[4 * qd + 2], a0[4 * qd + 3]};
        fin = a1;
      } else {
#pragma unroll
        for (int qd = 0; qd < 4; ++qd) *(LAS f32x4*)(LD + xb + qd * 1024) = f32x4{a1[4 * qd], a1[4 * qd + 1], a1[4 * qd + 2], a1[4 * qd + 3]};
        fin = a0;
      }
      BAR();
#pragma unroll
      for (int qd = 0; qd < 4; ++qd) {
        const f32x4 v = *(LAS f32x4*)(LD + xb2 + qd * 1024);
#pragma unroll
        for (int i = 0; i < 4; ++i) fin[4 * qd + i] += v[i];
      }
      finish_tile(fin, res, rs, opq(OFF_X1 + L * XB), opq(OFF_ST1 + L * STB), tt2, cb, kh, lane, false, out);
    }
    arrive_signal(LD, 0, 4, ws, C_X1 * 32 + L * 16 + g2, lane);
    STAMP(8);
    wait_cnt(ws, C_X1 * 32 + L * 16 + g, 16);
    STAMP(9);
    {
      const int xo = opq(OFF_X1 + L * XB + tt * 28 * 1024);
      const int so_ = opq(OFF_ST1 + L * STB + tt * 4096);
      const int sv_ = (opqv(lane) & 31) * 8 + h * 2048;
#pragma unroll
      for (int ks = 0; ks < XW; ++ks) x[ks] = ld16(rs, l16, xo + ks * 1024);
      if (lane == 0) __hip_atomic_fetch_add((LAS u32*)(LD + L_ARR + 8), 1u, __ATOMIC_RELAXED, __HIP_MEMORY_SCOPE_WORKGROUP);
      const int ho = opq(OFF_H + L * HB + (tt * 112 + 7 * j) * 1024);
      u32* fo = (u32*)(ws + OFF_FLG + ((L * 64 + tt) * 4) * 128);
      float nmean, gs, irs;
      const int csb = opqv(L_CS3 + 16 * (opqv(lane) >> 5));
      const int sbA = opqv(l16), sbB = opqv(opqv(l16) + 2 * UNIT);
      f32x4 Gcg[2][2], Gcu[2][2]; float Gt[2][8], Gyu[2][8]; u32 Go[2][4];
      auto glu = [&](const f32x16& a, auto N_, auto S_, auto G_) AI {
        constexpr int n = N_.value, s = S_.value, G = G_.value;
        if constexpr (s == 0) Gcu[G][0] = *(LAS f32x4*)(LD + csb + n * 128 + 32);
        if constexpr (s == 5) Gcg[G][1] = *(LAS f32x4*)(LD + csb + n * 128 + 64);
        if constexpr (s == 10) Gcu[G][1] = *(LAS f32x4*)(LD + csb + n * 128 + 96);
        if constexpr (s == 23 && G == 0 && n < 6) Gcg[0][0] = *(LAS f32x4*)(LD + csb + (n == 0 ? 2 : n + 1) * 128);
        static_for<8>([&](auto V_) AI {
          constexpr int v = V_.value, k = s - (2 + (5 * v) / 2), hf = v >> 2, i = v & 3;
          if constexpr (k == 0) { Gt[G][v] = fmaf(nmean, Gcg[G][hf][i], a[8 * hf + i]); PIN(Gt[G][v]); }
          if constexpr (k == 1) { Gt[G][v] = Gt[G][v] * gs; PIN(Gt[G][v]); }
          if constexpr (k == 2) { Gt[G][v] = __builtin_amdgcn_exp2f(Gt[G][v]); PIN(Gt[G][v]); }
          if constexpr (k == 3) { Gt[G][v] = fmaf(Gt[G][v], irs, irs); PIN(Gt[G][v]); }
          if constexpr (k == 4) { Gt[G][v] = __builtin_amdgcn_rcpf(Gt[G][v]); PIN(Gt[G][v]); }
          if constexpr (k == 5) { Gyu[G][v] = fmaf(nmean, Gcu[G][hf][i], a[8 * hf + 4 + i]); PIN(Gyu[G][v]); }
          if constexpr (k == 6) { Gt[G][v] = Gt[G][v] * Gyu[G][v]; PIN(Gt[G][v]); }
          if constexpr (k == 7 && (v & 1)) { Go[G][v >> 1] = pk2(Gt[G][v - 1], Gt[G][v]); PIN(Go[G][v >> 1]); }
        });
        if constexpr (s == 27) {
          if constexpr (n == 3 || n == 5) {
            VMCNT(0);
            if (lane == 0) __hip_atomic_fetch_add(fo + 32 * (n == 3 ? 0 : 1), 1u, __ATOMIC_RELAXED, __HIP_MEMORY_SCOPE_AGENT);
          }
          st16(rs, l16, ho + n * 1024, v4u{Go[G][0], Go[G][1], Go[G][2], Go[G][3]});
        }
      };
      f32x16 acc[7];
      pipe<196>(
          [&](auto I) AI { constexpr int i = I.value, u = i / 28, sl = u & 3; return lds16(LD, (sl < 2 ? sbA : sbB) + (sl & 1) * UNIT + (i % 28) * 1024); },
          [&](auto I) AI { if constexpr (I.value > 0 && I.value % 28 == 0) { USTAMP(18 + 2 * (I.value / 28)); RAWBAR(); USTAMP(19 + 2 * (I.value / 28)); } },
          [&](auto I, v4u a) AI {
            constexpr int i = I.value, u = i / 28, s = i % 28;
            if constexpr (u < 2) {
              constexpr int n = i & 1, ks = i >> 1;
              if constexpr (ks == 0) { f32x16 z = {}; acc[n] = mfma(a, x[ks], z); } else acc[n] = mfma(a, x[ks], acc[n]);
              if constexpr (n == 0 && ks + XW < 28) x[ks + XW] = ld16(rs, l16, xo + (ks + XW) * 1024);
              if constexpr (n == 1 && ks >= 8 && ks < 16) st[ks - 8] = ld8(rs, sv_ + (ks - 8) * 256, so_);
              if constexpr (i == 50) Gcg[0][0] = *(LAS f32x4*)(LD + csb);
              if constexpr (i == 51) Gcg[1][0] = *(LAS f32x4*)(LD + csb + 128);
              if constexpr (i == 53) {
                ln_stats(st, mean, rstd);
                nmean = -mean; gs = -LOG2E * rstd; irs = __builtin_amdgcn_rcpf(rstd);
              }
            } else {
              if constexpr (s == 0) { f32x16 z = {}; acc[u] = mfma(a, x[s], z); } else acc[u] = mfma(a, x[s], acc[u]);
              if constexpr (u == 2) { glu(acc[0], IC<0>{}, IC<s>{}, IC<0>{}); glu(acc[1], IC<1>{}, IC<s>{}, IC<1>{}); }
              else glu(acc[u - 1], IC<u - 1>{}, IC<s>{}, IC<0>{});
            }
          });
      RAWBAR();
      STAMP(10);
      static_for<28>([&](auto S_) AI { glu(acc[6], IC<6>{}, S_, IC<0>{}); });
      STAMP(11);
      u32* fp = (u32*)(ws + OFF_FLG + ((L * 64 + 2 * rb + (lane & 1)) * 4) * 128);
      u32 f = __hip_atomic_load(fp, __ATOMIC_RELAXED, __HIP_MEMORY_SCOPE_AGENT);
      const int wa0 = opqv(w * 2048 + opqv(l16)), wa1 = opqv(w * 2048 + opqv(l16) + 5 * 8192);
      const int wfb = opqv(L_FRESH + kh * 1024 + opqv(l16));
      v4u wq[20];
      auto ldw = [&](auto K) AI { constexpr int k = K.value, t = k >> 1; return lds16(LD, (t < 5 ? wa0 : wa1) + (t % 5) * 8192 + (k & 1) * 1024); };
      static_for<4>([&](auto K) AI { wq[K.value] = ldw(K); });
      VMCNT(0);
      if (lane == 0) __hip_atomic_fetch_add(fo + 64, 1u, __ATOMIC_RELAXED, __HIP_MEMORY_SCOPE_AGENT);
      wait_flags(ws, fp, f);
      STAMP(13);
      const int ho2 = opq(OFF_H + L * HB + 2 * rb * 112 * 1024);
      const int hc = opq(w + 4);
      const int hf_ = opq(OFF_H + L * HB + (tt2 * 112 + 6) * 1024);
      constexpr int HW = 8;
      v4u hx[36];
      auto ldh = [&](auto I) AI {
        constexpr int i = I.value, t = i >> 1;
        if constexpr (i < 20) return ld16(rs, l16, ho2 + (i & 1) * 112 * 1024 + p4_koff(t / 5, t % 5, hc));
        else return ld16(rs, l16, hf_ + (i - 20) * 7 * 1024);
      };
      auto lvl = [](int i) constexpr { return i < 20 ? p4_lvl((i >> 1) / 5, (i >> 1) % 5) : 2; };
      static_for<HW>([&](auto I) AI { hx[I.value] = ldh(I); });
      f32x16 pc[4] = {};
      f32x16 fin;
      v4u wfr[16];
      static_for<20>([&](auto I) AI {
        constexpr int i = I.value, t = i >> 1, tq = i & 1, nx = i + HW;
        if constexpr (i == 8) RAWBAR();
        if constexpr (i + 4 < 20) wq[i + 4] = ldw(IC<i + 4>{});
        else wfr[i - 16] = lds16(LD, wfb + (i - 16) * 2048);
        pc[tq] = mfma(wq[2 * t], hx[i], pc[tq]);
        pc[2 + tq] = mfma(wq[2 * t + 1], hx[i], pc[2 + tq]);
        if constexpr (lvl(nx + 2) > lvl(nx + 1)) f = __hip_atomic_load(fp + 32 * lvl(nx + 2), __ATOMIC_RELAXED, __HIP_MEMORY_SCOPE_AGENT);
        if constexpr (lvl(nx) > lvl(nx - 1)) wait_flags(ws, fp + 32 * lvl(nx), f);
        hx[nx] = ldh(IC<nx>{});
        SB();
      });
      RAWBAR();
      p4_put(pc[0], 0, w, LD, l16, fin);
      p4_put(pc[2], 2, w, LD, l16, fin);
      p4_put(pc[1], 1, w, LD, l16, fin);
      p4_put(pc[3], 3, w, LD, l16, fin);
      static_for<16>([&](auto I) AI {
        constexpr int i = I.value;
        if constexpr (i + 4 < 16) wfr[i + 4] = lds16(LD, wfb + (i + 4) * 2048);
        fin = mfma(wfr[i], hx[20 + i], fin);
        if constexpr (20 + i + HW < 36) hx[20 + i + HW] = ldh(IC<20 + i + HW>{});
        SB();
      });
      STAMP(14);
      BAR();
#pragma unroll
      for (int ww = 0; ww < 7; ++ww) {
        const int rr = opqv(p4_part(w, ww) + opqv(l16));
#pragma unroll
        for (int qd = 0; qd < 4; ++qd) {
          const f32x4 v = *(LAS f32x4*)(LD + rr + qd * 1024);
#pragma unroll
          for (int i = 0; i < 4; ++i) fin[4 * qd + i] += v[i];
        }
      }
      STAMP(15);
      finish_tile(fin, res, rs, OFF_XIN + XB, OFF_STIN + STB, tt2, cb, kh, lane, L == 1, out);
    }
    if (L == 0) {
      arrive_signal(LD, 0, 4, ws, C_X2 * 32 + g2, lane);
      STAMP(16);
      wait_cnt(ws, C_X2 * 32 + g, 16);
    }
    STAMP(17);
  }
}

extern "C" void kernel_launch(void* const* d_in, const int* in_sizes, int n_in, void* d_out, int out_size, void* d_ws,
                              size_t ws_size, hipStream_t stream) {
  const u16* x0 = (const u16*)d_in[0];
  const u16* wqkv = (const u16*)d_in[1];
  const u16* wphi = (const u16*)d_in[2];
  const u16* wo = (const u16*)d_in[3];
  const u16* wg = (const u16*)d_in[5];
  const u16* wu = (const u16*)d_in[6];
  const u16* wd = (const u16*)d_in[7];
  (void)hipFuncSetAttribute((const void*)k_main, hipFuncAttributeMaxDynamicSharedMemorySize, LDS_BYTES);
  k_prep<<<PREP_JOBS, 256, 0, stream>>>(x0, wqkv, wphi, wo, wg, wu, wd, (char*)d_ws);
  k_main<<<256, 512, LDS_BYTES, stream>>>((char*)d_ws, (u16*)d_out);
}
```

```cpp
#include <hip/hip_runtime.h>
#include <stdint.h>
#include <utility>
#include <type_traits>

typedef unsigned short u16;
typedef unsigned int u32;
typedef u32 v4u __attribute__((ext_vector_type(4)));
typedef u32 v2u __attribute__((ext_vector_type(2)));
typedef float f32x2 __attribute__((ext_vector_type(2)));
typedef float f32x4 __attribute__((ext_vector_type(4)));
typedef float f32x16 __attribute__((ext_vector_type(16)));
typedef __bf16 bf16x2 __attribute__((ext_vector_type(2)));
typedef __bf16 bf16x8 __attribute__((ext_vector_type(8)));
typedef short s16x4 __attribute__((ext_vector_type(4)));
#define LAS __attribute__((address_space(3)))
#define DI __device__ __forceinline__
#define AI __attribute__((always_inline))
template <int V> using IC = std::integral_constant<int, V>;

constexpr int S = 2048, D = 448, FF = 1792;
constexpr int UNIT = 28672;

constexpr int C_KV = 0;
constexpr int C_W2 = 32;
constexpr int C_W4 = 36;
constexpr int C_W3 = 40;
constexpr int C_ERR = 60;
constexpr int C_X2 = 64;
constexpr int C_X1 = 128;
constexpr int C_O = 256;
constexpr int C_H = 512;
constexpr int EPOCH_OFF = 1024 * 128;
constexpr int INIT_OFF = EPOCH_OFF + 256;
constexpr int CTL_BYTES = 1024 * 128 + 512;
constexpr int W2B = 65536;
constexpr int OFF_W2 = CTL_BYTES;
constexpr int OFF_W3 = OFF_W2 + 8 * W2B;
constexpr int OFF_W4 = OFF_W3 + 16 * 7 * UNIT;
constexpr int OFF_CS3 = OFF_W4 + 8 * 8 * UNIT;
constexpr int XB = S * D * 2;
constexpr int OB = S * 512 * 2;
constexpr int OFF_X2 = OFF_CS3 + 16 * 28 * 896;
constexpr int OFF_X1 = OFF_X2 + XB;
constexpr int OFF_O = OFF_X1 + 2 * XB;
constexpr int STB = S * 16 * 8;
constexpr int OFF_ST2 = OFF_O + 2 * OB;
constexpr int OFF_ST1 = OFF_ST2 + STB;
constexpr int KVB = 16 * 16 * 4096;
constexpr int OFF_KVP = OFF_ST1 + 2 * STB;
constexpr int HB = S * FF * 2;
constexpr int OFF_H = OFF_KVP + 2 * KVB;
constexpr int WS_USED = OFF_H + 2 * HB;

constexpr int L_SCR = 4 * UNIT;
constexpr int PHK_STRIDE = 136, V_STRIDE = 72;
constexpr int L_PHK = L_SCR;
constexpr int L_V = L_SCR + 128 * PHK_STRIDE;
constexpr int L_KVF = L_SCR + 28672;
constexpr int L_W2X = L_KVF + 4096;
constexpr int L_CONST = 155648;
constexpr int L_CS1 = L_CONST, L_CS3 = L_CONST + 384, L_WPHI = L_CONST + 1280, L_ARR = L_CONST + 3328;
constexpr int L_FRESH = 81920;
constexpr int L_STAGE = 2 * UNIT;
constexpr int LDS_BYTES = 163840;

constexpr float LOG2E = 1.4426950408889634f;
constexpr int LAG = 10;

DI u32 pk2(float a, float b) { f32x2 v = {a, b}; return __builtin_bit_cast(u32, __builtin_convertvector(v, bf16x2)); }
DI float bflo(u32 v) { return __uint_as_float(v << 16); }
DI float bfhi(u32 v) { return __uint_as_float(v & 0xffff0000u); }
DI f32x16 mfma(v4u a, v4u b, f32x16 c) {
  f32x16 d = __builtin_amdgcn_mfma_f32_32x32x16_bf16(__builtin_bit_cast(bf16x8, a), __builtin_bit_cast(bf16x8, b), c, 0, 0, 0);
  asm volatile("" : "+v"(d));
  return d;
}
DI float xsum(float v) {
  auto r = __builtin_amdgcn_permlane32_swap(__float_as_uint(v), __float_as_uint(v), false, false);
  return __uint_as_float(r[0]) + __uint_as_float(r[1]);
}
DI float xmax(float v) {
  auto r = __builtin_amdgcn_permlane32_swap(__float_as_uint(v), __float_as_uint(v), false, false);
  return fmaxf(__uint_as_float(r[0]), __uint_as_float(r[1]));
}
DI float xmin(float v) {
  auto r = __builtin_amdgcn_permlane32_swap(__float_as_uint(v), __float_as_uint(v), false, false);
  return fminf(__uint_as_float(r[0]), __uint_as_float(r[1]));
}
#define BAR() asm volatile("s_waitcnt lgkmcnt(0)\n\ts_barrier" ::: "memory")
#define RAWBAR() asm volatile("s_barrier" ::: "memory")
#define VMCNT(n) asm volatile("s_waitcnt vmcnt(" #n ")" ::: "memory")
#define SB() __builtin_amdgcn_sched_barrier(0)
#define PIN(x) asm volatile("" : "+v"(x))

typedef __amdgpu_buffer_rsrc_t rsrc_t;
DI v4u ld16(rsrc_t rs, int voff, int soff) { return __builtin_amdgcn_raw_buffer_load_b128(rs, voff, soff, 16); }
DI v2u ld8(rsrc_t rs, int voff, int soff) { return __builtin_amdgcn_raw_buffer_load_b64(rs, voff, soff, 16); }
DI u32 ld4(rsrc_t rs, int voff, int soff) { return __builtin_amdgcn_raw_buffer_load_b32(rs, voff, soff, 16); }
DI void st16(rsrc_t rs, int voff, int soff, v4u v) {
  __builtin_amdgcn_raw_buffer_store_b128(v, rs, voff, soff, 16);
  asm volatile("s_nop 1" ::"v"(v) : "memory");
}
DI void st8(rsrc_t rs, int voff, int soff, v2u v) { __builtin_amdgcn_raw_buffer_store_b64(v, rs, voff, soff, 16); }
DI void st4(rsrc_t rs, int voff, int soff, u32 v) { __builtin_amdgcn_raw_buffer_store_b32(v, rs, voff, soff, 16); }
DI int opq(int v) { asm volatile("" : "+s"(v)); return v; }
DI int opqv(int v) { asm volatile("" : "+v"(v)); return v; }
DI v4u lds16(LAS char* l, int off) { return *(LAS v4u*)(l + off); }
DI float dot2(u32 a, u32 b, float c) {
  return __builtin_amdgcn_fdot2_f32_bf16(__builtin_bit_cast(bf16x2, a), __builtin_bit_cast(bf16x2, b), c, false);
}
DI v4u tr_frag(LAS char* L, int base, int step) {
  v2u lo = __builtin_bit_cast(v2u, __builtin_amdgcn_ds_read_tr16_b64_v4i16((LAS s16x4*)(L + base)));
  v2u hi = __builtin_bit_cast(v2u, __builtin_amdgcn_ds_read_tr16_b64_v4i16((LAS s16x4*)(L + base + step)));
  return v4u{lo.x, lo.y, hi.x, hi.y};
}
DI float sum8(v4u o) {
  const u32 one = 0x3f803f80u;
  return dot2(o.x, one, dot2(o.y, one, 0.f)) + dot2(o.z, one, dot2(o.w, one, 0.f));
}

DI u32* cnt_ptr(char* ws, int idx) { return (u32*)(ws + idx * 128); }
DI bool spin_more(char* ws, u32& spins) {
  u32* e = cnt_ptr(ws, C_ERR);
  __builtin_amdgcn_s_sleep(1);
  ++spins;
  if ((spins & 255u) == 0u) {
    if (__builtin_amdgcn_readfirstlane(__hip_atomic_load(e, __ATOMIC_RELAXED, __HIP_MEMORY_SCOPE_AGENT)) != 0) return false;
    if (spins > 400000u) { __hip_atomic_store(e, 1u, __ATOMIC_RELAXED, __HIP_MEMORY_SCOPE_AGENT); return false; }
  }
  return true;
}
DI void wait_cnt(char* ws, int idx, u32 target) {
  u32* c = cnt_ptr(ws, idx);
  u32 spins = 0;
  while ((u32)__builtin_amdgcn_readfirstlane(__hip_atomic_load(c, __ATOMIC_RELAXED, __HIP_MEMORY_SCOPE_AGENT)) < target)
    if (!spin_more(ws, spins)) break;
  __builtin_amdgcn_fence(__ATOMIC_ACQUIRE, "wavefront");
  asm volatile("" ::: "memory");
}
DI void wait_flags(char* ws, u32* fp, u32 f) {
  u32 spins = 0;
  while (!__all(f >= 16u)) {
    if (!spin_more(ws, spins)) break;
    f = __hip_atomic_load(fp, __ATOMIC_RELAXED, __HIP_MEMORY_SCOPE_AGENT);
  }
  __builtin_amdgcn_fence(__ATOMIC_ACQUIRE, "wavefront");
  asm volatile("" ::: "memory");
}
DI void signal_cnt(char* ws, int idx) { __hip_atomic_fetch_add(cnt_ptr(ws, idx), 1u, __ATOMIC_RELAXED, __HIP_MEMORY_SCOPE_AGENT); }
DI void wave_signal(char* ws, int idx, int lane) { VMCNT(0); if (lane == 0) signal_cnt(ws, idx); }
DI void arrive_lds(LAS char* LD, int slot, int lane) {
  if (lane == 0) __hip_atomic_fetch_add((LAS u32*)(LD + L_ARR + slot * 4), 1u, __ATOMIC_RELAXED, __HIP_MEMORY_SCOPE_WORKGROUP);
}
DI void wait_arr(LAS char* LD, int slot, u32 target) {
  while ((u32)__builtin_amdgcn_readfirstlane(__hip_atomic_load((LAS u32*)(LD + L_ARR + slot * 4), __ATOMIC_RELAXED, __HIP_MEMORY_SCOPE_WORKGROUP)) < target)
    __builtin_amdgcn_s_sleep(1);
}

#define STAMP(i)
#define USTAMP(i)

#define DMA_STEP(GSTEP) \
      "s_add_u32 m0, m0, 0x1000\n\t" \
      "v_add_u32 %1, " GSTEP ", %1\n\t" \
      "global_load_lds_dwordx4 %1, %2 sc1\n\t"
#define DMA_FN(NAME, BODY) \
DI void NAME(const char* gsrc, u32 voff, u32 lds_dst) { \
  u32 keep; \
  asm volatile("s_mov_b32 %0, m0\n\ts_mov_b32 m0, %3\n\ts_nop 0\n\tglobal_load_lds_dwordx4 %1, %2 sc1\n\t" BODY "s_mov_b32 m0, %0" \
               : "=&s"(keep), "+v"(voff) : "s"(gsrc), "s"(lds_dst) : "memory", "scc"); }
DMA_FN(dma7, DMA_STEP("0x1000") DMA_STEP("0x1000") DMA_STEP("0x1000") DMA_STEP("0x1000") DMA_STEP("0x1000") DMA_STEP("0x1000"))
DMA_FN(dma2, DMA_STEP("0x1000"))
DMA_FN(dma7n, DMA_STEP("0x2a000") DMA_STEP("0x2a000") DMA_STEP("0x2a000") DMA_STEP("0x2a000") DMA_STEP("0x2a000") DMA_STEP("0x2a000"))

template <typename F, int... I>
DI void static_for_impl(F&& f, std::integer_sequence<int, I...>) { (f(IC<I>{}), ...); }
template <int N, typename F>
DI void static_for(F&& f) { static_for_impl(f, std::make_integer_sequence<int, N>{}); }

constexpr int PF = 6;
template <int N, typename RD, typename PRE, typename ST>
DI void pipe(RD rd, PRE pre, ST step) {
  v4u buf[PF];
  static_for<PF>([&](auto I) AI { buf[I.value] = rd(I); });
  SB();
  static_for<N>([&](auto I) AI {
    constexpr int i = I.value;
    pre(I);
    const v4u a = buf[i % PF];
    if constexpr (i + PF < N) buf[i % PF] = rd(IC<i + PF>{});
    step(I, a);
    SB();
  });
}

DI void ln_finish(float s, float q, float& mean, float& rstd) {
  s = xsum(s); q = xsum(q);
  mean = s * (1.f / 448.f);
  const float var = fmaxf(q * (1.f / 448.f) - mean * mean, 0.f);
  rstd = __builtin_amdgcn_rsqf(var + 1e-5f);
}
DI void ln_stats(const v2u (&st)[8], float& mean, float& rstd) {
  f32x2 a = __builtin_bit_cast(f32x2, st[0]), b = __builtin_bit_cast(f32x2, st[1]);
#pragma unroll
  for (int i = 2; i < 8; i += 2) { a += __builtin_bit_cast(f32x2, st[i]); b += __builtin_bit_cast(f32x2, st[i + 1]); }
  a += b;
  ln_finish(a[0], a[1], mean, rstd);
}
DI v4u pack8(const f32x16& a, int s) {
  v4u r = {pk2(a[8 * s], a[8 * s + 1]), pk2(a[8 * s + 2], a[8 * s + 3]), pk2(a[8 * s + 4], a[8 * s + 5]), pk2(a[8 * s + 6], a[8 * s + 7])};
  return r;
}
DI void dual_softmax(const f32x16& p, int h, float rstd, v4u (&out)[4]) {
  float mx = p[0], mn = p[0];
#pragma unroll
  for (int r = 1; r < 12; ++r) { mx = fmaxf(mx, p[r]); mn = fminf(mn, p[r]); }
#pragma unroll
  for (int r = 12; r < 16; ++r) { const float v = h ? p[0] : p[r]; mx = fmaxf(mx, v); mn = fminf(mn, v); }
  mx = xmax(mx); mn = xmin(mn);
  const float c = rstd * LOG2E;
#pragma unroll
  for (int sg = 0; sg < 2; ++sg) {
    const float cc = sg ? -c : c, off = sg ? mn * c : -mx * c;
    f32x16 e;
    float s0 = 0.f, s1 = 0.f;
#pragma unroll
    for (int r = 0; r < 16; ++r) {
      float v = __builtin_amdgcn_exp2f(fmaf(p[r], cc, off));
      if (r >= 12) v = h ? 0.f : v;
      e[r] = v;
      if (r & 1) s1 += v; else s0 += v;
    }
    const float rs_ = __builtin_amdgcn_rcpf(xsum(s0 + s1));
#pragma unroll
    for (int r = 0; r < 16; ++r) e[r] *= rs_;
    out[2 * sg] = pack8(e, 0); out[2 * sg + 1] = pack8(e, 1);
    PIN(out[2 * sg]); PIN(out[2 * sg + 1]);
    SB();
  }
}

DI void finish_tile(const f32x16& acc, v4u (&res)[2], rsrc_t rs, int xoff, int stoff, int tt, int cb, int nt, int lane, bool last, u16* out) {
  lane = opqv(lane);
  const int h = lane >> 5, tok = lane & 31;
#pragma unroll
  for (int ro = 0; ro < 2; ++ro) {
    v4u r = res[ro];
    v4u o;
    o.x = pk2(bflo(r.x) + acc[8 * ro + 0], bfhi(r.x) + acc[8 * ro + 1]);
    o.y = pk2(bflo(r.y) + acc[8 * ro + 2], bfhi(r.y) + acc[8 * ro + 3]);
    o.z = pk2(bflo(r.z) + acc[8 * ro + 4], bfhi(r.z) + acc[8 * ro + 5]);
    o.w = pk2(bflo(r.w) + acc[8 * ro + 6], bfhi(r.w) + acc[8 * ro + 7]);
    res[ro] = o;
    const int oi = nt * 4 + ro * 2 + h;
    if (oi < 7) {
      const int go = 7 * cb + oi;
      if (last) *(v4u*)(out + (size_t)(tt * 32 + tok) * D + go * 8) = o;
      else st16(rs, ((go >> 1) * 64 + tok + 32 * (go & 1)) * 16, xoff + tt * 28 * 1024, o);
    }
  }
  if (!last) {
    const bool v1 = !(nt == 1 && h == 1);
    const u32 one = 0x3f803f80u;
    float s0 = dot2(res[0].x, one, 0.f), s1 = dot2(res[1].x, one, 0.f), q0 = dot2(res[0].x, res[0].x, 0.f), q1 = dot2(res[1].x, res[1].x, 0.f);
    s0 = dot2(res[0].y, one, s0); s1 = dot2(res[1].y, one, s1); q0 = dot2(res[0].y, res[0].y, q0); q1 = dot2(res[1].y, res[1].y, q1);
    s0 = dot2(res[0].z, one, s0); s1 = dot2(res[1].z, one, s1); q0 = dot2(res[0].z, res[0].z, q0); q1 = dot2(res[1].z, res[1].z, q1);
    s0 = dot2(res[0].w, one, s0); s1 = dot2(res[1].w, one, s1); q0 = dot2(res[0].w, res[0].w, q0); q1 = dot2(res[1].w, res[1].w, q1);
    const float sum = xsum(s0 + (v1 ? s1 : 0.f));
    const float sq = xsum(q0 + (v1 ? q1 : 0.f));
    if (h == 0) st8(rs, tok * 8, stoff + ((tt * 8 + cb) * 2 + nt) * 256, v2u{__float_as_uint(sum), __float_as_uint(sq)});
  }
}

DI void kv_reduce(rsrc_t rs, LAS char* LD, int L, int j, int w, int lane) {
  const int ko = opq(OFF_KVP + L * KVB + j * 16 * 4096 + (w >> 1) * 1024 + (w & 1) * 512);
  const int lz = opqv(lane);
  const int vo = (lz >> 5) * 4096 + (lz & 31) * 16;
  v4u t[8];
#pragma unroll
  for (int i = 0; i < 8; ++i) t[i] = ld16(rs, vo, ko + i * 8192);
  float s[8] = {};
  const u32 lo1 = (u32)opq(0x00003f80), hi1 = (u32)opq(0x3f800000);
#pragma unroll
  for (int i = 0; i < 8; ++i) {
#pragma unroll
    for (int c = 0; c < 4; ++c) { s[2 * c] = dot2(t[i][c], lo1, s[2 * c]); s[2 * c + 1] = dot2(t[i][c], hi1, s[2 * c + 1]); }
  }
#pragma unroll
  for (int c = 0; c < 8; ++c) s[c] = xsum(s[c]);
  if (lz < 32) *(LAS v4u*)(LD + L_KVF + (w >> 1) * 1024 + (w & 1) * 512 + lz * 16) = v4u{pk2(s[0], s[1]), pk2(s[2], s[3]), pk2(s[4], s[5]), pk2(s[6], s[7])};
}

DI int p4_part(int a, int i) {
  return a == 0 ? i * 4096 : a == 1 ? UNIT + i * 4096 : a == 2 ? L_SCR + i * 4096 : (i < 6 ? 2 * UNIT + i * 4096 : L_SCR + UNIT);
}
DI void p4_put(const f32x16& acc, int a, int w, LAS char* LD, int l16, f32x16& keep) {
  if (w == a) { keep = acc; return; }
  const int off = opqv(p4_part(a, w - (w > a ? 1 : 0)) + opqv(l16));
#pragma unroll
  for (int qd = 0; qd < 4; ++qd) *(LAS f32x4*)(LD + off + qd * 1024) = f32x4{acc[4 * qd], acc[4 * qd + 1], acc[4 * qd + 2], acc[4 * qd + 3]};
}
DI int p4_koff(int blk, int tp, int c) {
  const int o = 48 * blk + (tp < 5 ? 8 * tp + c : 40 + 4 * (tp - 5) + c);
  return (7 * (o & 15) + (o >> 4)) * 1024;
}
constexpr int p4_lvl(int blk, int tp) { return blk == 0 ? 0 : (tp < 4 ? 1 : 2); }

struct Args {
  const u16* x0; const u16* wqkv; const u16* wphi; const u16* wo; const u16* wg; const u16* wu; const u16* wd;
  char* ws; u16* out;
};

DI void retile_load(const Args& A, int job, int lane, v4u (&t)[7]) {
  if (job < 64 || job >= 512) {
    const bool iswo = job < 64;
    const int a0 = iswo ? job >> 2 : (job - 512) >> 2, cg = job & 3;
    const u16* src = (iswo ? A.wo + (size_t)a0 * 28 * D : A.wd + (size_t)a0 * 32 * D) + 112 * cg;
    const int nrow = iswo ? 28 : 32;
#pragma unroll
    for (int i = 0; i < 7; ++i) {
      const int p = lane + 64 * i, r = p / 14, c = p % 14;
      t[i] = *(const v4u*)(src + (size_t)(r < nrow ? r : 0) * D + c * 8);
    }
  } else {
    const int a = job - 64, j = a / 28, ks = a % 28;
#pragma unroll
    for (int i = 0; i < 7; ++i) {
      const int p = lane + 64 * i, r = p / 28, c = p % 28;
      t[i] = *(const v4u*)((c < 14 ? A.wg : A.wu) + (size_t)(ks * 16 + r) * FF + 112 * j + (c < 14 ? c : c - 14) * 8);
    }
  }
}
DI int retile_store(int job, rsrc_t rs, LAS char* LD, int stage, int lane, const v4u (&t)[7]) {
  const int h = lane >> 5, q = (lane & 15) >> 2, c8 = ((lane >> 4) & 1) * 4 + (lane & 3);
  const int hh = c8 & 1, quad = c8 >> 1;
  if (job < 64 || job >= 512) {
    const bool iswo = job < 64;
    const int a0 = iswo ? job >> 2 : (job - 512) >> 2, cg = job & 3;
    const int nrow = iswo ? 28 : 32;
    constexpr int ST = 240;
#pragma unroll
    for (int i = 0; i < 7; ++i) {
      const int p = lane + 64 * i, r = p / 14, c = p % 14;
      *(LAS v4u*)(LD + stage + r * ST + c * 16) = r < nrow ? t[i] : v4u{0u, 0u, 0u, 0u};
    }
#pragma unroll
    for (int i = 0; i < 4; ++i) {
      const int cb = cg * 2 + (i >> 1), nt = i & 1;
      const int oi = nt * 4 + (quad >> 1) * 2 + hh;
      const int ro = (lane >> 4) & 1, rh = (lane >> 2) & 1;
      const bool valid = nt * 4 + ro * 2 + rh < 7;
      const int colb = (56 * (i >> 1) + 8 * (oi < 7 ? oi : 6) + 4 * (quad & 1)) * 2;
#pragma unroll
      for (int k2 = 0; k2 < 2; ++k2) {
        v4u o = iswo ? tr_frag(LD, stage + (16 * k2 + 4 * h + q) * ST + colb, 8 * ST) : tr_frag(LD, stage + (16 * k2 + 8 * h + q) * ST + colb, 4 * ST);
        if (!valid) o = v4u{0u, 0u, 0u, 0u};
        const int ks = a0 * 2 + k2;
        int off;
        if (iswo) off = OFF_W2 + cb * W2B + (((ks & 15) * 2 + (ks >> 4)) * 2 + nt) * 1024;
        else {
          const int jp = ks / 7, s = ks % 7;
          int fr;
          if (s == 6) fr = 80 + jp * 2 + nt;
          else {
            const int o_ = 16 * s + jp, blk = o_ / 48, r = o_ % 48;
            const int c = r < 40 ? (r & 7) : ((r - 40) & 3), tp = r < 40 ? (r >> 3) : 5 + ((r - 40) >> 2);
            if (c < 4) fr = 112 + (c * 14 + blk * 7 + tp) * 2 + nt;
            else fr = ((blk * 5 + tp) * 4 + (c - 4)) * 2 + nt;
          }
          off = OFF_W4 + cb * 8 * UNIT + fr * 1024;
        }
        st16(rs, lane * 16, off, o);
      }
    }
    return (iswo ? C_W2 : C_W4) + cg;
  } else {
    const int a = job - 64, j = a / 28, ks = a % 28;
    constexpr int ST = 464;
#pragma unroll
    for (int i = 0; i < 7; ++i) { const int p = lane + 64 * i, r = p / 28, c = p % 28; *(LAS v4u*)(LD + stage + r * ST + c * 16) = t[i]; }
    float cs[7];
#pragma unroll
    for (int n = 0; n < 7; ++n) {
      const v4u o = tr_frag(LD, stage + (8 * h + q) * ST + (quad & 1) * 224 + (16 * n + 8 * hh + 4 * (quad >> 1)) * 2, 4 * ST);
      st16(rs, lane * 16, OFF_W3 + j * 7 * UNIT + (n < 2 ? (ks * 2 + n) * 1024 : n * UNIT + ks * 1024), o);
      cs[n] = xsum(sum8(o));
    }
#pragma unroll
    for (int n2 = 0; n2 < 4; ++n2) {
      const float v = h ? cs[n2 < 3 ? 2 * n2 + 1 : 6] : cs[2 * n2];
      if (n2 < 3 || !h) st4(rs, lane * 4, OFF_CS3 + (j * 28 + ks) * 896 + n2 * 256, __float_as_uint(v));
    }
    return C_W3 + j;
  }
}

__global__ __launch_bounds__(512, 2) void k_main(const Args A) {
  extern __shared__ __attribute__((aligned(16))) char smem[];
  LAS char* LD = (LAS char*)smem;
  char* ws = A.ws;
  const int b = blockIdx.x, xcd = b & 7, bi = b >> 3;
  const int g = (xcd >> 2) * 8 + (bi >> 2), j = (xcd & 3) * 4 + (bi & 3);
  const int rb = xcd * 4 + (bi >> 3), cb = bi & 7;
  const int lane = threadIdx.x & 63, w = __builtin_amdgcn_readfirstlane(threadIdx.x >> 6);
  const int h = lane >> 5, tok = lane & 31;
  const rsrc_t rs = __builtin_amdgcn_make_buffer_rsrc((void*)ws, 0, WS_USED, 0x00020000);
  const u32 lbase = (u32)(size_t)smem;
  const int l16 = lane * 16;
  const int tq_ = (lane & 15) >> 2, tc8 = ((lane >> 4) & 1) * 4 + (lane & 3);
  const int w1lane = (8 * h + tq_) * 64 + min(tc8 + (j & 1), 7) * 8;

  if (w >= 4) {
    const int hw = w - 4;
    const u32 voff = hw * 1024 + lane * 16;
    const u32 voffn = (hw * 16 + (lane >> 2)) * (3 * D * 2) + (lane & 3) * 16;
    const char* w1 = (const char*)A.wqkv + 56 * j - (j & 1) * 8;
    const char* w2 = ws + OFF_W2 + cb * W2B;
    const char* w3 = ws + OFF_W3 + j * 7 * UNIT;
    const char* w4 = ws + OFF_W4 + cb * 8 * UNIT;
#define DMA(src, slot) dma7((src), voff, lbase + (slot) * UNIT + hw * 1024)
#define DMAN(which, slot) dma7n(w1 + (which) * 896, voffn, lbase + (slot) * UNIT + hw * 1024)
    v4u wr[28];
    unsigned long long tag = 0;
    auto hlayer = [&](auto LC) AI {
      constexpr int L = LC.value;
      if (L > 0) wait_arr(LD, 0, 4);
      if (L == 0 && hw == 3) {
        if (lane == 0) *(LAS v4u*)(LD + L_ARR) = v4u{0u, 0u, 0u, 0u};
        tag = __hip_atomic_load((unsigned long long*)(ws + EPOCH_OFF), __ATOMIC_RELAXED, __HIP_MEMORY_SCOPE_AGENT) + 1ull;
        if (b == 0) {
#pragma unroll
          for (int i = 0; i < 16; ++i) st4(rs, lane * 128, i * 8192, 0u);
          VMCNT(0);
          if (lane == 0) __hip_atomic_store((unsigned long long*)(ws + INIT_OFF), tag, __ATOMIC_RELAXED, __HIP_MEMORY_SCOPE_AGENT);
        }
      }
      DMAN(1, 4);
      DMAN(2, 0);
      v4u jt[7];
      const int job = hw >= 2 ? b * 2 + hw - 2 : (hw == 0 && b < 224 ? 512 + b : -1);
      VMCNT(7); BAR();
      auto colsum = [&](int slot, int which) AI {
        const int ib = opqv(slot * UNIT + opqv(w1lane));
        v4u fr[28];
#pragma unroll
        for (int ks = 0; ks < 28; ++ks) fr[ks] = tr_frag(LD, ib + ks * 1024, 256);
        SB();
        const u32 one = 0x3f803f80u;
        float c0 = 0.f, c1 = 0.f, c2 = 0.f, c3 = 0.f;
#pragma unroll
        for (int ks = 0; ks < 28; ++ks) { c0 = dot2(fr[ks].x, one, c0); c1 = dot2(fr[ks].y, one, c1); c2 = dot2(fr[ks].z, one, c2); c3 = dot2(fr[ks].w, one, c3); }
        const float cs = xsum((c0 + c1) + (c2 + c3));
        if (lane < 32) *(LAS float*)(LD + L_CS1 + which * 128 + lane * 4) = cs;
      };
      VMCNT(0); BAR();
      if (L == 0) {
        if (hw == 0) colsum(4, 0);
        if (hw == 1) colsum(0, 1);
        if (hw == 2) {
          const int f = lane & 31, fc = min(f, 27);
          u32 e[16];
#pragma unroll
          for (int i = 0; i < 16; ++i) {
            const int d0 = 16 * (i >> 3) + 8 * ((i >> 2) & 1) + 4 * h + (i & 3);
            e[i] = A.wphi[(j * 28 + min(d0, 27)) * 28 + fc];
          }
#pragma unroll
          for (int i = 0; i < 16; ++i) {
            const int d0 = 16 * (i >> 3) + 8 * ((i >> 2) & 1) + 4 * h + (i & 3);
            e[i] = (d0 < 28 && f < 28) ? e[i] : 0u;
          }
#pragma unroll
          for (int s = 0; s < 2; ++s)
            *(LAS v4u*)(LD + L_WPHI + s * 1024 + l16) = v4u{e[8 * s] | (e[8 * s + 1] << 16), e[8 * s + 2] | (e[8 * s + 3] << 16),
                                                           e[8 * s + 4] | (e[8 * s + 5] << 16), e[8 * s + 6] | (e[8 * s + 7] << 16)};
        }
        if (hw == 3) {
          u32 spins = 0;
          while (__builtin_amdgcn_readfirstlane((int)(__hip_atomic_load((unsigned long long*)(ws + INIT_OFF), __ATOMIC_RELAXED, __HIP_MEMORY_SCOPE_AGENT) != tag))) {
            __builtin_amdgcn_s_sleep(1);
            if (++spins > 400000u) break;
          }
        }
      } else DMAN(0, 1);
      BAR();
      if (L == 0) {
        DMAN(0, 1);
        if (job >= 0) retile_load(A, job, lane, jt);
      }
      BAR();
      int jsig = -1;
      if (L == 0) {
        if (job >= 0) { VMCNT(7); } else { VMCNT(0); }
      } else {
        DMA(w2, 2); DMA(w2 + UNIT, 3); dma2(w2 + 2 * UNIT, voff, lbase + L_W2X + hw * 1024);
        VMCNT(16);
      }
      BAR();
      if (hw < 2) {
        __builtin_amdgcn_s_setprio(2);
        f32x16 kp = {};
        const int lz = opqv(lane);
        const int g16 = (lz >> 4) & 1, q = (lz & 15) >> 2, p = lz & 3, hz = lz >> 5;
        const int ab0 = opqv(L_PHK + (8 * hz + q) * PHK_STRIDE + (g16 * 16 + 4 * p) * 2 + hw * 64);
        const int bb0 = opqv(L_V + (8 * hz + q) * V_STRIDE + (g16 * 16 + 4 * p) * 2);
        v4u ap[8], bv[8];
#pragma unroll
        for (int s = 0; s < 8; ++s) { ap[s] = tr_frag(LD, ab0 + 16 * s * PHK_STRIDE, 4 * PHK_STRIDE); bv[s] = tr_frag(LD, bb0 + 16 * s * V_STRIDE, 4 * V_STRIDE); }
        SB();
#pragma unroll
        for (int s = 0; s < 8; ++s) kp = mfma(ap[s], bv[s], kp);
        const int ko = opq(OFF_KVP + L * KVB + (j * 16 + g) * 4096 + hw * 2048);
        st16(rs, l16, ko, pack8(kp, 0));
        st16(rs, l16, ko + 1024, pack8(kp, 1));
        wave_signal(ws, C_KV + L * 16 + j, lane);
        __builtin_amdgcn_s_setprio(0);
      }
      if (L == 0) {
        if (hw == 3) {
          colsum(1, 2);
          arrive_lds(LD, 1, lane);
        }
        if (job >= 0) jsig = retile_store(job, rs, LD, L_STAGE + hw * 7680, lane, jt);
        BAR();
        if (jsig >= 0) wave_signal(ws, jsig, lane);
        BAR();
        wait_cnt(ws, C_W2 + (cb >> 1), 16);
        DMA(w2, 2); DMA(w2 + UNIT, 3); dma2(w2 + 2 * UNIT, voff, lbase + L_W2X + hw * 1024);
        VMCNT(0); BAR();
        wait_cnt(ws, C_W3 + j, 28);
        DMA(w3, 0);
        DMA(w3 + UNIT, 1);
        const int t = hw * 64 + lane;
        if (t < 224) {
          float a[28];
#pragma unroll
          for (int ks = 0; ks < 28; ++ks) a[ks] = __uint_as_float(ld4(rs, t * 4, OFF_CS3 + (j * 28 + ks) * 896));
          float s0 = 0.f, s1 = 0.f, s2 = 0.f, s3 = 0.f;
#pragma unroll
          for (int ks = 0; ks < 28; ks += 4) { s0 += a[ks]; s1 += a[ks + 1]; s2 += a[ks + 2]; s3 += a[ks + 3]; }
          *(LAS float*)(LD + L_CS3 + t * 4) = (s0 + s1) + (s2 + s3);
        }
      } else {
        DMA(w3, 0);
        BAR();
        kv_reduce(rs, LD, L, j, w, lane);
        BAR();
        DMA(w3 + UNIT, 1);
      }
      BAR();
      DMA(w3 + 2 * UNIT, 2);
      VMCNT(7); BAR();
      const int wo4 = opq(OFF_W4 + cb * 8 * UNIT + 4 * UNIT + hw * UNIT);
#define WR(lo, hi) _Pragma("unroll") for (int i = lo; i < hi; ++i) wr[i] = ld16(rs, l16, wo4 + i * 1024)
      if (L == 0) wait_cnt(ws, C_W4 + (cb >> 1), 56);
      wait_arr(LD, 2, 4 * L + 4);
      DMA(w3 + 3 * UNIT, 3);
      VMCNT(7); BAR();
      DMA(w3 + 4 * UNIT, 0);
      VMCNT(12); BAR();
      if (L == 0) {
        DMA(w3 + 5 * UNIT, 1); WR(0, 7);
        VMCNT(19); BAR();
        DMA(w3 + 6 * UNIT, 2); WR(7, 14);
        VMCNT(26); BAR();
        DMA(w4 + 3 * UNIT, 3); WR(14, 21);
        VMCNT(26); BAR();
        DMA(w4, 0); WR(21, 28);
        VMCNT(35); BAR();
      } else {
        DMA(w3 + 5 * UNIT, 1);
        VMCNT(12); BAR();
        DMA(w3 + 6 * UNIT, 2);
        VMCNT(12); BAR();
        DMA(w4 + 3 * UNIT, 3);
        VMCNT(12); BAR();
        DMA(w4, 0);
        VMCNT(14); BAR();
      }
      DMA(w4 + UNIT, 1);
      u32* fp = cnt_ptr(ws, C_H + (L * 64 + 2 * rb + (lane & 1)) * 4);
      u32 f = __hip_atomic_load(fp, __ATOMIC_RELAXED, __HIP_MEMORY_SCOPE_AGENT);
      VMCNT(0); BAR();
      DMA(w4 + 2 * UNIT, 2);
      {
        wait_flags(ws, fp, f);
        const int ho = opq(OFF_H + L * HB + 2 * rb * 112 * 1024);
        const int hc = opq(hw);
        constexpr int HW = 8;
        v4u hx[28];
        auto ldh = [&](auto I) AI { constexpr int i = I.value, t = i >> 1; return ld16(rs, l16, ho + (i & 1) * 112 * 1024 + p4_koff(t / 7, t % 7, hc)); };
        auto lvl = [](int i) constexpr { return p4_lvl((i >> 1) / 7, (i >> 1) % 7); };
        static_for<HW>([&](auto I) AI { hx[I.value] = ldh(I); });
        f32x16 acc[4] = {};
        f32x16 keep;
        static_for<28>([&](auto I) AI {
          constexpr int i = I.value, t = i >> 1, tq = i & 1, nx = i + HW;
          if constexpr (i == 8) RAWBAR();
          if constexpr (i == 18) RAWBAR();
          acc[tq] = mfma(wr[2 * t], hx[i], acc[tq]);
          acc[2 + tq] = mfma(wr[2 * t + 1], hx[i], acc[2 + tq]);
          if constexpr (nx + 3 < 28 && lvl(nx + 3) > lvl(nx + 2)) f = __hip_atomic_load(fp + 32 * lvl(nx + 3), __ATOMIC_RELAXED, __HIP_MEMORY_SCOPE_AGENT);
          if constexpr (nx < 28) {
            if constexpr (lvl(nx) > lvl(nx - 1)) wait_flags(ws, fp + 32 * lvl(nx), f);
            hx[nx] = ldh(IC<nx>{});
          }
          SB();
        });
        p4_put(acc[0], 0, w, LD, l16, keep);
        p4_put(acc[2], 2, w, LD, l16, keep);
        p4_put(acc[1], 1, w, LD, l16, keep);
        p4_put(acc[3], 3, w, LD, l16, keep);
        BAR();
      }
    };
    hlayer(IC<0>{});
    hlayer(IC<1>{});
    if (b == 0 && hw == 3 && lane == 0) __hip_atomic_store((unsigned long long*)(ws + EPOCH_OFF), tag, __ATOMIC_RELAXED, __HIP_MEMORY_SCOPE_AGENT);
    return;
  }

  const int tt = 4 * g + w;
  const int tt2 = 2 * rb + (w & 1);
  const int kh = w >> 1;
  const rsrc_t rx0 = __builtin_amdgcn_make_buffer_rsrc((void*)A.x0, 0, XB, 0x00020000);
  v4u res[2];
  constexpr int XW = 14;

  auto clayer = [&](auto LC) AI {
    constexpr int L = LC.value;
    v4u x[28];
    v2u st[8];
    float mean, rstd;
    v4u pq[4];
    STAMP(0);
    auto phase1 = [&](auto FIRST) AI {
      constexpr bool first = FIRST.value;
      const int xo = opq(first ? tt * 32 * (D * 2) : OFF_X2 + tt * 28 * 1024);
      auto ldx = [&](int ks) AI { return ld16(rs, l16, xo + ks * 1024); };
      const int so_ = opq(OFF_ST2 + tt * 4096);
      const int sv_ = (opqv(lane) & 31) * 8 + h * 2048;
      v4u gx[28]; int gv[7], gl[7];
      const int stg = L_STAGE + w * 7680;
      const int srd = opqv(stg + (opqv(lane) & 31) * 240 + h * 16);
      auto ldg = [&](int i) AI { return __builtin_amdgcn_raw_buffer_load_b128(rx0, gv[i % 7], xo + (i / 7) * 224, 0); };
      auto stage_w = [&](int c) AI {
#pragma unroll
        for (int i = 0; i < 7; ++i) *(LAS v4u*)(LD + gl[i]) = gx[7 * c + i];
      };
      auto stage_r = [&](int c) AI {
#pragma unroll
        for (int i = 0; i < 7; ++i) x[7 * c + i] = lds16(LD, srd + i * 32);
      };
      if constexpr (first) {
#pragma unroll
        for (int i = 0; i < 7; ++i) { const int p = lane + 64 * i, r = p / 14, c = p % 14; gv[i] = r * (D * 2) + c * 16; gl[i] = stg + r * 240 + c * 16; }
#pragma unroll
        for (int i = 0; i < 21; ++i) gx[i] = ldg(i);
        stage_w(0); stage_r(0);
      } else {
#pragma unroll
        for (int ks = 0; ks < XW; ++ks) x[ks] = ldx(ks);
      }
      USTAMP(18);
      BAR();
      USTAMP(19);
      f32x16 ak = {}, av = {}, p = {};
      const int sbk = opqv(opqv(w1lane) + 4 * UNIT), sbv = opqv(w1lane);
      const int csb = opqv(L_CS1 + 16 * (opqv(lane) >> 5)), wb = opqv(L_WPHI + opqv(l16));
      f32x4 cs[4]; v4u kf[2], wf[2];
      float nmean, s1 = 0.f, s2 = 0.f, q1 = 0.f, q2 = 0.f;
      auto isk = [](int i) constexpr { return i < LAG || (i < 56 - LAG && ((i - LAG) & 1) == 0); };
      auto kst = [](int i) constexpr { return i < LAG ? i : (i < 56 - LAG ? ((((i - LAG) & 1) == 0) ? LAG + (i - LAG) / 2 : (i - LAG) / 2) : i - 28); };
      pipe<56>([&](auto I) AI { return tr_frag(LD, (isk(I.value) ? sbk : sbv) + kst(I.value) * 1024, 256); },
               [&](auto I) AI { if constexpr (I.value == LAG + 1 - PF || I.value == 38 || I.value == 46) { USTAMP(20 + I.value / 10 * 2); RAWBAR(); USTAMP(21 + I.value / 10 * 2); }   },
               [&](auto I, v4u a) AI {
                 constexpr int i = I.value, ks = kst(i);
                 if constexpr (isk(i)) ak = mfma(a, x[ks], ak); else av = mfma(a, x[ks], av);
                 if constexpr (!first && isk(i) && ks + XW < 28) x[ks + XW] = ldx(ks + XW);
                 if constexpr (first) {
                   if constexpr (i < 7) gx[21 + i] = ldg(21 + i);
                   if constexpr (isk(i) && ks % 7 == 3 && ks < 21) { stage_w(ks / 7 + 1); stage_r(ks / 7 + 1); }
                   if constexpr (isk(i)) {
                     const u32 one = 0x3f803f80u;
                     s1 = dot2(x[ks].x, one, s1); q1 = dot2(x[ks].x, x[ks].x, q1); s2 = dot2(x[ks].y, one, s2); q2 = dot2(x[ks].y, x[ks].y, q2);
                     s1 = dot2(x[ks].z, one, s1); q1 = dot2(x[ks].z, x[ks].z, q1); s2 = dot2(x[ks].w, one, s2); q2 = dot2(x[ks].w, x[ks].w, q2);
                   }
                   if constexpr (i == 45) { ln_finish(s1 + s2, q1 + q2, mean, rstd); nmean = -mean; }
                 } else {
                   if constexpr (!isk(i) && ks >= 4 && ks < 12) st[ks - 4] = ld8(rs, sv_ + (ks - 4) * 256, so_);
                   if constexpr (i == 45) { ln_stats(st, mean, rstd); nmean = -mean; }
                 }
                 if constexpr (i >= 39 && i < 43) cs[i - 39] = *(LAS f32x4*)(LD + csb + 32 * (i - 39));
                 if constexpr (i == 43) wf[0] = lds16(LD, wb);
                 if constexpr (i == 44) wf[1] = lds16(LD, wb + 1024);
                 if constexpr (i >= 47 && i < 51) {
                   constexpr int qd = i - 47;
                   const float y0 = fmaf(nmean, cs[qd][0], ak[4 * qd]), y1 = fmaf(nmean, cs[qd][1], ak[4 * qd + 1]);
                   const float y2 = fmaf(nmean, cs[qd][2], ak[4 * qd + 2]), y3 = fmaf(nmean, cs[qd][3], ak[4 * qd + 3]);
                   kf[qd >> 1][2 * (qd & 1)] = pk2(y0, y1);
                   kf[qd >> 1][2 * (qd & 1) + 1] = pk2(y2, y3);
                 }
                 if constexpr (i == 49) p = mfma(wf[0], kf[0], p);
                 if constexpr (i == 51) p = mfma(wf[1], kf[1], p);
               });
      STAMP(1);
      const int lz = opqv(lane);
      const int trow = w * 32 + (lz & 31);
      const int pb = opqv(L_PHK + trow * PHK_STRIDE + 8 * (lz >> 5)), vb = opqv(L_V + trow * V_STRIDE + 8 * (lz >> 5));
      dual_softmax(p, h, rstd, pq);
#pragma unroll
      for (int s = 0; s < 4; ++s) {
        *(LAS v2u*)(LD + pb + 32 * s) = v2u{pq[s].x, pq[s].y};
        *(LAS v2u*)(LD + pb + 32 * s + 16) = v2u{pq[s].z, pq[s].w};
      }
      SB();
#pragma unroll
      for (int qd = 0; qd < 4; ++qd) cs[qd] = *(LAS f32x4*)(LD + csb + 128 + 32 * qd);
#pragma unroll
      for (int r = 0; r < 16; ++r) av[r] = fmaf(nmean, cs[r >> 2][r & 3], av[r]) * rstd;
      if (h) { av[12] = 1.f; av[13] = 0.f; av[14] = 0.f; av[15] = 0.f; }
#pragma unroll
      for (int qd = 0; qd < 4; ++qd) *(LAS v2u*)(LD + vb + 16 * qd) = v2u{pk2(av[4 * qd], av[4 * qd + 1]), pk2(av[4 * qd + 2], av[4 * qd + 3])};
      STAMP(2);
      BAR();
      if constexpr (first) {
#pragma unroll
        for (int ro = 0; ro < 2; ++ro) {
          const int oi = min(kh * 4 + ro * 2 + h, 6);
          res[ro] = __builtin_amdgcn_raw_buffer_load_b128(rx0, tok * (D * 2) + (7 * cb + oi) * 16, tt2 * 32 * (D * 2), 0);
        }
      }
      f32x16 aq = {};
      const int sbq = opqv(opqv(w1lane) + 1 * UNIT);
      pipe<28>([&](auto I) AI { return tr_frag(LD, sbq + I.value * 1024, 256); }, [&](auto) AI {},
               [&](auto I, v4u a) AI { aq = mfma(a, x[I.value], aq); });
      if constexpr (first) wait_arr(LD, 1, 1);
#pragma unroll
      for (int qd = 0; qd < 4; ++qd) cs[qd] = *(LAS f32x4*)(LD + csb + 256 + 32 * qd);
#pragma unroll
      for (int r = 0; r < 16; ++r) aq[r] = fmaf(nmean, cs[r >> 2][r & 3], aq[r]);
      f32x16 p2 = {};
      const int wb2 = opqv(L_WPHI + opqv(l16));
      p2 = mfma(lds16(LD, wb2), pack8(aq, 0), p2);
      p2 = mfma(lds16(LD, wb2 + 1024), pack8(aq, 1), p2);
      dual_softmax(p2, h, rstd, pq);
    };
    phase1(std::integral_constant<bool, L == 0>{});
    STAMP(3);
    if (w == 0) wait_cnt(ws, C_KV + L * 16 + j, 32);
    BAR();
    STAMP(4);
    kv_reduce(rs, LD, L, j, w, lane);
    if (L == 0) kv_reduce(rs, LD, L, j, w + 4, lane);
    BAR();
    STAMP(5);
    {
      f32x16 nm = {};
      const int kb = opqv(L_KVF + opqv(l16));
      v4u kf[4];
#pragma unroll
      for (int s = 0; s < 4; ++s) kf[s] = lds16(LD, kb + s * 1024);
#pragma unroll
      for (int s = 0; s < 4; ++s) nm = mfma(kf[s], pq[s], nm);
      auto r = __builtin_amdgcn_permlane32_swap(__float_as_uint(nm[12]), __float_as_uint(nm[12]), false, false);
      const float den = __uint_as_float(r[1]) + 1e-6f;
      const float rd = __builtin_amdgcn_rcpf(den);
#pragma unroll
      for (int i = 0; i < 16; ++i) nm[i] *= rd;
      const int oo = opq(OFF_O + L * OB + (tt * 32 + 2 * j) * 1024);
      st16(rs, l16, oo, pack8(nm, 0));
      st16(rs, l16, oo + 1024, pack8(nm, 1));
    }
    wave_signal(ws, C_O + (L * 64 + tt) * 2 + (j >> 3), lane);
    STAMP(6);
    wait_cnt(ws, C_O + (L * 64 + tt2) * 2 + kh, 8);
    STAMP(7);
    {
      const int xo = opq(OFF_O + L * OB + (tt2 * 32 + 16 * kh) * 1024);
#pragma unroll
      for (int t = 0; t < XW; ++t) x[t] = ld16(rs, l16, xo + t * 1024);
      if (L == 0) RAWBAR();
      f32x16 a0 = {}, a1 = {};
      const int sb2 = opqv(2 * UNIT + kh * 2048 + opqv(l16)), sbx = opqv(L_W2X + kh * 2048 + opqv(l16));
      pipe<32>([&](auto I) AI { constexpr int i = I.value, t = i >> 1; return lds16(LD, (t < 14 ? sb2 + t * 4096 : sbx + (t - 14) * 4096) + (i & 1) * 1024); },
               [&](auto I) AI { if constexpr (I.value == 14) RAWBAR();   },
               [&](auto I, v4u a) AI {
                 constexpr int i = I.value;
                 if constexpr (i & 1) a1 = mfma(a, x[i >> 1], a1); else a0 = mfma(a, x[i >> 1], a0);
                 if constexpr (!(i & 1) && (i >> 1) + XW < 16) x[(i >> 1) + XW] = ld16(rs, l16, xo + ((i >> 1) + XW) * 1024);
               });
      const int xb = opqv(L_SCR + w * 4096 + opqv(l16)), xb2 = opqv(L_SCR + (w ^ 2) * 4096 + opqv(l16));
      f32x16 fin;
      if (kh) {
#pragma unroll
        for (int qd = 0; qd < 4; ++qd) *(LAS f32x4*)(LD + xb + qd * 1024) = f32x4{a0[4 * qd], a0[4 * qd + 1], a0[4 * qd + 2], a0[4 * qd + 3]};
        fin = a1;
      } else {
#pragma unroll
        for (int qd = 0; qd < 4; ++qd) *(LAS f32x4*)(LD + xb + qd * 1024) = f32x4{a1[4 * qd], a1[4 * qd + 1], a1[4 * qd + 2], a1[4 * qd + 3]};
        fin = a0;
      }
      BAR();
#pragma unroll
      for (int qd = 0; qd < 4; ++qd) {
        const f32x4 v = *(LAS f32x4*)(LD + xb2 + qd * 1024);
#pragma unroll
        for (int i = 0; i < 4; ++i) fin[4 * qd + i] += v[i];
      }
      finish_tile(fin, res, rs, opq(OFF_X1 + L * XB), opq(OFF_ST1 + L * STB), tt2, cb, kh, lane, false, A.out);
    }
    wave_signal(ws, C_X1 + L * 64 + tt2, lane);
    STAMP(8);
    wait_cnt(ws, C_X1 + L * 64 + tt, 16);
    STAMP(9);
    {
      const int xo = opq(OFF_X1 + L * XB + tt * 28 * 1024);
      const int so_ = opq(OFF_ST1 + L * STB + tt * 4096);
      const int sv_ = (opqv(lane) & 31) * 8 + h * 2048;
#pragma unroll
      for (int ks = 0; ks < XW; ++ks) x[ks] = ld16(rs, l16, xo + ks * 1024);
      arrive_lds(LD, 2, lane);
      const int ho = opq(OFF_H + L * HB + (tt * 112 + 7 * j) * 1024);
      u32* fo = cnt_ptr(ws, C_H + (L * 64 + tt) * 4);
      u32* fp = cnt_ptr(ws, C_H + (L * 64 + 2 * rb + (lane & 1)) * 4);
      u32 f = 0;
      float nmean, gs, irs;
      const int csb = opqv(L_CS3 + 16 * (opqv(lane) >> 5));
      const int sbA = opqv(l16), sbB = opqv(opqv(l16) + 2 * UNIT);
      f32x4 Gcg[2][2], Gcu[2][2]; float Gt[2][8], Gyu[2][8]; u32 Go[2][4];
      auto glu = [&](const f32x16& a, auto N_, auto S_, auto G_) AI {
        constexpr int n = N_.value, s = S_.value, G = G_.value;
        if constexpr (s == 0) Gcu[G][0] = *(LAS f32x4*)(LD + csb + n * 128 + 32);
        if constexpr (s == 5) Gcg[G][1] = *(LAS f32x4*)(LD + csb + n * 128 + 64);
        if constexpr (s == 10) Gcu[G][1] = *(LAS f32x4*)(LD + csb + n * 128 + 96);
        if constexpr (s == 23 && G == 0 && n < 6) Gcg[0][0] = *(LAS f32x4*)(LD + csb + (n == 0 ? 2 : n + 1) * 128);
        static_for<8>([&](auto V_) AI {
          constexpr int v = V_.value, k = s - (2 + (5 * v) / 2), hf = v >> 2, i = v & 3;
          if constexpr (k == 0) { Gt[G][v] = fmaf(nmean, Gcg[G][hf][i], a[8 * hf + i]); PIN(Gt[G][v]); }
          if constexpr (k == 1) { Gt[G][v] = Gt[G][v] * gs; PIN(Gt[G][v]); }
          if constexpr (k == 2) { Gt[G][v] = __builtin_amdgcn_exp2f(Gt[G][v]); PIN(Gt[G][v]); }
          if constexpr (k == 3) { Gt[G][v] = fmaf(Gt[G][v], irs, irs); PIN(Gt[G][v]); }
          if constexpr (k == 4) { Gt[G][v] = __builtin_amdgcn_rcpf(Gt[G][v]); PIN(Gt[G][v]); }
          if constexpr (k == 5) { Gyu[G][v] = fmaf(nmean, Gcu[G][hf][i], a[8 * hf + 4 + i]); PIN(Gyu[G][v]); }
          if constexpr (k == 6) { Gt[G][v] = Gt[G][v] * Gyu[G][v]; PIN(Gt[G][v]); }
          if constexpr (k == 7 && (v & 1)) { Go[G][v >> 1] = pk2(Gt[G][v - 1], Gt[G][v]); PIN(Go[G][v >> 1]); }
        });
        if constexpr (s == 27) {
          if constexpr (n == 3 || n == 5) {
            VMCNT(0);
            if (lane == 0) __hip_atomic_fetch_add(fo + 32 * (n == 3 ? 0 : 1), 1u, __ATOMIC_RELAXED, __HIP_MEMORY_SCOPE_AGENT);
          }
          st16(rs, l16, ho + n * 1024, v4u{Go[G][0], Go[G][1], Go[G][2], Go[G][3]});
        }
      };
      f32x16 acc[7];
      pipe<196>(
          [&](auto I) AI { constexpr int i = I.value, u = i / 28, sl = u & 3; return lds16(LD, (sl < 2 ? sbA : sbB) + (sl & 1) * UNIT + (i % 28) * 1024); },
          [&](auto I) AI { if constexpr (I.value > 0 && I.value % 28 == 0) RAWBAR(); },
          [&](auto I, v4u a) AI {
            constexpr int i = I.value, u = i / 28, s = i % 28;
            if constexpr (u < 2) {
              constexpr int n = i & 1, ks = i >> 1;
              if constexpr (ks == 0) { f32x16 z = {}; acc[n] = mfma(a, x[ks], z); } else acc[n] = mfma(a, x[ks], acc[n]);
              if constexpr (n == 0 && ks + XW < 28) x[ks + XW] = ld16(rs, l16, xo + (ks + XW) * 1024);
              if constexpr (n == 1 && ks >= 8 && ks < 16) st[ks - 8] = ld8(rs, sv_ + (ks - 8) * 256, so_);
              if constexpr (i == 50) Gcg[0][0] = *(LAS f32x4*)(LD + csb);
              if constexpr (i == 51) Gcg[1][0] = *(LAS f32x4*)(LD + csb + 128);
              if constexpr (i == 53) {
                ln_stats(st, mean, rstd);
                nmean = -mean; gs = -LOG2E * rstd; irs = __builtin_amdgcn_rcpf(rstd);
              }
            } else {
              if constexpr (s == 0) { f32x16 z = {}; acc[u] = mfma(a, x[s], z); } else acc[u] = mfma(a, x[s], acc[u]);
              if constexpr (u == 2) { glu(acc[0], IC<0>{}, IC<s>{}, IC<0>{}); glu(acc[1], IC<1>{}, IC<s>{}, IC<1>{}); }
              else glu(acc[u - 1], IC<u - 1>{}, IC<s>{}, IC<0>{});
              if constexpr (i == 176) f = __hip_atomic_load(fp, __ATOMIC_RELAXED, __HIP_MEMORY_SCOPE_AGENT);
            }
          });
      RAWBAR();
      STAMP(10);
      const int ho2 = opq(OFF_H + L * HB + 2 * rb * 112 * 1024);
      const int hc = opq(w + 4);
      const int hf_ = opq(OFF_H + L * HB + (tt2 * 112 + 6) * 1024);
      constexpr int HW = 8;
      v4u hx[36];
      auto ldh = [&](auto I) AI {
        constexpr int i = I.value, t = i >> 1;
        if constexpr (i < 20) return ld16(rs, l16, ho2 + (i & 1) * 112 * 1024 + p4_koff(t / 5, t % 5, hc));
        else return ld16(rs, l16, hf_ + (i - 20) * 7 * 1024);
      };
      auto lvl = [](int i) constexpr { return i < 20 ? p4_lvl((i >> 1) / 5, (i >> 1) % 5) : 2; };
      wait_flags(ws, fp, f);
      static_for<HW>([&](auto I) AI { hx[I.value] = ldh(I); });
      static_for<28>([&](auto S_) AI { glu(acc[6], IC<6>{}, S_, IC<0>{}); });
      STAMP(11);
      const int wa0 = opqv(w * 2048 + opqv(l16)), wa1 = opqv(w * 2048 + opqv(l16) + 5 * 8192);
      const int wfb = opqv(L_FRESH + kh * 1024 + opqv(l16));
      v4u wq[20];
      auto ldw = [&](auto K) AI { constexpr int k = K.value, t = k >> 1; return lds16(LD, (t < 5 ? wa0 : wa1) + (t % 5) * 8192 + (k & 1) * 1024); };
      static_for<4>([&](auto K) AI { wq[K.value] = ldw(K); });
      f32x16 pc[4] = {};
      f32x16 fin;
      v4u wfr[16];
      static_for<20>([&](auto I) AI {
        constexpr int i = I.value, t = i >> 1, tq = i & 1, nx = i + HW;
        if constexpr (i == 8) RAWBAR();
        if constexpr (i + 4 < 20) wq[i + 4] = ldw(IC<i + 4>{});
        else wfr[i - 16] = lds16(LD, wfb + (i - 16) * 2048);
        if constexpr (i == 2) {
          VMCNT(3);
          if (lane == 0) __hip_atomic_fetch_add(fo + 64, 1u, __ATOMIC_RELAXED, __HIP_MEMORY_SCOPE_AGENT);
        }
        pc[tq] = mfma(wq[2 * t], hx[i], pc[tq]);
        pc[2 + tq] = mfma(wq[2 * t + 1], hx[i], pc[2 + tq]);
        if constexpr (lvl(nx + 2) > lvl(nx + 1)) f = __hip_atomic_load(fp + 32 * lvl(nx + 2), __ATOMIC_RELAXED, __HIP_MEMORY_SCOPE_AGENT);
        if constexpr (lvl(nx) > lvl(nx - 1)) wait_flags(ws, fp + 32 * lvl(nx), f);
        hx[nx] = ldh(IC<nx>{});
        SB();
      });
      RAWBAR();
      p4_put(pc[0], 0, w, LD, l16, fin);
      p4_put(pc[2], 2, w, LD, l16, fin);
      p4_put(pc[1], 1, w, LD, l16, fin);
      p4_put(pc[3], 3, w, LD, l16, fin);
      static_for<16>([&](auto I) AI {
        constexpr int i = I.value;
        if constexpr (i + 4 < 16) wfr[i + 4] = lds16(LD, wfb + (i + 4) * 2048);
        fin = mfma(wfr[i], hx[20 + i], fin);
        if constexpr (20 + i + HW < 36) hx[20 + i + HW] = ldh(IC<20 + i + HW>{});
        SB();
      });
      STAMP(14);
      BAR();
#pragma unroll
      for (int ww = 0; ww < 7; ++ww) {
        const int rr = opqv(p4_part(w, ww) + opqv(l16));
#pragma unroll
        for (int qd = 0; qd < 4; ++qd) {
          const f32x4 v = *(LAS f32x4*)(LD + rr + qd * 1024);
#pragma unroll
          for (int i = 0; i < 4; ++i) fin[4 * qd + i] += v[i];
        }
      }
      STAMP(15);
      finish_tile(fin, res, rs, OFF_X2, OFF_ST2, tt2, cb, kh, lane, L == 1, A.out);
    }
    if (L == 0) {
      wave_signal(ws, C_X2 + tt2, lane);
      arrive_lds(LD, 0, lane);
      STAMP(16);
      wait_cnt(ws, C_X2 + tt, 16);
    }
    STAMP(17);
  };
  clayer(IC<0>{});
  clayer(IC<1>{});
}

extern "C" void kernel_launch(void* const* d_in, const int* in_sizes, int n_in, void* d_out, int out_size, void* d_ws,
                              size_t ws_size, hipStream_t stream) {
  Args a;
  a.x0 = (const u16*)d_in[0]; a.wqkv = (const u16*)d_in[1]; a.wphi = (const u16*)d_in[2]; a.wo = (const u16*)d_in[3];
  a.wg = (const u16*)d_in[5]; a.wu = (const u16*)d_in[6]; a.wd = (const u16*)d_in[7];
  a.ws = (char*)d_ws; a.out = (u16*)d_out;
  (void)hipFuncSetAttribute((const void*)k_main, hipFuncAttributeMaxDynamicSharedMemorySize, LDS_BYTES);
  k_main<<<256, 512, LDS_BYTES, stream>>>(a);
}
```

```cpp
#include <hip/hip_runtime.h>
#include <stdint.h>
#include <utility>
#include <type_traits>

typedef unsigned short u16;
typedef unsigned int u32;
typedef u32 v4u __attribute__((ext_vector_type(4)));
typedef u32 v2u __attribute__((ext_vector_type(2)));
typedef float f32x2 __attribute__((ext_vector_type(2)));
typedef float f32x4 __attribute__((ext_vector_type(4)));
typedef float f32x16 __attribute__((ext_vector_type(16)));
typedef __bf16 bf16x2 __attribute__((ext_vector_type(2)));
typedef __bf16 bf16x8 __attribute__((ext_vector_type(8)));
typedef short s16x4 __attribute__((ext_vector_type(4)));
#define LAS __attribute__((address_space(3)))
#define DI __device__ __forceinline__
#define AI __attribute__((always_inline))
template <int V> using IC = std::integral_constant<int, V>;

constexpr int S = 2048, D = 448, FF = 1792;
constexpr int UNIT = 28672;

constexpr int C_KV = 0;
constexpr int C_W2 = 32;
constexpr int C_W4 = 36;
constexpr int C_W3 = 40;
constexpr int C_ERR = 60;
constexpr int C_X2 = 64;
constexpr int C_X1 = 128;
constexpr int C_O = 256;
constexpr int C_H = 512;
constexpr int EPOCH_OFF = 1024 * 128;
constexpr int INIT_OFF = EPOCH_OFF + 256;
constexpr int CTL_BYTES = 1024 * 128 + 512;
constexpr int W2B = 65536;
constexpr int OFF_W2 = CTL_BYTES;
constexpr int OFF_W3 = OFF_W2 + 8 * W2B;
constexpr int OFF_W4 = OFF_W3 + 16 * 7 * UNIT;
constexpr int OFF_CS3 = OFF_W4 + 8 * 8 * UNIT;
constexpr int XB = S * D * 2;
constexpr int OB = S * 512 * 2;
constexpr int OFF_X2 = OFF_CS3 + 16 * 28 * 896;
constexpr int OFF_X1 = OFF_X2 + XB;
constexpr int OFF_O = OFF_X1 + 2 * XB;
constexpr int STB = S * 16 * 8;
constexpr int OFF_ST2 = OFF_O + 2 * OB;
constexpr int OFF_ST1 = OFF_ST2 + STB;
constexpr int KVB = 16 * 16 * 4096;
constexpr int OFF_KVP = OFF_ST1 + 2 * STB;
constexpr int HB = S * FF * 2;
constexpr int OFF_H = OFF_KVP + 2 * KVB;
constexpr int WS_USED = OFF_H + 2 * HB;

constexpr int L_SCR = 4 * UNIT;
constexpr int PHK_STRIDE = 136, V_STRIDE = 72;
constexpr int L_PHK = L_SCR;
constexpr int L_V = L_SCR + 128 * PHK_STRIDE;
constexpr int L_KVF = L_SCR + 28672;
constexpr int L_W2X = L_KVF + 4096;
constexpr int L_CONST = 155648;
constexpr int L_CS1 = L_CONST, L_CS3 = L_CONST + 384, L_WPHI = L_CONST + 1280, L_ARR = L_CONST + 3328;
constexpr int L_FRESH = 81920;
constexpr int L_STAGE = 2 * UNIT;
constexpr int LDS_BYTES = 163840;

constexpr int CODE_PF_LINES = 1100;
constexpr float LOG2E = 1.4426950408889634f;
constexpr int LAG = 10;

DI u32 pk2(float a, float b) { f32x2 v = {a, b}; return __builtin_bit_cast(u32, __builtin_convertvector(v, bf16x2)); }
DI float bflo(u32 v) { return __uint_as_float(v << 16); }
DI float bfhi(u32 v) { return __uint_as_float(v & 0xffff0000u); }
DI f32x16 mfma(v4u a, v4u b, f32x16 c) {
  f32x16 d = __builtin_amdgcn_mfma_f32_32x32x16_bf16(__builtin_bit_cast(bf16x8, a), __builtin_bit_cast(bf16x8, b), c, 0, 0, 0);
  asm volatile("" : "+v"(d));
  return d;
}
DI float xsum(float v) {
  auto r = __builtin_amdgcn_permlane32_swap(__float_as_uint(v), __float_as_uint(v), false, false);
  return __uint_as_float(r[0]) + __uint_as_float(r[1]);
}
DI float xmax(float v) {
  auto r = __builtin_amdgcn_permlane32_swap(__float_as_uint(v), __float_as_uint(v), false, false);
  return fmaxf(__uint_as_float(r[0]), __uint_as_float(r[1]));
}
DI float xmin(float v) {
  auto r = __builtin_amdgcn_permlane32_swap(__float_as_uint(v), __float_as_uint(v), false, false);
  return fminf(__uint_as_float(r[0]), __uint_as_float(r[1]));
}
#define BAR() asm volatile("s_waitcnt lgkmcnt(0)\n\ts_barrier" ::: "memory")
#define RAWBAR() asm volatile("s_barrier" ::: "memory")
#define VMCNT(n) asm volatile("s_waitcnt vmcnt(" #n ")" ::: "memory")
#define SB() __builtin_amdgcn_sched_barrier(0)
#define PIN(x) asm volatile("" : "+v"(x))

typedef __amdgpu_buffer_rsrc_t rsrc_t;
DI v4u ld16(rsrc_t rs, int voff, int soff) { return __builtin_amdgcn_raw_buffer_load_b128(rs, voff, soff, 16); }
DI v2u ld8(rsrc_t rs, int voff, int soff) { return __builtin_amdgcn_raw_buffer_load_b64(rs, voff, soff, 16); }
DI u32 ld4(rsrc_t rs, int voff, int soff) { return __builtin_amdgcn_raw_buffer_load_b32(rs, voff, soff, 16); }
DI void st16(rsrc_t rs, int voff, int soff, v4u v) {
  __builtin_amdgcn_raw_buffer_store_b128(v, rs, voff, soff, 16);
  asm volatile("s_nop 1" ::"v"(v) : "memory");
}
DI void st8(rsrc_t rs, int voff, int soff, v2u v) { __builtin_amdgcn_raw_buffer_store_b64(v, rs, voff, soff, 16); }
DI void st4(rsrc_t rs, int voff, int soff, u32 v) { __builtin_amdgcn_raw_buffer_store_b32(v, rs, voff, soff, 16); }
DI int opq(int v) { asm volatile("" : "+s"(v)); return v; }
DI int opqv(int v) { asm volatile("" : "+v"(v)); return v; }
DI v4u lds16(LAS char* l, int off) { return *(LAS v4u*)(l + off); }
DI float dot2(u32 a, u32 b, float c) {
  return __builtin_amdgcn_fdot2_f32_bf16(__builtin_bit_cast(bf16x2, a), __builtin_bit_cast(bf16x2, b), c, false);
}
DI v4u tr_frag(LAS char* L, int base, int step) {
  v2u lo = __builtin_bit_cast(v2u, __builtin_amdgcn_ds_read_tr16_b64_v4i16((LAS s16x4*)(L + base)));
  v2u hi = __builtin_bit_cast(v2u, __builtin_amdgcn_ds_read_tr16_b64_v4i16((LAS s16x4*)(L + base + step)));
  return v4u{lo.x, lo.y, hi.x, hi.y};
}
DI float sum8(v4u o) {
  const u32 one = 0x3f803f80u;
  return dot2(o.x, one, dot2(o.y, one, 0.f)) + dot2(o.z, one, dot2(o.w, one, 0.f));
}

DI u32* cnt_ptr(char* ws, int idx) { return (u32*)(ws + idx * 128); }
DI bool spin_more(char* ws, u32& spins) {
  u32* e = cnt_ptr(ws, C_ERR);
  __builtin_amdgcn_s_sleep(1);
  ++spins;
  if ((spins & 255u) == 0u) {
    if (__builtin_amdgcn_readfirstlane(__hip_atomic_load(e, __ATOMIC_RELAXED, __HIP_MEMORY_SCOPE_AGENT)) != 0) return false;
    if (spins > 400000u) { __hip_atomic_store(e, 1u, __ATOMIC_RELAXED, __HIP_MEMORY_SCOPE_AGENT); return false; }
  }
  return true;
}
DI void wait_cnt(char* ws, int idx, u32 target) {
  u32* c = cnt_ptr(ws, idx);
  u32 spins = 0;
  while ((u32)__builtin_amdgcn_readfirstlane(__hip_atomic_load(c, __ATOMIC_RELAXED, __HIP_MEMORY_SCOPE_AGENT)) < target)
    if (!spin_more(ws, spins)) break;
  __builtin_amdgcn_fence(__ATOMIC_ACQUIRE, "wavefront");
  asm volatile("" ::: "memory");
}
DI void wait_flags(char* ws, u32* fp, u32 f) {
  u32 spins = 0;
  while (!__all(f >= 16u)) {
    if (!spin_more(ws, spins)) break;
    f = __hip_atomic_load(fp, __ATOMIC_RELAXED, __HIP_MEMORY_SCOPE_AGENT);
  }
  __builtin_amdgcn_fence(__ATOMIC_ACQUIRE, "wavefront");
  asm volatile("" ::: "memory");
}
DI void signal_cnt(char* ws, int idx) { __hip_atomic_fetch_add(cnt_ptr(ws, idx), 1u, __ATOMIC_RELAXED, __HIP_MEMORY_SCOPE_AGENT); }
DI void wave_signal(char* ws, int idx, int lane) { VMCNT(0); if (lane == 0) signal_cnt(ws, idx); }
DI void arrive_lds(LAS char* LD, int slot, int lane) {
  if (lane == 0) __hip_atomic_fetch_add((LAS u32*)(LD + L_ARR + slot * 4), 1u, __ATOMIC_RELAXED, __HIP_MEMORY_SCOPE_WORKGROUP);
}
DI void wait_arr(LAS char* LD, int slot, u32 target) {
  while ((u32)__builtin_amdgcn_readfirstlane(__hip_atomic_load((LAS u32*)(LD + L_ARR + slot * 4), __ATOMIC_RELAXED, __HIP_MEMORY_SCOPE_WORKGROUP)) < target)
    __builtin_amdgcn_s_sleep(1);
}

#define STAMP(i)
#define USTAMP(i)

#define DMA_STEP(GSTEP) \
      "s_add_u32 m0, m0, 0x1000\n\t" \
      "v_add_u32 %1, " GSTEP ", %1\n\t" \
      "global_load_lds_dwordx4 %1, %2 sc1\n\t"
#define DMA_FN(NAME, BODY) \
DI void NAME(const char* gsrc, u32 voff, u32 lds_dst) { \
  u32 keep; \
  asm volatile("s_mov_b32 %0, m0\n\ts_mov_b32 m0, %3\n\ts_nop 0\n\tglobal_load_lds_dwordx4 %1, %2 sc1\n\t" BODY "s_mov_b32 m0, %0" \
               : "=&s"(keep), "+v"(voff) : "s"(gsrc), "s"(lds_dst) : "memory", "scc"); }
DMA_FN(dma7, DMA_STEP("0x1000") DMA_STEP("0x1000") DMA_STEP("0x1000") DMA_STEP("0x1000") DMA_STEP("0x1000") DMA_STEP("0x1000"))
DMA_FN(dma2, DMA_STEP("0x1000"))
DMA_FN(dma7n, DMA_STEP("0x2a000") DMA_STEP("0x2a000") DMA_STEP("0x2a000") DMA_STEP("0x2a000") DMA_STEP("0x2a000") DMA_STEP("0x2a000"))

template <typename F, int... I>
DI void static_for_impl(F&& f, std::integer_sequence<int, I...>) { (f(IC<I>{}), ...); }
template <int N, typename F>
DI void static_for(F&& f) { static_for_impl(f, std::make_integer_sequence<int, N>{}); }

constexpr int PF = 6;
template <int N, typename RD, typename PRE, typename ST>
DI void pipe(RD rd, PRE pre, ST step) {
  v4u buf[PF];
  static_for<PF>([&](auto I) AI { buf[I.value] = rd(I); });
  SB();
  static_for<N>([&](auto I) AI {
    constexpr int i = I.value;
    pre(I);
    const v4u a = buf[i % PF];
    if constexpr (i + PF < N) buf[i % PF] = rd(IC<i + PF>{});
    step(I, a);
    SB();
  });
}

DI void ln_finish(float s, float q, float& mean, float& rstd) {
  s = xsum(s); q = xsum(q);
  mean = s * (1.f / 448.f);
  const float var = fmaxf(q * (1.f / 448.f) - mean * mean, 0.f);
  rstd = __builtin_amdgcn_rsqf(var + 1e-5f);
}
DI void ln_stats(const v2u (&st)[8], float& mean, float& rstd) {
  f32x2 a = __builtin_bit_cast(f32x2, st[0]), b = __builtin_bit_cast(f32x2, st[1]);
#pragma unroll
  for (int i = 2; i < 8; i += 2) { a += __builtin_bit_cast(f32x2, st[i]); b += __builtin_bit_cast(f32x2, st[i + 1]); }
  a += b;
  ln_finish(a[0], a[1], mean, rstd);
}
DI v4u pack8(const f32x16& a, int s) {
  v4u r = {pk2(a[8 * s], a[8 * s + 1]), pk2(a[8 * s + 2], a[8 * s + 3]), pk2(a[8 * s + 4], a[8 * s + 5]), pk2(a[8 * s + 6], a[8 * s + 7])};
  return r;
}
DI void dual_softmax(const f32x16& p, int h, float rstd, v4u (&out)[4]) {
  float mx = p[0], mn = p[0];
#pragma unroll
  for (int r = 1; r < 12; ++r) { mx = fmaxf(mx, p[r]); mn = fminf(mn, p[r]); }
#pragma unroll
  for (int r = 12; r < 16; ++r) { const float v = h ? p[0] : p[r]; mx = fmaxf(mx, v); mn = fminf(mn, v); }
  mx = xmax(mx); mn = xmin(mn);
  const float c = rstd * LOG2E;
#pragma unroll
  for (int sg = 0; sg < 2; ++sg) {
    const float cc = sg ? -c : c, off = sg ? mn * c : -mx * c;
    f32x16 e;
    float s0 = 0.f, s1 = 0.f;
#pragma unroll
    for (int r = 0; r < 16; ++r) {
      float v = __builtin_amdgcn_exp2f(fmaf(p[r], cc, off));
      if (r >= 12) v = h ? 0.f : v;
      e[r] = v;
      if (r & 1) s1 += v; else s0 += v;
    }
    const float rs_ = __builtin_amdgcn_rcpf(xsum(s0 + s1));
#pragma unroll
    for (int r = 0; r < 16; ++r) e[r] *= rs_;
    out[2 * sg] = pack8(e, 0); out[2 * sg + 1] = pack8(e, 1);
    PIN(out[2 * sg]); PIN(out[2 * sg + 1]);
    SB();
  }
}

DI void finish_tile(const f32x16& acc, v4u (&res)[2], rsrc_t rs, int xoff, int stoff, int tt, int cb, int nt, int lane, bool last, u16* out) {
  lane = opqv(lane);
  const int h = lane >> 5, tok = lane & 31;
#pragma unroll
  for (int ro = 0; ro < 2; ++ro) {
    v4u r = res[ro];
    v4u o;
    o.x = pk2(bflo(r.x) + acc[8 * ro + 0], bfhi(r.x) + acc[8 * ro + 1]);
    o.y = pk2(bflo(r.y) + acc[8 * ro + 2], bfhi(r.y) + acc[8 * ro + 3]);
    o.z = pk2(bflo(r.z) + acc[8 * ro + 4], bfhi(r.z) + acc[8 * ro + 5]);
    o.w = pk2(bflo(r.w) + acc[8 * ro + 6], bfhi(r.w) + acc[8 * ro + 7]);
    res[ro] = o;
    const int oi = nt * 4 + ro * 2 + h;
    if (oi < 7) {
      const int go = 7 * cb + oi;
      if (last) *(v4u*)(out + (size_t)(tt * 32 + tok) * D + go * 8) = o;
      else st16(rs, ((go >> 1) * 64 + tok + 32 * (go & 1)) * 16, xoff + tt * 28 * 1024, o);
    }
  }
  if (!last) {
    const bool v1 = !(nt == 1 && h == 1);
    const u32 one = 0x3f803f80u;
    float s0 = dot2(res[0].x, one, 0.f), s1 = dot2(res[1].x, one, 0.f), q0 = dot2(res[0].x, res[0].x, 0.f), q1 = dot2(res[1].x, res[1].x, 0.f);
    s0 = dot2(res[0].y, one, s0); s1 = dot2(res[1].y, one, s1); q0 = dot2(res[0].y, res[0].y, q0); q1 = dot2(res[1].y, res[1].y, q1);
    s0 = dot2(res[0].z, one, s0); s1 = dot2(res[1].z, one, s1); q0 = dot2(res[0].z, res[0].z, q0); q1 = dot2(res[1].z, res[1].z, q1);
    s0 = dot2(res[0].w, one, s0); s1 = dot2(res[1].w, one, s1); q0 = dot2(res[0].w, res[0].w, q0); q1 = dot2(res[1].w, res[1].w, q1);
    const float sum = xsum(s0 + (v1 ? s1 : 0.f));
    const float sq = xsum(q0 + (v1 ? q1 : 0.f));
    if (h == 0) st8(rs, tok * 8, stoff + ((tt * 8 + cb) * 2 + nt) * 256, v2u{__float_as_uint(sum), __float_as_uint(sq)});
  }
}

DI void kv_reduce(rsrc_t rs, LAS char* LD, int L, int j, int w, int lane) {
  const int ko = opq(OFF_KVP + L * KVB + j * 16 * 4096 + (w >> 1) * 1024 + (w & 1) * 512);
  const int lz = opqv(lane);
  const int vo = (lz >> 5) * 4096 + (lz & 31) * 16;
  v4u t[8];
#pragma unroll
  for (int i = 0; i < 8; ++i) t[i] = ld16(rs, vo, ko + i * 8192);
  float s[8] = {};
  const u32 lo1 = (u32)opq(0x00003f80), hi1 = (u32)opq(0x3f800000);
#pragma unroll
  for (int i = 0; i < 8; ++i) {
#pragma unroll
    for (int c = 0; c < 4; ++c) { s[2 * c] = dot2(t[i][c], lo1, s[2 * c]); s[2 * c + 1] = dot2(t[i][c], hi1, s[2 * c + 1]); }
  }
#pragma unroll
  for (int c = 0; c < 8; ++c) s[c] = xsum(s[c]);
  if (lz < 32) *(LAS v4u*)(LD + L_KVF + (w >> 1) * 1024 + (w & 1) * 512 + lz * 16) = v4u{pk2(s[0], s[1]), pk2(s[2], s[3]), pk2(s[4], s[5]), pk2(s[6], s[7])};
}

DI int p4_part(int a, int i) {
  return a == 0 ? i * 4096 : a == 1 ? UNIT + i * 4096 : a == 2 ? L_SCR + i * 4096 : (i < 6 ? 2 * UNIT + i * 4096 : L_SCR + UNIT);
}
DI void p4_put(const f32x16& acc, int a, int w, LAS char* LD, int l16, f32x16& keep) {
  if (w == a) { keep = acc; return; }
  const int off = opqv(p4_part(a, w - (w > a ? 1 : 0)) + opqv(l16));
#pragma unroll
  for (int qd = 0; qd < 4; ++qd) *(LAS f32x4*)(LD + off + qd * 1024) = f32x4{acc[4 * qd], acc[4 * qd + 1], acc[4 * qd + 2], acc[4 * qd + 3]};
}
DI int p4_koff(int blk, int tp, int c) {
  const int o = 48 * blk + (tp < 5 ? 8 * tp + c : 40 + 4 * (tp - 5) + c);
  return (7 * (o & 15) + (o >> 4)) * 1024;
}
constexpr int p4_lvl(int blk, int tp) { return blk == 0 ? 0 : (tp < 4 ? 1 : 2); }

struct Args {
  const u16* x0; const u16* wqkv; const u16* wphi; const u16* wo; const u16* wg; const u16* wu; const u16* wd;
  char* ws; u16* out;
};

DI void retile_load(const Args& A, int job, int lane, v4u (&t)[7]) {
  if (job < 64 || job >= 512) {
    const bool iswo = job < 64;
    const int a0 = iswo ? job >> 2 : (job - 512) >> 2, cg = job & 3;
    const u16* src = (iswo ? A.wo + (size_t)a0 * 28 * D : A.wd + (size_t)a0 * 32 * D) + 112 * cg;
    const int nrow = iswo ? 28 : 32;
#pragma unroll
    for (int i = 0; i < 7; ++i) {
      const int p = lane + 64 * i, r = p / 14, c = p % 14;
      t[i] = *(const v4u*)(src + (size_t)(r < nrow ? r : 0) * D + c * 8);
    }
  } else {
    const int a = job - 64, j = a / 28, ks = a % 28;
#pragma unroll
    for (int i = 0; i < 7; ++i) {
      const int p = lane + 64 * i, r = p / 28, c = p % 28;
      t[i] = *(const v4u*)((c < 14 ? A.wg : A.wu) + (size_t)(ks * 16 + r) * FF + 112 * j + (c < 14 ? c : c - 14) * 8);
    }
  }
}
DI int retile_store(int job, rsrc_t rs, LAS char* LD, int stage, int lane, const v4u (&t)[7]) {
  const int h = lane >> 5, q = (lane & 15) >> 2, c8 = ((lane >> 4) & 1) * 4 + (lane & 3);
  const int hh = c8 & 1, quad = c8 >> 1;
  if (job < 64 || job >= 512) {
    const bool iswo = job < 64;
    const int a0 = iswo ? job >> 2 : (job - 512) >> 2, cg = job & 3;
    const int nrow = iswo ? 28 : 32;
    constexpr int ST = 240;
#pragma unroll
    for (int i = 0; i < 7; ++i) {
      const int p = lane + 64 * i, r = p / 14, c = p % 14;
      *(LAS v4u*)(LD + stage + r * ST + c * 16) = r < nrow ? t[i] : v4u{0u, 0u, 0u, 0u};
    }
#pragma unroll
    for (int i = 0; i < 4; ++i) {
      const int cb = cg * 2 + (i >> 1), nt = i & 1;
      const int oi = nt * 4 + (quad >> 1) * 2 + hh;
      const int ro = (lane >> 4) & 1, rh = (lane >> 2) & 1;
      const bool valid = nt * 4 + ro * 2 + rh < 7;
      const int colb = (56 * (i >> 1) + 8 * (oi < 7 ? oi : 6) + 4 * (quad & 1)) * 2;
#pragma unroll
      for (int k2 = 0; k2 < 2; ++k2) {
        v4u o = iswo ? tr_frag(LD, stage + (16 * k2 + 4 * h + q) * ST + colb, 8 * ST) : tr_frag(LD, stage + (16 * k2 + 8 * h + q) * ST + colb, 4 * ST);
        if (!valid) o = v4u{0u, 0u, 0u, 0u};
        const int ks = a0 * 2 + k2;
        int off;
        if (iswo) off = OFF_W2 + cb * W2B + (((ks & 15) * 2 + (ks >> 4)) * 2 + nt) * 1024;
        else {
          const int jp = ks / 7, s = ks % 7;
          int fr;
          if (s == 6) fr = 80 + jp * 2 + nt;
          else {
            const int o_ = 16 * s + jp, blk = o_ / 48, r = o_ % 48;
            const int c = r < 40 ? (r & 7) : ((r - 40) & 3), tp = r < 40 ? (r >> 3) : 5 + ((r - 40) >> 2);
            if (c < 4) fr = 112 + (c * 14 + blk * 7 + tp) * 2 + nt;
            else fr = ((blk * 5 + tp) * 4 + (c - 4)) * 2 + nt;
          }
          off = OFF_W4 + cb * 8 * UNIT + fr * 1024;
        }
        st16(rs, lane * 16, off, o);
      }
    }
    return (iswo ? C_W2 : C_W4) + cg;
  } else {
    const int a = job - 64, j = a / 28, ks = a % 28;
    constexpr int ST = 464;
#pragma unroll
    for (int i = 0; i < 7; ++i) { const int p = lane + 64 * i, r = p / 28, c = p % 28; *(LAS v4u*)(LD + stage + r * ST + c * 16) = t[i]; }
    float cs[7];
#pragma unroll
    for (int n = 0; n < 7; ++n) {
      const v4u o = tr_frag(LD, stage + (8 * h + q) * ST + (quad & 1) * 224 + (16 * n + 8 * hh + 4 * (quad >> 1)) * 2, 4 * ST);
      st16(rs, lane * 16, OFF_W3 + j * 7 * UNIT + (n < 2 ? (ks * 2 + n) * 1024 : n * UNIT + ks * 1024), o);
      cs[n] = xsum(sum8(o));
    }
#pragma unroll
    for (int n2 = 0; n2 < 4; ++n2) {
      const float v = h ? cs[n2 < 3 ? 2 * n2 + 1 : 6] : cs[2 * n2];
      if (n2 < 3 || !h) st4(rs, lane * 4, OFF_CS3 + (j * 28 + ks) * 896 + n2 * 256, __float_as_uint(v));
    }
    return C_W3 + j;
  }
}

__global__ __launch_bounds__(512, 2) void k_main(const Args A) {
  extern __shared__ __attribute__((aligned(16))) char smem[];
  LAS char* LD = (LAS char*)smem;
  char* ws = A.ws;
  const int b = blockIdx.x, xcd = b & 7, bi = b >> 3;
  const int g = (xcd >> 2) * 8 + (bi >> 2), j = (xcd & 3) * 4 + (bi & 3);
  const int rb = xcd * 4 + (bi >> 3), cb = bi & 7;
  const int lane = threadIdx.x & 63, w = __builtin_amdgcn_readfirstlane(threadIdx.x >> 6);
  const int h = lane >> 5, tok = lane & 31;
  const rsrc_t rs = __builtin_amdgcn_make_buffer_rsrc((void*)ws, 0, WS_USED, 0x00020000);
  const u32 lbase = (u32)(size_t)smem;
  const int l16 = lane * 16;
  const int tq_ = (lane & 15) >> 2, tc8 = ((lane >> 4) & 1) * 4 + (lane & 3);
  const int w1lane = (8 * h + tq_) * 64 + min(tc8 + (j & 1), 7) * 8;

  u32 codepf = 0;
  if (w == 7) {
    typedef const __attribute__((address_space(1))) u32* gptr;
    const unsigned long long pc = __builtin_amdgcn_s_getpc();
    codepf = *(gptr)(pc + (unsigned long long)min(bi * 64 + lane, CODE_PF_LINES - 1) * 64);
  }
  if (w >= 4) {
    const int hw = w - 4;
    const u32 voff = hw * 1024 + lane * 16;
    const u32 voffn = (hw * 16 + (lane >> 2)) * (3 * D * 2) + (lane & 3) * 16;
    const char* w1 = (const char*)A.wqkv + 56 * j - (j & 1) * 8;
    const char* w2 = ws + OFF_W2 + cb * W2B;
    const char* w3 = ws + OFF_W3 + j * 7 * UNIT;
    const char* w4 = ws + OFF_W4 + cb * 8 * UNIT;
#define DMA(src, slot) dma7((src), voff, lbase + (slot) * UNIT + hw * 1024)
#define DMAN(which, slot) dma7n(w1 + (which) * 896, voffn, lbase + (slot) * UNIT + hw * 1024)
    v4u wr[28];
    unsigned long long tag = 0;
    auto hlayer = [&](auto LC) AI {
      constexpr int L = LC.value;
      if (L > 0) wait_arr(LD, 0, 4);
      if (L == 0 && hw == 3) {
        if (lane == 0) *(LAS v4u*)(LD + L_ARR) = v4u{0u, 0u, 0u, 0u};
        tag = __hip_atomic_load((unsigned long long*)(ws + EPOCH_OFF), __ATOMIC_RELAXED, __HIP_MEMORY_SCOPE_AGENT) + 1ull;
        if (b == 0) {
#pragma unroll
          for (int i = 0; i < 16; ++i) st4(rs, lane * 128, i * 8192, 0u);
          VMCNT(0);
          if (lane == 0) __hip_atomic_store((unsigned long long*)(ws + INIT_OFF), tag, __ATOMIC_RELAXED, __HIP_MEMORY_SCOPE_AGENT);
        }
      }
      DMAN(1, 4);
      DMAN(2, 0);
      v4u jt[7];
      const int job = hw >= 2 ? b * 2 + hw - 2 : (hw == 0 && b < 224 ? 512 + b : -1);
      VMCNT(7); BAR();
      auto colsum = [&](int slot, int which) AI {
        const int ib = opqv(slot * UNIT + opqv(w1lane));
        v4u fr[28];
#pragma unroll
        for (int ks = 0; ks < 28; ++ks) fr[ks] = tr_frag(LD, ib + ks * 1024, 256);
        SB();
        const u32 one = 0x3f803f80u;
        float c0 = 0.f, c1 = 0.f, c2 = 0.f, c3 = 0.f;
#pragma unroll
        for (int ks = 0; ks < 28; ++ks) { c0 = dot2(fr[ks].x, one, c0); c1 = dot2(fr[ks].y, one, c1); c2 = dot2(fr[ks].z, one, c2); c3 = dot2(fr[ks].w, one, c3); }
        const float cs = xsum((c0 + c1) + (c2 + c3));
        if (lane < 32) *(LAS float*)(LD + L_CS1 + which * 128 + lane * 4) = cs;
      };
      VMCNT(0); BAR();
      if (L == 0) {
        asm volatile("" ::"v"(codepf));
        if (hw == 0) colsum(4, 0);
        if (hw == 1) colsum(0, 1);
        if (hw == 2) {
          const int f = lane & 31, fc = min(f, 27);
          u32 e[16];
#pragma unroll
          for (int i = 0; i < 16; ++i) {
            const int d0 = 16 * (i >> 3) + 8 * ((i >> 2) & 1) + 4 * h + (i & 3);
            e[i] = A.wphi[(j * 28 + min(d0, 27)) * 28 + fc];
          }
#pragma unroll
          for (int i = 0; i < 16; ++i) {
            const int d0 = 16 * (i >> 3) + 8 * ((i >> 2) & 1) + 4 * h + (i & 3);
            e[i] = (d0 < 28 && f < 28) ? e[i] : 0u;
          }
#pragma unroll
          for (int s = 0; s < 2; ++s)
            *(LAS v4u*)(LD + L_WPHI + s * 1024 + l16) = v4u{e[8 * s] | (e[8 * s + 1] << 16), e[8 * s + 2] | (e[8 * s + 3] << 16),
                                                           e[8 * s + 4] | (e[8 * s + 5] << 16), e[8 * s + 6] | (e[8 * s + 7] << 16)};
        }
        if (hw == 3) {
          u32 spins = 0;
          while (__builtin_amdgcn_readfirstlane((int)(__hip_atomic_load((unsigned long long*)(ws + INIT_OFF), __ATOMIC_RELAXED, __HIP_MEMORY_SCOPE_AGENT) != tag))) {
            __builtin_amdgcn_s_sleep(1);
            if (++spins > 400000u) break;
          }
        }
      } else DMAN(0, 1);
      BAR();
      if (L == 0) {
        DMAN(0, 1);
        if (job >= 0) retile_load(A, job, lane, jt);
      }
      BAR();
      int jsig = -1;
      if (L == 0) {
        if (job >= 0) { VMCNT(7); } else { VMCNT(0); }
      } else {
        DMA(w2, 2); DMA(w2 + UNIT, 3); dma2(w2 + 2 * UNIT, voff, lbase + L_W2X + hw * 1024);
        VMCNT(16);
      }
      BAR();
      if (hw < 2) {
        __builtin_amdgcn_s_setprio(2);
        f32x16 kp = {};
        const int lz = opqv(lane);
        const int g16 = (lz >> 4) & 1, q = (lz & 15) >> 2, p = lz & 3, hz = lz >> 5;
        const int ab0 = opqv(L_PHK + (8 * hz + q) * PHK_STRIDE + (g16 * 16 + 4 * p) * 2 + hw * 64);
        const int bb0 = opqv(L_V + (8 * hz + q) * V_STRIDE + (g16 * 16 + 4 * p) * 2);
        v4u ap[8], bv[8];
#pragma unroll
        for (int s = 0; s < 8; ++s) { ap[s] = tr_frag(LD, ab0 + 16 * s * PHK_STRIDE, 4 * PHK_STRIDE); bv[s] = tr_frag(LD, bb0 + 16 * s * V_STRIDE, 4 * V_STRIDE); }
        SB();
#pragma unroll
        for (int s = 0; s < 8; ++s) kp = mfma(ap[s], bv[s], kp);
        const int ko = opq(OFF_KVP + L * KVB + (j * 16 + g) * 4096 + hw * 2048);
        st16(rs, l16, ko, pack8(kp, 0));
        st16(rs, l16, ko + 1024, pack8(kp, 1));
        wave_signal(ws, C_KV + L * 16 + j, lane);
        __builtin_amdgcn_s_setprio(0);
      }
      if (L == 0) {
        if (hw == 3) {
          colsum(1, 2);
          arrive_lds(LD, 1, lane);
        }
        if (job >= 0) jsig = retile_store(job, rs, LD, L_STAGE + hw * 7680, lane, jt);
        BAR();
        if (jsig >= 0) wave_signal(ws, jsig, lane);
        BAR();
        wait_cnt(ws, C_W2 + (cb >> 1), 16);
        DMA(w2, 2); DMA(w2 + UNIT, 3); dma2(w2 + 2 * UNIT, voff, lbase + L_W2X + hw * 1024);
        VMCNT(0); BAR();
        wait_cnt(ws, C_W3 + j, 28);
        DMA(w3, 0);
        DMA(w3 + UNIT, 1);
        const int t = hw * 64 + lane;
        if (t < 224) {
          float a[28];
#pragma unroll
          for (int ks = 0; ks < 28; ++ks) a[ks] = __uint_as_float(ld4(rs, t * 4, OFF_CS3 + (j * 28 + ks) * 896));
          float s0 = 0.f, s1 = 0.f, s2 = 0.f, s3 = 0.f;
#pragma unroll
          for (int ks = 0; ks < 28; ks += 4) { s0 += a[ks]; s1 += a[ks + 1]; s2 += a[ks + 2]; s3 += a[ks + 3]; }
          *(LAS float*)(LD + L_CS3 + t * 4) = (s0 + s1) + (s2 + s3);
        }
      } else {
        DMA(w3, 0);
        BAR();
        kv_reduce(rs, LD, L, j, w, lane);
        BAR();
        DMA(w3 + UNIT, 1);
      }
      BAR();
      DMA(w3 + 2 * UNIT, 2);
      VMCNT(7); BAR();
      const int wo4 = opq(OFF_W4 + cb * 8 * UNIT + 4 * UNIT + hw * UNIT);
#define WR(lo, hi) _Pragma("unroll") for (int i = lo; i < hi; ++i) wr[i] = ld16(rs, l16, wo4 + i * 1024)
      if (L == 0) wait_cnt(ws, C_W4 + (cb >> 1), 56);
      wait_arr(LD, 2, 4 * L + 4);
      DMA(w3 + 3 * UNIT, 3);
      VMCNT(7); BAR();
      DMA(w3 + 4 * UNIT, 0);
      VMCNT(12); BAR();
      if (L == 0) {
        DMA(w3 + 5 * UNIT, 1); WR(0, 7);
        VMCNT(19); BAR();
        DMA(w3 + 6 * UNIT, 2); WR(7, 14);
        VMCNT(26); BAR();
        DMA(w4 + 3 * UNIT, 3); WR(14, 21);
        VMCNT(26); BAR();
        DMA(w4, 0); WR(21, 28);
        VMCNT(35); BAR();
      } else {
        DMA(w3 + 5 * UNIT, 1);
        VMCNT(12); BAR();
        DMA(w3 + 6 * UNIT, 2);
        VMCNT(12); BAR();
        DMA(w4 + 3 * UNIT, 3);
        VMCNT(12); BAR();
        DMA(w4, 0);
        VMCNT(14); BAR();
      }
      DMA(w4 + UNIT, 1);
      u32* fp = cnt_ptr(ws, C_H + (L * 64 + 2 * rb + (lane & 1)) * 4);
      u32 f = __hip_atomic_load(fp, __ATOMIC_RELAXED, __HIP_MEMORY_SCOPE_AGENT);
      VMCNT(0); BAR();
      DMA(w4 + 2 * UNIT, 2);
      {
        wait_flags(ws, fp, f);
        const int ho = opq(OFF_H + L * HB + 2 * rb * 112 * 1024);
        const int hc = opq(hw);
        constexpr int HW = 8;
        v4u hx[28];
        auto ldh = [&](auto I) AI { constexpr int i = I.value, t = i >> 1; return ld16(rs, l16, ho + (i & 1) * 112 * 1024 + p4_koff(t / 7, t % 7, hc)); };
        auto lvl = [](int i) constexpr { return p4_lvl((i >> 1) / 7, (i >> 1) % 7); };
        static_for<HW>([&](auto I) AI { hx[I.value] = ldh(I); });
        f32x16 acc[4] = {};
        f32x16 keep;
        static_for<28>([&](auto I) AI {
          constexpr int i = I.value, t = i >> 1, tq = i & 1, nx = i + HW;
          if constexpr (i == 8) RAWBAR();
          if constexpr (i == 18) RAWBAR();
          acc[tq] = mfma(wr[2 * t], hx[i], acc[tq]);
          acc[2 + tq] = mfma(wr[2 * t + 1], hx[i], acc[2 + tq]);
          if constexpr (nx + 3 < 28 && lvl(nx + 3) > lvl(nx + 2)) f = __hip_atomic_load(fp + 32 * lvl(nx + 3), __ATOMIC_RELAXED, __HIP_MEMORY_SCOPE_AGENT);
          if constexpr (nx < 28) {
            if constexpr (lvl(nx) > lvl(nx - 1)) wait_flags(ws, fp + 32 * lvl(nx), f);
            hx[nx] = ldh(IC<nx>{});
          }
          SB();
        });
        p4_put(acc[0], 0, w, LD, l16, keep);
        p4_put(acc[2], 2, w, LD, l16, keep);
        p4_put(acc[1], 1, w, LD, l16, keep);
        p4_put(acc[3], 3, w, LD, l16, keep);
        BAR();
      }
    };
    hlayer(IC<0>{});
    hlayer(IC<1>{});
    if (b == 0 && hw == 3 && lane == 0) __hip_atomic_store((unsigned long long*)(ws + EPOCH_OFF), tag, __ATOMIC_RELAXED, __HIP_MEMORY_SCOPE_AGENT);
    return;
  }

  const int tt = 4 * g + w;
  const int tt2 = 2 * rb + (w & 1);
  const int kh = w >> 1;
  const rsrc_t rx0 = __builtin_amdgcn_make_buffer_rsrc((void*)A.x0, 0, XB, 0x00020000);
  v4u res[2];
  constexpr int XW = 14;

  auto clayer = [&](auto LC) AI {
    constexpr int L = LC.value;
    v4u x[28];
    v2u st[8];
    float mean, rstd;
    v4u pq[4];
    STAMP(0);
    auto phase1 = [&](auto FIRST) AI {
      constexpr bool first = FIRST.value;
      const int xo = opq(first ? tt * 32 * (D * 2) : OFF_X2 + tt * 28 * 1024);
      auto ldx = [&](int ks) AI { return ld16(rs, l16, xo + ks * 1024); };
      const int so_ = opq(OFF_ST2 + tt * 4096);
      const int sv_ = (opqv(lane) & 31) * 8 + h * 2048;
      v4u gx[28]; int gv[7], gl[7];
      const int stg = L_STAGE + w * 7680;
      const int srd = opqv(stg + (opqv(lane) & 31) * 240 + h * 16);
      auto ldg = [&](int i) AI { return __builtin_amdgcn_raw_buffer_load_b128(rx0, gv[i % 7], xo + (i / 7) * 224, 0); };
      auto stage_w = [&](int c) AI {
#pragma unroll
        for (int i = 0; i < 7; ++i) *(LAS v4u*)(LD + gl[i]) = gx[7 * c + i];
      };
      auto stage_r = [&](int c) AI {
#pragma unroll
        for (int i = 0; i < 7; ++i) x[7 * c + i] = lds16(LD, srd + i * 32);
      };
      if constexpr (first) {
#pragma unroll
        for (int i = 0; i < 7; ++i) { const int p = lane + 64 * i, r = p / 14, c = p % 14; gv[i] = r * (D * 2) + c * 16; gl[i] = stg + r * 240 + c * 16; }
#pragma unroll
        for (int i = 0; i < 21; ++i) gx[i] = ldg(i);
        stage_w(0); stage_r(0);
      } else {
#pragma unroll
        for (int ks = 0; ks < XW; ++ks) x[ks] = ldx(ks);
      }
      USTAMP(18);
      BAR();
      USTAMP(19);
      f32x16 ak = {}, av = {}, p = {};
      const int sbk = opqv(opqv(w1lane) + 4 * UNIT), sbv = opqv(w1lane);
      const int csb = opqv(L_CS1 + 16 * (opqv(lane) >> 5)), wb = opqv(L_WPHI + opqv(l16));
      f32x4 cs[4]; v4u kf[2], wf[2];
      float nmean, s1 = 0.f, s2 = 0.f, q1 = 0.f, q2 = 0.f;
      auto isk = [](int i) constexpr { return i < LAG || (i < 56 - LAG && ((i - LAG) & 1) == 0); };
      auto kst = [](int i) constexpr { return i < LAG ? i : (i < 56 - LAG ? ((((i - LAG) & 1) == 0) ? LAG + (i - LAG) / 2 : (i - LAG) / 2) : i - 28); };
      pipe<56>([&](auto I) AI { return tr_frag(LD, (isk(I.value) ? sbk : sbv) + kst(I.value) * 1024, 256); },
               [&](auto I) AI { if constexpr (I.value == LAG + 1 - PF || I.value == 38 || I.value == 46) { USTAMP(20 + I.value / 10 * 2); RAWBAR(); USTAMP(21 + I.value / 10 * 2); }   },
               [&](auto I, v4u a) AI {
                 constexpr int i = I.value, ks = kst(i);
                 if constexpr (isk(i)) ak = mfma(a, x[ks], ak); else av = mfma(a, x[ks], av);
                 if constexpr (!first && isk(i) && ks + XW < 28) x[ks + XW] = ldx(ks + XW);
                 if constexpr (first) {
                   if constexpr (i < 7) gx[21 + i] = ldg(21 + i);
                   if constexpr (isk(i) && ks % 7 == 3 && ks < 21) { stage_w(ks / 7 + 1); stage_r(ks / 7 + 1); }
                   if constexpr (isk(i)) {
                     const u32 one = 0x3f803f80u;
                     s1 = dot2(x[ks].x, one, s1); q1 = dot2(x[ks].x, x[ks].x, q1); s2 = dot2(x[ks].y, one, s2); q2 = dot2(x[ks].y, x[ks].y, q2);
                     s1 = dot2(x[ks].z, one, s1); q1 = dot2(x[ks].z, x[ks].z, q1); s2 = dot2(x[ks].w, one, s2); q2 = dot2(x[ks].w, x[ks].w, q2);
                   }
                   if constexpr (i == 45) { ln_finish(s1 + s2, q1 + q2, mean, rstd); nmean = -mean; }
                 } else {
                   if constexpr (!isk(i) && ks >= 4 && ks < 12) st[ks - 4] = ld8(rs, sv_ + (ks - 4) * 256, so_);
                   if constexpr (i == 45) { ln_stats(st, mean, rstd); nmean = -mean; }
                 }
                 if constexpr (i >= 39 && i < 43) cs[i - 39] = *(LAS f32x4*)(LD + csb + 32 * (i - 39));
                 if constexpr (i == 43) wf[0] = lds16(LD, wb);
                 if constexpr (i == 44) wf[1] = lds16(LD, wb + 1024);
                 if constexpr (i >= 47 && i < 51) {
                   constexpr int qd = i - 47;
                   const float y0 = fmaf(nmean, cs[qd][0], ak[4 * qd]), y1 = fmaf(nmean, cs[qd][1], ak[4 * qd + 1]);
                   const float y2 = fmaf(nmean, cs[qd][2], ak[4 * qd + 2]), y3 = fmaf(nmean, cs[qd][3], ak[4 * qd + 3]);
                   kf[qd >> 1][2 * (qd & 1)] = pk2(y0, y1);
                   kf[qd >> 1][2 * (qd & 1) + 1] = pk2(y2, y3);
                 }
                 if constexpr (i == 49) p = mfma(wf[0], kf[0], p);
                 if constexpr (i == 51) p = mfma(wf[1], kf[1], p);
               });
      STAMP(1);
      const int lz = opqv(lane);
      const int trow = w * 32 + (lz & 31);
      const int pb = opqv(L_PHK + trow * PHK_STRIDE + 8 * (lz >> 5)), vb = opqv(L_V + trow * V_STRIDE + 8 * (lz >> 5));
      dual_softmax(p, h, rstd, pq);
#pragma unroll
      for (int s = 0; s < 4; ++s) {
        *(LAS v2u*)(LD + pb + 32 * s) = v2u{pq[s].x, pq[s].y};
        *(LAS v2u*)(LD + pb + 32 * s + 16) = v2u{pq[s].z, pq[s].w};
      }
      SB();
#pragma unroll
      for (int qd = 0; qd < 4; ++qd) cs[qd] = *(LAS f32x4*)(LD + csb + 128 + 32 * qd);
#pragma unroll
      for (int r = 0; r < 16; ++r) av[r] = fmaf(nmean, cs[r >> 2][r & 3], av[r]) * rstd;
      if (h) { av[12] = 1.f; av[13] = 0.f; av[14] = 0.f; av[15] = 0.f; }
#pragma unroll
      for (int qd = 0; qd < 4; ++qd) *(LAS v2u*)(LD + vb + 16 * qd) = v2u{pk2(av[4 * qd], av[4 * qd + 1]), pk2(av[4 * qd + 2], av[4 * qd + 3])};
      STAMP(2);
      BAR();
      if constexpr (first) {
#pragma unroll
        for (int ro = 0; ro < 2; ++ro) {
          const int oi = min(kh * 4 + ro * 2 + h, 6);
          res[ro] = __builtin_amdgcn_raw_buffer_load_b128(rx0, tok * (D * 2) + (7 * cb + oi) * 16, tt2 * 32 * (D * 2), 0);
        }
      }
      f32x16 aq = {};
      const int sbq = opqv(opqv(w1lane) + 1 * UNIT);
      pipe<28>([&](auto I) AI { return tr_frag(LD, sbq + I.value * 1024, 256); }, [&](auto) AI {},
               [&](auto I, v4u a) AI { aq = mfma(a, x[I.value], aq); });
      if constexpr (first) wait_arr(LD, 1, 1);
#pragma unroll
      for (int qd = 0; qd < 4; ++qd) cs[qd] = *(LAS f32x4*)(LD + csb + 256 + 32 * qd);
#pragma unroll
      for (int r = 0; r < 16; ++r) aq[r] = fmaf(nmean, cs[r >> 2][r & 3], aq[r]);
      f32x16 p2 = {};
      const int wb2 = opqv(L_WPHI + opqv(l16));
      p2 = mfma(lds16(LD, wb2), pack8(aq, 0), p2);
      p2 = mfma(lds16(LD, wb2 + 1024), pack8(aq, 1), p2);
      dual_softmax(p2, h, rstd, pq);
    };
    phase1(std::integral_constant<bool, L == 0>{});
    STAMP(3);
    if (w == 0) wait_cnt(ws, C_KV + L * 16 + j, 32);
    BAR();
    STAMP(4);
    kv_reduce(rs, LD, L, j, w, lane);
    if (L == 0) kv_reduce(rs, LD, L, j, w + 4, lane);
    BAR();
    STAMP(5);
    {
      f32x16 nm = {};
      const int kb = opqv(L_KVF + opqv(l16));
      v4u kf[4];
#pragma unroll
      for (int s = 0; s < 4; ++s) kf[s] = lds16(LD, kb + s * 1024);
#pragma unroll
      for (int s = 0; s < 4; ++s) nm = mfma(kf[s], pq[s], nm);
      auto r = __builtin_amdgcn_permlane32_swap(__float_as_uint(nm[12]), __float_as_uint(nm[12]), false, false);
      const float den = __uint_as_float(r[1]) + 1e-6f;
      const float rd = __builtin_amdgcn_rcpf(den);
#pragma unroll
      for (int i = 0; i < 16; ++i) nm[i] *= rd;
      const int oo = opq(OFF_O + L * OB + (tt * 32 + 2 * j) * 1024);
      st16(rs, l16, oo, pack8(nm, 0));
      st16(rs, l16, oo + 1024, pack8(nm, 1));
    }
    wave_signal(ws, C_O + (L * 64 + tt) * 2 + (j >> 3), lane);
    STAMP(6);
    wait_cnt(ws, C_O + (L * 64 + tt2) * 2 + kh, 8);
    STAMP(7);
    {
      const int xo = opq(OFF_O + L * OB + (tt2 * 32 + 16 * kh) * 1024);
#pragma unroll
      for (int t = 0; t < XW; ++t) x[t] = ld16(rs, l16, xo + t * 1024);
      if (L == 0) RAWBAR();
      f32x16 a0 = {}, a1 = {};
      const int sb2 = opqv(2 * UNIT + kh * 2048 + opqv(l16)), sbx = opqv(L_W2X + kh * 2048 + opqv(l16));
      pipe<32>([&](auto I) AI { constexpr int i = I.value, t = i >> 1; return lds16(LD, (t < 14 ? sb2 + t * 4096 : sbx + (t - 14) * 4096) + (i & 1) * 1024); },
               [&](auto I) AI { if constexpr (I.value == 14) RAWBAR();   },
               [&](auto I, v4u a) AI {
                 constexpr int i = I.value;
                 if constexpr (i & 1) a1 = mfma(a, x[i >> 1], a1); else a0 = mfma(a, x[i >> 1], a0);
                 if constexpr (!(i & 1) && (i >> 1) + XW < 16) x[(i >> 1) + XW] = ld16(rs, l16, xo + ((i >> 1) + XW) * 1024);
               });
      const int xb = opqv(L_SCR + w * 4096 + opqv(l16)), xb2 = opqv(L_SCR + (w ^ 2) * 4096 + opqv(l16));
      f32x16 fin;
      if (kh) {
#pragma unroll
        for (int qd = 0; qd < 4; ++qd) *(LAS f32x4*)(LD + xb + qd * 1024) = f32x4{a0[4 * qd], a0[4 * qd + 1], a0[4 * qd + 2], a0[4 * qd + 3]};
        fin = a1;
      } else {
#pragma unroll
        for (int qd = 0; qd < 4; ++qd) *(LAS f32x4*)(LD + xb + qd * 1024) = f32x4{a1[4 * qd], a1[4 * qd + 1], a1[4 * qd + 2], a1[4 * qd + 3]};
        fin = a0;
      }
      BAR();
#pragma unroll
      for (int qd = 0; qd < 4; ++qd) {
        const f32x4 v = *(LAS f32x4*)(LD + xb2 + qd * 1024);
#pragma unroll
        for (int i = 0; i < 4; ++i) fin[4 * qd + i] += v[i];
      }
      finish_tile(fin, res, rs, opq(OFF_X1 + L * XB), opq(OFF_ST1 + L * STB), tt2, cb, kh, lane, false, A.out);
    }
    wave_signal(ws, C_X1 + L * 64 + tt2, lane);
    STAMP(8);
    wait_cnt(ws, C_X1 + L * 64 + tt, 16);
    STAMP(9);
    {
      const int xo = opq(OFF_X1 + L * XB + tt * 28 * 1024);
      const int so_ = opq(OFF_ST1 + L * STB + tt * 4096);
      const int sv_ = (opqv(lane) & 31) * 8 + h * 2048;
#pragma unroll
      for (int ks = 0; ks < XW; ++ks) x[ks] = ld16(rs, l16, xo + ks * 1024);
      arrive_lds(LD, 2, lane);
      const int ho = opq(OFF_H + L * HB + (tt * 112 + 7 * j) * 1024);
      u32* fo = cnt_ptr(ws, C_H + (L * 64 + tt) * 4);
      u32* fp = cnt_ptr(ws, C_H + (L * 64 + 2 * rb + (lane & 1)) * 4);
      u32 f = 0;
      float nmean, gs, irs;
      const int csb = opqv(L_CS3 + 16 * (opqv(lane) >> 5));
      const int sbA = opqv(l16), sbB = opqv(opqv(l16) + 2 * UNIT);
      f32x4 Gcg[2][2], Gcu[2][2]; float Gt[2][8], Gyu[2][8]; u32 Go[2][4];
      auto glu = [&](const f32x16& a, auto N_, auto S_, auto G_) AI {
        constexpr int n = N_.value, s = S_.value, G = G_.value;
        if constexpr (s == 0) Gcu[G][0] = *(LAS f32x4*)(LD + csb + n * 128 + 32);
        if constexpr (s == 5) Gcg[G][1] = *(LAS f32x4*)(LD + csb + n * 128 + 64);
        if constexpr (s == 10) Gcu[G][1] = *(LAS f32x4*)(LD + csb + n * 128 + 96);
        if constexpr (s == 23 && G == 0 && n < 6) Gcg[0][0] = *(LAS f32x4*)(LD + csb + (n == 0 ? 2 : n + 1) * 128);
        static_for<8>([&](auto V_) AI {
          constexpr int v = V_.value, k = s - (2 + (5 * v) / 2), hf = v >> 2, i = v & 3;
          if constexpr (k == 0) { Gt[G][v] = fmaf(nmean, Gcg[G][hf][i], a[8 * hf + i]); PIN(Gt[G][v]); }
          if constexpr (k == 1) { Gt[G][v] = Gt[G][v] * gs; PIN(Gt[G][v]); }
          if constexpr (k == 2) { Gt[G][v] = __builtin_amdgcn_exp2f(Gt[G][v]); PIN(Gt[G][v]); }
          if constexpr (k == 3) { Gt[G][v] = fmaf(Gt[G][v], irs, irs); PIN(Gt[G][v]); }
          if constexpr (k == 4) { Gt[G][v] = __builtin_amdgcn_rcpf(Gt[G][v]); PIN(Gt[G][v]); }
          if constexpr (k == 5) { Gyu[G][v] = fmaf(nmean, Gcu[G][hf][i], a[8 * hf + 4 + i]); PIN(Gyu[G][v]); }
          if constexpr (k == 6) { Gt[G][v] = Gt[G][v] * Gyu[G][v]; PIN(Gt[G][v]); }
          if constexpr (k == 7 && (v & 1)) { Go[G][v >> 1] = pk2(Gt[G][v - 1], Gt[G][v]); PIN(Go[G][v >> 1]); }
        });
        if constexpr (s == 27) {
          if constexpr (n == 3 || n == 5) {
            VMCNT(0);
            if (lane == 0) __hip_atomic_fetch_add(fo + 32 * (n == 3 ? 0 : 1), 1u, __ATOMIC_RELAXED, __HIP_MEMORY_SCOPE_AGENT);
          }
          st16(rs, l16, ho + n * 1024, v4u{Go[G][0], Go[G][1], Go[G][2], Go[G][3]});
        }
      };
      f32x16 acc[7];
      pipe<196>(
          [&](auto I) AI { constexpr int i = I.value, u = i / 28, sl = u & 3; return lds16(LD, (sl < 2 ? sbA : sbB) + (sl & 1) * UNIT + (i % 28) * 1024); },
          [&](auto I) AI { if constexpr (I.value > 0 && I.value % 28 == 0) RAWBAR(); },
          [&](auto I, v4u a) AI {
            constexpr int i = I.value, u = i / 28, s = i % 28;
            if constexpr (u < 2) {
              constexpr int n = i & 1, ks = i >> 1;
              if constexpr (ks == 0) { f32x16 z = {}; acc[n] = mfma(a, x[ks], z); } else acc[n] = mfma(a, x[ks], acc[n]);
              if constexpr (n == 0 && ks + XW < 28) x[ks + XW] = ld16(rs, l16, xo + (ks + XW) * 1024);
              if constexpr (n == 1 && ks >= 8 && ks < 16) st[ks - 8] = ld8(rs, sv_ + (ks - 8) * 256, so_);
              if constexpr (i == 50) Gcg[0][0] = *(LAS f32x4*)(LD + csb);
              if constexpr (i == 51) Gcg[1][0] = *(LAS f32x4*)(LD + csb + 128);
              if constexpr (i == 53) {
                ln_stats(st, mean, rstd);
                nmean = -mean; gs = -LOG2E * rstd; irs = __builtin_amdgcn_rcpf(rstd);
              }
            } else {
              if constexpr (s == 0) { f32x16 z = {}; acc[u] = mfma(a, x[s], z); } else acc[u] = mfma(a, x[s], acc[u]);
              if constexpr (u == 2) { glu(acc[0], IC<0>{}, IC<s>{}, IC<0>{}); glu(acc[1], IC<1>{}, IC<s>{}, IC<1>{}); }
              else glu(acc[u - 1], IC<u - 1>{}, IC<s>{}, IC<0>{});
              if constexpr (i == 176) f = __hip_atomic_load(fp, __ATOMIC_RELAXED, __HIP_MEMORY_SCOPE_AGENT);
            }
          });
      RAWBAR();
      STAMP(10);
      const int ho2 = opq(OFF_H + L * HB + 2 * rb * 112 * 1024);
      const int hc = opq(w + 4);
      const int hf_ = opq(OFF_H + L * HB + (tt2 * 112 + 6) * 1024);
      constexpr int HW = 8;
      v4u hx[36];
      auto ldh = [&](auto I) AI {
        constexpr int i = I.value, t = i >> 1;
        if constexpr (i < 20) return ld16(rs, l16, ho2 + (i & 1) * 112 * 1024 + p4_koff(t / 5, t % 5, hc));
        else return ld16(rs, l16, hf_ + (i - 20) * 7 * 1024);
      };
      auto lvl = [](int i) constexpr { return i < 20 ? p4_lvl((i >> 1) / 5, (i >> 1) % 5) : 2; };
      wait_flags(ws, fp, f);
      static_for<HW>([&](auto I) AI { hx[I.value] = ldh(I); });
      static_for<28>([&](auto S_) AI { glu(acc[6], IC<6>{}, S_, IC<0>{}); });
      STAMP(11);
      const int wa0 = opqv(w * 2048 + opqv(l16)), wa1 = opqv(w * 2048 + opqv(l16) + 5 * 8192);
      const int wfb = opqv(L_FRESH + kh * 1024 + opqv(l16));
      v4u wq[20];
      auto ldw = [&](auto K) AI { constexpr int k = K.value, t = k >> 1; return lds16(LD, (t < 5 ? wa0 : wa1) + (t % 5) * 8192 + (k & 1) * 1024); };
      static_for<4>([&](auto K) AI { wq[K.value] = ldw(K); });
      f32x16 pc[4] = {};
      f32x16 fin;
      v4u wfr[16];
      static_for<20>([&](auto I) AI {
        constexpr int i = I.value, t = i >> 1, tq = i & 1, nx = i + HW;
        if constexpr (i == 8) RAWBAR();
        if constexpr (i + 4 < 20) wq[i + 4] = ldw(IC<i + 4>{});
        else wfr[i - 16] = lds16(LD, wfb + (i - 16) * 2048);
        if constexpr (i == 2) {
          VMCNT(3);
          if (lane == 0) __hip_atomic_fetch_add(fo + 64, 1u, __ATOMIC_RELAXED, __HIP_MEMORY_SCOPE_AGENT);
        }
        pc[tq] = mfma(wq[2 * t], hx[i], pc[tq]);
        pc[2 + tq] = mfma(wq[2 * t + 1], hx[i], pc[2 + tq]);
        if constexpr (lvl(nx + 2) > lvl(nx + 1)) f = __hip_atomic_load(fp + 32 * lvl(nx + 2), __ATOMIC_RELAXED, __HIP_MEMORY_SCOPE_AGENT);
        if constexpr (lvl(nx) > lvl(nx - 1)) wait_flags(ws, fp + 32 * lvl(nx), f);
        hx[nx] = ldh(IC<nx>{});
        SB();
      });
      RAWBAR();
      p4_put(pc[0], 0, w, LD, l16, fin);
      p4_put(pc[2], 2, w, LD, l16, fin);
      p4_put(pc[1], 1, w, LD, l16, fin);
      p4_put(pc[3], 3, w, LD, l16, fin);
      static_for<16>([&](auto I) AI {
        constexpr int i = I.value;
        if constexpr (i + 4 < 16) wfr[i + 4] = lds16(LD, wfb + (i + 4) * 2048);
        fin = mfma(wfr[i], hx[20 + i], fin);
        if constexpr (20 + i + HW < 36) hx[20 + i + HW] = ldh(IC<20 + i + HW>{});
        SB();
      });
      STAMP(14);
      BAR();
#pragma unroll
      for (int ww = 0; ww < 7; ++ww) {
        const int rr = opqv(p4_part(w, ww) + opqv(l16));
#pragma unroll
        for (int qd = 0; qd < 4; ++qd) {
          const f32x4 v = *(LAS f32x4*)(LD + rr + qd * 1024);
#pragma unroll
          for (int i = 0; i < 4; ++i) fin[4 * qd + i] += v[i];
        }
      }
      STAMP(15);
      finish_tile(fin, res, rs, OFF_X2, OFF_ST2, tt2, cb, kh, lane, L == 1, A.out);
    }
    if (L == 0) {
      wave_signal(ws, C_X2 + tt2, lane);
      arrive_lds(LD, 0, lane);
      STAMP(16);
      wait_cnt(ws, C_X2 + tt, 16);
    }
    STAMP(17);
  };
  clayer(IC<0>{});
  clayer(IC<1>{});
}

extern "C" void kernel_launch(void* const* d_in, const int* in_sizes, int n_in, void* d_out, int out_size, void* d_ws,
                              size_t ws_size, hipStream_t stream) {
  Args a;
  a.x0 = (const u16*)d_in[0]; a.wqkv = (const u16*)d_in[1]; a.wphi = (const u16*)d_in[2]; a.wo = (const u16*)d_in[3];
  a.wg = (const u16*)d_in[5]; a.wu = (const u16*)d_in[6]; a.wd = (const u16*)d_in[7];
  a.ws = (char*)d_ws; a.out = (u16*)d_out;
  (void)hipFuncSetAttribute((const void*)k_main, hipFuncAttributeMaxDynamicSharedMemorySize, LDS_BYTES);
  k_main<<<256, 512, LDS_BYTES, stream>>>(a);
}
```

```cpp
#include <hip/hip_runtime.h>
#include <stdint.h>
#include <utility>
#include <type_traits>

typedef unsigned short u16;
typedef unsigned int u32;
typedef u32 v4u __attribute__((ext_vector_type(4)));
typedef u32 v2u __attribute__((ext_vector_type(2)));
typedef float f32x2 __attribute__((ext_vector_type(2)));
typedef float f32x4 __attribute__((ext_vector_type(4)));
typedef float f32x16 __attribute__((ext_vector_type(16)));
typedef __bf16 bf16x2 __attribute__((ext_vector_type(2)));
typedef __bf16 bf16x8 __attribute__((ext_vector_type(8)));
typedef short s16x4 __attribute__((ext_vector_type(4)));
#define LAS __attribute__((address_space(3)))
#define DI __device__ __forceinline__
#define AI __attribute__((always_inline))
template <int V> using IC = std::integral_constant<int, V>;

constexpr int S = 2048, D = 448, FF = 1792;
constexpr int UNIT = 28672;

constexpr int C_KV = 0;
constexpr int C_W2 = 32;
constexpr int C_W4 = 36;
constexpr int C_W3 = 40;
constexpr int C_ERR = 60;
constexpr int C_X2 = 64;
constexpr int C_X1 = 128;
constexpr int C_O = 256;
constexpr int C_H = 512;
constexpr int EPOCH_OFF = 1024 * 128;
constexpr int INIT_OFF = EPOCH_OFF + 256;
constexpr int CTL_BYTES = 1024 * 128 + 512;
constexpr int W2B = 65536;
constexpr int OFF_W2 = CTL_BYTES;
constexpr int OFF_W3 = OFF_W2 + 8 * W2B;
constexpr int OFF_W4 = OFF_W3 + 16 * 7 * UNIT;
constexpr int OFF_CS3 = OFF_W4 + 8 * 8 * UNIT;
constexpr int XB = S * D * 2;
constexpr int OB = S * 512 * 2;
constexpr int OFF_X2 = OFF_CS3 + 16 * 28 * 896;
constexpr int OFF_X1 = OFF_X2 + XB;
constexpr int OFF_O = OFF_X1 + 2 * XB;
constexpr int STB = S * 16 * 8;
constexpr int OFF_ST2 = OFF_O + 2 * OB;
constexpr int OFF_ST1 = OFF_ST2 + STB;
constexpr int KVB = 16 * 16 * 4096;
constexpr int OFF_KVP = OFF_ST1 + 2 * STB;
constexpr int HB = S * FF * 2;
constexpr int OFF_H = OFF_KVP + 2 * KVB;
constexpr int WS_USED = OFF_H + 2 * HB;

constexpr int L_SCR = 4 * UNIT;
constexpr int PHK_STRIDE = 136, V_STRIDE = 72;
constexpr int L_PHK = L_SCR;
constexpr int L_V = L_SCR + 128 * PHK_STRIDE;
constexpr int L_KVF = L_SCR + 28672;
constexpr int L_W2X = L_KVF + 4096;
constexpr int L_CONST = 155648;
constexpr int L_CS1 = L_CONST, L_CS3 = L_CONST + 384, L_WPHI = L_CONST + 1280, L_ARR = L_CONST + 3328;
constexpr int L_FRESH = 81920;
constexpr int L_STAGE = 2 * UNIT;
constexpr int LDS_BYTES = 163840;

constexpr float LOG2E = 1.4426950408889634f;
constexpr int LAG = 10;

DI u32 pk2(float a, float b) { f32x2 v = {a, b}; return __builtin_bit_cast(u32, __builtin_convertvector(v, bf16x2)); }
DI float bflo(u32 v) { return __uint_as_float(v << 16); }
DI float bfhi(u32 v) { return __uint_as_float(v & 0xffff0000u); }
DI f32x16 mfma(v4u a, v4u b, f32x16 c) {
  f32x16 d = __builtin_amdgcn_mfma_f32_32x32x16_bf16(__builtin_bit_cast(bf16x8, a), __builtin_bit_cast(bf16x8, b), c, 0, 0, 0);
  asm volatile("" : "+v"(d));
  return d;
}
DI float xsum(float v) {
  auto r = __builtin_amdgcn_permlane32_swap(__float_as_uint(v), __float_as_uint(v), false, false);
  return __uint_as_float(r[0]) + __uint_as_float(r[1]);
}
DI float xmax(float v) {
  auto r = __builtin_amdgcn_permlane32_swap(__float_as_uint(v), __float_as_uint(v), false, false);
  return fmaxf(__uint_as_float(r[0]), __uint_as_float(r[1]));
}
DI float xmin(float v) {
  auto r = __builtin_amdgcn_permlane32_swap(__float_as_uint(v), __float_as_uint(v), false, false);
  return fminf(__uint_as_float(r[0]), __uint_as_float(r[1]));
}
#define BAR() asm volatile("s_waitcnt lgkmcnt(0)\n\ts_barrier" ::: "memory")
#define RAWBAR() asm volatile("s_barrier" ::: "memory")
#define VMCNT(n) asm volatile("s_waitcnt vmcnt(" #n ")" ::: "memory")
#define SB() __builtin_amdgcn_sched_barrier(0)
#define PIN(x) asm volatile("" : "+v"(x))

typedef __amdgpu_buffer_rsrc_t rsrc_t;
DI v4u ld16(rsrc_t rs, int voff, int soff) { return __builtin_amdgcn_raw_buffer_load_b128(rs, voff, soff, 16); }
DI v2u ld8(rsrc_t rs, int voff, int soff) { return __builtin_amdgcn_raw_buffer_load_b64(rs, voff, soff, 16); }
DI u32 ld4(rsrc_t rs, int voff, int soff) { return __builtin_amdgcn_raw_buffer_load_b32(rs, voff, soff, 16); }
DI void st16(rsrc_t rs, int voff, int soff, v4u v) {
  __builtin_amdgcn_raw_buffer_store_b128(v, rs, voff, soff, 16);
  asm volatile("s_nop 1" ::"v"(v) : "memory");
}
DI void st8(rsrc_t rs, int voff, int soff, v2u v) { __builtin_amdgcn_raw_buffer_store_b64(v, rs, voff, soff, 16); }
DI void st4(rsrc_t rs, int voff, int soff, u32 v) { __builtin_amdgcn_raw_buffer_store_b32(v, rs, voff, soff, 16); }
DI int opq(int v) { asm volatile("" : "+s"(v)); return v; }
DI int opqv(int v) { asm volatile("" : "+v"(v)); return v; }
DI v4u lds16(LAS char* l, int off) { return *(LAS v4u*)(l + off); }
DI float dot2(u32 a, u32 b, float c) {
  return __builtin_amdgcn_fdot2_f32_bf16(__builtin_bit_cast(bf16x2, a), __builtin_bit_cast(bf16x2, b), c, false);
}
DI v4u tr_frag(LAS char* L, int base, int step) {
  v2u lo = __builtin_bit_cast(v2u, __builtin_amdgcn_ds_read_tr16_b64_v4i16((LAS s16x4*)(L + base)));
  v2u hi = __builtin_bit_cast(v2u, __builtin_amdgcn_ds_read_tr16_b64_v4i16((LAS s16x4*)(L + base + step)));
  return v4u{lo.x, lo.y, hi.x, hi.y};
}
DI float sum8(v4u o) {
  const u32 one = 0x3f803f80u;
  return dot2(o.x, one, dot2(o.y, one, 0.f)) + dot2(o.z, one, dot2(o.w, one, 0.f));
}

DI u32* cnt_ptr(char* ws, int idx) { return (u32*)(ws + idx * 128); }
DI bool spin_more(char* ws, u32& spins) {
  u32* e = cnt_ptr(ws, C_ERR);
  __builtin_amdgcn_s_sleep(1);
  ++spins;
  if ((spins & 255u) == 0u) {
    if (__builtin_amdgcn_readfirstlane(__hip_atomic_load(e, __ATOMIC_RELAXED, __HIP_MEMORY_SCOPE_AGENT)) != 0) return false;
    if (spins > 400000u) { __hip_atomic_store(e, 1u, __ATOMIC_RELAXED, __HIP_MEMORY_SCOPE_AGENT); return false; }
  }
  return true;
}
DI void wait_cnt(char* ws, int idx, u32 target) {
  u32* c = cnt_ptr(ws, idx);
  u32 spins = 0;
  while ((u32)__builtin_amdgcn_readfirstlane(__hip_atomic_load(c, __ATOMIC_RELAXED, __HIP_MEMORY_SCOPE_AGENT)) < target)
    if (!spin_more(ws, spins)) break;
  __builtin_amdgcn_fence(__ATOMIC_ACQUIRE, "wavefront");
  asm volatile("" ::: "memory");
}
DI void wait_flags(char* ws, u32* fp, u32 f) {
  u32 spins = 0;
  while (!__all(f >= 16u)) {
    if (!spin_more(ws, spins)) break;
    f = __hip_atomic_load(fp, __ATOMIC_RELAXED, __HIP_MEMORY_SCOPE_AGENT);
  }
  __builtin_amdgcn_fence(__ATOMIC_ACQUIRE, "wavefront");
  asm volatile("" ::: "memory");
}
DI void signal_cnt(char* ws, int idx) { __hip_atomic_fetch_add(cnt_ptr(ws, idx), 1u, __ATOMIC_RELAXED, __HIP_MEMORY_SCOPE_AGENT); }
DI void wave_signal(char* ws, int idx, int lane) { VMCNT(0); if (lane == 0) signal_cnt(ws, idx); }
DI void arrive_lds(LAS char* LD, int slot, int lane) {
  if (lane == 0) __hip_atomic_fetch_add((LAS u32*)(LD + L_ARR + slot * 4), 1u, __ATOMIC_RELAXED, __HIP_MEMORY_SCOPE_WORKGROUP);
}
DI void wait_arr(LAS char* LD, int slot, u32 target) {
  while ((u32)__builtin_amdgcn_readfirstlane(__hip_atomic_load((LAS u32*)(LD + L_ARR + slot * 4), __ATOMIC_RELAXED, __HIP_MEMORY_SCOPE_WORKGROUP)) < target)
    __builtin_amdgcn_s_sleep(1);
}

#define STAMP(i)
#define USTAMP(i)

#define DMA_STEP(GSTEP) \
      "s_add_u32 m0, m0, 0x1000\n\t" \
      "v_add_u32 %1, " GSTEP ", %1\n\t" \
      "global_load_lds_dwordx4 %1, %2 sc1\n\t"
#define DMA_FN(NAME, BODY) \
DI void NAME(const char* gsrc, u32 voff, u32 lds_dst) { \
  u32 keep; \
  asm volatile("s_mov_b32 %0, m0\n\ts_mov_b32 m0, %3\n\ts_nop 0\n\tglobal_load_lds_dwordx4 %1, %2 sc1\n\t" BODY "s_mov_b32 m0, %0" \
               : "=&s"(keep), "+v"(voff) : "s"(gsrc), "s"(lds_dst) : "memory", "scc"); }
DMA_FN(dma7, DMA_STEP("0x1000") DMA_STEP("0x1000") DMA_STEP("0x1000") DMA_STEP("0x1000") DMA_STEP("0x1000") DMA_STEP("0x1000"))
DMA_FN(dma2, DMA_STEP("0x1000"))
DMA_FN(dma7n, DMA_STEP("0x2a000") DMA_STEP("0x2a000") DMA_STEP("0x2a000") DMA_STEP("0x2a000") DMA_STEP("0x2a000") DMA_STEP("0x2a000"))

template <typename F, int... I>
DI void static_for_impl(F&& f, std::integer_sequence<int, I...>) { (f(IC<I>{}), ...); }
template <int N, typename F>
DI void static_for(F&& f) { static_for_impl(f, std::make_integer_sequence<int, N>{}); }

constexpr int PF = 6;
template <int N, typename RD, typename PRE, typename ST>
DI void pipe(RD rd, PRE pre, ST step) {
  v4u buf[PF];
  static_for<PF>([&](auto I) AI { buf[I.value] = rd(I); });
  SB();
  static_for<N>([&](auto I) AI {
    constexpr int i = I.value;
    pre(I);
    const v4u a = buf[i % PF];
    if constexpr (i + PF < N) buf[i % PF] = rd(IC<i + PF>{});
    step(I, a);
    SB();
  });
}

DI void ln_finish(float s, float q, float& mean, float& rstd) {
  s = xsum(s); q = xsum(q);
  mean = s * (1.f / 448.f);
  const float var = fmaxf(q * (1.f / 448.f) - mean * mean, 0.f);
  rstd = __builtin_amdgcn_rsqf(var + 1e-5f);
}
DI void ln_stats(const v2u (&st)[8], float& mean, float& rstd) {
  f32x2 a = __builtin_bit_cast(f32x2, st[0]), b = __builtin_bit_cast(f32x2, st[1]);
#pragma unroll
  for (int i = 2; i < 8; i += 2) { a += __builtin_bit_cast(f32x2, st[i]); b += __builtin_bit_cast(f32x2, st[i + 1]); }
  a += b;
  ln_finish(a[0], a[1], mean, rstd);
}
DI v4u pack8(const f32x16& a, int s) {
  v4u r = {pk2(a[8 * s], a[8 * s + 1]), pk2(a[8 * s + 2], a[8 * s + 3]), pk2(a[8 * s + 4], a[8 * s + 5]), pk2(a[8 * s + 6], a[8 * s + 7])};
  return r;
}
DI void dual_softmax(const f32x16& p, int h, float rstd, v4u (&out)[4]) {
  float mx = p[0], mn = p[0];
#pragma unroll
  for (int r = 1; r < 12; ++r) { mx = fmaxf(mx, p[r]); mn = fminf(mn, p[r]); }
#pragma unroll
  for (int r = 12; r < 16; ++r) { const float v = h ? p[0] : p[r]; mx = fmaxf(mx, v); mn = fminf(mn, v); }
  mx = xmax(mx); mn = xmin(mn);
  const float c = rstd * LOG2E;
#pragma unroll
  for (int sg = 0; sg < 2; ++sg) {
    const float cc = sg ? -c : c, off = sg ? mn * c : -mx * c;
    f32x16 e;
    float s0 = 0.f, s1 = 0.f;
#pragma unroll
    for (int r = 0; r < 16; ++r) {
      float v = __builtin_amdgcn_exp2f(fmaf(p[r], cc, off));
      if (r >= 12) v = h ? 0.f : v;
      e[r] = v;
      if (r & 1) s1 += v; else s0 += v;
    }
    const float rs_ = __builtin_amdgcn_rcpf(xsum(s0 + s1));
#pragma unroll
    for (int r = 0; r < 16; ++r) e[r] *= rs_;
    out[2 * sg] = pack8(e, 0); out[2 * sg + 1] = pack8(e, 1);
    PIN(out[2 * sg]); PIN(out[2 * sg + 1]);
    SB();
  }
}

DI void finish_tile(const f32x16& acc, v4u (&res)[2], rsrc_t rs, int xoff, int stoff, int tt, int cb, int nt, int lane, bool last, u16* out) {
  lane = opqv(lane);
  const int h = lane >> 5, tok = lane & 31;
#pragma unroll
  for (int ro = 0; ro < 2; ++ro) {
    v4u r = res[ro];
    v4u o;
    o.x = pk2(bflo(r.x) + acc[8 * ro + 0], bfhi(r.x) + acc[8 * ro + 1]);
    o.y = pk2(bflo(r.y) + acc[8 * ro + 2], bfhi(r.y) + acc[8 * ro + 3]);
    o.z = pk2(bflo(r.z) + acc[8 * ro + 4], bfhi(r.z) + acc[8 * ro + 5]);
    o.w = pk2(bflo(r.w) + acc[8 * ro + 6], bfhi(r.w) + acc[8 * ro + 7]);
    res[ro] = o;
    const int oi = nt * 4 + ro * 2 + h;
    if (oi < 7) {
      const int go = 7 * cb + oi;
      if (last) *(v4u*)(out + (size_t)(tt * 32 + tok) * D + go * 8) = o;
      else st16(rs, ((go >> 1) * 64 + tok + 32 * (go & 1)) * 16, xoff + tt * 28 * 1024, o);
    }
  }
  if (!last) {
    const bool v1 = !(nt == 1 && h == 1);
    const u32 one = 0x3f803f80u;
    float s0 = dot2(res[0].x, one, 0.f), s1 = dot2(res[1].x, one, 0.f), q0 = dot2(res[0].x, res[0].x, 0.f), q1 = dot2(res[1].x, res[1].x, 0.f);
    s0 = dot2(res[0].y, one, s0); s1 = dot2(res[1].y, one, s1); q0 = dot2(res[0].y, res[0].y, q0); q1 = dot2(res[1].y, res[1].y, q1);
    s0 = dot2(res[0].z, one, s0); s1 = dot2(res[1].z, one, s1); q0 = dot2(res[0].z, res[0].z, q0); q1 = dot2(res[1].z, res[1].z, q1);
    s0 = dot2(res[0].w, one, s0); s1 = dot2(res[1].w, one, s1); q0 = dot2(res[0].w, res[0].w, q0); q1 = dot2(res[1].w, res[1].w, q1);
    const float sum = xsum(s0 + (v1 ? s1 : 0.f));
    const float sq = xsum(q0 + (v1 ? q1 : 0.f));
    if (h == 0) st8(rs, tok * 8, stoff + ((tt * 8 + cb) * 2 + nt) * 256, v2u{__float_as_uint(sum), __float_as_uint(sq)});
  }
}

DI void kv_reduce(rsrc_t rs, LAS char* LD, int L, int j, int w, int lane) {
  const int ko = opq(OFF_KVP + L * KVB + j * 16 * 4096 + (w >> 1) * 1024 + (w & 1) * 512);
  const int lz = opqv(lane);
  const int vo = (lz >> 5) * 4096 + (lz & 31) * 16;
  v4u t[8];
#pragma unroll
  for (int i = 0; i < 8; ++i) t[i] = ld16(rs, vo, ko + i * 8192);
  float s[8] = {};
  const u32 lo1 = (u32)opq(0x00003f80), hi1 = (u32)opq(0x3f800000);
#pragma unroll
  for (int i = 0; i < 8; ++i) {
#pragma unroll
    for (int c = 0; c < 4; ++c) { s[2 * c] = dot2(t[i][c], lo1, s[2 * c]); s[2 * c + 1] = dot2(t[i][c], hi1, s[2 * c + 1]); }
  }
#pragma unroll
  for (int c = 0; c < 8; ++c) s[c] = xsum(s[c]);
  if (lz < 32) *(LAS v4u*)(LD + L_KVF + (w >> 1) * 1024 + (w & 1) * 512 + lz * 16) = v4u{pk2(s[0], s[1]), pk2(s[2], s[3]), pk2(s[4], s[5]), pk2(s[6], s[7])};
}

DI int p4_part(int a, int i) {
  return a == 0 ? i * 4096 : a == 1 ? UNIT + i * 4096 : a == 2 ? L_SCR + i * 4096 : (i < 6 ? 2 * UNIT + i * 4096 : L_SCR + UNIT);
}
DI void p4_put(const f32x16& acc, int a, int w, LAS char* LD, int l16, f32x16& keep) {
  if (w == a) { keep = acc; return; }
  const int off = opqv(p4_part(a, w - (w > a ? 1 : 0)) + opqv(l16));
#pragma unroll
  for (int qd = 0; qd < 4; ++qd) *(LAS f32x4*)(LD + off + qd * 1024) = f32x4{acc[4 * qd], acc[4 * qd + 1], acc[4 * qd + 2], acc[4 * qd + 3]};
}
DI int p4_koff(int blk, int tp, int c) {
  const int o = 48 * blk + (tp < 5 ? 8 * tp + c : 40 + 4 * (tp - 5) + c);
  return (7 * (o & 15) + (o >> 4)) * 1024;
}
constexpr int p4_lvl(int blk, int tp) { return blk == 0 ? 0 : (tp < 4 ? 1 : 2); }

struct Args {
  const u16* x0; const u16* wqkv; const u16* wphi; const u16* wo; const u16* wg; const u16* wu; const u16* wd;
  char* ws; u16* out;
};

DI void retile_load(const Args& A, int job, int lane, v4u (&t)[7]) {
  if (job < 64 || job >= 512) {
    const bool iswo = job < 64;
    const int a0 = iswo ? job >> 2 : (job - 512) >> 2, cg = job & 3;
    const u16* src = (iswo ? A.wo + (size_t)a0 * 28 * D : A.wd + (size_t)a0 * 32 * D) + 112 * cg;
    const int nrow = iswo ? 28 : 32;
#pragma unroll
    for (int i = 0; i < 7; ++i) {
      const int p = lane + 64 * i, r = p / 14, c = p % 14;
      t[i] = *(const v4u*)(src + (size_t)(r < nrow ? r : 0) * D + c * 8);
    }
  } else {
    const int a = job - 64, j = a / 28, ks = a % 28;
#pragma unroll
    for (int i = 0; i < 7; ++i) {
      const int p = lane + 64 * i, r = p / 28, c = p % 28;
      t[i] = *(const v4u*)((c < 14 ? A.wg : A.wu) + (size_t)(ks * 16 + r) * FF + 112 * j + (c < 14 ? c : c - 14) * 8);
    }
  }
}
DI int retile_store(int job, rsrc_t rs, LAS char* LD, int stage, int lane, const v4u (&t)[7]) {
  const int h = lane >> 5, q = (lane & 15) >> 2, c8 = ((lane >> 4) & 1) * 4 + (lane & 3);
  const int hh = c8 & 1, quad = c8 >> 1;
  if (job < 64 || job >= 512) {
    const bool iswo = job < 64;
    const int a0 = iswo ? job >> 2 : (job - 512) >> 2, cg = job & 3;
    const int nrow = iswo ? 28 : 32;
    constexpr int ST = 240;
#pragma unroll
    for (int i = 0; i < 7; ++i) {
      const int p = lane + 64 * i, r = p / 14, c = p % 14;
      *(LAS v4u*)(LD + stage + r * ST + c * 16) = r < nrow ? t[i] : v4u{0u, 0u, 0u, 0u};
    }
#pragma unroll
    for (int i = 0; i < 4; ++i) {
      const int cb = cg * 2 + (i >> 1), nt = i & 1;
      const int oi = nt * 4 + (quad >> 1) * 2 + hh;
      const int ro = (lane >> 4) & 1, rh = (lane >> 2) & 1;
      const bool valid = nt * 4 + ro * 2 + rh < 7;
      const int colb = (56 * (i >> 1) + 8 * (oi < 7 ? oi : 6) + 4 * (quad & 1)) * 2;
#pragma unroll
      for (int k2 = 0; k2 < 2; ++k2) {
        v4u o = iswo ? tr_frag(LD, stage + (16 * k2 + 4 * h + q) * ST + colb, 8 * ST) : tr_frag(LD, stage + (16 * k2 + 8 * h + q) * ST + colb, 4 * ST);
        if (!valid) o = v4u{0u, 0u, 0u, 0u};
        const int ks = a0 * 2 + k2;
        int off;
        if (iswo) off = OFF_W2 + cb * W2B + (((ks & 15) * 2 + (ks >> 4)) * 2 + nt) * 1024;
        else {
          const int jp = ks / 7, s = ks % 7;
          int fr;
          if (s == 6) fr = 80 + jp * 2 + nt;
          else {
            const int o_ = 16 * s + jp, blk = o_ / 48, r = o_ % 48;
            const int c = r < 40 ? (r & 7) : ((r - 40) & 3), tp = r < 40 ? (r >> 3) : 5 + ((r - 40) >> 2);
            if (c < 4) fr = 112 + (c * 14 + blk * 7 + tp) * 2 + nt;
            else fr = ((blk * 5 + tp) * 4 + (c - 4)) * 2 + nt;
          }
          off = OFF_W4 + cb * 8 * UNIT + fr * 1024;
        }
        st16(rs, lane * 16, off, o);
      }
    }
    return (iswo ? C_W2 : C_W4) + cg;
  } else {
    const int a = job - 64, j = a / 28, ks = a % 28;
    constexpr int ST = 464;
#pragma unroll
    for (int i = 0; i < 7; ++i) { const int p = lane + 64 * i, r = p / 28, c = p % 28; *(LAS v4u*)(LD + stage + r * ST + c * 16) = t[i]; }
    float cs[7];
#pragma unroll
    for (int n = 0; n < 7; ++n) {
      const v4u o = tr_frag(LD, stage + (8 * h + q) * ST + (quad & 1) * 224 + (16 * n + 8 * hh + 4 * (quad >> 1)) * 2, 4 * ST);
      st16(rs, lane * 16, OFF_W3 + j * 7 * UNIT + (n < 2 ? (ks * 2 + n) * 1024 : n * UNIT + ks * 1024), o);
      cs[n] = xsum(sum8(o));
    }
#pragma unroll
    for (int n2 = 0; n2 < 4; ++n2) {
      const float v = h ? cs[n2 < 3 ? 2 * n2 + 1 : 6] : cs[2 * n2];
      if (n2 < 3 || !h) st4(rs, lane * 4, OFF_CS3 + (j * 28 + ks) * 896 + n2 * 256, __float_as_uint(v));
    }
    return C_W3 + j;
  }
}

__global__ __launch_bounds__(512, 2) void k_main(const Args A) {
  extern __shared__ __attribute__((aligned(16))) char smem[];
  LAS char* LD = (LAS char*)smem;
  char* ws = A.ws;
  const int b = blockIdx.x, xcd = b & 7, bi = b >> 3;
  const int g = (xcd >> 2) * 8 + (bi >> 2), j = (xcd & 3) * 4 + (bi & 3);
  const int rb = xcd * 4 + (bi >> 3), cb = bi & 7;
  const int lane = threadIdx.x & 63, w = __builtin_amdgcn_readfirstlane(threadIdx.x >> 6);
  const int h = lane >> 5, tok = lane & 31;
  const rsrc_t rs = __builtin_amdgcn_make_buffer_rsrc((void*)ws, 0, WS_USED, 0x00020000);
  const u32 lbase = (u32)(size_t)smem;
  const int l16 = lane * 16;
  const int tq_ = (lane & 15) >> 2, tc8 = ((lane >> 4) & 1) * 4 + (lane & 3);
  const int w1lane = (8 * h + tq_) * 64 + min(tc8 + (j & 1), 7) * 8;

  u32 tlbpf = 0;
  if (w == 7) tlbpf = __builtin_amdgcn_raw_buffer_load_b32(rs, lane * (WS_USED / 64 & ~16383) + bi * 16384, 0, 0);
  if (w >= 4) {
    const int hw = w - 4;
    const u32 voff = hw * 1024 + lane * 16;
    const u32 voffn = (hw * 16 + (lane >> 2)) * (3 * D * 2) + (lane & 3) * 16;
    const char* w1 = (const char*)A.wqkv + 56 * j - (j & 1) * 8;
    const char* w2 = ws + OFF_W2 + cb * W2B;
    const char* w3 = ws + OFF_W3 + j * 7 * UNIT;
    const char* w4 = ws + OFF_W4 + cb * 8 * UNIT;
#define DMA(src, slot) dma7((src), voff, lbase + (slot) * UNIT + hw * 1024)
#define DMAN(which, slot) dma7n(w1 + (which) * 896, voffn, lbase + (slot) * UNIT + hw * 1024)
    v4u wr[28];
    unsigned long long tag = 0;
    auto hlayer = [&](auto LC) AI {
      constexpr int L = LC.value;
      if (L > 0) wait_arr(LD, 0, 4);
      if (L == 0 && hw == 3) {
        if (lane == 0) *(LAS v4u*)(LD + L_ARR) = v4u{0u, 0u, 0u, 0u};
        tag = __hip_atomic_load((unsigned long long*)(ws + EPOCH_OFF), __ATOMIC_RELAXED, __HIP_MEMORY_SCOPE_AGENT) + 1ull;
        if (b == 0) {
#pragma unroll
          for (int i = 0; i < 16; ++i) st4(rs, lane * 128, i * 8192, 0u);
          VMCNT(0);
          if (lane == 0) __hip_atomic_store((unsigned long long*)(ws + INIT_OFF), tag, __ATOMIC_RELAXED, __HIP_MEMORY_SCOPE_AGENT);
        }
      }
      DMAN(1, 4);
      DMAN(2, 0);
      v4u jt[7];
      const int job = hw >= 2 ? b * 2 + hw - 2 : (hw == 0 && b < 224 ? 512 + b : -1);
      VMCNT(7); BAR();
      auto colsum = [&](int slot, int which) AI {
        const int ib = opqv(slot * UNIT + opqv(w1lane));
        v4u fr[28];
#pragma unroll
        for (int ks = 0; ks < 28; ++ks) fr[ks] = tr_frag(LD, ib + ks * 1024, 256);
        SB();
        const u32 one = 0x3f803f80u;
        float c0 = 0.f, c1 = 0.f, c2 = 0.f, c3 = 0.f;
#pragma unroll
        for (int ks = 0; ks < 28; ++ks) { c0 = dot2(fr[ks].x, one, c0); c1 = dot2(fr[ks].y, one, c1); c2 = dot2(fr[ks].z, one, c2); c3 = dot2(fr[ks].w, one, c3); }
        const float cs = xsum((c0 + c1) + (c2 + c3));
        if (lane < 32) *(LAS float*)(LD + L_CS1 + which * 128 + lane * 4) = cs;
      };
      VMCNT(0); BAR();
      if (L == 0) {
        asm volatile("" ::"v"(tlbpf));
        if (hw == 0) colsum(4, 0);
        if (hw == 1) colsum(0, 1);
        if (hw == 2) {
          const int f = lane & 31, fc = min(f, 27);
          u32 e[16];
#pragma unroll
          for (int i = 0; i < 16; ++i) {
            const int d0 = 16 * (i >> 3) + 8 * ((i >> 2) & 1) + 4 * h + (i & 3);
            e[i] = A.wphi[(j * 28 + min(d0, 27)) * 28 + fc];
          }
#pragma unroll
          for (int i = 0; i < 16; ++i) {
            const int d0 = 16 * (i >> 3) + 8 * ((i >> 2) & 1) + 4 * h + (i & 3);
            e[i] = (d0 < 28 && f < 28) ? e[i] : 0u;
          }
#pragma unroll
          for (int s = 0; s < 2; ++s)
            *(LAS v4u*)(LD + L_WPHI + s * 1024 + l16) = v4u{e[8 * s] | (e[8 * s + 1] << 16), e[8 * s + 2] | (e[8 * s + 3] << 16),
                                                           e[8 * s + 4] | (e[8 * s + 5] << 16), e[8 * s + 6] | (e[8 * s + 7] << 16)};
        }
        if (hw == 3) {
          u32 spins = 0;
          while (__builtin_amdgcn_readfirstlane((int)(__hip_atomic_load((unsigned long long*)(ws + INIT_OFF), __ATOMIC_RELAXED, __HIP_MEMORY_SCOPE_AGENT) != tag))) {
            __builtin_amdgcn_s_sleep(1);
            if (++spins > 400000u) break;
          }
        }
      } else DMAN(0, 1);
      BAR();
      if (L == 0) {
        DMAN(0, 1);
        if (job >= 0) retile_load(A, job, lane, jt);
      }
      BAR();
      int jsig = -1;
      if (L == 0) {
        if (job >= 0) { VMCNT(7); } else { VMCNT(0); }
      } else {
        DMA(w2, 2); DMA(w2 + UNIT, 3); dma2(w2 + 2 * UNIT, voff, lbase + L_W2X + hw * 1024);
        VMCNT(16);
      }
      BAR();
      if (hw < 2) {
        __builtin_amdgcn_s_setprio(2);
        f32x16 kp = {};
        const int lz = opqv(lane);
        const int g16 = (lz >> 4) & 1, q = (lz & 15) >> 2, p = lz & 3, hz = lz >> 5;
        const int ab0 = opqv(L_PHK + (8 * hz + q) * PHK_STRIDE + (g16 * 16 + 4 * p) * 2 + hw * 64);
        const int bb0 = opqv(L_V + (8 * hz + q) * V_STRIDE + (g16 * 16 + 4 * p) * 2);
        v4u ap[8], bv[8];
#pragma unroll
        for (int s = 0; s < 8; ++s) { ap[s] = tr_frag(LD, ab0 + 16 * s * PHK_STRIDE, 4 * PHK_STRIDE); bv[s] = tr_frag(LD, bb0 + 16 * s * V_STRIDE, 4 * V_STRIDE); }
        SB();
#pragma unroll
        for (int s = 0; s < 8; ++s) kp = mfma(ap[s], bv[s], kp);
        const int ko = opq(OFF_KVP + L * KVB + (j * 16 + g) * 4096 + hw * 2048);
        st16(rs, l16, ko, pack8(kp, 0));
        st16(rs, l16, ko + 1024, pack8(kp, 1));
        wave_signal(ws, C_KV + L * 16 + j, lane);
        __builtin_amdgcn_s_setprio(0);
      }
      if (L == 0) {
        if (hw == 3) {
          colsum(1, 2);
          arrive_lds(LD, 1, lane);
        }
        if (job >= 0) jsig = retile_store(job, rs, LD, L_STAGE + hw * 7680, lane, jt);
        BAR();
        if (jsig >= 0) wave_signal(ws, jsig, lane);
        BAR();
        wait_cnt(ws, C_W2 + (cb >> 1), 16);
        DMA(w2, 2); DMA(w2 + UNIT, 3); dma2(w2 + 2 * UNIT, voff, lbase + L_W2X + hw * 1024);
        VMCNT(0); BAR();
        wait_cnt(ws, C_W3 + j, 28);
        DMA(w3, 0);
        DMA(w3 + UNIT, 1);
        const int t = hw * 64 + lane;
        if (t < 224) {
          float a[28];
#pragma unroll
          for (int ks = 0; ks < 28; ++ks) a[ks] = __uint_as_float(ld4(rs, t * 4, OFF_CS3 + (j * 28 + ks) * 896));
          float s0 = 0.f, s1 = 0.f, s2 = 0.f, s3 = 0.f;
#pragma unroll
          for (int ks = 0; ks < 28; ks += 4) { s0 += a[ks]; s1 += a[ks + 1]; s2 += a[ks + 2]; s3 += a[ks + 3]; }
          *(LAS float*)(LD + L_CS3 + t * 4) = (s0 + s1) + (s2 + s3);
        }
      } else {
        DMA(w3, 0);
        BAR();
        kv_reduce(rs, LD, L, j, w, lane);
        BAR();
        DMA(w3 + UNIT, 1);
      }
      BAR();
      DMA(w3 + 2 * UNIT, 2);
      VMCNT(7); BAR();
      const int wo4 = opq(OFF_W4 + cb * 8 * UNIT + 4 * UNIT + hw * UNIT);
#define WR(lo, hi) _Pragma("unroll") for (int i = lo; i < hi; ++i) wr[i] = ld16(rs, l16, wo4 + i * 1024)
      if (L == 0) wait_cnt(ws, C_W4 + (cb >> 1), 56);
      wait_arr(LD, 2, 4 * L + 4);
      DMA(w3 + 3 * UNIT, 3);
      VMCNT(7); BAR();
      DMA(w3 + 4 * UNIT, 0);
      VMCNT(12); BAR();
      if (L == 0) {
        DMA(w3 + 5 * UNIT, 1); WR(0, 7);
        VMCNT(19); BAR();
        DMA(w3 + 6 * UNIT, 2); WR(7, 14);
        VMCNT(26); BAR();
        DMA(w4 + 3 * UNIT, 3); WR(14, 21);
        VMCNT(26); BAR();
        DMA(w4, 0); WR(21, 28);
        VMCNT(35); BAR();
      } else {
        DMA(w3 + 5 * UNIT, 1);
        VMCNT(12); BAR();
        DMA(w3 + 6 * UNIT, 2);
        VMCNT(12); BAR();
        DMA(w4 + 3 * UNIT, 3);
        VMCNT(12); BAR();
        DMA(w4, 0);
        VMCNT(14); BAR();
      }
      DMA(w4 + UNIT, 1);
      u32* fp = cnt_ptr(ws, C_H + (L * 64 + 2 * rb + (lane & 1)) * 4);
      u32 f = __hip_atomic_load(fp, __ATOMIC_RELAXED, __HIP_MEMORY_SCOPE_AGENT);
      VMCNT(0); BAR();
      DMA(w4 + 2 * UNIT, 2);
      {
        wait_flags(ws, fp, f);
        const int ho = opq(OFF_H + L * HB + 2 * rb * 112 * 1024);
        const int hc = opq(hw);
        constexpr int HW = 8;
        v4u hx[28];
        auto ldh = [&](auto I) AI { constexpr int i = I.value, t = i >> 1; return ld16(rs, l16, ho + (i & 1) * 112 * 1024 + p4_koff(t / 7, t % 7, hc)); };
        auto lvl = [](int i) constexpr { return p4_lvl((i >> 1) / 7, (i >> 1) % 7); };
        static_for<HW>([&](auto I) AI { hx[I.value] = ldh(I); });
        f32x16 acc[4] = {};
        f32x16 keep;
        static_for<28>([&](auto I) AI {
          constexpr int i = I.value, t = i >> 1, tq = i & 1, nx = i + HW;
          if constexpr (i == 8) RAWBAR();
          if constexpr (i == 18) RAWBAR();
          acc[tq] = mfma(wr[2 * t], hx[i], acc[tq]);
          acc[2 + tq] = mfma(wr[2 * t + 1], hx[i], acc[2 + tq]);
          if constexpr (nx + 3 < 28 && lvl(nx + 3) > lvl(nx + 2)) f = __hip_atomic_load(fp + 32 * lvl(nx + 3), __ATOMIC_RELAXED, __HIP_MEMORY_SCOPE_AGENT);
          if constexpr (nx < 28) {
            if constexpr (lvl(nx) > lvl(nx - 1)) wait_flags(ws, fp + 32 * lvl(nx), f);
            hx[nx] = ldh(IC<nx>{});
          }
          SB();
        });
        p4_put(acc[0], 0, w, LD, l16, keep);
        p4_put(acc[2], 2, w, LD, l16, keep);
        p4_put(acc[1], 1, w, LD, l16, keep);
        p4_put(acc[3], 3, w, LD, l16, keep);
        BAR();
      }
    };
    hlayer(IC<0>{});
    hlayer(IC<1>{});
    if (b == 0 && hw == 3 && lane == 0) __hip_atomic_store((unsigned long long*)(ws + EPOCH_OFF), tag, __ATOMIC_RELAXED, __HIP_MEMORY_SCOPE_AGENT);
    return;
  }

  const int tt = 4 * g + w;
  const int tt2 = 2 * rb + (w & 1);
  const int kh = w >> 1;
  const rsrc_t rx0 = __builtin_amdgcn_make_buffer_rsrc((void*)A.x0, 0, XB, 0x00020000);
  v4u res[2];
  constexpr int XW = 14;

  auto clayer = [&](auto LC) AI {
    constexpr int L = LC.value;
    v4u x[28];
    v2u st[8];
    float mean, rstd;
    v4u pq[4];
    STAMP(0);
    auto phase1 = [&](auto FIRST) AI {
      constexpr bool first = FIRST.value;
      const int xo = opq(first ? tt * 32 * (D * 2) : OFF_X2 + tt * 28 * 1024);
      auto ldx = [&](int ks) AI { return ld16(rs, l16, xo + ks * 1024); };
      const int so_ = opq(OFF_ST2 + tt * 4096);
      const int sv_ = (opqv(lane) & 31) * 8 + h * 2048;
      v4u gx[28]; int gv[7], gl[7];
      const int stg = L_STAGE + w * 7680;
      const int srd = opqv(stg + (opqv(lane) & 31) * 240 + h * 16);
      auto ldg = [&](int i) AI { return __builtin_amdgcn_raw_buffer_load_b128(rx0, gv[i % 7], xo + (i / 7) * 224, 0); };
      auto stage_w = [&](int c) AI {
#pragma unroll
        for (int i = 0; i < 7; ++i) *(LAS v4u*)(LD + gl[i]) = gx[7 * c + i];
      };
      auto stage_r = [&](int c) AI {
#pragma unroll
        for (int i = 0; i < 7; ++i) x[7 * c + i] = lds16(LD, srd + i * 32);
      };
      if constexpr (first) {
#pragma unroll
        for (int i = 0; i < 7; ++i) { const int p = lane + 64 * i, r = p / 14, c = p % 14; gv[i] = r * (D * 2) + c * 16; gl[i] = stg + r * 240 + c * 16; }
#pragma unroll
        for (int i = 0; i < 21; ++i) gx[i] = ldg(i);
        stage_w(0); stage_r(0);
      } else {
#pragma unroll
        for (int ks = 0; ks < XW; ++ks) x[ks] = ldx(ks);
      }
      USTAMP(18);
      BAR();
      USTAMP(19);
      f32x16 ak = {}, av = {}, p = {};
      const int sbk = opqv(opqv(w1lane) + 4 * UNIT), sbv = opqv(w1lane);
      const int csb = opqv(L_CS1 + 16 * (opqv(lane) >> 5)), wb = opqv(L_WPHI + opqv(l16));
      f32x4 cs[4]; v4u kf[2], wf[2];
      float nmean, s1 = 0.f, s2 = 0.f, q1 = 0.f, q2 = 0.f;
      auto isk = [](int i) constexpr { return i < LAG || (i < 56 - LAG && ((i - LAG) & 1) == 0); };
      auto kst = [](int i) constexpr { return i < LAG ? i : (i < 56 - LAG ? ((((i - LAG) & 1) == 0) ? LAG + (i - LAG) / 2 : (i - LAG) / 2) : i - 28); };
      pipe<56>([&](auto I) AI { return tr_frag(LD, (isk(I.value) ? sbk : sbv) + kst(I.value) * 1024, 256); },
               [&](auto I) AI { if constexpr (I.value == LAG + 1 - PF || I.value == 38 || I.value == 46) { USTAMP(20 + I.value / 10 * 2); RAWBAR(); USTAMP(21 + I.value / 10 * 2); }   },
               [&](auto I, v4u a) AI {
                 constexpr int i = I.value, ks = kst(i);
                 if constexpr (isk(i)) ak = mfma(a, x[ks], ak); else av = mfma(a, x[ks], av);
                 if constexpr (!first && isk(i) && ks + XW < 28) x[ks + XW] = ldx(ks + XW);
                 if constexpr (first) {
                   if constexpr (i < 7) gx[21 + i] = ldg(21 + i);
                   if constexpr (isk(i) && ks % 7 == 3 && ks < 21) { stage_w(ks / 7 + 1); stage_r(ks / 7 + 1); }
                   if constexpr (isk(i)) {
                     const u32 one = 0x3f803f80u;
                     s1 = dot2(x[ks].x, one, s1); q1 = dot2(x[ks].x, x[ks].x, q1); s2 = dot2(x[ks].y, one, s2); q2 = dot2(x[ks].y, x[ks].y, q2);
                     s1 = dot2(x[ks].z, one, s1); q1 = dot2(x[ks].z, x[ks].z, q1); s2 = dot2(x[ks].w, one, s2); q2 = dot2(x[ks].w, x[ks].w, q2);
                   }
                   if constexpr (i == 45) { ln_finish(s1 + s2, q1 + q2, mean, rstd); nmean = -mean; }
                 } else {
                   if constexpr (!isk(i) && ks >= 4 && ks < 12) st[ks - 4] = ld8(rs, sv_ + (ks - 4) * 256, so_);
                   if constexpr (i == 45) { ln_stats(st, mean, rstd); nmean = -mean; }
                 }
                 if constexpr (i >= 39 && i < 43) cs[i - 39] = *(LAS f32x4*)(LD + csb + 32 * (i - 39));
                 if constexpr (i == 43) wf[0] = lds16(LD, wb);
                 if constexpr (i == 44) wf[1] = lds16(LD, wb + 1024);
                 if constexpr (i >= 47 && i < 51) {
                   constexpr int qd = i - 47;
                   const float y0 = fmaf(nmean, cs[qd][0], ak[4 * qd]), y1 = fmaf(nmean, cs[qd][1], ak[4 * qd + 1]);
                   const float y2 = fmaf(nmean, cs[qd][2], ak[4 * qd + 2]), y3 = fmaf(nmean, cs[qd][3], ak[4 * qd + 3]);
                   kf[qd >> 1][2 * (qd & 1)] = pk2(y0, y1);
                   kf[qd >> 1][2 * (qd & 1) + 1] = pk2(y2, y3);
                 }
                 if constexpr (i == 49) p = mfma(wf[0], kf[0], p);
                 if constexpr (i == 51) p = mfma(wf[1], kf[1], p);
               });
      STAMP(1);
      const int lz = opqv(lane);
      const int trow = w * 32 + (lz & 31);
      const int pb = opqv(L_PHK + trow * PHK_STRIDE + 8 * (lz >> 5)), vb = opqv(L_V + trow * V_STRIDE + 8 * (lz >> 5));
      dual_softmax(p, h, rstd, pq);
#pragma unroll
      for (int s = 0; s < 4; ++s) {
        *(LAS v2u*)(LD + pb + 32 * s) = v2u{pq[s].x, pq[s].y};
        *(LAS v2u*)(LD + pb + 32 * s + 16) = v2u{pq[s].z, pq[s].w};
      }
      SB();
#pragma unroll
      for (int qd = 0; qd < 4; ++qd) cs[qd] = *(LAS f32x4*)(LD + csb + 128 + 32 * qd);
#pragma unroll
      for (int r = 0; r < 16; ++r) av[r] = fmaf(nmean, cs[r >> 2][r & 3], av[r]) * rstd;
      if (h) { av[12] = 1.f; av[13] = 0.f; av[14] = 0.f; av[15] = 0.f; }
#pragma unroll
      for (int qd = 0; qd < 4; ++qd) *(LAS v2u*)(LD + vb + 16 * qd) = v2u{pk2(av[4 * qd], av[4 * qd + 1]), pk2(av[4 * qd + 2], av[4 * qd + 3])};
      STAMP(2);
      BAR();
      if constexpr (first) {
#pragma unroll
        for (int ro = 0; ro < 2; ++ro) {
          const int oi = min(kh * 4 + ro * 2 + h, 6);
          res[ro] = __builtin_amdgcn_raw_buffer_load_b128(rx0, tok * (D * 2) + (7 * cb + oi) * 16, tt2 * 32 * (D * 2), 0);
        }
      }
      f32x16 aq = {};
      const int sbq = opqv(opqv(w1lane) + 1 * UNIT);
      pipe<28>([&](auto I) AI { return tr_frag(LD, sbq + I.value * 1024, 256); }, [&](auto) AI {},
               [&](auto I, v4u a) AI { aq = mfma(a, x[I.value], aq); });
      if constexpr (first) wait_arr(LD, 1, 1);
#pragma unroll
      for (int qd = 0; qd < 4; ++qd) cs[qd] = *(LAS f32x4*)(LD + csb + 256 + 32 * qd);
#pragma unroll
      for (int r = 0; r < 16; ++r) aq[r] = fmaf(nmean, cs[r >> 2][r & 3], aq[r]);
      f32x16 p2 = {};
      const int wb2 = opqv(L_WPHI + opqv(l16));
      p2 = mfma(lds16(LD, wb2), pack8(aq, 0), p2);
      p2 = mfma(lds16(LD, wb2 + 1024), pack8(aq, 1), p2);
      dual_softmax(p2, h, rstd, pq);
    };
    phase1(std::integral_constant<bool, L == 0>{});
    STAMP(3);
    if (w == 0) wait_cnt(ws, C_KV + L * 16 + j, 32);
    BAR();
    STAMP(4);
    kv_reduce(rs, LD, L, j, w, lane);
    if (L == 0) kv_reduce(rs, LD, L, j, w + 4, lane);
    BAR();
    STAMP(5);
    {
      f32x16 nm = {};
      const int kb = opqv(L_KVF + opqv(l16));
      v4u kf[4];
#pragma unroll
      for (int s = 0; s < 4; ++s) kf[s] = lds16(LD, kb + s * 1024);
#pragma unroll
      for (int s = 0; s < 4; ++s) nm = mfma(kf[s], pq[s], nm);
      auto r = __builtin_amdgcn_permlane32_swap(__float_as_uint(nm[12]), __float_as_uint(nm[12]), false, false);
      const float den = __uint_as_float(r[1]) + 1e-6f;
      const float rd = __builtin_amdgcn_rcpf(den);
#pragma unroll
      for (int i = 0; i < 16; ++i) nm[i] *= rd;
      const int oo = opq(OFF_O + L * OB + (tt * 32 + 2 * j) * 1024);
      st16(rs, l16, oo, pack8(nm, 0));
      st16(rs, l16, oo + 1024, pack8(nm, 1));
    }
    wave_signal(ws, C_O + (L * 64 + tt) * 2 + (j >> 3), lane);
    STAMP(6);
    wait_cnt(ws, C_O + (L * 64 + tt2) * 2 + kh, 8);
    STAMP(7);
    {
      const int xo = opq(OFF_O + L * OB + (tt2 * 32 + 16 * kh) * 1024);
#pragma unroll
      for (int t = 0; t < XW; ++t) x[t] = ld16(rs, l16, xo + t * 1024);
      if (L == 0) RAWBAR();
      f32x16 a0 = {}, a1 = {};
      const int sb2 = opqv(2 * UNIT + kh * 2048 + opqv(l16)), sbx = opqv(L_W2X + kh * 2048 + opqv(l16));
      pipe<32>([&](auto I) AI { constexpr int i = I.value, t = i >> 1; return lds16(LD, (t < 14 ? sb2 + t * 4096 : sbx + (t - 14) * 4096) + (i & 1) * 1024); },
               [&](auto I) AI { if constexpr (I.value == 14) RAWBAR();   },
               [&](auto I, v4u a) AI {
                 constexpr int i = I.value;
                 if constexpr (i & 1) a1 = mfma(a, x[i >> 1], a1); else a0 = mfma(a, x[i >> 1], a0);
                 if constexpr (!(i & 1) && (i >> 1) + XW < 16) x[(i >> 1) + XW] = ld16(rs, l16, xo + ((i >> 1) + XW) * 1024);
               });
      const int xb = opqv(L_SCR + w * 4096 + opqv(l16)), xb2 = opqv(L_SCR + (w ^ 2) * 4096 + opqv(l16));
      f32x16 fin;
      if (kh) {
#pragma unroll
        for (int qd = 0; qd < 4; ++qd) *(LAS f32x4*)(LD + xb + qd * 1024) = f32x4{a0[4 * qd], a0[4 * qd + 1], a0[4 * qd + 2], a0[4 * qd + 3]};
        fin = a1;
      } else {
#pragma unroll
        for (int qd = 0; qd < 4; ++qd) *(LAS f32x4*)(LD + xb + qd * 1024) = f32x4{a1[4 * qd], a1[4 * qd + 1], a1[4 * qd + 2], a1[4 * qd + 3]};
        fin = a0;
      }
      BAR();
#pragma unroll
      for (int qd = 0; qd < 4; ++qd) {
        const f32x4 v = *(LAS f32x4*)(LD + xb2 + qd * 1024);
#pragma unroll
        for (int i = 0; i < 4; ++i) fin[4 * qd + i] += v[i];
      }
      finish_tile(fin, res, rs, opq(OFF_X1 + L * XB), opq(OFF_ST1 + L * STB), tt2, cb, kh, lane, false, A.out);
    }
    wave_signal(ws, C_X1 + L * 64 + tt2, lane);
    STAMP(8);
    wait_cnt(ws, C_X1 + L * 64 + tt, 16);
    STAMP(9);
    {
      const int xo = opq(OFF_X1 + L * XB + tt * 28 * 1024);
      const int so_ = opq(OFF_ST1 + L * STB + tt * 4096);
      const int sv_ = (opqv(lane) & 31) * 8 + h * 2048;
#pragma unroll
      for (int ks = 0; ks < XW; ++ks) x[ks] = ld16(rs, l16, xo + ks * 1024);
      arrive_lds(LD, 2, lane);
      const int ho = opq(OFF_H + L * HB + (tt * 112 + 7 * j) * 1024);
      u32* fo = cnt_ptr(ws, C_H + (L * 64 + tt) * 4);
      u32* fp = cnt_ptr(ws, C_H + (L * 64 + 2 * rb + (lane & 1)) * 4);
      u32 f = 0;
      float nmean, gs, irs;
      const int csb = opqv(L_CS3 + 16 * (opqv(lane) >> 5));
      const int sbA = opqv(l16), sbB = opqv(opqv(l16) + 2 * UNIT);
      f32x4 Gcg[2][2], Gcu[2][2]; float Gt[2][8], Gyu[2][8]; u32 Go[2][4];
      auto glu = [&](const f32x16& a, auto N_, auto S_, auto G_) AI {
        constexpr int n = N_.value, s = S_.value, G = G_.value;
        if constexpr (s == 0) Gcu[G][0] = *(LAS f32x4*)(LD + csb + n * 128 + 32);
        if constexpr (s == 5) Gcg[G][1] = *(LAS f32x4*)(LD + csb + n * 128 + 64);
        if constexpr (s == 10) Gcu[G][1] = *(LAS f32x4*)(LD + csb + n * 128 + 96);
        if constexpr (s == 23 && G == 0 && n < 6) Gcg[0][0] = *(LAS f32x4*)(LD + csb + (n == 0 ? 2 : n + 1) * 128);
        static_for<8>([&](auto V_) AI {
          constexpr int v = V_.value, k = s - (2 + (5 * v) / 2), hf = v >> 2, i = v & 3;
          if constexpr (k == 0) { Gt[G][v] = fmaf(nmean, Gcg[G][hf][i], a[8 * hf + i]); PIN(Gt[G][v]); }
          if constexpr (k == 1) { Gt[G][v] = Gt[G][v] * gs; PIN(Gt[G][v]); }
          if constexpr (k == 2) { Gt[G][v] = __builtin_amdgcn_exp2f(Gt[G][v]); PIN(Gt[G][v]); }
          if constexpr (k == 3) { Gt[G][v] = fmaf(Gt[G][v], irs, irs); PIN(Gt[G][v]); }
          if constexpr (k == 4) { Gt[G][v] = __builtin_amdgcn_rcpf(Gt[G][v]); PIN(Gt[G][v]); }
          if constexpr (k == 5) { Gyu[G][v] = fmaf(nmean, Gcu[G][hf][i], a[8 * hf + 4 + i]); PIN(Gyu[G][v]); }
          if constexpr (k == 6) { Gt[G][v] = Gt[G][v] * Gyu[G][v]; PIN(Gt[G][v]); }
          if constexpr (k == 7 && (v & 1)) { Go[G][v >> 1] = pk2(Gt[G][v - 1], Gt[G][v]); PIN(Go[G][v >> 1]); }
        });
        if constexpr (s == 27) {
          if constexpr (n == 3 || n == 5) {
            VMCNT(0);
            if (lane == 0) __hip_atomic_fetch_add(fo + 32 * (n == 3 ? 0 : 1), 1u, __ATOMIC_RELAXED, __HIP_MEMORY_SCOPE_AGENT);
          }
          st16(rs, l16, ho + n * 1024, v4u{Go[G][0], Go[G][1], Go[G][2], Go[G][3]});
        }
      };
      f32x16 acc[7];
      pipe<196>(
          [&](auto I) AI { constexpr int i = I.value, u = i / 28, sl = u & 3; return lds16(LD, (sl < 2 ? sbA : sbB) + (sl & 1) * UNIT + (i % 28) * 1024); },
          [&](auto I) AI { if constexpr (I.value > 0 && I.value % 28 == 0) RAWBAR(); },
          [&](auto I, v4u a) AI {
            constexpr int i = I.value, u = i / 28, s = i % 28;
            if constexpr (u < 2) {
              constexpr int n = i & 1, ks = i >> 1;
              if constexpr (ks == 0) { f32x16 z = {}; acc[n] = mfma(a, x[ks], z); } else acc[n] = mfma(a, x[ks], acc[n]);
              if constexpr (n == 0 && ks + XW < 28) x[ks + XW] = ld16(rs, l16, xo + (ks + XW) * 1024);
              if constexpr (n == 1 && ks >= 8 && ks < 16) st[ks - 8] = ld8(rs, sv_ + (ks - 8) * 256, so_);
              if constexpr (i == 50) Gcg[0][0] = *(LAS f32x4*)(LD + csb);
              if constexpr (i == 51) Gcg[1][0] = *(LAS f32x4*)(LD + csb + 128);
              if constexpr (i == 53) {
                ln_stats(st, mean, rstd);
                nmean = -mean; gs = -LOG2E * rstd; irs = __builtin_amdgcn_rcpf(rstd);
              }
            } else {
              if constexpr (s == 0) { f32x16 z = {}; acc[u] = mfma(a, x[s], z); } else acc[u] = mfma(a, x[s], acc[u]);
              if constexpr (u == 2) { glu(acc[0], IC<0>{}, IC<s>{}, IC<0>{}); glu(acc[1], IC<1>{}, IC<s>{}, IC<1>{}); }
              else glu(acc[u - 1], IC<u - 1>{}, IC<s>{}, IC<0>{});
              if constexpr (i == 176) f = __hip_atomic_load(fp, __ATOMIC_RELAXED, __HIP_MEMORY_SCOPE_AGENT);
            }
          });
      RAWBAR();
      STAMP(10);
      const int ho2 = opq(OFF_H + L * HB + 2 * rb * 112 * 1024);
      const int hc = opq(w + 4);
      const int hf_ = opq(OFF_H + L * HB + (tt2 * 112 + 6) * 1024);
      constexpr int HW = 8;
      v4u hx[36];
      auto ldh = [&](auto I) AI {
        constexpr int i = I.value, t = i >> 1;
        if constexpr (i < 20) return ld16(rs, l16, ho2 + (i & 1) * 112 * 1024 + p4_koff(t / 5, t % 5, hc));
        else return ld16(rs, l16, hf_ + (i - 20) * 7 * 1024);
      };
      auto lvl = [](int i) constexpr { return i < 20 ? p4_lvl((i >> 1) / 5, (i >> 1) % 5) : 2; };
      wait_flags(ws, fp, f);
      static_for<HW>([&](auto I) AI { hx[I.value] = ldh(I); });
      static_for<28>([&](auto S_) AI { glu(acc[6], IC<6>{}, S_, IC<0>{}); });
      STAMP(11);
      const int wa0 = opqv(w * 2048 + opqv(l16)), wa1 = opqv(w * 2048 + opqv(l16) + 5 * 8192);
      const int wfb = opqv(L_FRESH + kh * 1024 + opqv(l16));
      v4u wq[20];
      auto ldw = [&](auto K) AI { constexpr int k = K.value, t = k >> 1; return lds16(LD, (t < 5 ? wa0 : wa1) + (t % 5) * 8192 + (k & 1) * 1024); };
      static_for<4>([&](auto K) AI { wq[K.value] = ldw(K); });
      f32x16 pc[4] = {};
      f32x16 fin;
      v4u wfr[16];
      static_for<20>([&](auto I) AI {
        constexpr int i = I.value, t = i >> 1, tq = i & 1, nx = i + HW;
        if constexpr (i == 8) RAWBAR();
        if constexpr (i + 4 < 20) wq[i + 4] = ldw(IC<i + 4>{});
        else wfr[i - 16] = lds16(LD, wfb + (i - 16) * 2048);
        if constexpr (i == 2) {
          VMCNT(3);
          if (lane == 0) __hip_atomic_fetch_add(fo + 64, 1u, __ATOMIC_RELAXED, __HIP_MEMORY_SCOPE_AGENT);
        }
        pc[tq] = mfma(wq[2 * t], hx[i], pc[tq]);
        pc[2 + tq] = mfma(wq[2 * t + 1], hx[i], pc[2 + tq]);
        if constexpr (lvl(nx + 2) > lvl(nx + 1)) f = __hip_atomic_load(fp + 32 * lvl(nx + 2), __ATOMIC_RELAXED, __HIP_MEMORY_SCOPE_AGENT);
        if constexpr (lvl(nx) > lvl(nx - 1)) wait_flags(ws, fp + 32 * lvl(nx), f);
        hx[nx] = ldh(IC<nx>{});
        SB();
      });
      RAWBAR();
      p4_put(pc[0], 0, w, LD, l16, fin);
      p4_put(pc[2], 2, w, LD, l16, fin);
      p4_put(pc[1], 1, w, LD, l16, fin);
      p4_put(pc[3], 3, w, LD, l16, fin);
      static_for<16>([&](auto I) AI {
        constexpr int i = I.value;
        if constexpr (i + 4 < 16) wfr[i + 4] = lds16(LD, wfb + (i + 4) * 2048);
        fin = mfma(wfr[i], hx[20 + i], fin);
        if constexpr (20 + i + HW < 36) hx[20 + i + HW] = ldh(IC<20 + i + HW>{});
        SB();
      });
      STAMP(14);
      BAR();
#pragma unroll
      for (int ww = 0; ww < 7; ++ww) {
        const int rr = opqv(p4_part(w, ww) + opqv(l16));
#pragma unroll
        for (int qd = 0; qd < 4; ++qd) {
          const f32x4 v = *(LAS f32x4*)(LD + rr + qd * 1024);
#pragma unroll
          for (int i = 0; i < 4; ++i) fin[4 * qd + i] += v[i];
        }
      }
      STAMP(15);
      finish_tile(fin, res, rs, OFF_X2, OFF_ST2, tt2, cb, kh, lane, L == 1, A.out);
    }
    if (L == 0) {
      wave_signal(ws, C_X2 + tt2, lane);
      arrive_lds(LD, 0, lane);
      STAMP(16);
      wait_cnt(ws, C_X2 + tt, 16);
    }
    STAMP(17);
  };
  clayer(IC<0>{});
  clayer(IC<1>{});
}

extern "C" void kernel_launch(void* const* d_in, const int* in_sizes, int n_in, void* d_out, int out_size, void* d_ws,
                              size_t ws_size, hipStream_t stream) {
  Args a;
  a.x0 = (const u16*)d_in[0]; a.wqkv = (const u16*)d_in[1]; a.wphi = (const u16*)d_in[2]; a.wo = (const u16*)d_in[3];
  a.wg = (const u16*)d_in[5]; a.wu = (const u16*)d_in[6]; a.wd = (const u16*)d_in[7];
  a.ws = (char*)d_ws; a.out = (u16*)d_out;
  (void)hipFuncSetAttribute((const void*)k_main, hipFuncAttributeMaxDynamicSharedMemorySize, LDS_BYTES);
  k_main<<<256, 512, LDS_BYTES, stream>>>(a);
}
```

```cpp
#include <hip/hip_runtime.h>
#include <stdint.h>
#include <utility>
#include <type_traits>

typedef unsigned short u16;
typedef unsigned int u32;
typedef u32 v4u __attribute__((ext_vector_type(4)));
typedef u32 v2u __attribute__((ext_vector_type(2)));
typedef float f32x2 __attribute__((ext_vector_type(2)));
typedef float f32x4 __attribute__((ext_vector_type(4)));
typedef float f32x16 __attribute__((ext_vector_type(16)));
typedef __bf16 bf16x2 __attribute__((ext_vector_type(2)));
typedef __bf16 bf16x8 __attribute__((ext_vector_type(8)));
typedef short s16x4 __attribute__((ext_vector_type(4)));
#define LAS __attribute__((address_space(3)))
#define DI __device__ __forceinline__
#define AI __attribute__((always_inline))
template <int V> using IC = std::integral_constant<int, V>;

constexpr int S = 2048, D = 448, FF = 1792;
constexpr int UNIT = 28672;

constexpr int C_KV = 0;
constexpr int C_W2 = 32;
constexpr int C_W4 = 36;
constexpr int C_W3 = 40;
constexpr int C_ERR = 60;
constexpr int C_X2 = 64;
constexpr int C_X1 = 128;
constexpr int C_O = 256;
constexpr int C_H = 512;
constexpr int EPOCH_OFF = 1024 * 128;
constexpr int INIT_OFF = EPOCH_OFF + 256;
constexpr int CTL_BYTES = 1024 * 128 + 512;
constexpr int W2B = 65536;
constexpr int OFF_W2 = CTL_BYTES;
constexpr int OFF_W3 = OFF_W2 + 8 * W2B;
constexpr int OFF_W4 = OFF_W3 + 16 * 7 * UNIT;
constexpr int OFF_CS3 = OFF_W4 + 8 * 8 * UNIT;
constexpr int XB = S * D * 2;
constexpr int OB = S * 512 * 2;
constexpr int OFF_X2 = OFF_CS3 + 16 * 28 * 896;
constexpr int OFF_X1 = OFF_X2 + XB;
constexpr int OFF_O = OFF_X1 + 2 * XB;
constexpr int STB = S * 16 * 8;
constexpr int OFF_ST2 = OFF_O + 2 * OB;
constexpr int OFF_ST1 = OFF_ST2 + STB;
constexpr int KVB = 16 * 16 * 4096;
constexpr int OFF_KVP = OFF_ST1 + 2 * STB;
constexpr int HB = S * FF * 2;
constexpr int OFF_H = OFF_KVP + 2 * KVB;
constexpr int WS_USED = OFF_H + 2 * HB;

constexpr int L_SCR = 4 * UNIT;
constexpr int PHK_STRIDE = 136, V_STRIDE = 72;
constexpr int L_PHK = L_SCR;
constexpr int L_V = L_SCR + 128 * PHK_STRIDE;
constexpr int L_KVF = L_SCR + 28672;
constexpr int L_W2X = L_KVF + 4096;
constexpr int L_CONST = 155648;
constexpr int L_CS1 = L_CONST, L_CS3 = L_CONST + 384, L_WPHI = L_CONST + 1280, L_ARR = L_CONST + 3328;
constexpr int L_FRESH = 81920;
constexpr int L_STAGE = 2 * UNIT;
constexpr int LDS_BYTES = 163840;

constexpr float LOG2E = 1.4426950408889634f;
constexpr int LAG = 10;

DI u32 pk2(float a, float b) { f32x2 v = {a, b}; return __builtin_bit_cast(u32, __builtin_convertvector(v, bf16x2)); }
DI float bflo(u32 v) { return __uint_as_float(v << 16); }
DI float bfhi(u32 v) { return __uint_as_float(v & 0xffff0000u); }
DI f32x16 mfma(v4u a, v4u b, f32x16 c) {
  f32x16 d = __builtin_amdgcn_mfma_f32_32x32x16_bf16(__builtin_bit_cast(bf16x8, a), __builtin_bit_cast(bf16x8, b), c, 0, 0, 0);
  asm volatile("" : "+v"(d));
  return d;
}
DI float xsum(float v) {
  auto r = __builtin_amdgcn_permlane32_swap(__float_as_uint(v), __float_as_uint(v), false, false);
  return __uint_as_float(r[0]) + __uint_as_float(r[1]);
}
DI float xmax(float v) {
  auto r = __builtin_amdgcn_permlane32_swap(__float_as_uint(v), __float_as_uint(v), false, false);
  return fmaxf(__uint_as_float(r[0]), __uint_as_float(r[1]));
}
DI float xmin(float v) {
  auto r = __builtin_amdgcn_permlane32_swap(__float_as_uint(v), __float_as_uint(v), false, false);
  return fminf(__uint_as_float(r[0]), __uint_as_float(r[1]));
}
#define BAR() asm volatile("s_waitcnt lgkmcnt(0)\n\ts_barrier" ::: "memory")
#define RAWBAR() asm volatile("s_barrier" ::: "memory")
#define VMCNT(n) asm volatile("s_waitcnt vmcnt(" #n ")" ::: "memory")
#define SB() __builtin_amdgcn_sched_barrier(0)
#define PIN(x) asm volatile("" : "+v"(x))

typedef __amdgpu_buffer_rsrc_t rsrc_t;
DI v4u ld16(rsrc_t rs, int voff, int soff) { return __builtin_amdgcn_raw_buffer_load_b128(rs, voff, soff, 16); }
DI v2u ld8(rsrc_t rs, int voff, int soff) { return __builtin_amdgcn_raw_buffer_load_b64(rs, voff, soff, 16); }
DI u32 ld4(rsrc_t rs, int voff, int soff) { return __builtin_amdgcn_raw_buffer_load_b32(rs, voff, soff, 16); }
DI void st16(rsrc_t rs, int voff, int soff, v4u v) {
  __builtin_amdgcn_raw_buffer_store_b128(v, rs, voff, soff, 16);
  asm volatile("s_nop 1" ::"v"(v) : "memory");
}
DI void st8(rsrc_t rs, int voff, int soff, v2u v) { __builtin_amdgcn_raw_buffer_store_b64(v, rs, voff, soff, 16); }
DI void st4(rsrc_t rs, int voff, int soff, u32 v) { __builtin_amdgcn_raw_buffer_store_b32(v, rs, voff, soff, 16); }
DI int opq(int v) { asm volatile("" : "+s"(v)); return v; }
DI int opqv(int v) { asm volatile("" : "+v"(v)); return v; }
DI v4u lds16(LAS char* l, int off) { return *(LAS v4u*)(l + off); }
DI float dot2(u32 a, u32 b, float c) {
  return __builtin_amdgcn_fdot2_f32_bf16(__builtin_bit_cast(bf16x2, a), __builtin_bit_cast(bf16x2, b), c, false);
}
DI v4u tr_frag(LAS char* L, int base, int step) {
  v2u lo = __builtin_bit_cast(v2u, __builtin_amdgcn_ds_read_tr16_b64_v4i16((LAS s16x4*)(L + base)));
  v2u hi = __builtin_bit_cast(v2u, __builtin_amdgcn_ds_read_tr16_b64_v4i16((LAS s16x4*)(L + base + step)));
  return v4u{lo.x, lo.y, hi.x, hi.y};
}
DI float sum8(v4u o) {
  const u32 one = 0x3f803f80u;
  return dot2(o.x, one, dot2(o.y, one, 0.f)) + dot2(o.z, one, dot2(o.w, one, 0.f));
}

DI u32* cnt_ptr(char* ws, int idx) { return (u32*)(ws + idx * 128); }
#define POLL_D0 16
#define POLL_IV 1
DI bool spin_more(char* ws, u32& spins) {
  u32* e = cnt_ptr(ws, C_ERR);
  __builtin_amdgcn_s_sleep(POLL_IV);
  ++spins;
  if ((spins & 255u) == 0u) {
    if (__builtin_amdgcn_readfirstlane(__hip_atomic_load(e, __ATOMIC_RELAXED, __HIP_MEMORY_SCOPE_AGENT)) != 0) return false;
    if (spins > 400000u) { __hip_atomic_store(e, 1u, __ATOMIC_RELAXED, __HIP_MEMORY_SCOPE_AGENT); return false; }
  }
  return true;
}
DI void wait_cnt(char* ws, int idx, u32 target, bool delay = false) {
  u32* c = cnt_ptr(ws, idx);
  u32 spins = 0;
  if (delay && POLL_D0) __builtin_amdgcn_s_sleep(POLL_D0);
  while ((u32)__builtin_amdgcn_readfirstlane(__hip_atomic_load(c, __ATOMIC_RELAXED, __HIP_MEMORY_SCOPE_AGENT)) < target)
    if (!spin_more(ws, spins)) break;
  __builtin_amdgcn_fence(__ATOMIC_ACQUIRE, "wavefront");
  asm volatile("" ::: "memory");
}
DI void wait_flags(char* ws, u32* fp, u32 f) {
  u32 spins = 0;
  while (!__all(f >= 16u)) {
    if (!spin_more(ws, spins)) break;
    f = __hip_atomic_load(fp, __ATOMIC_RELAXED, __HIP_MEMORY_SCOPE_AGENT);
  }
  __builtin_amdgcn_fence(__ATOMIC_ACQUIRE, "wavefront");
  asm volatile("" ::: "memory");
}
DI void signal_cnt(char* ws, int idx) { __hip_atomic_fetch_add(cnt_ptr(ws, idx), 1u, __ATOMIC_RELAXED, __HIP_MEMORY_SCOPE_AGENT); }
DI void wave_signal(char* ws, int idx, int lane) { VMCNT(0); if (lane == 0) signal_cnt(ws, idx); }
DI void arrive_lds(LAS char* LD, int slot, int lane) {
  if (lane == 0) __hip_atomic_fetch_add((LAS u32*)(LD + L_ARR + slot * 4), 1u, __ATOMIC_RELAXED, __HIP_MEMORY_SCOPE_WORKGROUP);
}
DI void wait_arr(LAS char* LD, int slot, u32 target) {
  while ((u32)__builtin_amdgcn_readfirstlane(__hip_atomic_load((LAS u32*)(LD + L_ARR + slot * 4), __ATOMIC_RELAXED, __HIP_MEMORY_SCOPE_WORKGROUP)) < target)
    __builtin_amdgcn_s_sleep(1);
}

#define STAMP(i)
#define VSTAMP(i)
#define USTAMP(i)

#define DMA_STEP(GSTEP) \
      "s_add_u32 m0, m0, 0x1000\n\t" \
      "v_add_u32 %1, " GSTEP ", %1\n\t" \
      "global_load_lds_dwordx4 %1, %2 sc1\n\t"
#define DMA_FN(NAME, BODY) \
DI void NAME(const char* gsrc, u32 voff, u32 lds_dst) { \
  u32 keep; \
  asm volatile("s_mov_b32 %0, m0\n\ts_mov_b32 m0, %3\n\ts_nop 0\n\tglobal_load_lds_dwordx4 %1, %2 sc1\n\t" BODY "s_mov_b32 m0, %0" \
               : "=&s"(keep), "+v"(voff) : "s"(gsrc), "s"(lds_dst) : "memory", "scc"); }
DMA_FN(dma7, DMA_STEP("0x1000") DMA_STEP("0x1000") DMA_STEP("0x1000") DMA_STEP("0x1000") DMA_STEP("0x1000") DMA_STEP("0x1000"))
DMA_FN(dma2, DMA_STEP("0x1000"))
DMA_FN(dma7n, DMA_STEP("0x2a000") DMA_STEP("0x2a000") DMA_STEP("0x2a000") DMA_STEP("0x2a000") DMA_STEP("0x2a000") DMA_STEP("0x2a000"))

template <typename F, int... I>
DI void static_for_impl(F&& f, std::integer_sequence<int, I...>) { (f(IC<I>{}), ...); }
template <int N, typename F>
DI void static_for(F&& f) { static_for_impl(f, std::make_integer_sequence<int, N>{}); }

constexpr int PF = 6;
template <int N, typename RD, typename PRE, typename ST>
DI void pipe(RD rd, PRE pre, ST step) {
  v4u buf[PF];
  static_for<PF>([&](auto I) AI { buf[I.value] = rd(I); });
  SB();
  static_for<N>([&](auto I) AI {
    constexpr int i = I.value;
    pre(I);
    const v4u a = buf[i % PF];
    if constexpr (i + PF < N) buf[i % PF] = rd(IC<i + PF>{});
    step(I, a);
    SB();
  });
}

DI void ln_finish(float s, float q, float& mean, float& rstd) {
  s = xsum(s); q = xsum(q);
  mean = s * (1.f / 448.f);
  const float var = fmaxf(q * (1.f / 448.f) - mean * mean, 0.f);
  rstd = __builtin_amdgcn_rsqf(var + 1e-5f);
}
DI void ln_stats(const v2u (&st)[8], float& mean, float& rstd) {
  f32x2 a = __builtin_bit_cast(f32x2, st[0]), b = __builtin_bit_cast(f32x2, st[1]);
#pragma unroll
  for (int i = 2; i < 8; i += 2) { a += __builtin_bit_cast(f32x2, st[i]); b += __builtin_bit_cast(f32x2, st[i + 1]); }
  a += b;
  ln_finish(a[0], a[1], mean, rstd);
}
DI v4u pack8(const f32x16& a, int s) {
  v4u r = {pk2(a[8 * s], a[8 * s + 1]), pk2(a[8 * s + 2], a[8 * s + 3]), pk2(a[8 * s + 4], a[8 * s + 5]), pk2(a[8 * s + 6], a[8 * s + 7])};
  return r;
}
DI void dual_softmax(const f32x16& p, int h, float rstd, v4u (&out)[4]) {
  float mx = p[0], mn = p[0];
#pragma unroll
  for (int r = 1; r < 12; ++r) { mx = fmaxf(mx, p[r]); mn = fminf(mn, p[r]); }
#pragma unroll
  for (int r = 12; r < 16; ++r) { const float v = h ? p[0] : p[r]; mx = fmaxf(mx, v); mn = fminf(mn, v); }
  mx = xmax(mx); mn = xmin(mn);
  const float c = rstd * LOG2E;
#pragma unroll
  for (int sg = 0; sg < 2; ++sg) {
    const float cc = sg ? -c : c, off = sg ? mn * c : -mx * c;
    f32x16 e;
    float s0 = 0.f, s1 = 0.f;
#pragma unroll
    for (int r = 0; r < 16; ++r) {
      float v = __builtin_amdgcn_exp2f(fmaf(p[r], cc, off));
      if (r >= 12) v = h ? 0.f : v;
      e[r] = v;
      if (r & 1) s1 += v; else s0 += v;
    }
    const float rs_ = __builtin_amdgcn_rcpf(xsum(s0 + s1));
#pragma unroll
    for (int r = 0; r < 16; ++r) e[r] *= rs_;
    out[2 * sg] = pack8(e, 0); out[2 * sg + 1] = pack8(e, 1);
    PIN(out[2 * sg]); PIN(out[2 * sg + 1]);
    SB();
  }
}

DI void finish_tile(const f32x16& acc, v4u (&res)[2], rsrc_t rs, int xoff, int stoff, int tt, int cb, int nt, int lane, bool last, u16* out) {
  lane = opqv(lane);
  const int h = lane >> 5, tok = lane & 31;
#pragma unroll
  for (int ro = 0; ro < 2; ++ro) {
    v4u r = res[ro];
    v4u o;
    o.x = pk2(bflo(r.x) + acc[8 * ro + 0], bfhi(r.x) + acc[8 * ro + 1]);
    o.y = pk2(bflo(r.y) + acc[8 * ro + 2], bfhi(r.y) + acc[8 * ro + 3]);
    o.z = pk2(bflo(r.z) + acc[8 * ro + 4], bfhi(r.z) + acc[8 * ro + 5]);
    o.w = pk2(bflo(r.w) + acc[8 * ro + 6], bfhi(r.w) + acc[8 * ro + 7]);
    res[ro] = o;
    const int oi = nt * 4 + ro * 2 + h;
    if (oi < 7) {
      const int go = 7 * cb + oi;
      if (last) *(v4u*)(out + (size_t)(tt * 32 + tok) * D + go * 8) = o;
      else st16(rs, ((go >> 1) * 64 + tok + 32 * (go & 1)) * 16, xoff + tt * 28 * 1024, o);
    }
  }
  if (!last) {
    const bool v1 = !(nt == 1 && h == 1);
    const u32 one = 0x3f803f80u;
    float s0 = dot2(res[0].x, one, 0.f), s1 = dot2(res[1].x, one, 0.f), q0 = dot2(res[0].x, res[0].x, 0.f), q1 = dot2(res[1].x, res[1].x, 0.f);
    s0 = dot2(res[0].y, one, s0); s1 = dot2(res[1].y, one, s1); q0 = dot2(res[0].y, res[0].y, q0); q1 = dot2(res[1].y, res[1].y, q1);
    s0 = dot2(res[0].z, one, s0); s1 = dot2(res[1].z, one, s1); q0 = dot2(res[0].z, res[0].z, q0); q1 = dot2(res[1].z, res[1].z, q1);
    s0 = dot2(res[0].w, one, s0); s1 = dot2(res[1].w, one, s1); q0 = dot2(res[0].w, res[0].w, q0); q1 = dot2(res[1].w, res[1].w, q1);
    const float sum = xsum(s0 + (v1 ? s1 : 0.f));
    const float sq = xsum(q0 + (v1 ? q1 : 0.f));
    if (h == 0) st8(rs, tok * 8, stoff + ((tt * 8 + cb) * 2 + nt) * 256, v2u{__float_as_uint(sum), __float_as_uint(sq)});
  }
}

DI void kv_reduce(rsrc_t rs, LAS char* LD, int L, int j, int w, int lane) {
  const int ko = opq(OFF_KVP + L * KVB + j * 16 * 4096 + (w >> 1) * 1024 + (w & 1) * 512);
  const int lz = opqv(lane);
  const int vo = (lz >> 5) * 4096 + (lz & 31) * 16;
  v4u t[8];
#pragma unroll
  for (int i = 0; i < 8; ++i) t[i] = ld16(rs, vo, ko + i * 8192);
  float s[8] = {};
  const u32 lo1 = (u32)opq(0x00003f80), hi1 = (u32)opq(0x3f800000);
#pragma unroll
  for (int i = 0; i < 8; ++i) {
#pragma unroll
    for (int c = 0; c < 4; ++c) { s[2 * c] = dot2(t[i][c], lo1, s[2 * c]); s[2 * c + 1] = dot2(t[i][c], hi1, s[2 * c + 1]); }
  }
#pragma unroll
  for (int c = 0; c < 8; ++c) s[c] = xsum(s[c]);
  if (lz < 32) *(LAS v4u*)(LD + L_KVF + (w >> 1) * 1024 + (w & 1) * 512 + lz * 16) = v4u{pk2(s[0], s[1]), pk2(s[2], s[3]), pk2(s[4], s[5]), pk2(s[6], s[7])};
}

DI int p4_part(int a, int i) {
  return a == 0 ? i * 4096 : a == 1 ? UNIT + i * 4096 : a == 2 ? L_SCR + i * 4096 : (i < 6 ? 2 * UNIT + i * 4096 : L_SCR + UNIT);
}
DI void p4_put(const f32x16& acc, int a, int w, LAS char* LD, int l16, f32x16& keep) {
  if (w == a) { keep = acc; return; }
  const int off = opqv(p4_part(a, w - (w > a ? 1 : 0)) + opqv(l16));
#pragma unroll
  for (int qd = 0; qd < 4; ++qd) *(LAS f32x4*)(LD + off + qd * 1024) = f32x4{acc[4 * qd], acc[4 * qd + 1], acc[4 * qd + 2], acc[4 * qd + 3]};
}
DI int p4_koff(int blk, int tp, int c) {
  const int o = 48 * blk + (tp < 5 ? 8 * tp + c : 40 + 4 * (tp - 5) + c);
  return (7 * (o & 15) + (o >> 4)) * 1024;
}
constexpr int p4_lvl(int blk, int tp) { return blk == 0 ? 0 : (tp < 4 ? 1 : 2); }

struct Args {
  const u16* x0; const u16* wqkv; const u16* wphi; const u16* wo; const u16* wg; const u16* wu; const u16* wd;
  char* ws; u16* out;
};

DI void retile_load(const Args& A, int job, int lane, v4u (&t)[7]) {
  if (job < 64 || job >= 512) {
    const bool iswo = job < 64;
    const int a0 = iswo ? job >> 2 : (job - 512) >> 2, cg = job & 3;
    const u16* src = (iswo ? A.wo + (size_t)a0 * 28 * D : A.wd + (size_t)a0 * 32 * D) + 112 * cg;
    const int nrow = iswo ? 28 : 32;
#pragma unroll
    for (int i = 0; i < 7; ++i) {
      const int p = lane + 64 * i, r = p / 14, c = p % 14;
      t[i] = *(const v4u*)(src + (size_t)(r < nrow ? r : 0) * D + c * 8);
    }
  } else {
    const int a = job - 64, j = a / 28, ks = a % 28;
#pragma unroll
    for (int i = 0; i < 7; ++i) {
      const int p = lane + 64 * i, r = p / 28, c = p % 28;
      t[i] = *(const v4u*)((c < 14 ? A.wg : A.wu) + (size_t)(ks * 16 + r) * FF + 112 * j + (c < 14 ? c : c - 14) * 8);
    }
  }
}
DI int retile_store(int job, rsrc_t rs, LAS char* LD, int stage, int lane, const v4u (&t)[7]) {
  const int h = lane >> 5, q = (lane & 15) >> 2, c8 = ((lane >> 4) & 1) * 4 + (lane & 3);
  const int hh = c8 & 1, quad = c8 >> 1;
  if (job < 64 || job >= 512) {
    const bool iswo = job < 64;
    const int a0 = iswo ? job >> 2 : (job - 512) >> 2, cg = job & 3;
    const int nrow = iswo ? 28 : 32;
    constexpr int ST = 240;
#pragma unroll
    for (int i = 0; i < 7; ++i) {
      const int p = lane + 64 * i, r = p / 14, c = p % 14;
      *(LAS v4u*)(LD + stage + r * ST + c * 16) = r < nrow ? t[i] : v4u{0u, 0u, 0u, 0u};
    }
#pragma unroll
    for (int i = 0; i < 4; ++i) {
      const int cb = cg * 2 + (i >> 1), nt = i & 1;
      const int oi = nt * 4 + (quad >> 1) * 2 + hh;
      const int ro = (lane >> 4) & 1, rh = (lane >> 2) & 1;
      const bool valid = nt * 4 + ro * 2 + rh < 7;
      const int colb = (56 * (i >> 1) + 8 * (oi < 7 ? oi : 6) + 4 * (quad & 1)) * 2;
#pragma unroll
      for (int k2 = 0; k2 < 2; ++k2) {
        v4u o = iswo ? tr_frag(LD, stage + (16 * k2 + 4 * h + q) * ST + colb, 8 * ST) : tr_frag(LD, stage + (16 * k2 + 8 * h + q) * ST + colb, 4 * ST);
        if (!valid) o = v4u{0u, 0u, 0u, 0u};
        const int ks = a0 * 2 + k2;
        int off;
        if (iswo) off = OFF_W2 + cb * W2B + (((ks & 15) * 2 + (ks >> 4)) * 2 + nt) * 1024;
        else {
          const int jp = ks / 7, s = ks % 7;
          int fr;
          if (s == 6) fr = 80 + jp * 2 + nt;
          else {
            const int o_ = 16 * s + jp, blk = o_ / 48, r = o_ % 48;
            const int c = r < 40 ? (r & 7) : ((r - 40) & 3), tp = r < 40 ? (r >> 3) : 5 + ((r - 40) >> 2);
            if (c < 4) fr = 112 + (c * 14 + blk * 7 + tp) * 2 + nt;
            else fr = ((blk * 5 + tp) * 4 + (c - 4)) * 2 + nt;
          }
          off = OFF_W4 + cb * 8 * UNIT + fr * 1024;
        }
        st16(rs, lane * 16, off, o);
      }
    }
    return (iswo ? C_W2 : C_W4) + cg;
  } else {
    const int a = job - 64, j = a / 28, ks = a % 28;
    constexpr int ST = 464;
#pragma unroll
    for (int i = 0; i < 7; ++i) { const int p = lane + 64 * i, r = p / 28, c = p % 28; *(LAS v4u*)(LD + stage + r * ST + c * 16) = t[i]; }
    float cs[7];
#pragma unroll
    for (int n = 0; n < 7; ++n) {
      const v4u o = tr_frag(LD, stage + (8 * h + q) * ST + (quad & 1) * 224 + (16 * n + 8 * hh + 4 * (quad >> 1)) * 2, 4 * ST);
      st16(rs, lane * 16, OFF_W3 + j * 7 * UNIT + (n < 2 ? (ks * 2 + n) * 1024 : n * UNIT + ks * 1024), o);
      cs[n] = xsum(sum8(o));
    }
#pragma unroll
    for (int n2 = 0; n2 < 4; ++n2) {
      const float v = h ? cs[n2 < 3 ? 2 * n2 + 1 : 6] : cs[2 * n2];
      if (n2 < 3 || !h) st4(rs, lane * 4, OFF_CS3 + (j * 28 + ks) * 896 + n2 * 256, __float_as_uint(v));
    }
    return C_W3 + j;
  }
}

__global__ __launch_bounds__(512, 2) void k_main(const Args A) {
  extern __shared__ __attribute__((aligned(16))) char smem[];
  LAS char* LD = (LAS char*)smem;
  char* ws = A.ws;
  const int b = blockIdx.x, xcd = b & 7, bi = b >> 3;
  const int g = (xcd >> 2) * 8 + (bi >> 2), j = (xcd & 3) * 4 + (bi & 3);
  const int rb = xcd * 4 + (bi >> 3), cb = bi & 7;
  const int lane = threadIdx.x & 63, w = __builtin_amdgcn_readfirstlane(threadIdx.x >> 6);
  const int h = lane >> 5, tok = lane & 31;
  const rsrc_t rs = __builtin_amdgcn_make_buffer_rsrc((void*)ws, 0, WS_USED, 0x00020000);
  const u32 lbase = (u32)(size_t)smem;
  const int l16 = lane * 16;
  const int tq_ = (lane & 15) >> 2, tc8 = ((lane >> 4) & 1) * 4 + (lane & 3);
  const int w1lane = (8 * h + tq_) * 64 + min(tc8 + (j & 1), 7) * 8;

  if (w >= 4) {
    const int hw = w - 4;
    const u32 voff = hw * 1024 + lane * 16;
    const u32 voffn = (hw * 16 + (lane >> 2)) * (3 * D * 2) + (lane & 3) * 16;
    const char* w1 = (const char*)A.wqkv + 56 * j - (j & 1) * 8;
    const char* w2 = ws + OFF_W2 + cb * W2B;
    const char* w3 = ws + OFF_W3 + j * 7 * UNIT;
    const char* w4 = ws + OFF_W4 + cb * 8 * UNIT;
#define DMA(src, slot) dma7((src), voff, lbase + (slot) * UNIT + hw * 1024)
#define DMAN(which, slot) dma7n(w1 + (which) * 896, voffn, lbase + (slot) * UNIT + hw * 1024)
    v4u wr[28];
    unsigned long long tag = 0;
    auto hlayer = [&](auto LC) AI {
      constexpr int L = LC.value;
      if (L > 0) wait_arr(LD, 0, 4);
      if (L == 0 && hw == 3) {
        if (lane == 0) *(LAS v4u*)(LD + L_ARR) = v4u{0u, 0u, 0u, 0u};
        tag = __hip_atomic_load((unsigned long long*)(ws + EPOCH_OFF), __ATOMIC_RELAXED, __HIP_MEMORY_SCOPE_AGENT) + 1ull;
        if (b == 0) {
#pragma unroll
          for (int i = 0; i < 16; ++i) st4(rs, lane * 128, i * 8192, 0u);
          VMCNT(0);
          if (lane == 0) __hip_atomic_store((unsigned long long*)(ws + INIT_OFF), tag, __ATOMIC_RELAXED, __HIP_MEMORY_SCOPE_AGENT);
        }
      }
      DMAN(1, 4);
      DMAN(2, 0);
      v4u jt[7];
      const int job = hw >= 2 ? b * 2 + hw - 2 : (hw == 0 && b < 224 ? 512 + b : -1);
      VMCNT(7); BAR();
      auto colsum = [&](int slot, int which) AI {
        const int ib = opqv(slot * UNIT + opqv(w1lane));
        v4u fr[28];
#pragma unroll
        for (int ks = 0; ks < 28; ++ks) fr[ks] = tr_frag(LD, ib + ks * 1024, 256);
        SB();
        const u32 one = 0x3f803f80u;
        float c0 = 0.f, c1 = 0.f, c2 = 0.f, c3 = 0.f;
#pragma unroll
        for (int ks = 0; ks < 28; ++ks) { c0 = dot2(fr[ks].x, one, c0); c1 = dot2(fr[ks].y, one, c1); c2 = dot2(fr[ks].z, one, c2); c3 = dot2(fr[ks].w, one, c3); }
        const float cs = xsum((c0 + c1) + (c2 + c3));
        if (lane < 32) *(LAS float*)(LD + L_CS1 + which * 128 + lane * 4) = cs;
      };
      VMCNT(0); BAR();
      if (L == 0) {
        if (hw == 0) colsum(4, 0);
        if (hw == 1) colsum(0, 1);
        if (hw == 2) {
          const int f = lane & 31, fc = min(f, 27);
          u32 e[16];
#pragma unroll
          for (int i = 0; i < 16; ++i) {
            const int d0 = 16 * (i >> 3) + 8 * ((i >> 2) & 1) + 4 * h + (i & 3);
            e[i] = A.wphi[(j * 28 + min(d0, 27)) * 28 + fc];
          }
#pragma unroll
          for (int i = 0; i < 16; ++i) {
            const int d0 = 16 * (i >> 3) + 8 * ((i >> 2) & 1) + 4 * h + (i & 3);
            e[i] = (d0 < 28 && f < 28) ? e[i] : 0u;
          }
#pragma unroll
          for (int s = 0; s < 2; ++s)
            *(LAS v4u*)(LD + L_WPHI + s * 1024 + l16) = v4u{e[8 * s] | (e[8 * s + 1] << 16), e[8 * s + 2] | (e[8 * s + 3] << 16),
                                                           e[8 * s + 4] | (e[8 * s + 5] << 16), e[8 * s + 6] | (e[8 * s + 7] << 16)};
        }
        if (hw == 3) {
          u32 spins = 0;
          while (__builtin_amdgcn_readfirstlane((int)(__hip_atomic_load((unsigned long long*)(ws + INIT_OFF), __ATOMIC_RELAXED, __HIP_MEMORY_SCOPE_AGENT) != tag))) {
            __builtin_amdgcn_s_sleep(1);
            if (++spins > 400000u) break;
          }
        }
      } else DMAN(0, 1);
      BAR();
      if (L == 0) {
        DMAN(0, 1);
        if (job >= 0) retile_load(A, job, lane, jt);
      }
      BAR();
      int jsig = -1;
      if (L == 0) {
        if (job >= 0) { VMCNT(7); } else { VMCNT(0); }
      } else {
        DMA(w2, 2); DMA(w2 + UNIT, 3); dma2(w2 + 2 * UNIT, voff, lbase + L_W2X + hw * 1024);
        VMCNT(16);
      }
      BAR();
      if (hw < 2) {
        __builtin_amdgcn_s_setprio(2);
        f32x16 kp = {};
        const int lz = opqv(lane);
        const int g16 = (lz >> 4) & 1, q = (lz & 15) >> 2, p = lz & 3, hz = lz >> 5;
        const int ab0 = opqv(L_PHK + (8 * hz + q) * PHK_STRIDE + (g16 * 16 + 4 * p) * 2 + hw * 64);
        const int bb0 = opqv(L_V + (8 * hz + q) * V_STRIDE + (g16 * 16 + 4 * p) * 2);
        v4u ap[8], bv[8];
#pragma unroll
        for (int s = 0; s < 8; ++s) { ap[s] = tr_frag(LD, ab0 + 16 * s * PHK_STRIDE, 4 * PHK_STRIDE); bv[s] = tr_frag(LD, bb0 + 16 * s * V_STRIDE, 4 * V_STRIDE); }
        SB();
#pragma unroll
        for (int s = 0; s < 8; ++s) kp = mfma(ap[s], bv[s], kp);
        const int ko = opq(OFF_KVP + L * KVB + (j * 16 + g) * 4096 + hw * 2048);
        st16(rs, l16, ko, pack8(kp, 0));
        st16(rs, l16, ko + 1024, pack8(kp, 1));
        wave_signal(ws, C_KV + L * 16 + j, lane);
        __builtin_amdgcn_s_setprio(0);
      }
      if (L == 0) {
        if (hw == 3) {
          colsum(1, 2);
          arrive_lds(LD, 1, lane);
        }
        if (job >= 0) jsig = retile_store(job, rs, LD, L_STAGE + hw * 7680, lane, jt);
        BAR();
        if (jsig >= 0) wave_signal(ws, jsig, lane);
        BAR();
        wait_cnt(ws, C_W2 + (cb >> 1), 16);
        DMA(w2, 2); DMA(w2 + UNIT, 3); dma2(w2 + 2 * UNIT, voff, lbase + L_W2X + hw * 1024);
        VMCNT(0); BAR();
        wait_cnt(ws, C_W3 + j, 28);
        DMA(w3, 0);
        DMA(w3 + UNIT, 1);
        const int t = hw * 64 + lane;
        if (t < 224) {
          float a[28];
#pragma unroll
          for (int ks = 0; ks < 28; ++ks) a[ks] = __uint_as_float(ld4(rs, t * 4, OFF_CS3 + (j * 28 + ks) * 896));
          float s0 = 0.f, s1 = 0.f, s2 = 0.f, s3 = 0.f;
#pragma unroll
          for (int ks = 0; ks < 28; ks += 4) { s0 += a[ks]; s1 += a[ks + 1]; s2 += a[ks + 2]; s3 += a[ks + 3]; }
          *(LAS float*)(LD + L_CS3 + t * 4) = (s0 + s1) + (s2 + s3);
        }
      } else {
        DMA(w3, 0);
        BAR();
        kv_reduce(rs, LD, L, j, w, lane);
        BAR();
        DMA(w3 + UNIT, 1);
      }
      BAR();
      DMA(w3 + 2 * UNIT, 2);
      VMCNT(7); BAR();
      const int wo4 = opq(OFF_W4 + cb * 8 * UNIT + 4 * UNIT + hw * UNIT);
#define WR(lo, hi) _Pragma("unroll") for (int i = lo; i < hi; ++i) wr[i] = ld16(rs, l16, wo4 + i * 1024)
      if (L == 0) wait_cnt(ws, C_W4 + (cb >> 1), 56);
      wait_arr(LD, 2, 4 * L + 4);
      DMA(w3 + 3 * UNIT, 3);
      VMCNT(7); BAR();
      DMA(w3 + 4 * UNIT, 0);
      VMCNT(12); BAR();
      if (L == 0) {
        DMA(w3 + 5 * UNIT, 1); WR(0, 7);
        VMCNT(19); BAR();
        DMA(w3 + 6 * UNIT, 2); WR(7, 14);
        VMCNT(26); BAR();
        DMA(w4 + 3 * UNIT, 3); WR(14, 21);
        VMCNT(26); BAR();
        DMA(w4, 0); WR(21, 28);
        VMCNT(35); BAR();
      } else {
        DMA(w3 + 5 * UNIT, 1);
        VMCNT(12); BAR();
        DMA(w3 + 6 * UNIT, 2);
        VMCNT(12); BAR();
        DMA(w4 + 3 * UNIT, 3);
        VMCNT(12); BAR();
        DMA(w4, 0);
        VMCNT(14); BAR();
      }
      DMA(w4 + UNIT, 1);
      u32* fp = cnt_ptr(ws, C_H + (L * 64 + 2 * rb + (lane & 1)) * 4);
      u32 f = __hip_atomic_load(fp, __ATOMIC_RELAXED, __HIP_MEMORY_SCOPE_AGENT);
      VMCNT(0); BAR();
      DMA(w4 + 2 * UNIT, 2);
      {
        wait_flags(ws, fp, f);
        const int ho = opq(OFF_H + L * HB + 2 * rb * 112 * 1024);
        const int hc = opq(hw);
        constexpr int HW = 8;
        v4u hx[28];
        auto ldh = [&](auto I) AI { constexpr int i = I.value, t = i >> 1; return ld16(rs, l16, ho + (i & 1) * 112 * 1024 + p4_koff(t / 7, t % 7, hc)); };
        auto lvl = [](int i) constexpr { return p4_lvl((i >> 1) / 7, (i >> 1) % 7); };
        static_for<HW>([&](auto I) AI { hx[I.value] = ldh(I); });
        f32x16 acc[4] = {};
        f32x16 keep;
        static_for<28>([&](auto I) AI {
          constexpr int i = I.value, t = i >> 1, tq = i & 1, nx = i + HW;
          if constexpr (i == 8) RAWBAR();
          if constexpr (i == 18) RAWBAR();
          acc[tq] = mfma(wr[2 * t], hx[i], acc[tq]);
          acc[2 + tq] = mfma(wr[2 * t + 1], hx[i], acc[2 + tq]);
          if constexpr (nx + 3 < 28 && lvl(nx + 3) > lvl(nx + 2)) f = __hip_atomic_load(fp + 32 * lvl(nx + 3), __ATOMIC_RELAXED, __HIP_MEMORY_SCOPE_AGENT);
          if constexpr (nx < 28) {
            if constexpr (lvl(nx) > lvl(nx - 1)) wait_flags(ws, fp + 32 * lvl(nx), f);
            hx[nx] = ldh(IC<nx>{});
          }
          SB();
        });
        p4_put(acc[0], 0, w, LD, l16, keep);
        p4_put(acc[2], 2, w, LD, l16, keep);
        p4_put(acc[1], 1, w, LD, l16, keep);
        p4_put(acc[3], 3, w, LD, l16, keep);
        BAR();
      }
    };
    hlayer(IC<0>{});
    hlayer(IC<1>{});
    if (b == 0 && hw == 3 && lane == 0) __hip_atomic_store((unsigned long long*)(ws + EPOCH_OFF), tag, __ATOMIC_RELAXED, __HIP_MEMORY_SCOPE_AGENT);
    return;
  }

  const int tt = 4 * g + w;
  const int tt2 = 2 * rb + (w & 1);
  const int kh = w >> 1;
  const rsrc_t rx0 = __builtin_amdgcn_make_buffer_rsrc((void*)A.x0, 0, XB, 0x00020000);
  v4u res[2];
  constexpr int XW = 14;

  auto clayer = [&](auto LC) AI {
    constexpr int L = LC.value;
    v4u x[28];
    v2u st[8];
    float mean, rstd;
    v4u pq[4];
    STAMP(0);
    auto phase1 = [&](auto FIRST) AI {
      constexpr bool first = FIRST.value;
      const int xo = opq(first ? tt * 32 * (D * 2) : OFF_X2 + tt * 28 * 1024);
      auto ldx = [&](int ks) AI { return ld16(rs, l16, xo + ks * 1024); };
      const int so_ = opq(OFF_ST2 + tt * 4096);
      const int sv_ = (opqv(lane) & 31) * 8 + h * 2048;
      v4u gx[28]; int gv[7], gl[7];
      const int stg = L_STAGE + w * 7680;
      const int srd = opqv(stg + (opqv(lane) & 31) * 240 + h * 16);
      auto ldg = [&](int i) AI { return __builtin_amdgcn_raw_buffer_load_b128(rx0, gv[i % 7], xo + (i / 7) * 224, 0); };
      auto stage_w = [&](int c) AI {
#pragma unroll
        for (int i = 0; i < 7; ++i) *(LAS v4u*)(LD + gl[i]) = gx[7 * c + i];
      };
      auto stage_r = [&](int c) AI {
#pragma unroll
        for (int i = 0; i < 7; ++i) x[7 * c + i] = lds16(LD, srd + i * 32);
      };
      if constexpr (first) {
#pragma unroll
        for (int i = 0; i < 7; ++i) { const int p = lane + 64 * i, r = p / 14, c = p % 14; gv[i] = r * (D * 2) + c * 16; gl[i] = stg + r * 240 + c * 16; }
#pragma unroll
        for (int i = 0; i < 21; ++i) gx[i] = ldg(i);
        stage_w(0); stage_r(0);
      } else {
#pragma unroll
        for (int ks = 0; ks < XW; ++ks) x[ks] = ldx(ks);
      }
      USTAMP(18);
      BAR();
      USTAMP(19);
      f32x16 ak = {}, av = {}, p = {};
      const int sbk = opqv(opqv(w1lane) + 4 * UNIT), sbv = opqv(w1lane);
      const int csb = opqv(L_CS1 + 16 * (opqv(lane) >> 5)), wb = opqv(L_WPHI + opqv(l16));
      f32x4 cs[4]; v4u kf[2], wf[2];
      float nmean, s1 = 0.f, s2 = 0.f, q1 = 0.f, q2 = 0.f;
      auto isk = [](int i) constexpr { return i < LAG || (i < 56 - LAG && ((i - LAG) & 1) == 0); };
      auto kst = [](int i) constexpr { return i < LAG ? i : (i < 56 - LAG ? ((((i - LAG) & 1) == 0) ? LAG + (i - LAG) / 2 : (i - LAG) / 2) : i - 28); };
      pipe<56>([&](auto I) AI { return tr_frag(LD, (isk(I.value) ? sbk : sbv) + kst(I.value) * 1024, 256); },
               [&](auto I) AI { if constexpr (I.value == LAG + 1 - PF || I.value == 38 || I.value == 46) { USTAMP(20 + I.value / 10 * 2); RAWBAR(); USTAMP(21 + I.value / 10 * 2); }   },
               [&](auto I, v4u a) AI {
                 constexpr int i = I.value, ks = kst(i);
                 if constexpr (isk(i)) ak = mfma(a, x[ks], ak); else av = mfma(a, x[ks], av);
                 if constexpr (!first && isk(i) && ks + XW < 28) x[ks + XW] = ldx(ks + XW);
                 if constexpr (first) {
                   if constexpr (i < 7) gx[21 + i] = ldg(21 + i);
                   if constexpr (isk(i) && ks % 7 == 3 && ks < 21) { stage_w(ks / 7 + 1); stage_r(ks / 7 + 1); }
                   if constexpr (isk(i)) {
                     const u32 one = 0x3f803f80u;
                     s1 = dot2(x[ks].x, one, s1); q1 = dot2(x[ks].x, x[ks].x, q1); s2 = dot2(x[ks].y, one, s2); q2 = dot2(x[ks].y, x[ks].y, q2);
                     s1 = dot2(x[ks].z, one, s1); q1 = dot2(x[ks].z, x[ks].z, q1); s2 = dot2(x[ks].w, one, s2); q2 = dot2(x[ks].w, x[ks].w, q2);
                   }
                   if constexpr (i == 45) { ln_finish(s1 + s2, q1 + q2, mean, rstd); nmean = -mean; }
                 } else {
                   if constexpr (!isk(i) && ks >= 4 && ks < 12) st[ks - 4] = ld8(rs, sv_ + (ks - 4) * 256, so_);
                   if constexpr (i == 45) { ln_stats(st, mean, rstd); nmean = -mean; }
                 }
                 if constexpr (i >= 39 && i < 43) cs[i - 39] = *(LAS f32x4*)(LD + csb + 32 * (i - 39));
                 if constexpr (i == 43) wf[0] = lds16(LD, wb);
                 if constexpr (i == 44) wf[1] = lds16(LD, wb + 1024);
                 if constexpr (i >= 47 && i < 51) {
                   constexpr int qd = i - 47;
                   const float y0 = fmaf(nmean, cs[qd][0], ak[4 * qd]), y1 = fmaf(nmean, cs[qd][1], ak[4 * qd + 1]);
                   const float y2 = fmaf(nmean, cs[qd][2], ak[4 * qd + 2]), y3 = fmaf(nmean, cs[qd][3], ak[4 * qd + 3]);
                   kf[qd >> 1][2 * (qd & 1)] = pk2(y0, y1);
                   kf[qd >> 1][2 * (qd & 1) + 1] = pk2(y2, y3);
                 }
                 if constexpr (i == 49) p = mfma(wf[0], kf[0], p);
                 if constexpr (i == 51) p = mfma(wf[1], kf[1], p);
               });
      STAMP(1);
      const int lz = opqv(lane);
      const int trow = w * 32 + (lz & 31);
      const int pb = opqv(L_PHK + trow * PHK_STRIDE + 8 * (lz >> 5)), vb = opqv(L_V + trow * V_STRIDE + 8 * (lz >> 5));
      dual_softmax(p, h, rstd, pq);
#pragma unroll
      for (int s = 0; s < 4; ++s) {
        *(LAS v2u*)(LD + pb + 32 * s) = v2u{pq[s].x, pq[s].y};
        *(LAS v2u*)(LD + pb + 32 * s + 16) = v2u{pq[s].z, pq[s].w};
      }
      SB();
#pragma unroll
      for (int qd = 0; qd < 4; ++qd) cs[qd] = *(LAS f32x4*)(LD + csb + 128 + 32 * qd);
#pragma unroll
      for (int r = 0; r < 16; ++r) av[r] = fmaf(nmean, cs[r >> 2][r & 3], av[r]) * rstd;
      if (h) { av[12] = 1.f; av[13] = 0.f; av[14] = 0.f; av[15] = 0.f; }
#pragma unroll
      for (int qd = 0; qd < 4; ++qd) *(LAS v2u*)(LD + vb + 16 * qd) = v2u{pk2(av[4 * qd], av[4 * qd + 1]), pk2(av[4 * qd + 2], av[4 * qd + 3])};
      STAMP(2);
      BAR();
      if constexpr (first) {
#pragma unroll
        for (int ro = 0; ro < 2; ++ro) {
          const int oi = min(kh * 4 + ro * 2 + h, 6);
          res[ro] = __builtin_amdgcn_raw_buffer_load_b128(rx0, tok * (D * 2) + (7 * cb + oi) * 16, tt2 * 32 * (D * 2), 0);
        }
      }
      f32x16 aq = {};
      const int sbq = opqv(opqv(w1lane) + 1 * UNIT);
      pipe<28>([&](auto I) AI { return tr_frag(LD, sbq + I.value * 1024, 256); }, [&](auto) AI {},
               [&](auto I, v4u a) AI { aq = mfma(a, x[I.value], aq); });
      if constexpr (first) wait_arr(LD, 1, 1);
#pragma unroll
      for (int qd = 0; qd < 4; ++qd) cs[qd] = *(LAS f32x4*)(LD + csb + 256 + 32 * qd);
#pragma unroll
      for (int r = 0; r < 16; ++r) aq[r] = fmaf(nmean, cs[r >> 2][r & 3], aq[r]);
      f32x16 p2 = {};
      const int wb2 = opqv(L_WPHI + opqv(l16));
      p2 = mfma(lds16(LD, wb2), pack8(aq, 0), p2);
      p2 = mfma(lds16(LD, wb2 + 1024), pack8(aq, 1), p2);
      dual_softmax(p2, h, rstd, pq);
    };
    phase1(std::integral_constant<bool, L == 0>{});
    STAMP(3);
    if (w == 0) wait_cnt(ws, C_KV + L * 16 + j, 32);
    BAR();
    STAMP(4);
    kv_reduce(rs, LD, L, j, w, lane);
    if (L == 0) kv_reduce(rs, LD, L, j, w + 4, lane);
    BAR();
    STAMP(5);
    {
      f32x16 nm = {};
      const int kb = opqv(L_KVF + opqv(l16));
      v4u kf[4];
#pragma unroll
      for (int s = 0; s < 4; ++s) kf[s] = lds16(LD, kb + s * 1024);
#pragma unroll
      for (int s = 0; s < 4; ++s) nm = mfma(kf[s], pq[s], nm);
      auto r = __builtin_amdgcn_permlane32_swap(__float_as_uint(nm[12]), __float_as_uint(nm[12]), false, false);
      const float den = __uint_as_float(r[1]) + 1e-6f;
      const float rd = __builtin_amdgcn_rcpf(den);
#pragma unroll
      for (int i = 0; i < 16; ++i) nm[i] *= rd;
      const int oo = opq(OFF_O + L * OB + (tt * 32 + 2 * j) * 1024);
      st16(rs, l16, oo, pack8(nm, 0));
      st16(rs, l16, oo + 1024, pack8(nm, 1));
    }
    wave_signal(ws, C_O + (L * 64 + tt) * 2 + (j >> 3), lane);
    STAMP(6);
    wait_cnt(ws, C_O + (L * 64 + tt2) * 2 + kh, 8, true);
    STAMP(7);
    {
      const int xo = opq(OFF_O + L * OB + (tt2 * 32 + 16 * kh) * 1024);
#pragma unroll
      for (int t = 0; t < XW; ++t) x[t] = ld16(rs, l16, xo + t * 1024);
      if (L == 0) RAWBAR();
      f32x16 a0 = {}, a1 = {};
      const int sb2 = opqv(2 * UNIT + kh * 2048 + opqv(l16)), sbx = opqv(L_W2X + kh * 2048 + opqv(l16));
      pipe<32>([&](auto I) AI { constexpr int i = I.value, t = i >> 1; return lds16(LD, (t < 14 ? sb2 + t * 4096 : sbx + (t - 14) * 4096) + (i & 1) * 1024); },
               [&](auto I) AI { if constexpr (I.value == 14) RAWBAR();   },
               [&](auto I, v4u a) AI {
                 constexpr int i = I.value;
                 if constexpr (i & 1) a1 = mfma(a, x[i >> 1], a1); else a0 = mfma(a, x[i >> 1], a0);
                 if constexpr (!(i & 1) && (i >> 1) + XW < 16) x[(i >> 1) + XW] = ld16(rs, l16, xo + ((i >> 1) + XW) * 1024);
               });
      const int xb = opqv(L_SCR + w * 4096 + opqv(l16)), xb2 = opqv(L_SCR + (w ^ 2) * 4096 + opqv(l16));
      f32x16 fin;
      if (kh) {
#pragma unroll
        for (int qd = 0; qd < 4; ++qd) *(LAS f32x4*)(LD + xb + qd * 1024) = f32x4{a0[4 * qd], a0[4 * qd + 1], a0[4 * qd + 2], a0[4 * qd + 3]};
        fin = a1;
      } else {
#pragma unroll
        for (int qd = 0; qd < 4; ++qd) *(LAS f32x4*)(LD + xb + qd * 1024) = f32x4{a1[4 * qd], a1[4 * qd + 1], a1[4 * qd + 2], a1[4 * qd + 3]};
        fin = a0;
      }
      BAR();
#pragma unroll
      for (int qd = 0; qd < 4; ++qd) {
        const f32x4 v = *(LAS f32x4*)(LD + xb2 + qd * 1024);
#pragma unroll
        for (int i = 0; i < 4; ++i) fin[4 * qd + i] += v[i];
      }
      finish_tile(fin, res, rs, opq(OFF_X1 + L * XB), opq(OFF_ST1 + L * STB), tt2, cb, kh, lane, false, A.out);
    }
    wave_signal(ws, C_X1 + L * 64 + tt2, lane);
    STAMP(8);
    wait_cnt(ws, C_X1 + L * 64 + tt, 16, true);
    STAMP(9);
    {
      const int xo = opq(OFF_X1 + L * XB + tt * 28 * 1024);
      const int so_ = opq(OFF_ST1 + L * STB + tt * 4096);
      const int sv_ = (opqv(lane) & 31) * 8 + h * 2048;
#pragma unroll
      for (int ks = 0; ks < XW; ++ks) x[ks] = ld16(rs, l16, xo + ks * 1024);
      arrive_lds(LD, 2, lane);
      const int ho = opq(OFF_H + L * HB + (tt * 112 + 7 * j) * 1024);
      u32* fo = cnt_ptr(ws, C_H + (L * 64 + tt) * 4);
      u32* fp = cnt_ptr(ws, C_H + (L * 64 + 2 * rb + (lane & 1)) * 4);
      u32 f = 0;
      float nmean, gs, irs;
      const int csb = opqv(L_CS3 + 16 * (opqv(lane) >> 5));
      const int sbA = opqv(l16), sbB = opqv(opqv(l16) + 2 * UNIT);
      f32x4 Gcg[2][2], Gcu[2][2]; float Gt[2][8], Gyu[2][8]; u32 Go[2][4];
      auto glu = [&](const f32x16& a, auto N_, auto S_, auto G_) AI {
        constexpr int n = N_.value, s = S_.value, G = G_.value;
        if constexpr (s == 0) Gcu[G][0] = *(LAS f32x4*)(LD + csb + n * 128 + 32);
        if constexpr (s == 5) Gcg[G][1] = *(LAS f32x4*)(LD + csb + n * 128 + 64);
        if constexpr (s == 10) Gcu[G][1] = *(LAS f32x4*)(LD + csb + n * 128 + 96);
        if constexpr (s == 23 && G == 0 && n < 6) Gcg[0][0] = *(LAS f32x4*)(LD + csb + (n == 0 ? 2 : n + 1) * 128);
        static_for<8>([&](auto V_) AI {
          constexpr int v = V_.value, k = s - (2 + (5 * v) / 2), hf = v >> 2, i = v & 3;
          if constexpr (k == 0) { Gt[G][v] = fmaf(nmean, Gcg[G][hf][i], a[8 * hf + i]); PIN(Gt[G][v]); }
          if constexpr (k == 1) { Gt[G][v] = Gt[G][v] * gs; PIN(Gt[G][v]); }
          if constexpr (k == 2) { Gt[G][v] = __builtin_amdgcn_exp2f(Gt[G][v]); PIN(Gt[G][v]); }
          if constexpr (k == 3) { Gt[G][v] = fmaf(Gt[G][v], irs, irs); PIN(Gt[G][v]); }
          if constexpr (k == 4) { Gt[G][v] = __builtin_amdgcn_rcpf(Gt[G][v]); PIN(Gt[G][v]); }
          if constexpr (k == 5) { Gyu[G][v] = fmaf(nmean, Gcu[G][hf][i], a[8 * hf + 4 + i]); PIN(Gyu[G][v]); }
          if constexpr (k == 6) { Gt[G][v] = Gt[G][v] * Gyu[G][v]; PIN(Gt[G][v]); }
          if constexpr (k == 7 && (v & 1)) { Go[G][v >> 1] = pk2(Gt[G][v - 1], Gt[G][v]); PIN(Go[G][v >> 1]); }
        });
        if constexpr (s == 27) {
          if constexpr (n == 3 || n == 5) {
            VMCNT(0);
            if (lane == 0) __hip_atomic_fetch_add(fo + 32 * (n == 3 ? 0 : 1), 1u, __ATOMIC_RELAXED, __HIP_MEMORY_SCOPE_AGENT);
          }
          st16(rs, l16, ho + n * 1024, v4u{Go[G][0], Go[G][1], Go[G][2], Go[G][3]});
        }
      };
      f32x16 acc[7];
      pipe<196>(
          [&](auto I) AI { constexpr int i = I.value, u = i / 28, sl = u & 3; return lds16(LD, (sl < 2 ? sbA : sbB) + (sl & 1) * UNIT + (i % 28) * 1024); },
          [&](auto I) AI { if constexpr (I.value > 0 && I.value % 28 == 0) { VSTAMP(30 + I.value / 28); RAWBAR(); } },
          [&](auto I, v4u a) AI {
            constexpr int i = I.value, u = i / 28, s = i % 28;
            if constexpr (u < 2) {
              constexpr int n = i & 1, ks = i >> 1;
              if constexpr (ks == 0) { f32x16 z = {}; acc[n] = mfma(a, x[ks], z); } else acc[n] = mfma(a, x[ks], acc[n]);
              if constexpr (n == 0 && ks + XW < 28) x[ks + XW] = ld16(rs, l16, xo + (ks + XW) * 1024);
              if constexpr (n == 1 && ks >= 8 && ks < 16) st[ks - 8] = ld8(rs, sv_ + (ks - 8) * 256, so_);
              if constexpr (i == 50) Gcg[0][0] = *(LAS f32x4*)(LD + csb);
              if constexpr (i == 51) Gcg[1][0] = *(LAS f32x4*)(LD + csb + 128);
              if constexpr (i == 53) {
                ln_stats(st, mean, rstd);
                nmean = -mean; gs = -LOG2E * rstd; irs = __builtin_amdgcn_rcpf(rstd);
              }
            } else {
              if constexpr (s == 0) { f32x16 z = {}; acc[u] = mfma(a, x[s], z); } else acc[u] = mfma(a, x[s], acc[u]);
              if constexpr (u == 2) { glu(acc[0], IC<0>{}, IC<s>{}, IC<0>{}); glu(acc[1], IC<1>{}, IC<s>{}, IC<1>{}); }
              else glu(acc[u - 1], IC<u - 1>{}, IC<s>{}, IC<0>{});
              if constexpr (i == 176) f = __hip_atomic_load(fp, __ATOMIC_RELAXED, __HIP_MEMORY_SCOPE_AGENT);
            }
          });
      RAWBAR();
      STAMP(10);
      const int ho2 = opq(OFF_H + L * HB + 2 * rb * 112 * 1024);
      const int hc = opq(w + 4);
      const int hf_ = opq(OFF_H + L * HB + (tt2 * 112 + 6) * 1024);
      constexpr int HW = 8;
      v4u hx[36];
      auto ldh = [&](auto I) AI {
        constexpr int i = I.value, t = i >> 1;
        if constexpr (i < 20) return ld16(rs, l16, ho2 + (i & 1) * 112 * 1024 + p4_koff(t / 5, t % 5, hc));
        else return ld16(rs, l16, hf_ + (i - 20) * 7 * 1024);
      };
      auto lvl = [](int i) constexpr { return i < 20 ? p4_lvl((i >> 1) / 5, (i >> 1) % 5) : 2; };
      wait_flags(ws, fp, f);
      static_for<HW>([&](auto I) AI { hx[I.value] = ldh(I); });
      static_for<28>([&](auto S_) AI { glu(acc[6], IC<6>{}, S_, IC<0>{}); });
      STAMP(11);
      const int wa0 = opqv(w * 2048 + opqv(l16)), wa1 = opqv(w * 2048 + opqv(l16) + 5 * 8192);
      const int wfb = opqv(L_FRESH + kh * 1024 + opqv(l16));
      v4u wq[20];
      auto ldw = [&](auto K) AI { constexpr int k = K.value, t = k >> 1; return lds16(LD, (t < 5 ? wa0 : wa1) + (t % 5) * 8192 + (k & 1) * 1024); };
      static_for<4>([&](auto K) AI { wq[K.value] = ldw(K); });
      f32x16 pc[4] = {};
      f32x16 fin;
      v4u wfr[16];
      static_for<20>([&](auto I) AI {
        constexpr int i = I.value, t = i >> 1, tq = i & 1, nx = i + HW;
        if constexpr (i == 8) RAWBAR();
        if constexpr (i + 4 < 20) wq[i + 4] = ldw(IC<i + 4>{});
        else wfr[i - 16] = lds16(LD, wfb + (i - 16) * 2048);
        if constexpr (i == 2) {
          VMCNT(3);
          if (lane == 0) __hip_atomic_fetch_add(fo + 64, 1u, __ATOMIC_RELAXED, __HIP_MEMORY_SCOPE_AGENT);
        }
        pc[tq] = mfma(wq[2 * t], hx[i], pc[tq]);
        pc[2 + tq] = mfma(wq[2 * t + 1], hx[i], pc[2 + tq]);
        if constexpr (lvl(nx + 2) > lvl(nx + 1)) f = __hip_atomic_load(fp + 32 * lvl(nx + 2), __ATOMIC_RELAXED, __HIP_MEMORY_SCOPE_AGENT);
        if constexpr (lvl(nx) > lvl(nx - 1)) wait_flags(ws, fp + 32 * lvl(nx), f);
        hx[nx] = ldh(IC<nx>{});
        SB();
      });
      RAWBAR();
      p4_put(pc[0], 0, w, LD, l16, fin);
      p4_put(pc[2], 2, w, LD, l16, fin);
      p4_put(pc[1], 1, w, LD, l16, fin);
      p4_put(pc[3], 3, w, LD, l16, fin);
      static_for<16>([&](auto I) AI {
        constexpr int i = I.value;
        if constexpr (i + 4 < 16) wfr[i + 4] = lds16(LD, wfb + (i + 4) * 2048);
        fin = mfma(wfr[i], hx[20 + i], fin);
        if constexpr (20 + i + HW < 36) hx[20 + i + HW] = ldh(IC<20 + i + HW>{});
        SB();
      });
      STAMP(14);
      BAR();
#pragma unroll
      for (int ww = 0; ww < 7; ++ww) {
        const int rr = opqv(p4_part(w, ww) + opqv(l16));
#pragma unroll
        for (int qd = 0; qd < 4; ++qd) {
          const f32x4 v = *(LAS f32x4*)(LD + rr + qd * 1024);
#pragma unroll
          for (int i = 0; i < 4; ++i) fin[4 * qd + i] += v[i];
        }
      }
      STAMP(15);
      finish_tile(fin, res, rs, OFF_X2, OFF_ST2, tt2, cb, kh, lane, L == 1, A.out);
    }
    if (L == 0) {
      wave_signal(ws, C_X2 + tt2, lane);
      arrive_lds(LD, 0, lane);
      STAMP(16);
      wait_cnt(ws, C_X2 + tt, 16, true);
    }
    STAMP(17);
  };
  clayer(IC<0>{});
  clayer(IC<1>{});
}

extern "C" void kernel_launch(void* const* d_in, const int* in_sizes, int n_in, void* d_out, int out_size, void* d_ws,
                              size_t ws_size, hipStream_t stream) {
  Args a;
  a.x0 = (const u16*)d_in[0]; a.wqkv = (const u16*)d_in[1]; a.wphi = (const u16*)d_in[2]; a.wo = (const u16*)d_in[3];
  a.wg = (const u16*)d_in[5]; a.wu = (const u16*)d_in[6]; a.wd = (const u16*)d_in[7];
  a.ws = (char*)d_ws; a.out = (u16*)d_out;
  (void)hipFuncSetAttribute((const void*)k_main, hipFuncAttributeMaxDynamicSharedMemorySize, LDS_BYTES);
  k_main<<<256, 512, LDS_BYTES, stream>>>(a);
}
```

```cpp
#include <hip/hip_runtime.h>
#include <stdint.h>
#include <utility>
#include <type_traits>

typedef unsigned short u16;
typedef unsigned int u32;
typedef u32 v4u __attribute__((ext_vector_type(4)));
typedef u32 v2u __attribute__((ext_vector_type(2)));
typedef float f32x2 __attribute__((ext_vector_type(2)));
typedef float f32x4 __attribute__((ext_vector_type(4)));
typedef float f32x16 __attribute__((ext_vector_type(16)));
typedef __bf16 bf16x2 __attribute__((ext_vector_type(2)));
typedef __bf16 bf16x8 __attribute__((ext_vector_type(8)));
typedef short s16x4 __attribute__((ext_vector_type(4)));
#define LAS __attribute__((address_space(3)))
#define DI __device__ __forceinline__
#define AI __attribute__((always_inline))
template <int V> using IC = std::integral_constant<int, V>;

constexpr int S = 2048, D = 448, FF = 1792;
constexpr int UNIT = 28672;

constexpr int C_KV = 0;
constexpr int C_W2 = 32;
constexpr int C_W4 = 36;
constexpr int C_W3 = 40;
constexpr int C_ERR = 60;
constexpr int C_X2 = 64;
constexpr int C_X1 = 128;
constexpr int C_O = 256;
constexpr int C_H = 512;
constexpr int C_W1 = 1536;
constexpr int NZ = 25;
constexpr int EPOCH_OFF = NZ * 64 * 128;
constexpr int INIT_OFF = EPOCH_OFF + 256;
constexpr int CTL_BYTES = EPOCH_OFF + 4096;
constexpr int W2B = 65536;
constexpr int OFF_W1 = CTL_BYTES;
constexpr int OFF_W2 = OFF_W1 + 16 * 3 * UNIT;
constexpr int OFF_W3 = OFF_W2 + 8 * W2B;
constexpr int OFF_W4 = OFF_W3 + 16 * 7 * UNIT;
constexpr int OFF_CS3 = OFF_W4 + 8 * 8 * UNIT;
constexpr int XB = S * D * 2;
constexpr int OB = S * 512 * 2;
constexpr int OFF_X2 = OFF_CS3 + 16 * 28 * 896;
constexpr int OFF_X1 = OFF_X2 + XB;
constexpr int OFF_O = OFF_X1 + 2 * XB;
constexpr int STB = S * 16 * 8;
constexpr int OFF_ST2 = OFF_O + 2 * OB;
constexpr int OFF_ST1 = OFF_ST2 + STB;
constexpr int KVB = 16 * 16 * 4096;
constexpr int OFF_KVP = OFF_ST1 + 2 * STB;
constexpr int HB = S * FF * 2;
constexpr int OFF_H = OFF_KVP + 2 * KVB;
constexpr int WS_USED = OFF_H + 2 * HB;

constexpr int L_SCR = 4 * UNIT;
constexpr int PHK_STRIDE = 136, V_STRIDE = 72;
constexpr int L_PHK = L_SCR;
constexpr int L_V = L_SCR + 128 * PHK_STRIDE;
constexpr int L_KVF = L_SCR + 28672;
constexpr int L_W2X = L_KVF + 4096;
constexpr int L_CONST = 155648;
constexpr int L_CS1 = L_CONST, L_CS3 = L_CONST + 384, L_WPHI = L_CONST + 1280, L_ARR = L_CONST + 3328;
constexpr int L_STAGE = 2 * UNIT;
constexpr int STG = 8704;
constexpr int LDS_BYTES = 163840;

constexpr float LOG2E = 1.4426950408889634f;
constexpr int LAG = 4;

DI u32 pk2(float a, float b) { f32x2 v = {a, b}; return __builtin_bit_cast(u32, __builtin_convertvector(v, bf16x2)); }
DI float bflo(u32 v) { return __uint_as_float(v << 16); }
DI float bfhi(u32 v) { return __uint_as_float(v & 0xffff0000u); }
DI f32x16 mfma(v4u a, v4u b, f32x16 c) {
  f32x16 d = __builtin_amdgcn_mfma_f32_32x32x16_bf16(__builtin_bit_cast(bf16x8, a), __builtin_bit_cast(bf16x8, b), c, 0, 0, 0);
  asm volatile("" : "+v"(d));
  return d;
}
DI float xsum(float v) {
  auto r = __builtin_amdgcn_permlane32_swap(__float_as_uint(v), __float_as_uint(v), false, false);
  return __uint_as_float(r[0]) + __uint_as_float(r[1]);
}
DI float xmax(float v) {
  auto r = __builtin_amdgcn_permlane32_swap(__float_as_uint(v), __float_as_uint(v), false, false);
  return fmaxf(__uint_as_float(r[0]), __uint_as_float(r[1]));
}
DI float xmin(float v) {
  auto r = __builtin_amdgcn_permlane32_swap(__float_as_uint(v), __float_as_uint(v), false, false);
  return fminf(__uint_as_float(r[0]), __uint_as_float(r[1]));
}
#define BAR() asm volatile("s_waitcnt lgkmcnt(0)\n\ts_barrier" ::: "memory")
#define RAWBAR() asm volatile("s_barrier" ::: "memory")
#define VMCNT(n) asm volatile("s_waitcnt vmcnt(" #n ")" ::: "memory")
#define SB() __builtin_amdgcn_sched_barrier(0)
#define PIN(x) asm volatile("" : "+v"(x))

typedef __amdgpu_buffer_rsrc_t rsrc_t;
DI v4u ld16(rsrc_t rs, int voff, int soff) { return __builtin_amdgcn_raw_buffer_load_b128(rs, voff, soff, 16); }
DI v2u ld8(rsrc_t rs, int voff, int soff) { return __builtin_amdgcn_raw_buffer_load_b64(rs, voff, soff, 16); }
DI u32 ld4(rsrc_t rs, int voff, int soff) { return __builtin_amdgcn_raw_buffer_load_b32(rs, voff, soff, 16); }
DI void st16(rsrc_t rs, int voff, int soff, v4u v) {
  __builtin_amdgcn_raw_buffer_store_b128(v, rs, voff, soff, 16);
  asm volatile("s_nop 1" ::"v"(v) : "memory");
}
DI void st8(rsrc_t rs, int voff, int soff, v2u v) { __builtin_amdgcn_raw_buffer_store_b64(v, rs, voff, soff, 16); }
DI void st4(rsrc_t rs, int voff, int soff, u32 v) { __builtin_amdgcn_raw_buffer_store_b32(v, rs, voff, soff, 16); }
DI int opq(int v) { asm volatile("" : "+s"(v)); return v; }
DI int opqv(int v) { asm volatile("" : "+v"(v)); return v; }
DI v4u lds16(LAS char* l, int off) { return *(LAS v4u*)(l + off); }
DI float dot2(u32 a, u32 b, float c) {
  return __builtin_amdgcn_fdot2_f32_bf16(__builtin_bit_cast(bf16x2, a), __builtin_bit_cast(bf16x2, b), c, false);
}
DI v4u tr_frag(LAS char* L, int base, int step) {
  v2u lo = __builtin_bit_cast(v2u, __builtin_amdgcn_ds_read_tr16_b64_v4i16((LAS s16x4*)(L + base)));
  v2u hi = __builtin_bit_cast(v2u, __builtin_amdgcn_ds_read_tr16_b64_v4i16((LAS s16x4*)(L + base + step)));
  return v4u{lo.x, lo.y, hi.x, hi.y};
}
DI float sum8(v4u o) {
  const u32 one = 0x3f803f80u;
  return dot2(o.x, one, dot2(o.y, one, 0.f)) + dot2(o.z, one, dot2(o.w, one, 0.f));
}

DI u32* cnt_ptr(char* ws, int idx) { return (u32*)(ws + idx * 128); }
#define POLL_D0 16
#define LATE_ARR
#define WOV 0
#define WOS 20
#define POLL_IV 1
DI bool spin_more(char* ws, u32& spins) {
  u32* e = cnt_ptr(ws, C_ERR);
  __builtin_amdgcn_s_sleep(POLL_IV);
  ++spins;
  if ((spins & 255u) == 0u) {
    if (__builtin_amdgcn_readfirstlane(__hip_atomic_load(e, __ATOMIC_RELAXED, __HIP_MEMORY_SCOPE_AGENT)) != 0) return false;
    if (spins > 400000u) { __hip_atomic_store(e, 1u, __ATOMIC_RELAXED, __HIP_MEMORY_SCOPE_AGENT); return false; }
  }
  return true;
}
DI void wait_cnt(char* ws, int idx, u32 target, bool delay = false) {
  u32* c = cnt_ptr(ws, idx);
  u32 spins = 0;
  if (delay && POLL_D0) __builtin_amdgcn_s_sleep(POLL_D0);
  while ((u32)__builtin_amdgcn_readfirstlane(__hip_atomic_load(c, __ATOMIC_RELAXED, __HIP_MEMORY_SCOPE_AGENT)) < target)
    if (!spin_more(ws, spins)) break;
  __builtin_amdgcn_fence(__ATOMIC_ACQUIRE, "wavefront");
  asm volatile("" ::: "memory");
}
DI void wait_flags(char* ws, u32* fp, u32 f, u32 target = 16u) {
  if (__builtin_expect(!__all(f >= target), 0)) {
    u32 spins = 0;
    while (spin_more(ws, spins))
      if (__all(__hip_atomic_load(fp, __ATOMIC_RELAXED, __HIP_MEMORY_SCOPE_AGENT) >= target)) break;
  }
  __builtin_amdgcn_fence(__ATOMIC_ACQUIRE, "wavefront");
  asm volatile("" ::: "memory");
}
DI void signal_cnt(char* ws, int idx) {
  unsigned long long sv;
  asm volatile("s_mov_b64 %0, exec\n\ts_mov_b64 exec, 1\n\tglobal_atomic_add %1, %2, %3\n\ts_mov_b64 exec, %0"
               : "=&s"(sv) : "v"(idx * 128), "v"(1), "s"(ws) : "memory");
}
DI void wave_signal(char* ws, int idx, int lane) { VMCNT(0); signal_cnt(ws, idx); }
DI void arrive_lds(LAS char* LD, int slot, int lane) {
  if (lane == 0) __hip_atomic_fetch_add((LAS u32*)(LD + L_ARR + slot * 4), 1u, __ATOMIC_RELAXED, __HIP_MEMORY_SCOPE_WORKGROUP);
}
DI void wait_arr(LAS char* LD, int slot, u32 target) {
  while ((u32)__builtin_amdgcn_readfirstlane(__hip_atomic_load((LAS u32*)(LD + L_ARR + slot * 4), __ATOMIC_RELAXED, __HIP_MEMORY_SCOPE_WORKGROUP)) < target)
    __builtin_amdgcn_s_sleep(1);
}

#define STAMP(i)
#define HSTAMP(i)
#define VSTAMP(i)
#define USTAMP(i)

#define DMA_STEP(GSTEP) \
      "s_add_u32 m0, m0, 0x1000\n\t" \
      "v_add_u32 %1, " GSTEP ", %1\n\t" \
      "global_load_lds_dwordx4 %1, %2 sc1\n\t"
#define DMA_FN(NAME, BODY) \
DI void NAME(const char* gsrc, u32 voff, u32 lds_dst) { \
  u32 keep; \
  asm volatile("s_mov_b32 %0, m0\n\ts_mov_b32 m0, %3\n\ts_nop 0\n\tglobal_load_lds_dwordx4 %1, %2 sc1\n\t" BODY "s_mov_b32 m0, %0" \
               : "=&s"(keep), "+v"(voff) : "s"(gsrc), "s"(lds_dst) : "memory", "scc"); }
DMA_FN(dma7, DMA_STEP("0x1000") DMA_STEP("0x1000") DMA_STEP("0x1000") DMA_STEP("0x1000") DMA_STEP("0x1000") DMA_STEP("0x1000"))
DMA_FN(dma2, DMA_STEP("0x1000"))
DMA_FN(dma7n, DMA_STEP("0x2a000") DMA_STEP("0x2a000") DMA_STEP("0x2a000") DMA_STEP("0x2a000") DMA_STEP("0x2a000") DMA_STEP("0x2a000"))

template <int LO, int GO>
DI void dma1(const char* gsrc, u32 voff, u32 lb) {
  u32 keep, tmp;
  asm volatile("s_mov_b32 %0, m0\n\ts_add_u32 m0, %4, %5\n\tv_add_u32 %1, %6, %2\n\tglobal_load_lds_dwordx4 %1, %3 sc1\n\ts_mov_b32 m0, %0"
               : "=&s"(keep), "=&v"(tmp) : "v"(voff), "s"(gsrc), "s"(lb), "i"(LO), "i"(GO) : "memory", "scc");
}

template <typename F, int... I>
DI void static_for_impl(F&& f, std::integer_sequence<int, I...>) { (f(IC<I>{}), ...); }
template <int N, typename F>
DI void static_for(F&& f) { static_for_impl(f, std::make_integer_sequence<int, N>{}); }

constexpr int PF = 6;
template <int N, typename RD, typename PRE, typename ST>
DI void pipe(RD rd, PRE pre, ST step) {
  v4u buf[PF];
  static_for<PF>([&](auto I) AI { buf[I.value] = rd(I); });
  SB();
  static_for<N>([&](auto I) AI {
    constexpr int i = I.value;
    pre(I);
    const v4u a = buf[i % PF];
    if constexpr (i + PF < N) buf[i % PF] = rd(IC<i + PF>{});
    step(I, a);
    SB();
  });
}

DI void ln_finish(float s, float q, float& mean, float& rstd) {
  s = xsum(s); q = xsum(q);
  mean = s * (1.f / 448.f);
  const float var = fmaxf(q * (1.f / 448.f) - mean * mean, 0.f);
  rstd = __builtin_amdgcn_rsqf(var + 1e-5f);
}
DI void ln_stats(const v2u (&st)[8], float& mean, float& rstd) {
  f32x2 a = __builtin_bit_cast(f32x2, st[0]), b = __builtin_bit_cast(f32x2, st[1]);
#pragma unroll
  for (int i = 2; i < 8; i += 2) { a += __builtin_bit_cast(f32x2, st[i]); b += __builtin_bit_cast(f32x2, st[i + 1]); }
  a += b;
  ln_finish(a[0], a[1], mean, rstd);
}
DI v4u pack8(const f32x16& a, int s) {
  v4u r = {pk2(a[8 * s], a[8 * s + 1]), pk2(a[8 * s + 2], a[8 * s + 3]), pk2(a[8 * s + 4], a[8 * s + 5]), pk2(a[8 * s + 6], a[8 * s + 7])};
  return r;
}
template <int SG>
DI void half_softmax(const f32x16& p, int h, float rstd, v4u& o0, v4u& o1) {
  auto mm = [](float a, float b) AI { return SG ? fminf(a, b) : fmaxf(a, b); };
  float m0 = mm(mm(p[0], p[1]), p[2]), m1 = mm(mm(p[3], p[4]), p[5]), m2 = mm(mm(p[6], p[7]), p[8]), m3 = mm(mm(p[9], p[10]), p[11]);
  float m4 = mm(mm(mm(p[12], p[13]), p[14]), p[15]);
  m4 = h ? p[0] : m4;
  float mx = mm(mm(mm(m0, m1), m2), mm(m3, m4));
  mx = SG ? xmin(mx) : xmax(mx);
  const float c = rstd * LOG2E;
  const float cc = SG ? -c : c, off = -mx * cc;
  f32x16 e;
  float s0 = 0.f, s1 = 0.f;
#pragma unroll
  for (int r = 0; r < 16; ++r) {
    float v = __builtin_amdgcn_exp2f(fmaf(p[r], cc, off));
    if (r >= 12) v = h ? 0.f : v;
    e[r] = v;
    if (r & 1) s1 += v; else s0 += v;
  }
  const float rs_ = __builtin_amdgcn_rcpf(xsum(s0 + s1));
#pragma unroll
  for (int r = 0; r < 16; ++r) e[r] *= rs_;
  o0 = pack8(e, 0); o1 = pack8(e, 1);
  PIN(o0); PIN(o1);
  SB();
}

DI void finish_tile(const f32x16& acc, v4u (&res)[2], rsrc_t rs, int xoff, int stoff, int tt, int cb, int nt, int lane, bool last, u16* out) {
  lane = opqv(lane);
  const int h = lane >> 5, tok = lane & 31;
#pragma unroll
  for (int ro = 0; ro < 2; ++ro) {
    v4u r = res[ro];
    v4u o;
    o.x = pk2(bflo(r.x) + acc[8 * ro + 0], bfhi(r.x) + acc[8 * ro + 1]);
    o.y = pk2(bflo(r.y) + acc[8 * ro + 2], bfhi(r.y) + acc[8 * ro + 3]);
    o.z = pk2(bflo(r.z) + acc[8 * ro + 4], bfhi(r.z) + acc[8 * ro + 5]);
    o.w = pk2(bflo(r.w) + acc[8 * ro + 6], bfhi(r.w) + acc[8 * ro + 7]);
    res[ro] = o;
    const int oi = nt * 4 + ro * 2 + h;
    if (oi < 7) {
      const int go = 7 * cb + oi;
      if (last) *(v4u*)(out + (size_t)(tt * 32 + tok) * D + go * 8) = o;
      else st16(rs, ((go >> 1) * 64 + tok + 32 * (go & 1)) * 16, xoff + tt * 28 * 1024, o);
    }
  }
  if (!last) {
    const bool v1 = !(nt == 1 && h == 1);
    const u32 one = 0x3f803f80u;
    float s0 = dot2(res[0].x, one, 0.f), s1 = dot2(res[1].x, one, 0.f), q0 = dot2(res[0].x, res[0].x, 0.f), q1 = dot2(res[1].x, res[1].x, 0.f);
    s0 = dot2(res[0].y, one, s0); s1 = dot2(res[1].y, one, s1); q0 = dot2(res[0].y, res[0].y, q0); q1 = dot2(res[1].y, res[1].y, q1);
    s0 = dot2(res[0].z, one, s0); s1 = dot2(res[1].z, one, s1); q0 = dot2(res[0].z, res[0].z, q0); q1 = dot2(res[1].z, res[1].z, q1);
    s0 = dot2(res[0].w, one, s0); s1 = dot2(res[1].w, one, s1); q0 = dot2(res[0].w, res[0].w, q0); q1 = dot2(res[1].w, res[1].w, q1);
    const float sum = xsum(s0 + (v1 ? s1 : 0.f));
    const float sq = xsum(q0 + (v1 ? q1 : 0.f));
    if (h == 0) st8(rs, tok * 8, stoff + ((tt * 8 + cb) * 2 + nt) * 256, v2u{__float_as_uint(sum), __float_as_uint(sq)});
  }
}

DI void kv_reduce(rsrc_t rs, LAS char* LD, int L, int j, int w, int lane) {
  const int ko = opq(OFF_KVP + L * KVB + j * 16 * 4096 + (w >> 1) * 1024 + (w & 1) * 512);
  const int lz = opqv(lane);
  const int vo = (lz >> 5) * 4096 + (lz & 31) * 16;
  v4u t[8];
#pragma unroll
  for (int i = 0; i < 8; ++i) t[i] = ld16(rs, vo, ko + i * 8192);
  float s[8] = {};
  const u32 lo1 = (u32)opq(0x00003f80), hi1 = (u32)opq(0x3f800000);
#pragma unroll
  for (int i = 0; i < 8; ++i) {
#pragma unroll
    for (int c = 0; c < 4; ++c) { s[2 * c] = dot2(t[i][c], lo1, s[2 * c]); s[2 * c + 1] = dot2(t[i][c], hi1, s[2 * c + 1]); }
  }
#pragma unroll
  for (int c = 0; c < 8; ++c) s[c] = xsum(s[c]);
  if (lz < 32) *(LAS v4u*)(LD + L_KVF + (w >> 1) * 1024 + (w & 1) * 512 + lz * 16) = v4u{pk2(s[0], s[1]), pk2(s[2], s[3]), pk2(s[4], s[5]), pk2(s[6], s[7])};
}

constexpr int p4_oh(int t) { return t < 7 ? 4 * t : 16 + 4 * t; }
constexpr int p4_oc(int t) { return t < 8 ? 28 + 2 * t : 56 + 2 * t; }
constexpr int p4_koff(int o) { return (7 * (o & 15) + (o >> 4)) * 1024; }
DI int p4_part(int a, int hw) { const int i = a * 4 + hw; return i < 10 ? L_SCR + i * 4096 : (i - 10) * 4096; }
DI void put16(const f32x16& acc, LAS char* LD, int off) {
#pragma unroll
  for (int qd = 0; qd < 4; ++qd) *(LAS f32x4*)(LD + off + qd * 1024) = f32x4{acc[4 * qd], acc[4 * qd + 1], acc[4 * qd + 2], acc[4 * qd + 3]};
}
DI void add16(f32x16& acc, LAS char* LD, int off) {
#pragma unroll
  for (int qd = 0; qd < 4; ++qd) {
    const f32x4 v = *(LAS f32x4*)(LD + off + qd * 1024);
#pragma unroll
    for (int i = 0; i < 4; ++i) acc[4 * qd + i] += v[i];
  }
}

struct Args {
  const u16* x0; const u16* wqkv; const u16* wphi; const u16* wo; const u16* wg; const u16* wu; const u16* wd;
  char* ws; u16* out;
};

DI void retile_addr(const Args& A, int job, int lane, const v4u* (&pa)[7]) {
  if (job < 64 || job >= 512) {
    const bool iswo = job < 64;
    const int a0 = iswo ? job >> 2 : (job - 512) >> 2, cg = job & 3;
    const u16* src = (iswo ? A.wo + (size_t)a0 * 28 * D : A.wd + (size_t)a0 * 32 * D) + 112 * cg;
    const int nrow = iswo ? 28 : 32;
#pragma unroll
    for (int i = 0; i < 7; ++i) {
      const int p = lane + 64 * i, r = p / 14, c = p % 14;
      pa[i] = (const v4u*)(src + (size_t)(r < nrow ? r : 0) * D + c * 8);
    }
  } else {
    const int a = job - 64, j = a / 28, ks = a % 28;
#pragma unroll
    for (int i = 0; i < 7; ++i) {
      const int p = lane + 64 * i, r = p / 28, c = p % 28;
      pa[i] = (const v4u*)((c < 14 ? A.wg : A.wu) + (size_t)(ks * 16 + r) * FF + 112 * j + (c < 14 ? c : c - 14) * 8);
    }
  }
#pragma unroll
  for (int i = 0; i < 7; ++i) PIN(pa[i]);
}
DI int retile_store(int job, rsrc_t rs, LAS char* LD, int stage, int lane, const v4u (&t)[7]) {
  const int h = lane >> 5, q = (lane & 15) >> 2, c8 = ((lane >> 4) & 1) * 4 + (lane & 3);
  const int hh = c8 & 1, quad = c8 >> 1;
  if (job < 64 || job >= 512) {
    const bool iswo = job < 64;
    const int a0 = iswo ? job >> 2 : (job - 512) >> 2, cg = job & 3;
    const int nrow = iswo ? 28 : 32;
    constexpr int ST = 240;
#pragma unroll
    for (int i = 0; i < 7; ++i) {
      const int p = lane + 64 * i, r = p / 14, c = p % 14;
      *(LAS v4u*)(LD + stage + r * ST + c * 16) = r < nrow ? t[i] : v4u{0u, 0u, 0u, 0u};
    }
#pragma unroll
    for (int i = 0; i < 4; ++i) {
      const int cb = cg * 2 + (i >> 1), nt = i & 1;
      const int oi = nt * 4 + (quad >> 1) * 2 + hh;
      const int ro = (lane >> 4) & 1, rh = (lane >> 2) & 1;
      const bool valid = nt * 4 + ro * 2 + rh < 7;
      const int colb = (56 * (i >> 1) + 8 * (oi < 7 ? oi : 6) + 4 * (quad & 1)) * 2;
#pragma unroll
      for (int k2 = 0; k2 < 2; ++k2) {
        v4u o = iswo ? tr_frag(LD, stage + (16 * k2 + 4 * h + q) * ST + colb, 8 * ST) : tr_frag(LD, stage + (16 * k2 + 8 * h + q) * ST + colb, 4 * ST);
        if (!valid) o = v4u{0u, 0u, 0u, 0u};
        const int ks = a0 * 2 + k2;
        int off;
        if (iswo) off = OFF_W2 + cb * W2B + (((ks & 15) * 2 + (ks >> 4)) * 2 + nt) * 1024;
        else {
          const int o_ = 16 * (ks % 7) + ks / 7;
          int fr;
          if (o_ < 28 || (o_ >= 44 && o_ < 72)) fr = 112 + ((o_ & 3) * 14 + (o_ < 28 ? o_ >> 2 : (o_ - 16) >> 2)) * 2 + nt;
          else fr = (o_ < 44 ? o_ - 28 : o_ - 56) * 2 + nt;
          off = OFF_W4 + cb * 8 * UNIT + fr * 1024;
        }
        st16(rs, lane * 16, off, o);
      }
    }
    return (iswo ? C_W2 : C_W4) + cg;
  } else {
    const int a = job - 64, j = a / 28, ks = a % 28;
    constexpr int ST = 464;
#pragma unroll
    for (int i = 0; i < 7; ++i) { const int p = lane + 64 * i, r = p / 28, c = p % 28; *(LAS v4u*)(LD + stage + r * ST + c * 16) = t[i]; }
    float cs[7];
#pragma unroll
    for (int n = 0; n < 7; ++n) {
      const v4u o = tr_frag(LD, stage + (8 * h + q) * ST + (quad & 1) * 224 + (16 * n + 8 * hh + 4 * (quad >> 1)) * 2, 4 * ST);
      st16(rs, lane * 16, OFF_W3 + j * 7 * UNIT + (n < 2 ? (ks * 2 + n) * 1024 : n * UNIT + ks * 1024), o);
      cs[n] = xsum(sum8(o));
    }
#pragma unroll
    for (int n2 = 0; n2 < 4; ++n2) {
      const float v = h ? cs[n2 < 3 ? 2 * n2 + 1 : 6] : cs[2 * n2];
      if (n2 < 3 || !h) st4(rs, lane * 4, OFF_CS3 + (j * 28 + ks) * 896 + n2 * 256, __float_as_uint(v));
    }
    return C_W3 + j;
  }
}

__global__ __launch_bounds__(512, 2) void k_main(const Args A) {
  extern __shared__ __attribute__((aligned(16))) char smem[];
  LAS char* LD = (LAS char*)smem;
  char* ws = A.ws;
  const int b = blockIdx.x, xcd = b & 7, bi = b >> 3;
  const int g = (xcd >> 2) * 8 + (bi >> 2), j = (xcd & 3) * 4 + (bi & 3);
  const int rb = xcd * 4 + (bi >> 3), cb = bi & 7;
  const int lane = threadIdx.x & 63, w = __builtin_amdgcn_readfirstlane(threadIdx.x >> 6);
  const int h = lane >> 5, tok = lane & 31;
  const rsrc_t rs = __builtin_amdgcn_make_buffer_rsrc((void*)ws, 0, WS_USED, 0x00020000);
  const u32 lbase = (u32)(size_t)smem;
  const int l16 = lane * 16;
  const int tq_ = (lane & 15) >> 2, tc8 = ((lane >> 4) & 1) * 4 + (lane & 3);
  const int w1lane = (8 * h + tq_) * 64 + min(tc8 + (j & 1), 7) * 8;

  const int hw = w - 4;
  const u32 voffn = (hw * 16 + (lane >> 2)) * (3 * D * 2) + (lane & 3) * 16;
  const char* w1 = (const char*)A.wqkv + 56 * j - (j & 1) * 8;
  unsigned long long ep;
  const u32 lb = lbase + hw * 1024;
#define KP(p) dma1<4 * UNIT + (p) * 4096, 896 + (p) * 0x2a000>(w1, voffn, lb)
#define VP(p) dma1<(p) * 4096, 1792 + (p) * 0x2a000>(w1, voffn, lb)
  if (opq(w) >= 4) {
    HSTAMP(0);
    if (hw == 1) {
      asm volatile("global_load_dwordx2 %0, %1, %2 sc1" : "=v"(ep) : "v"(EPOCH_OFF), "s"(ws) : "memory");
      if (b < NZ) st4(rs, lane * 128, b * 8192, 0u);
    }
    if (hw == 2) {
      dma1<L_KVF, 0>((const char*)A.wphi + j * 1568, l16, lbase);
      dma1<L_KVF + 1024, 0>((const char*)A.wphi + j * 1568, min(1024 + l16, 1552), lbase);
    }
    KP(0); KP(1); VP(0); KP(2); VP(1); KP(3); VP(2);
    HSTAMP(1);
  }

  if (w >= 4) {
    const u32 voff = hw * 1024 + lane * 16;
    const char* w1f = ws + OFF_W1 + j * 3 * UNIT;
    const char* w2 = ws + OFF_W2 + cb * W2B;
    const char* w3 = ws + OFF_W3 + j * 7 * UNIT;
    const char* w4 = ws + OFF_W4 + cb * 8 * UNIT;
#define DMA(src, slot) dma7((src), voff, lbase + (slot) * UNIT + hw * 1024)
#define DMAN(which, slot) dma7n(w1 + (which) * 896, voffn, lbase + (slot) * UNIT + hw * 1024)
    v4u wr[28];
    unsigned long long tag = 0;
    auto hlayer = [&](auto LC) AI {
      constexpr int L = LC.value;
      v4u jt[7];
      const int job = hw >= 2 ? b * 2 + hw - 2 : (hw == 0 && b < 224 ? 512 + b : -1);
      float c0 = 0.f, c1 = 0.f, c2 = 0.f, c3 = 0.f;
      auto colpart = [&](int slot, auto K0, auto K1) AI {
        constexpr int k0 = K0.value, n = K1.value - K0.value;
        const int ib = opqv(slot * UNIT + opqv(w1lane));
        v4u fr[n];
#pragma unroll
        for (int ks = 0; ks < n; ++ks) fr[ks] = tr_frag(LD, ib + (k0 + ks) * 1024, 256);
        SB();
        const u32 one = 0x3f803f80u;
#pragma unroll
        for (int ks = 0; ks < n; ++ks) { c0 = dot2(fr[ks].x, one, c0); c1 = dot2(fr[ks].y, one, c1); c2 = dot2(fr[ks].z, one, c2); c3 = dot2(fr[ks].w, one, c3); }
        SB();
      };
      auto colfin = [&](int which) AI {
        const float cs = xsum((c0 + c1) + (c2 + c3));
        if (lane < 32) *(LAS float*)(LD + L_CS1 + which * 128 + lane * 4) = cs;
      };
      int jsig = -1;
      float csa[28];
      auto export_img = [&](int slot, int which) AI {
        const int ib = opqv(slot * UNIT + opqv(w1lane) + g * 1024);
        const int eo = opq(OFF_W1 + (j * 3 + which) * UNIT + g * 1024);
        st16(rs, l16, eo, tr_frag(LD, ib, 256));
        if (g < 12) st16(rs, l16, eo + 16384, tr_frag(LD, ib + 16384, 256));
      };
      if constexpr (L == 0) {
        if (hw == 1 && lane == 0) *(LAS v4u*)(LD + L_ARR) = v4u{0u, 0u, 0u, 0u};
        HSTAMP(2);
        VMCNT(4); BAR();
        HSTAMP(3);
        KP(4); VP(3);
        if (hw == 1) {
          PIN(ep);
          tag = ep + 1ull;
          if (b < NZ && lane == 0) __hip_atomic_store((unsigned long long*)(ws + INIT_OFF + b * 128), tag, __ATOMIC_RELAXED, __HIP_MEMORY_SCOPE_AGENT);
        }
        if (hw == 2) {
          const int wb = opqv(L_KVF + (4 * h + tq_) * 56 + tc8 * 8);
          const v4u f0 = tr_frag(LD, wb, 8 * 56);
          v4u f1 = tr_frag(LD, wb + 16 * 56, 8 * 56);
          if (h) { f1.z = 0u; f1.w = 0u; }
          *(LAS v4u*)(LD + L_WPHI + l16) = f0;
          *(LAS v4u*)(LD + L_WPHI + 1024 + l16) = f1;
        }
        if (hw == 0) colpart(4, IC<0>{}, IC<8>{});
        VMCNT(4); BAR();
        HSTAMP(4);
        KP(5); VP(4);
        const v4u* pa[7];
        if (job >= 0) retile_addr(A, job, lane, pa);
        if (hw == 1) colpart(0, IC<0>{}, IC<8>{});
        VMCNT(4); BAR();
        HSTAMP(5);
        KP(6); VP(5); VP(6);
        if (hw == 0) colpart(4, IC<8>{}, IC<16>{});
        if (hw == 1) colpart(0, IC<8>{}, IC<12>{});
        VMCNT(5); BAR();
        HSTAMP(6);
        if (hw == 0) colpart(4, IC<16>{}, IC<20>{});
        if (hw == 1) colpart(0, IC<12>{}, IC<16>{});
        VMCNT(3); BAR();
        HSTAMP(7);
        if (hw == 0) colpart(4, IC<20>{}, IC<24>{});
        if (hw == 1) colpart(0, IC<16>{}, IC<20>{});
        VMCNT(0); BAR();
        HSTAMP(8);
        if (hw == 0) { colpart(4, IC<24>{}, IC<28>{}); colfin(0); }
        if (hw == 1) { colpart(0, IC<20>{}, IC<28>{}); colfin(1); }
        if (hw == 2) export_img(4, 1);
        if (hw == 3) export_img(0, 2);
        HSTAMP(9);
        BAR();
        HSTAMP(10);
        DMAN(0, 1);
        if (job >= 0) {
#pragma unroll
          for (int i = 0; i < 7; ++i) jt[i] = *pa[i];
        }
        SB();
      } else {
        wait_arr(LD, 0, 4);
        DMA(w1f + 2 * UNIT, 0);
        DMA(w1f, 1);
        VMCNT(7); BAR();
      }
      {
        const int wb = opqv(L_WPHI + opqv(l16));
        const v4u wf0 = lds16(LD, wb), wf1 = lds16(LD, wb + 1024);
        HSTAMP(11);
        BAR();
        HSTAMP(12);
        __builtin_amdgcn_s_setprio(1);
        const int lz = opqv(lane);
        const int xk = opqv(L_W2X + hw * 1024 + lz * 16);
        const v4u kf0 = lds16(LD, xk), kf1 = lds16(LD, xk + 4096);
        const float rstd = *(LAS float*)(LD + L_KVF + hw * 256 + lz * 4);
        f32x16 p = {};
        p = mfma(wf0, kf0, p);
        p = mfma(wf1, kf1, p);
        v4u o0, o1;
        half_softmax<1>(p, h, rstd, o0, o1);
        const int pb = opqv(L_PHK + (hw * 32 + (lz & 31)) * PHK_STRIDE + 8 * (lz >> 5) + 64);
        *(LAS v2u*)(LD + pb) = v2u{o0.x, o0.y}; *(LAS v2u*)(LD + pb + 16) = v2u{o0.z, o0.w};
        *(LAS v2u*)(LD + pb + 32) = v2u{o1.x, o1.y}; *(LAS v2u*)(LD + pb + 48) = v2u{o1.z, o1.w};
        __builtin_amdgcn_s_setprio(0);
      }
      HSTAMP(13);
      if constexpr (L == 0) {
        if (hw == 1) {
          u32 spins = 0;
          while (!__all(__hip_atomic_load((unsigned long long*)(ws + INIT_OFF + min(lane, NZ - 1) * 128), __ATOMIC_RELAXED, __HIP_MEMORY_SCOPE_AGENT) == tag)) {
            __builtin_amdgcn_s_sleep(1);
            if (++spins > 400000u) break;
          }
        }
        if (job >= 0) { VMCNT(7); } else { VMCNT(0); }
      } else VMCNT(0);
      HSTAMP(14);
      BAR();
      if (hw < 2) {
        __builtin_amdgcn_s_setprio(2);
        f32x16 kp = {};
        const int lz = opqv(lane);
        const int g16 = (lz >> 4) & 1, q = (lz & 15) >> 2, p = lz & 3, hz = lz >> 5;
        const int ab0 = opqv(L_PHK + (8 * hz + q) * PHK_STRIDE + (g16 * 16 + 4 * p) * 2 + hw * 64);
        const int bb0 = opqv(L_V + (8 * hz + q) * V_STRIDE + (g16 * 16 + 4 * p) * 2);
        v4u ap[8], bv[8];
#pragma unroll
        for (int s = 0; s < 8; ++s) { ap[s] = tr_frag(LD, ab0 + 16 * s * PHK_STRIDE, 4 * PHK_STRIDE); bv[s] = tr_frag(LD, bb0 + 16 * s * V_STRIDE, 4 * V_STRIDE); }
        SB();
#pragma unroll
        for (int s = 0; s < 8; ++s) kp = mfma(ap[s], bv[s], kp);
        const int ko = opq(OFF_KVP + L * KVB + (j * 16 + g) * 4096 + hw * 2048);
        st16(rs, l16, ko, pack8(kp, 0));
        st16(rs, l16, ko + 1024, pack8(kp, 1));
        wave_signal(ws, C_KV + L * 16 + j, lane);
        __builtin_amdgcn_s_setprio(0);
      }
      if constexpr (L == 0) {
        HSTAMP(15);
        if (hw == 3) {
          colpart(1, IC<0>{}, IC<28>{}); colfin(2);
          arrive_lds(LD, 1, lane);
        }
        if (hw == 1) { export_img(1, 0); wave_signal(ws, C_W1 + j, lane); }
        if (job >= 0) jsig = retile_store(job, rs, LD, L_STAGE + hw * STG, lane, jt);
        HSTAMP(16);
        if (hw >= 2) { wave_signal(ws, jsig, lane); signal_cnt(ws, C_W1 + j); }
        HSTAMP(17);
        BAR();
        HSTAMP(18);
        kv_reduce(rs, LD, L, j, w, lane);
        if (hw == 0 && jsig >= 0) wave_signal(ws, jsig, lane);
        HSTAMP(19);
        BAR();
        HSTAMP(20);
        {
          u32* fw = cnt_ptr(ws, (lane & 1) ? C_W3 + j : C_W2 + (cb >> 1));
          const u32 bias = (lane & 1) ? 12u : 0u;
          u32 spins = 0;
          for (;;) {
            const u32 f = __hip_atomic_load(fw, __ATOMIC_RELAXED, __HIP_MEMORY_SCOPE_AGENT);
            if (__all(f >= 16u + bias) || !spin_more(ws, spins)) break;
          }
          __builtin_amdgcn_fence(__ATOMIC_ACQUIRE, "wavefront");
          asm volatile("" ::: "memory");
        }
        HSTAMP(21);
        DMA(w2, 2); DMA(w2 + UNIT, 3); dma2(w2 + 2 * UNIT, voff, lbase + L_W2X + hw * 1024);
#pragma unroll
        for (int ks = 0; ks < 28; ++ks) csa[ks] = __uint_as_float(ld4(rs, min(hw * 64 + lane, 223) * 4, OFF_CS3 + (j * 28 + ks) * 896));
        DMA(w3, 0);
        DMA(w3 + UNIT, 1);
        HSTAMP(22);
        VMCNT(42); HSTAMP(23); BAR();
        HSTAMP(24);
      } else {
        DMA(w2, 2); DMA(w2 + UNIT, 3); dma2(w2 + 2 * UNIT, voff, lbase + L_W2X + hw * 1024);
        DMA(w3, 0);
        BAR();
        kv_reduce(rs, LD, L, j, w, lane);
        BAR();
        DMA(w3 + UNIT, 1);
      }
      BAR();
      DMA(w3 + 2 * UNIT, 2);
      VMCNT(7); BAR();
      const int wo4 = opq(OFF_W4 + cb * 8 * UNIT + 4 * UNIT + hw * UNIT);
#define WR(lo, hi) _Pragma("unroll") for (int i = lo; i < hi; ++i) wr[i] = ld16(rs, l16, wo4 + i * 1024)
      if constexpr (L == 0) {
        float s0 = 0.f, s1 = 0.f, s2 = 0.f, s3 = 0.f;
#pragma unroll
        for (int ks = 0; ks < 28; ks += 4) { s0 += csa[ks]; s1 += csa[ks + 1]; s2 += csa[ks + 2]; s3 += csa[ks + 3]; }
        if (hw * 64 + lane < 224) *(LAS float*)(LD + L_CS3 + (hw * 64 + lane) * 4) = (s0 + s1) + (s2 + s3);
        wait_cnt(ws, C_W4 + (cb >> 1), 56);
        wait_cnt(ws, C_W1 + j, 48);
        WR(0, 28);
      }
      wait_arr(LD, 2, 4 * L + 4);
      DMA(w3 + 3 * UNIT, 3);
      VMCNT(7); BAR();
      DMA(w3 + 4 * UNIT, 0);
      VMCNT(7); BAR();
      const int hto = opq(OFF_H + L * HB + ((4 * g + hw) * 112 + 7 * j) * 1024);
      const int lvo = opq(C_H + (L * 64 + 4 * g + hw) * 8);
      const f32x2 mr = *(LAS f32x2*)(LD + L_W2X + hw * 512 + opqv(lane) * 8);
      const float nmean = mr[0], gs = -LOG2E * mr[1], irs = __builtin_amdgcn_rcpf(mr[1]);
      v4u ho4;
      auto glu = [&](int n, const char* src, auto SL) AI {
        constexpr int sl = SL.value;
        const int gb = opqv(L_SCR + ((n & 1) * 4 + hw) * 4096 + opqv(l16)), csb = opqv(L_CS3 + n * 128 + 16 * (opqv(lane) >> 5));
        f32x4 a[4], c[4];
#pragma unroll
        for (int qd = 0; qd < 4; ++qd) { a[qd] = *(LAS f32x4*)(LD + gb + qd * 1024); c[qd] = *(LAS f32x4*)(LD + csb + 32 * qd); }
        float t[8], y[8];
        static_for<7>([&](auto K) AI {
          constexpr int k = K.value;
          dma1<sl * UNIT + k * 4096, k * 4096>(src, voff, lb);
          static_for<8>([&](auto V) AI {
            constexpr int v = V.value, hf = v >> 2, i = v & 3;
            if constexpr (k == 0) t[v] = fmaf(nmean, c[2 * hf][i], a[2 * hf][i]);
            if constexpr (k == 1) t[v] *= gs;
            if constexpr (k == 2) t[v] = __builtin_amdgcn_exp2f(t[v]);
            if constexpr (k == 3) t[v] = fmaf(t[v], irs, irs);
            if constexpr (k == 4) t[v] = __builtin_amdgcn_rcpf(t[v]);
            if constexpr (k == 5) { y[v] = fmaf(nmean, c[2 * hf + 1][i], a[2 * hf + 1][i]); PIN(y[v]); }
            if constexpr (k == 6) t[v] *= y[v];
            if constexpr (k != 5) PIN(t[v]);
          });
        });
        ho4 = v4u{pk2(t[0], t[1]), pk2(t[2], t[3]), pk2(t[4], t[5]), pk2(t[6], t[7])};
        PIN(ho4);
      };
#define HST(n) st16(rs, l16, hto + (n) * 1024, ho4)
      glu(1, w3 + 5 * UNIT, IC<1>{}); HST(1);
      VMCNT(8); BAR();
      DMA(w3 + 6 * UNIT, 2);
      VMCNT(7); signal_cnt(ws, lvo);
      BAR();
      {
        u32* fp = cnt_ptr(ws, C_H + (L * 64 + 2 * rb + (lane & 1)) * 8);
        const int ho = opq(OFF_H + L * HB + 2 * rb * 112 * 1024 + hw * 7168);
        v4u hx[28];
        auto ldh = [&](auto I) AI { constexpr int i = I.value; hx[i] = ld16(rs, l16, ho + (i & 1) * 112 * 1024 + p4_koff(p4_oh(i >> 1))); };
        DMA(w4 + 3 * UNIT, 3);
        wait_flags(ws, fp, __hip_atomic_load(fp, __ATOMIC_RELAXED, __HIP_MEMORY_SCOPE_AGENT), 32u);
        VMCNT(0);
        static_for<7>([&](auto I) AI { ldh(I); });
        BAR();
        DMA(w4, 0);
        static_for<7>([&](auto I) AI { ldh(IC<7 + I.value>{}); });
        BAR();
        DMA(w4 + UNIT, 1);
        u32* fp2 = fp + 32 + 32 * ((lane >> 1) & 1);
        u32 f = __hip_atomic_load(fp2, __ATOMIC_RELAXED, __HIP_MEMORY_SCOPE_AGENT);
        f32x16 acc[4] = {};
        static_for<28>([&](auto I) AI {
          constexpr int i = I.value, t = i >> 1, tq = i & 1;
          if constexpr (i == 4) {
            wait_flags(ws, fp2, f);
            VMCNT(0); RAWBAR();
            DMA(w4 + 2 * UNIT, 2);
          }
          if constexpr (i == 14) { VMCNT(14); RAWBAR(); }
          acc[tq] = mfma(wr[2 * t], hx[i], acc[tq]);
          acc[2 + tq] = mfma(wr[2 * t + 1], hx[i], acc[2 + tq]);
          if constexpr (i >= 4 && i < 8) { ldh(IC<14 + 2 * (i - 4)>{}); ldh(IC<15 + 2 * (i - 4)>{}); }
          if constexpr (i == 7) f = __hip_atomic_load(fp + 96, __ATOMIC_RELAXED, __HIP_MEMORY_SCOPE_AGENT);
          if constexpr (i == 10) wait_flags(ws, fp + 96, f);
          if constexpr (i >= 8 && i < 14) ldh(IC<14 + i>{});
          SB();
        });
#pragma unroll
        for (int a = 0; a < 4; ++a) put16(acc[a], LD, opqv(p4_part(a, hw) + opqv(l16)));
        BAR();
        BAR();
      }
      if (L == 0) DMA(w1f + UNIT, 3);
    };
    hlayer(IC<0>{});
    hlayer(IC<1>{});
    if (b == 0 && hw == 1 && lane == 0) __hip_atomic_store((unsigned long long*)(ws + EPOCH_OFF), tag, __ATOMIC_RELAXED, __HIP_MEMORY_SCOPE_AGENT);
    return;
  }

  const int tt = 4 * g + w;
  const int tt2 = 2 * rb + (w & 1);
  const int kh = w >> 1;
  const rsrc_t rx0 = __builtin_amdgcn_make_buffer_rsrc((void*)A.x0, 0, XB, 0x00020000);
  v4u res[2];
  constexpr int XW = 14;

  auto clayer = [&](auto LC) AI {
    constexpr int L = LC.value;
    v4u x[28];
    v2u st[8];
    float mean, rstd;
    v4u pq[4];
    STAMP(0);
    auto phase1 = [&](auto FIRST) AI {
      constexpr bool first = FIRST.value;
      const int xo = opq(first ? tt * 32 * (D * 2) : OFF_X2 + tt * 28 * 1024);
      auto ldx = [&](int ks) AI { return ld16(rs, l16, xo + ks * 1024); };
      const int so_ = opq(OFF_ST2 + tt * 4096);
      const int sv_ = (opqv(lane) & 31) * 8 + h * 2048;
      v4u gx[28];
      const int stg = L_STAGE + w * STG;
      const int gv = (lane >> 4) * (D * 2) + (lane & 15) * 16, gv3 = (lane >> 3) * (D * 2) + (lane & 7) * 16;
      const int gl = stg + (lane >> 4) * 272 + (lane & 15) * 16, gl3 = stg + (lane >> 3) * 272 + (lane & 7) * 16;
      const int srd = opqv(stg + (opqv(lane) & 31) * 272 + h * 16);
      auto ldg = [&](int i) AI {
        return i < 24 ? __builtin_amdgcn_raw_buffer_load_b128(rx0, gv, xo + (i & 7) * 4 * (D * 2) + (i >> 3) * 256, 0)
                      : __builtin_amdgcn_raw_buffer_load_b128(rx0, gv3, xo + (i & 7) * 8 * (D * 2) + 768, 0);
      };
      auto stage = [&](int c) AI {
#pragma unroll
        for (int i = 0; i < (c < 3 ? 8 : 4); ++i) *(LAS v4u*)(LD + (c < 3 ? gl + i * 4 * 272 : gl3 + i * 8 * 272)) = gx[8 * c + i];
#pragma unroll
        for (int i = 0; i < (c < 3 ? 8 : 4); ++i) x[8 * c + i] = lds16(LD, srd + i * 32);
      };
      if constexpr (first) {
#pragma unroll
        for (int i = 0; i < 16; ++i) gx[i] = ldg(i);
        stage(0);
      } else {
#pragma unroll
        for (int ks = 0; ks < XW; ++ks) x[ks] = ldx(ks);
      }
      USTAMP(18);
      BAR();
      USTAMP(19);
      f32x16 ak = {}, av = {}, p = {};
      const int sbk = first ? opqv(opqv(w1lane) + 4 * UNIT) : opqv(opqv(l16) + 3 * UNIT), sbv = first ? opqv(w1lane) : opqv(l16);
      const int csb = opqv(L_CS1 + 16 * (opqv(lane) >> 5)), wb = opqv(L_WPHI + opqv(l16));
      const int xk = opqv(L_W2X + w * 1024 + opqv(l16));
      f32x4 cs[4]; v4u kf[2], wf[2];
      float nmean, s1 = 0.f, s2 = 0.f, q1 = 0.f, q2 = 0.f;
      auto isk = [](int i) constexpr { return i < LAG || (i < 56 - LAG && ((i - LAG) & 1) == 0); };
      auto kst = [](int i) constexpr { return i < LAG ? i : (i < 56 - LAG ? ((((i - LAG) & 1) == 0) ? LAG + (i - LAG) / 2 : (i - LAG) / 2) : i - 28); };
      auto rdw = [&](int base, int ks) AI { if constexpr (first) return tr_frag(LD, base + ks * 1024, 256); else return lds16(LD, base + ks * 1024); };
      pipe<56>([&](auto I) AI { return rdw(isk(I.value) ? sbk : sbv, kst(I.value)); },
               [&](auto I) AI {
                 constexpr int i = I.value;
                 if constexpr (first && (i == 6 || i == 14 || i == 22 || i == 30 || i == 38 || i == 46)) { USTAMP(40 + i / 8 * 2); RAWBAR(); USTAMP(41 + i / 8 * 2); }
                 if constexpr (i == 54) { USTAMP(20); BAR(); USTAMP(21); }
               },
               [&](auto I, v4u a) AI {
                 constexpr int i = I.value, ks = kst(i);
                 if constexpr (i == 54) { p = mfma(wf[0], kf[0], p); p = mfma(wf[1], kf[1], p); }
                 if constexpr (isk(i)) ak = mfma(a, x[ks], ak); else av = mfma(a, x[ks], av);
                 if constexpr (!first && isk(i) && ks + XW < 28) x[ks + XW] = ldx(ks + XW);
                 if constexpr (first) {
                   if constexpr (i < 12) gx[16 + i] = ldg(16 + i);
                   if constexpr (isk(i) && ks % 8 == 6) stage(ks / 8 + 1);
                   if constexpr (isk(i)) {
                     const u32 one = 0x3f803f80u;
                     s1 = dot2(x[ks].x, one, s1); q1 = dot2(x[ks].x, x[ks].x, q1); s2 = dot2(x[ks].y, one, s2); q2 = dot2(x[ks].y, x[ks].y, q2);
                     s1 = dot2(x[ks].z, one, s1); q1 = dot2(x[ks].z, x[ks].z, q1); s2 = dot2(x[ks].w, one, s2); q2 = dot2(x[ks].w, x[ks].w, q2);
                   }
                   if constexpr (i == 51) { ln_finish(s1 + s2, q1 + q2, mean, rstd); nmean = -mean; }
                 } else {
                   if constexpr (!isk(i) && ks >= 4 && ks < 12) st[ks - 4] = ld8(rs, sv_ + (ks - 4) * 256, so_);
                   if constexpr (i == 45) { ln_stats(st, mean, rstd); nmean = -mean; }
                 }
                 if constexpr (i >= 46 && i < 50) cs[i - 46] = *(LAS f32x4*)(LD + csb + 32 * (i - 46));
                 if constexpr (i == 50) wf[0] = lds16(LD, wb);
                 if constexpr (i == 51) wf[1] = lds16(LD, wb + 1024);
                 if constexpr (i == 52 || i == 53) {
#pragma unroll
                   for (int qd = 2 * (i - 52); qd < 2 * (i - 52) + 2; ++qd) {
                     const float y0 = fmaf(nmean, cs[qd][0], ak[4 * qd]), y1 = fmaf(nmean, cs[qd][1], ak[4 * qd + 1]);
                     const float y2 = fmaf(nmean, cs[qd][2], ak[4 * qd + 2]), y3 = fmaf(nmean, cs[qd][3], ak[4 * qd + 3]);
                     kf[qd >> 1][2 * (qd & 1)] = pk2(y0, y1);
                     kf[qd >> 1][2 * (qd & 1) + 1] = pk2(y2, y3);
                   }
                   *(LAS v4u*)(LD + xk + (i - 52) * 4096) = kf[i - 52];
                   if constexpr (i == 52) *(LAS float*)(LD + L_KVF + w * 256 + opqv(lane) * 4) = rstd;
                 }
                 if constexpr (i == 55) {
#pragma unroll
                   for (int qd = 0; qd < 4; ++qd) cs[qd] = *(LAS f32x4*)(LD + csb + 128 + 32 * qd);
                 }
               });
      STAMP(1);
      const int lz = opqv(lane);
      const int trow = w * 32 + (lz & 31);
      const int pb = opqv(L_PHK + trow * PHK_STRIDE + 8 * (lz >> 5)), vb = opqv(L_V + trow * V_STRIDE + 8 * (lz >> 5));
      half_softmax<0>(p, h, rstd, pq[0], pq[1]);
#pragma unroll
      for (int s = 0; s < 2; ++s) {
        *(LAS v2u*)(LD + pb + 32 * s) = v2u{pq[s].x, pq[s].y};
        *(LAS v2u*)(LD + pb + 32 * s + 16) = v2u{pq[s].z, pq[s].w};
      }
      SB();
#pragma unroll
      for (int r = 0; r < 16; ++r) av[r] = fmaf(nmean, cs[r >> 2][r & 3], av[r]) * rstd;
      if (h) { av[12] = 1.f; av[13] = 0.f; av[14] = 0.f; av[15] = 0.f; }
#pragma unroll
      for (int qd = 0; qd < 4; ++qd) *(LAS v2u*)(LD + vb + 16 * qd) = v2u{pk2(av[4 * qd], av[4 * qd + 1]), pk2(av[4 * qd + 2], av[4 * qd + 3])};
      STAMP(2);
      BAR();
      if constexpr (first) {
#pragma unroll
        for (int ro = 0; ro < 2; ++ro) {
          const int oi = min(kh * 4 + ro * 2 + h, 6);
          res[ro] = __builtin_amdgcn_raw_buffer_load_b128(rx0, tok * (D * 2) + (7 * cb + oi) * 16, tt2 * 32 * (D * 2), 0);
        }
      }
      f32x16 aq = {};
      const int sbq = first ? opqv(opqv(w1lane) + 1 * UNIT) : opqv(opqv(l16) + 1 * UNIT);
      pipe<28>([&](auto I) AI { return rdw(sbq, I.value); }, [&](auto) AI {},
               [&](auto I, v4u a) AI { aq = mfma(a, x[I.value], aq); });
      if constexpr (first) wait_arr(LD, 1, 1);
#pragma unroll
      for (int qd = 0; qd < 4; ++qd) cs[qd] = *(LAS f32x4*)(LD + csb + 256 + 32 * qd);
#pragma unroll
      for (int r = 0; r < 16; ++r) aq[r] = fmaf(nmean, cs[r >> 2][r & 3], aq[r]);
      f32x16 p2 = {};
      const int wb2 = opqv(L_WPHI + opqv(l16));
      p2 = mfma(lds16(LD, wb2), pack8(aq, 0), p2);
      p2 = mfma(lds16(LD, wb2 + 1024), pack8(aq, 1), p2);
      half_softmax<0>(p2, h, rstd, pq[0], pq[1]);
      half_softmax<1>(p2, h, rstd, pq[2], pq[3]);
    };
    phase1(std::integral_constant<bool, L == 0>{});
    STAMP(3);
    if (w == 0) wait_cnt(ws, C_KV + L * 16 + j, 32);
    BAR();
    STAMP(4);
    kv_reduce(rs, LD, L, j, w, lane);
    BAR();
    STAMP(5);
    {
      f32x16 nm = {};
      const int kb = opqv(L_KVF + opqv(l16));
      v4u kf[4];
#pragma unroll
      for (int s = 0; s < 4; ++s) kf[s] = lds16(LD, kb + s * 1024);
#pragma unroll
      for (int s = 0; s < 4; ++s) nm = mfma(kf[s], pq[s], nm);
      auto r = __builtin_amdgcn_permlane32_swap(__float_as_uint(nm[12]), __float_as_uint(nm[12]), false, false);
      const float den = __uint_as_float(r[1]) + 1e-6f;
      const float rd = __builtin_amdgcn_rcpf(den);
#pragma unroll
      for (int i = 0; i < 16; ++i) nm[i] *= rd;
      const int oo = opq(OFF_O + L * OB + (tt * 32 + 2 * j) * 1024);
      st16(rs, l16, oo, pack8(nm, 0));
      st16(rs, l16, oo + 1024, pack8(nm, 1));
    }
    wave_signal(ws, C_O + (L * 64 + tt) * 2 + (j >> 3), lane);
    STAMP(6);
    wait_cnt(ws, C_O + (L * 64 + tt2) * 2 + kh, 8, true);
    STAMP(7);
    {
      const int xo = opq(OFF_O + L * OB + (tt2 * 32 + 16 * kh) * 1024);
#pragma unroll
      for (int t = 0; t < XW; ++t) x[t] = ld16(rs, l16, xo + t * 1024);
      if (L == 0) RAWBAR();
      f32x16 a0 = {}, a1 = {};
      const int sb2 = opqv(2 * UNIT + kh * 2048 + opqv(l16)), sbx = opqv(L_W2X + kh * 2048 + opqv(l16));
      pipe<32>([&](auto I) AI { constexpr int i = I.value, t = i >> 1; return lds16(LD, (t < 14 ? sb2 + t * 4096 : sbx + (t - 14) * 4096) + (i & 1) * 1024); },
               [&](auto I) AI { if constexpr (I.value == 14) RAWBAR();   },
               [&](auto I, v4u a) AI {
                 constexpr int i = I.value;
                 if constexpr (i & 1) a1 = mfma(a, x[i >> 1], a1); else a0 = mfma(a, x[i >> 1], a0);
                 if constexpr (!(i & 1) && (i >> 1) + XW < 16) x[(i >> 1) + XW] = ld16(rs, l16, xo + ((i >> 1) + XW) * 1024);
               });
      const int xb = opqv(L_SCR + w * 4096 + opqv(l16)), xb2 = opqv(L_SCR + (w ^ 2) * 4096 + opqv(l16));
      f32x16 fin;
      if (kh) {
#pragma unroll
        for (int qd = 0; qd < 4; ++qd) *(LAS f32x4*)(LD + xb + qd * 1024) = f32x4{a0[4 * qd], a0[4 * qd + 1], a0[4 * qd + 2], a0[4 * qd + 3]};
        fin = a1;
      } else {
#pragma unroll
        for (int qd = 0; qd < 4; ++qd) *(LAS f32x4*)(LD + xb + qd * 1024) = f32x4{a1[4 * qd], a1[4 * qd + 1], a1[4 * qd + 2], a1[4 * qd + 3]};
        fin = a0;
      }
      BAR();
#pragma unroll
      for (int qd = 0; qd < 4; ++qd) {
        const f32x4 v = *(LAS f32x4*)(LD + xb2 + qd * 1024);
#pragma unroll
        for (int i = 0; i < 4; ++i) fin[4 * qd + i] += v[i];
      }
      finish_tile(fin, res, rs, opq(OFF_X1 + L * XB), opq(OFF_ST1 + L * STB), tt2, cb, kh, lane, false, A.out);
    }
    wave_signal(ws, C_X1 + L * 64 + tt2, lane);
    STAMP(8);
    wait_cnt(ws, C_X1 + L * 64 + tt, 16, true);
    STAMP(9);
    {
      const int xo = opq(OFF_X1 + L * XB + tt * 28 * 1024);
      const int so_ = opq(OFF_ST1 + L * STB + tt * 4096);
      const int sv_ = (opqv(lane) & 31) * 8 + h * 2048;
#pragma unroll
      for (int ks = 0; ks < XW; ++ks) x[ks] = ld16(rs, l16, xo + ks * 1024);
      arrive_lds(LD, 2, lane);
      const int ho = opq(OFF_H + L * HB + (tt * 112 + 7 * j) * 1024);
      const int lvo = opq(C_H + (L * 64 + tt) * 8);
      u32* fp = cnt_ptr(ws, C_H + (L * 64 + tt2) * 8);
      u32* fp01 = fp + 32 * (lane & 1);
      u32 f;
      float nmean, gs, irs;
      const int csb = opqv(L_CS3 + 16 * (opqv(lane) >> 5));
      const int sbA = opqv(l16), sbB = opqv(opqv(l16) + 2 * UNIT);
      const int gb = opqv(L_SCR + 16384 + w * 4096 + opqv(l16));
      f32x4 Gcg[2], Gcu[2]; float Gt[8], Gyu[8]; u32 Go[4];
      auto glu = [&](const f32x16& a, auto N_, auto S_) AI {
        constexpr int n = N_.value, s = S_.value;
        if constexpr (s == 0) Gcu[0] = *(LAS f32x4*)(LD + csb + n * 128 + 32);
        if constexpr (s == 5) Gcg[1] = *(LAS f32x4*)(LD + csb + n * 128 + 64);
        if constexpr (s == 10) Gcu[1] = *(LAS f32x4*)(LD + csb + n * 128 + 96);
        if constexpr (s == 23 && n < 6) Gcg[0] = *(LAS f32x4*)(LD + csb + (n == 0 ? 2 : n + 1) * 128);
        static_for<8>([&](auto V_) AI {
          constexpr int v = V_.value, k = s - (2 + (5 * v) / 2), hf = v >> 2, i = v & 3;
          if constexpr (k == 0) { Gt[v] = fmaf(nmean, Gcg[hf][i], a[8 * hf + i]); PIN(Gt[v]); }
          if constexpr (k == 1) { Gt[v] = Gt[v] * gs; PIN(Gt[v]); }
          if constexpr (k == 2) { Gt[v] = __builtin_amdgcn_exp2f(Gt[v]); PIN(Gt[v]); }
          if constexpr (k == 3) { Gt[v] = fmaf(Gt[v], irs, irs); PIN(Gt[v]); }
          if constexpr (k == 4) { Gt[v] = __builtin_amdgcn_rcpf(Gt[v]); PIN(Gt[v]); }
          if constexpr (k == 5) { Gyu[v] = fmaf(nmean, Gcu[hf][i], a[8 * hf + 4 + i]); PIN(Gyu[v]); }
          if constexpr (k == 6) { Gt[v] = Gt[v] * Gyu[v]; PIN(Gt[v]); }
          if constexpr (k == 7 && (v & 1)) { Go[v >> 1] = pk2(Gt[v - 1], Gt[v]); PIN(Go[v >> 1]); }
        });
        if constexpr (s == 27) st16(rs, l16, ho + n * 1024, v4u{Go[0], Go[1], Go[2], Go[3]});
      };
      const int ho2 = opq(OFF_H + L * HB + tt2 * 112 * 1024 + kh * 7168);
      v4u hx[28];
      auto ldh = [&](auto T) AI { hx[T.value] = ld16(rs, l16, ho2 + p4_koff(p4_oc(T.value))); };
      f32x16 acc[7];
      pipe<196>(
          [&](auto I) AI { constexpr int i = I.value, u = i / 28, sl = u & 3; return lds16(LD, (sl < 2 ? sbA : sbB) + (sl & 1) * UNIT + (i % 28) * 1024); },
          [&](auto I) AI {
            if constexpr (I.value > 28 && I.value % 28 == 6) {
              VSTAMP(30 + I.value / 28);
              if constexpr (I.value == 62) asm volatile("s_waitcnt lgkmcnt(1)" ::: "memory");
              RAWBAR();
            }
          },
          [&](auto I, v4u a) AI {
            constexpr int i = I.value, u = i / 28, s = i % 28;
            if constexpr (u < 2) {
              constexpr int n = i & 1, ks = i >> 1;
              if constexpr (ks == 0) { f32x16 z = {}; acc[n] = mfma(a, x[ks], z); } else acc[n] = mfma(a, x[ks], acc[n]);
              if constexpr (n == 0 && ks + XW < 28) x[ks + XW] = ld16(rs, l16, xo + (ks + XW) * 1024);
              if constexpr (n == 1 && ks >= 8 && ks < 16) st[ks - 8] = ld8(rs, sv_ + (ks - 8) * 256, so_);
              if constexpr (i == 50) Gcg[0] = *(LAS f32x4*)(LD + csb);
              if constexpr (i == 53) {
                ln_stats(st, mean, rstd);
                nmean = -mean; gs = -LOG2E * rstd; irs = __builtin_amdgcn_rcpf(rstd);
                *(LAS f32x2*)(LD + L_W2X + w * 512 + opqv(lane) * 8) = f32x2{nmean, rstd};
              }
            } else {
              if constexpr (s == 0) { f32x16 z = {}; acc[u] = mfma(a, x[s], z); } else acc[u] = mfma(a, x[s], acc[u]);
              if constexpr (u == 2 && s >= 1 && s <= 4) {
                constexpr int qd = s - 1;
                *(LAS f32x4*)(LD + gb + qd * 1024) = f32x4{acc[1][4 * qd], acc[1][4 * qd + 1], acc[1][4 * qd + 2], acc[1][4 * qd + 3]};
              }
              glu(acc[u == 2 ? 0 : u - 1], IC<(u == 2 ? 0 : u - 1)>{}, IC<s>{});
              if constexpr (i == 104) { VMCNT(0); signal_cnt(ws, lvo); }
              if constexpr (i == 132) { VMCNT(0); signal_cnt(ws, lvo + 1); }
              if constexpr (i == 148) f = __hip_atomic_load(fp01, __ATOMIC_RELAXED, __HIP_MEMORY_SCOPE_AGENT);
              if constexpr (i == 162) {
                wait_flags(ws, fp01, f, (lane & 1) ? 16u : 32u);
                VMCNT(0); signal_cnt(ws, lvo + 2);
              }
              if constexpr (i >= 174 && i < 190 && !(i & 1)) ldh(IC<(i - 174) / 2>{});
              if constexpr (i == 189) { VMCNT(8); signal_cnt(ws, lvo + 3); }
            }
          });
      STAMP(10);
      static_for<28>([&](auto S_) AI { glu(acc[6], IC<6>{}, S_); });
      asm volatile("global_load_dword %0, %1, off sc1" : "=v"(f) : "v"(fp + 96) : "memory");
      RAWBAR();
      STAMP(11);
      const int wo_ = opqv(kh * 3072 + opqv(l16)), wp_ = opqv(kh * 1024 + 1024 + opqv(l16));
      const int wo2 = opqv(wo_ + 2 * UNIT), wp2 = opqv(wp_ + 2 * UNIT);
      v4u wq[56];
      auto ldw = [&](auto K) AI {
        constexpr int k = K.value, t = k >> 1;
        wq[k] = lds16(LD, ((k & 1) ? (t < 14 ? wp_ : wp2) : (t < 14 ? wo_ : wo2)) + (t % 14) * 4096);
      };
      static_for<4>([&](auto K) AI { ldw(K); });
      f32x16 fin = {}, oth = {};
      static_for<28>([&](auto T) AI {
        constexpr int t = T.value;
        if constexpr (t == 1) { VMCNT(2); signal_cnt(ws, lvo + 4); }
        if constexpr (t == 3) {
          VMCNT(1); signal_cnt(ws, lvo + 5);
          { u32 f2 = f; PIN(f2); wait_flags(ws, fp + 96, f2); }
          static_for<4>([&](auto K) AI { ldh(IC<8 + K.value>{}); });
        }
        if constexpr (t == 8) { RAWBAR(); f = __hip_atomic_load(fp + 128, __ATOMIC_RELAXED, __HIP_MEMORY_SCOPE_AGENT); }
        if constexpr (t == 12) {
          STAMP(12);
          wait_flags(ws, fp + 128, f);
          f = __hip_atomic_load(fp + 160, __ATOMIC_RELAXED, __HIP_MEMORY_SCOPE_AGENT);
          static_for<8>([&](auto K) AI { ldh(IC<12 + K.value>{}); });
          RAWBAR();
          STAMP(13);
#pragma unroll
          for (int c = 0; c < 4; ++c) add16(fin, LD, opqv(p4_part(w, c) + opqv(l16)));
          PIN(fin);
          SB();
          wait_flags(ws, fp + 160, f);
          static_for<8>([&](auto K) AI { ldh(IC<20 + K.value>{}); });
          STAMP(22);
        }
        if constexpr (t + 2 < 28) { ldw(IC<2 * t + 4>{}); ldw(IC<2 * t + 5>{}); }
        oth = mfma(wq[2 * t + 1], hx[t], oth);
        fin = mfma(wq[2 * t], hx[t], fin);
        SB();
      });
      STAMP(14);
      put16(oth, LD, opqv(UNIT + w * 4096 + opqv(l16)));
      BAR();
      add16(fin, LD, opqv(UNIT + (w ^ 2) * 4096 + opqv(l16)));
      STAMP(15);
      finish_tile(fin, res, rs, OFF_X2, OFF_ST2, tt2, cb, kh, lane, L == 1, A.out);
    }
    if (L == 0) {
      wave_signal(ws, C_X2 + tt2, lane);
      arrive_lds(LD, 0, lane);
      STAMP(16);
      wait_cnt(ws, C_X2 + tt, 16, true);
    }
    STAMP(17);
  };
  clayer(IC<0>{});
  clayer(IC<1>{});
}

extern "C" void kernel_launch(void* const* d_in, const int* in_sizes, int n_in, void* d_out, int out_size, void* d_ws,
                              size_t ws_size, hipStream_t stream) {
  Args a;
  a.x0 = (const u16*)d_in[0]; a.wqkv = (const u16*)d_in[1]; a.wphi = (const u16*)d_in[2]; a.wo = (const u16*)d_in[3];
  a.wg = (const u16*)d_in[5]; a.wu = (const u16*)d_in[6]; a.wd = (const u16*)d_in[7];
  a.ws = (char*)d_ws; a.out = (u16*)d_out;
  (void)hipFuncSetAttribute((const void*)k_main, hipFuncAttributeMaxDynamicSharedMemorySize, LDS_BYTES);
  k_main<<<256, 512, LDS_BYTES, stream>>>(a);
}
```

```cpp
#include <hip/hip_runtime.h>
#include <stdint.h>
#include <utility>
#include <type_traits>

typedef unsigned short u16;
typedef unsigned int u32;
typedef u32 v4u __attribute__((ext_vector_type(4)));
typedef u32 v2u __attribute__((ext_vector_type(2)));
typedef float f32x2 __attribute__((ext_vector_type(2)));
typedef float f32x4 __attribute__((ext_vector_type(4)));
typedef float f32x16 __attribute__((ext_vector_type(16)));
typedef __bf16 bf16x2 __attribute__((ext_vector_type(2)));
typedef __bf16 bf16x8 __attribute__((ext_vector_type(8)));
typedef short s16x4 __attribute__((ext_vector_type(4)));
#define LAS __attribute__((address_space(3)))
#define DI __device__ __forceinline__
#define AI __attribute__((always_inline))
template <int V> using IC = std::integral_constant<int, V>;

constexpr int S = 2048, D = 448, FF = 1792;
constexpr int UNIT = 28672;

constexpr int C_KV = 0;
constexpr int C_W2 = 32;
constexpr int C_W4 = 36;
constexpr int C_W3 = 40;
constexpr int C_ERR = 60;
constexpr int C_X2 = 64;
constexpr int C_X1 = 128;
constexpr int C_O = 256;
constexpr int C_H = 512;
constexpr int C_W1 = 1536;
constexpr int NZ = 25;
constexpr int EPOCH_OFF = NZ * 64 * 128;
constexpr int INIT_OFF = EPOCH_OFF + 256;
constexpr int CTL_BYTES = EPOCH_OFF + 4096;
constexpr int W2B = 65536;
constexpr int OFF_W1 = CTL_BYTES;
constexpr int OFF_W2 = OFF_W1 + 16 * 3 * UNIT;
constexpr int OFF_W3 = OFF_W2 + 8 * W2B;
constexpr int OFF_W4 = OFF_W3 + 16 * 7 * UNIT;
constexpr int OFF_CS3 = OFF_W4 + 8 * 8 * UNIT;
constexpr int XB = S * D * 2;
constexpr int OB = S * 512 * 2;
constexpr int OFF_X2 = OFF_CS3 + 16 * 28 * 896;
constexpr int OFF_X1 = OFF_X2 + XB;
constexpr int OFF_O = OFF_X1 + 2 * XB;
constexpr int STB = S * 16 * 8;
constexpr int OFF_ST2 = OFF_O + 2 * OB;
constexpr int OFF_ST1 = OFF_ST2 + STB;
constexpr int KVB = 16 * 16 * 4096;
constexpr int OFF_KVP = OFF_ST1 + 2 * STB;
constexpr int HB = S * FF * 2;
constexpr int OFF_H = OFF_KVP + 2 * KVB;
constexpr int WS_USED = OFF_H + 2 * HB;

constexpr int L_SCR = 4 * UNIT;
constexpr int PHK_STRIDE = 136, V_STRIDE = 72;
constexpr int L_PHK = L_SCR;
constexpr int L_V = L_SCR + 128 * PHK_STRIDE;
constexpr int L_KVF = L_SCR + 28672;
constexpr int L_W2X = L_KVF + 4096;
constexpr int L_CONST = 155648;
constexpr int L_CS1 = L_CONST, L_CS3 = L_CONST + 384, L_WPHI = L_CONST + 1280, L_ARR = L_CONST + 3328;
constexpr int L_STAGE = 2 * UNIT;
constexpr int STG = 8704;
constexpr int LDS_BYTES = 163840;

constexpr float LOG2E = 1.4426950408889634f;
#define PKV 12
constexpr int PK = PKV, MK = 28 - PK;
constexpr int LAG = 4;

DI u32 pk2(float a, float b) { f32x2 v = {a, b}; return __builtin_bit_cast(u32, __builtin_convertvector(v, bf16x2)); }
DI float bflo(u32 v) { return __uint_as_float(v << 16); }
DI float bfhi(u32 v) { return __uint_as_float(v & 0xffff0000u); }
DI f32x16 mfma(v4u a, v4u b, f32x16 c) {
  f32x16 d = __builtin_amdgcn_mfma_f32_32x32x16_bf16(__builtin_bit_cast(bf16x8, a), __builtin_bit_cast(bf16x8, b), c, 0, 0, 0);
  asm volatile("" : "+v"(d));
  __builtin_amdgcn_sched_barrier(0);
  return d;
}
DI float xsum(float v) {
  auto r = __builtin_amdgcn_permlane32_swap(__float_as_uint(v), __float_as_uint(v), false, false);
  return __uint_as_float(r[0]) + __uint_as_float(r[1]);
}
DI float xmax(float v) {
  auto r = __builtin_amdgcn_permlane32_swap(__float_as_uint(v), __float_as_uint(v), false, false);
  return fmaxf(__uint_as_float(r[0]), __uint_as_float(r[1]));
}
DI float xmin(float v) {
  auto r = __builtin_amdgcn_permlane32_swap(__float_as_uint(v), __float_as_uint(v), false, false);
  return fminf(__uint_as_float(r[0]), __uint_as_float(r[1]));
}
#define BAR() asm volatile("s_waitcnt lgkmcnt(0)\n\ts_barrier" ::: "memory")
#define RAWBAR() asm volatile("s_barrier" ::: "memory")
#define VMCNT(n) asm volatile("s_waitcnt vmcnt(" #n ")" ::: "memory")
#define SB() __builtin_amdgcn_sched_barrier(0)
#define PIN(x) asm volatile("" : "+v"(x))

typedef __amdgpu_buffer_rsrc_t rsrc_t;
DI v4u ld16(rsrc_t rs, int voff, int soff) { return __builtin_amdgcn_raw_buffer_load_b128(rs, voff, soff, 16); }
DI v2u ld8(rsrc_t rs, int voff, int soff) { return __builtin_amdgcn_raw_buffer_load_b64(rs, voff, soff, 16); }
DI u32 ld4(rsrc_t rs, int voff, int soff) { return __builtin_amdgcn_raw_buffer_load_b32(rs, voff, soff, 16); }
DI void st16(rsrc_t rs, int voff, int soff, v4u v) {
  __builtin_amdgcn_raw_buffer_store_b128(v, rs, voff, soff, 16);
  asm volatile("s_nop 1" ::"v"(v) : "memory");
}
DI void st8(rsrc_t rs, int voff, int soff, v2u v) { __builtin_amdgcn_raw_buffer_store_b64(v, rs, voff, soff, 16); }
DI void st4(rsrc_t rs, int voff, int soff, u32 v) { __builtin_amdgcn_raw_buffer_store_b32(v, rs, voff, soff, 16); }
DI int opq(int v) { asm volatile("" : "+s"(v)); return v; }
DI int opqv(int v) { asm volatile("" : "+v"(v)); return v; }
DI v4u lds16(LAS char* l, int off) { return *(LAS v4u*)(l + off); }
DI float dot2(u32 a, u32 b, float c) {
  return __builtin_amdgcn_fdot2_f32_bf16(__builtin_bit_cast(bf16x2, a), __builtin_bit_cast(bf16x2, b), c, false);
}
DI v4u tr_frag(LAS char* L, int base, int step) {
  v2u lo = __builtin_bit_cast(v2u, __builtin_amdgcn_ds_read_tr16_b64_v4i16((LAS s16x4*)(L + base)));
  v2u hi = __builtin_bit_cast(v2u, __builtin_amdgcn_ds_read_tr16_b64_v4i16((LAS s16x4*)(L + base + step)));
  return v4u{lo.x, lo.y, hi.x, hi.y};
}
DI float sum8(v4u o) {
  const u32 one = 0x3f803f80u;
  return dot2(o.x, one, dot2(o.y, one, 0.f)) + dot2(o.z, one, dot2(o.w, one, 0.f));
}

DI u32* cnt_ptr(char* ws, int idx) { return (u32*)(ws + idx * 128); }
#define POLL_D0 16
#define LATE_ARR
#define WOV 0
#define WOS 20
#define H1 0
#define H3 6
#define H4 10
#define PT5 8
#define POLL_IV 1
DI bool spin_more(char* ws, u32& spins) {
  u32* e = cnt_ptr(ws, C_ERR);
  __builtin_amdgcn_s_sleep(POLL_IV);
  ++spins;
  if ((spins & 255u) == 0u) {
    if (__builtin_amdgcn_readfirstlane(__hip_atomic_load(e, __ATOMIC_RELAXED, __HIP_MEMORY_SCOPE_AGENT)) != 0) return false;
    if (spins > 400000u) { __hip_atomic_store(e, 1u, __ATOMIC_RELAXED, __HIP_MEMORY_SCOPE_AGENT); return false; }
  }
  return true;
}
DI void wait_cnt(char* ws, int idx, u32 target, bool delay = false) {
  u32* c = cnt_ptr(ws, idx);
  u32 spins = 0;
  if (delay && POLL_D0) __builtin_amdgcn_s_sleep(POLL_D0);
  while ((u32)__builtin_amdgcn_readfirstlane(__hip_atomic_load(c, __ATOMIC_RELAXED, __HIP_MEMORY_SCOPE_AGENT)) < target)
    if (!spin_more(ws, spins)) break;
  __builtin_amdgcn_fence(__ATOMIC_ACQUIRE, "wavefront");
  asm volatile("" ::: "memory");
}
DI void wait_flags(char* ws, u32* fp, u32 f, u32 target = 16u) {
  if (__builtin_expect(!__all(f >= target), 0)) {
    u32 spins = 0;
    while (spin_more(ws, spins))
      if (__all(__hip_atomic_load(fp, __ATOMIC_RELAXED, __HIP_MEMORY_SCOPE_AGENT) >= target)) break;
  }
  __builtin_amdgcn_fence(__ATOMIC_ACQUIRE, "wavefront");
  asm volatile("" ::: "memory");
}
DI u32 poll_issue(const u32* p) { u32 f; asm volatile("global_load_dword %0, %1, off sc1" : "=v"(f) : "v"(p) : "memory"); return f; }
DI void signal_cnt(char* ws, int idx) {
  unsigned long long sv;
  asm volatile("s_mov_b64 %0, exec\n\ts_mov_b64 exec, 1\n\tglobal_atomic_add %1, %2, %3\n\ts_mov_b64 exec, %0"
               : "=&s"(sv) : "v"(idx * 128), "v"(1), "s"(ws) : "memory");
}
DI void wave_signal(char* ws, int idx, int lane) { VMCNT(0); signal_cnt(ws, idx); }
DI void arrive_lds(LAS char* LD, int slot, int lane) {
  if (lane == 0) __hip_atomic_fetch_add((LAS u32*)(LD + L_ARR + slot * 4), 1u, __ATOMIC_RELAXED, __HIP_MEMORY_SCOPE_WORKGROUP);
}
DI void wait_arr(LAS char* LD, int slot, u32 target) {
  while ((u32)__builtin_amdgcn_readfirstlane(__hip_atomic_load((LAS u32*)(LD + L_ARR + slot * 4), __ATOMIC_RELAXED, __HIP_MEMORY_SCOPE_WORKGROUP)) < target)
    __builtin_amdgcn_s_sleep(1);
}

#define STAMP(i)
#define HSTAMP(i)
#define VSTAMP(i)
#define WSTAMP(i)
#define USTAMP(i)

#define DMA_STEP(GSTEP) \
      "s_add_u32 m0, m0, 0x1000\n\t" \
      "v_add_u32 %1, " GSTEP ", %1\n\t" \
      "global_load_lds_dwordx4 %1, %2 sc1\n\t"
#define DMA_FN(NAME, BODY) \
DI void NAME(const char* gsrc, u32 voff, u32 lds_dst) { \
  u32 keep; \
  asm volatile("s_mov_b32 %0, m0\n\ts_mov_b32 m0, %3\n\ts_nop 0\n\tglobal_load_lds_dwordx4 %1, %2 sc1\n\t" BODY "s_mov_b32 m0, %0" \
               : "=&s"(keep), "+v"(voff) : "s"(gsrc), "s"(lds_dst) : "memory", "scc"); }
DMA_FN(dma7, DMA_STEP("0x1000") DMA_STEP("0x1000") DMA_STEP("0x1000") DMA_STEP("0x1000") DMA_STEP("0x1000") DMA_STEP("0x1000"))
DMA_FN(dma2, DMA_STEP("0x1000"))
#define PACE 1
#define STR2(x) #x
#define STR(x) STR2(x)
#define PSTEP "s_sleep " STR(PACE) "\n\t" DMA_STEP("0x1000")
DMA_FN(dma7p, PSTEP PSTEP PSTEP PSTEP PSTEP PSTEP)
DMA_FN(dma7n, DMA_STEP("0x2a000") DMA_STEP("0x2a000") DMA_STEP("0x2a000") DMA_STEP("0x2a000") DMA_STEP("0x2a000") DMA_STEP("0x2a000"))

template <int LO, int GO>
DI void dma1(const char* gsrc, u32 voff, u32 lb) {
  u32 keep, tmp;
  asm volatile("s_mov_b32 %0, m0\n\ts_add_u32 m0, %4, %5\n\tv_add_u32 %1, %6, %2\n\tglobal_load_lds_dwordx4 %1, %3 sc1\n\ts_mov_b32 m0, %0"
               : "=&s"(keep), "=&v"(tmp) : "v"(voff), "s"(gsrc), "s"(lb), "i"(LO), "i"(GO) : "memory", "scc");
}

template <typename F, int... I>
DI void static_for_impl(F&& f, std::integer_sequence<int, I...>) { (f(IC<I>{}), ...); }
template <int N, typename F>
DI void static_for(F&& f) { static_for_impl(f, std::make_integer_sequence<int, N>{}); }

constexpr int PF = 6;
#define WEV 2
template <int N, int RPS = 1, typename RD, typename PRE, typename ST>
DI void pipe(RD rd, PRE pre, ST step) {
  v4u buf[PF];
  static_for<PF>([&](auto I) AI { buf[I.value] = rd(I); });
  SB();
  static_for<N>([&](auto I) AI {
    constexpr int i = I.value;
    pre(I);
    if constexpr (WEV > 1 && i % WEV == 0 && i + PF <= N) __builtin_amdgcn_s_waitcnt(0xC07F | (((PF - WEV) * RPS) << 8));
    const v4u a = buf[i % PF];
    step(I, a, [&]() AI { if constexpr (i + PF < N) buf[i % PF] = rd(IC<i + PF>{}); SB(); });
    SB();
  });
}

DI void ln_finish(float s, float q, float& mean, float& rstd) {
  s = xsum(s); q = xsum(q);
  mean = s * (1.f / 448.f);
  const float var = fmaxf(q * (1.f / 448.f) - mean * mean, 0.f);
  rstd = __builtin_amdgcn_rsqf(var + 1e-5f);
}
DI void ln_stats(const v2u (&st)[8], float& mean, float& rstd) {
  f32x2 a = __builtin_bit_cast(f32x2, st[0]), b = __builtin_bit_cast(f32x2, st[1]);
#pragma unroll
  for (int i = 2; i < 8; i += 2) { a += __builtin_bit_cast(f32x2, st[i]); b += __builtin_bit_cast(f32x2, st[i + 1]); }
  a += b;
  ln_finish(a[0], a[1], mean, rstd);
}
DI v4u pack8(const f32x16& a, int s) {
  v4u r = {pk2(a[8 * s], a[8 * s + 1]), pk2(a[8 * s + 2], a[8 * s + 3]), pk2(a[8 * s + 4], a[8 * s + 5]), pk2(a[8 * s + 6], a[8 * s + 7])};
  return r;
}
template <int SG>
DI void half_softmax(const f32x16& p, int h, float rstd, v4u& o0, v4u& o1) {
  auto mm = [](float a, float b) AI { return SG ? fminf(a, b) : fmaxf(a, b); };
  float m0 = mm(mm(p[0], p[1]), p[2]), m1 = mm(mm(p[3], p[4]), p[5]), m2 = mm(mm(p[6], p[7]), p[8]), m3 = mm(mm(p[9], p[10]), p[11]);
  float m4 = mm(mm(mm(p[12], p[13]), p[14]), p[15]);
  m4 = h ? p[0] : m4;
  float mx = mm(mm(mm(m0, m1), m2), mm(m3, m4));
  mx = SG ? xmin(mx) : xmax(mx);
  const float c = rstd * LOG2E;
  const float cc = SG ? -c : c, off = -mx * cc;
  f32x16 e;
  float s0 = 0.f, s1 = 0.f;
#pragma unroll
  for (int r = 0; r < 16; ++r) {
    float v = __builtin_amdgcn_exp2f(fmaf(p[r], cc, off));
    if (r >= 12) v = h ? 0.f : v;
    e[r] = v;
    if (r & 1) s1 += v; else s0 += v;
  }
  const float rs_ = __builtin_amdgcn_rcpf(xsum(s0 + s1));
#pragma unroll
  for (int r = 0; r < 16; ++r) e[r] *= rs_;
  o0 = pack8(e, 0); o1 = pack8(e, 1);
  PIN(o0); PIN(o1);
  SB();
}

DI void finish_tile(const f32x16& acc, v4u (&res)[2], rsrc_t rs, int xoff, int stoff, int tt, int cb, int nt, int lane, bool last, u16* out) {
  lane = opqv(lane);
  const int h = lane >> 5, tok = lane & 31;
#pragma unroll
  for (int ro = 0; ro < 2; ++ro) {
    v4u r = res[ro];
    v4u o;
    o.x = pk2(bflo(r.x) + acc[8 * ro + 0], bfhi(r.x) + acc[8 * ro + 1]);
    o.y = pk2(bflo(r.y) + acc[8 * ro + 2], bfhi(r.y) + acc[8 * ro + 3]);
    o.z = pk2(bflo(r.z) + acc[8 * ro + 4], bfhi(r.z) + acc[8 * ro + 5]);
    o.w = pk2(bflo(r.w) + acc[8 * ro + 6], bfhi(r.w) + acc[8 * ro + 7]);
    res[ro] = o;
    const int oi = nt * 4 + ro * 2 + h;
    if (oi < 7) {
      const int go = 7 * cb + oi;
      if (last) *(v4u*)(out + (size_t)(tt * 32 + tok) * D + go * 8) = o;
      else st16(rs, ((go >> 1) * 64 + tok + 32 * (go & 1)) * 16, xoff + tt * 28 * 1024, o);
    }
  }
  if (!last) {
    const bool v1 = !(nt == 1 && h == 1);
    const u32 one = 0x3f803f80u;
    float s0 = dot2(res[0].x, one, 0.f), s1 = dot2(res[1].x, one, 0.f), q0 = dot2(res[0].x, res[0].x, 0.f), q1 = dot2(res[1].x, res[1].x, 0.f);
    s0 = dot2(res[0].y, one, s0); s1 = dot2(res[1].y, one, s1); q0 = dot2(res[0].y, res[0].y, q0); q1 = dot2(res[1].y, res[1].y, q1);
    s0 = dot2(res[0].z, one, s0); s1 = dot2(res[1].z, one, s1); q0 = dot2(res[0].z, res[0].z, q0); q1 = dot2(res[1].z, res[1].z, q1);
    s0 = dot2(res[0].w, one, s0); s1 = dot2(res[1].w, one, s1); q0 = dot2(res[0].w, res[0].w, q0); q1 = dot2(res[1].w, res[1].w, q1);
    const float sum = xsum(s0 + (v1 ? s1 : 0.f));
    const float sq = xsum(q0 + (v1 ? q1 : 0.f));
    if (h == 0) st8(rs, tok * 8, stoff + ((tt * 8 + cb) * 2 + nt) * 256, v2u{__float_as_uint(sum), __float_as_uint(sq)});
  }
}

DI void kv_reduce(rsrc_t rs, LAS char* LD, int L, int j, int w, int lane) {
  const int ko = opq(OFF_KVP + L * KVB + j * 16 * 4096 + (w >> 1) * 1024 + (w & 1) * 512);
  const int lz = opqv(lane);
  const int vo = (lz >> 5) * 4096 + (lz & 31) * 16;
  v4u t[8];
#pragma unroll
  for (int i = 0; i < 8; ++i) t[i] = ld16(rs, vo, ko + i * 8192);
  float s[8] = {};
  const u32 lo1 = (u32)opq(0x00003f80), hi1 = (u32)opq(0x3f800000);
#pragma unroll
  for (int i = 0; i < 8; ++i) {
#pragma unroll
    for (int c = 0; c < 4; ++c) { s[2 * c] = dot2(t[i][c], lo1, s[2 * c]); s[2 * c + 1] = dot2(t[i][c], hi1, s[2 * c + 1]); }
  }
#pragma unroll
  for (int c = 0; c < 8; ++c) s[c] = xsum(s[c]);
  if (lz < 32) *(LAS v4u*)(LD + L_KVF + (w >> 1) * 1024 + (w & 1) * 512 + lz * 16) = v4u{pk2(s[0], s[1]), pk2(s[2], s[3]), pk2(s[4], s[5]), pk2(s[6], s[7])};
}

constexpr int p4_oh(int t) { return t < 7 ? 4 * t : 24 + 4 * t; }
constexpr int p4_oc(int t) { return t < 12 ? 28 + 2 * t : 56 + 2 * t; }
constexpr int p4_koff(int o) { return (7 * (o & 15) + (o >> 4)) * 1024; }
constexpr int p4_wbase(int t) { return (t < 7 ? 0 : t < 14 ? L_SCR : t < 21 ? 3 * UNIT : UNIT) + (t % 7) * 4096; }
DI int p4_part(int a, int hw) {
  const int i = ((a >> 1) | ((a & 1) << 1)) * 4 + hw;
  return i < 7 ? 2 * UNIT + i * 4096 : i < 10 ? L_KVF + (i - 7) * 4096 : (i - 10) * 4096;
}
DI void put16(const f32x16& acc, LAS char* LD, int off) {
#pragma unroll
  for (int qd = 0; qd < 4; ++qd) *(LAS f32x4*)(LD + off + qd * 1024) = f32x4{acc[4 * qd], acc[4 * qd + 1], acc[4 * qd + 2], acc[4 * qd + 3]};
}
DI void add16(f32x16& acc, LAS char* LD, int off) {
#pragma unroll
  for (int qd = 0; qd < 4; ++qd) {
    const f32x4 v = *(LAS f32x4*)(LD + off + qd * 1024);
#pragma unroll
    for (int i = 0; i < 4; ++i) acc[4 * qd + i] += v[i];
  }
}

struct Args {
  const u16* x0; const u16* wqkv; const u16* wphi; const u16* wo; const u16* wg; const u16* wu; const u16* wd;
  char* ws; u16* out;
};

struct Retile { const v4u* pa[7]; int rd0, rd1, step, o0, o1, ocb, sig; };
DI void retile_prep(const Args& A, int job, int lane, int stage, Retile& R) {
  const int h = lane >> 5, q = (lane & 15) >> 2, c8 = ((lane >> 4) & 1) * 4 + (lane & 3);
  const int hh = c8 & 1, quad = c8 >> 1;
  if (job < 64 || job >= 512) {
    const bool iswo = job < 64;
    const int a0 = iswo ? job >> 2 : (job - 512) >> 2, cg = job & 3;
    const u16* src = (iswo ? A.wo + (size_t)a0 * 28 * D : A.wd + (size_t)a0 * 32 * D) + 112 * cg;
    const int nrow = iswo ? 28 : 32;
#pragma unroll
    for (int i = 0; i < 7; ++i) {
      const int p = lane + 64 * i, r = p / 14, c = p % 14;
      R.pa[i] = (const v4u*)(src + (size_t)(r < nrow ? r : 0) * D + c * 8);
    }
    const int rb = stage + ((iswo ? 4 : 8) * h + q) * 224 + 8 * (quad & 1);
    const int oi = (quad >> 1) * 2 + hh;
    R.rd0 = rb + 16 * oi; R.rd1 = rb + 16 * min(4 + oi, 6); R.step = (iswo ? 8 : 4) * 224;
    int ob[2];
#pragma unroll
    for (int k2 = 0; k2 < 2; ++k2) {
      const int ks = a0 * 2 + k2, o_ = 16 * (ks % 7) + ks / 7;
      int fr;
      if (o_ < 28 || (o_ >= 52 && o_ < 80)) fr = 112 + ((o_ & 3) * 14 + (o_ < 28 ? o_ >> 2 : (o_ - 24) >> 2)) * 2;
      else fr = (o_ < 52 ? o_ - 28 : o_ - 56) * 2;
      ob[k2] = iswo ? OFF_W2 + cg * 2 * W2B + ((ks & 15) * 2 + (ks >> 4)) * 2048 : OFF_W4 + cg * 16 * UNIT + fr * 1024;
    }
    R.o0 = opq(ob[0]); R.o1 = opq(ob[1]); R.ocb = opq(iswo ? W2B : 8 * UNIT); R.sig = opq((iswo ? C_W2 : C_W4) + cg);
  } else {
    const int a = job - 64, j = a / 28, ks = a % 28;
#pragma unroll
    for (int i = 0; i < 7; ++i) {
      const int p = lane + 64 * i, r = p / 28, c = p % 28;
      R.pa[i] = (const v4u*)((c < 14 ? A.wg : A.wu) + (size_t)(ks * 16 + r) * FF + 112 * j + (c < 14 ? c : c - 14) * 8);
    }
    R.rd0 = stage + (8 * h + q) * 448 + (quad & 1) * 224 + (8 * hh + 4 * (quad >> 1)) * 2; R.rd1 = 0; R.step = 0;
    R.o0 = opq(OFF_W3 + j * 7 * UNIT + (ks < PK ? ks * 7 : 6 * PK + ks) * 1024); R.o1 = opq(ks < PK ? 1024 : MK * 1024);
    R.ocb = opq(OFF_CS3 + (j * 28 + ks) * 896); R.sig = opq(C_W3 + j);
  }
#pragma unroll
  for (int i = 0; i < 7; ++i) PIN(R.pa[i]);
  PIN(R.rd0); PIN(R.rd1);
}
DI void retile_load(const Retile& R, u32 lds_dst) {
#pragma unroll
  for (int i = 0; i < 7; ++i) {
    u32 keep;
    asm volatile("s_mov_b32 %0, m0\n\ts_add_u32 m0, %2, %3\n\ts_nop 0\n\tglobal_load_lds_dwordx4 %1, off\n\ts_mov_b32 m0, %0"
                 : "=&s"(keep) : "v"(R.pa[i]), "s"(lds_dst), "i"(i * 1024) : "memory", "scc");
  }
}
DI void retile_store(int job, const Retile& R, rsrc_t rs, LAS char* LD, int lane) {
  const int h = lane >> 5;
  if (job < 64 || job >= 512) {
    const bool pad = (lane & 0x14) == 0x14;
    const bool kpad = job < 64 && h;
    v4u o[8];
#pragma unroll
    for (int i = 0; i < 8; ++i) {
      const int ad = ((i & 2) ? R.rd1 : R.rd0) + (i >> 2) * 112 + (i & 1) * 16 * 224;
      const v2u lo = __builtin_bit_cast(v2u, __builtin_amdgcn_ds_read_tr16_b64_v4i16((LAS s16x4*)(LD + ad)));
      const v2u hi = __builtin_bit_cast(v2u, __builtin_amdgcn_ds_read_tr16_b64_v4i16((LAS s16x4*)(LD + ad + R.step)));
      o[i] = v4u{lo.x, lo.y, hi.x, hi.y};
    }
#pragma unroll
    for (int i = 0; i < 8; ++i) {
      if ((i & 2) && pad) o[i] = v4u{0u, 0u, 0u, 0u};
      if ((i & 1) && kpad) { o[i].z = 0u; o[i].w = 0u; }
      st16(rs, lane * 16, ((i & 1) ? R.o1 : R.o0) + (i >> 2) * R.ocb + ((i >> 1) & 1) * 1024, o[i]);
    }
  } else {
    v4u o[7];
#pragma unroll
    for (int n = 0; n < 7; ++n) o[n] = tr_frag(LD, R.rd0 + n * 32, 4 * 448);
    float cs[7];
#pragma unroll
    for (int n = 0; n < 7; ++n) {
      st16(rs, lane * 16, R.o0 + n * R.o1, o[n]);
      cs[n] = xsum(sum8(o[n]));
    }
#pragma unroll
    for (int n2 = 0; n2 < 4; ++n2) {
      const float v = h ? cs[n2 < 3 ? 2 * n2 + 1 : 6] : cs[2 * n2];
      if (n2 < 3 || !h) st4(rs, lane * 4, R.ocb + n2 * 256, __float_as_uint(v));
    }
  }
}

__global__ __launch_bounds__(512, 2) void k_main(const Args A) {
  extern __shared__ __attribute__((aligned(16))) char smem[];
  LAS char* LD = (LAS char*)smem;
  char* ws = A.ws;
  const int b = blockIdx.x, xcd = b & 7, bi = b >> 3;
  const int g = (xcd >> 2) * 8 + (bi >> 2), j = (xcd & 3) * 4 + (bi & 3);
  const int rb = xcd * 4 + (bi >> 3), cb = bi & 7;
  const int lane = threadIdx.x & 63, w = __builtin_amdgcn_readfirstlane(threadIdx.x >> 6);
  const int h = lane >> 5, tok = lane & 31;
  const rsrc_t rs = __builtin_amdgcn_make_buffer_rsrc((void*)ws, 0, WS_USED, 0x00020000);
  const u32 lbase = (u32)(size_t)smem;
  const int l16 = lane * 16;
  const int tq_ = (lane & 15) >> 2, tc8 = ((lane >> 4) & 1) * 4 + (lane & 3);
  const int w1lane = (8 * h + tq_) * 64 + min(tc8 + (j & 1), 7) * 8;

  const int hw = w - 4;
  const u32 voffn = (hw * 16 + (lane >> 2)) * (3 * D * 2) + (lane & 3) * 16;
  const char* w1 = (const char*)A.wqkv + 56 * j - (j & 1) * 8;
  unsigned long long ep;
  const u32 lb = lbase + hw * 1024;
#define KP(p) dma1<4 * UNIT + (p) * 4096, 896 + (p) * 0x2a000>(w1, voffn, lb)
#define VP(p) dma1<(p) * 4096, 1792 + (p) * 0x2a000>(w1, voffn, lb)
#define QP(p) dma1<UNIT + (p) * 4096, (p) * 0x2a000>(w1, voffn, lb)
  if (opq(w) >= 4) {
    HSTAMP(0);
    if (hw == 1) {
      asm volatile("global_load_dwordx2 %0, %1, %2 sc1" : "=v"(ep) : "v"(EPOCH_OFF), "s"(ws) : "memory");
      if (b < NZ) st4(rs, lane * 128, b * 8192, 0u);
    }
    if (hw == 2) {
      dma1<L_KVF, 0>((const char*)A.wphi + j * 1568, l16, lbase);
      dma1<L_KVF + 1024, 0>((const char*)A.wphi + j * 1568, min(1024 + l16, 1552), lbase);
    }
    KP(0); KP(1); VP(0); KP(2); VP(1); KP(3); VP(2);
    if (hw == 3) {
      unsigned long long pcv;
      asm volatile("s_getpc_b64 %0" : "=s"(pcv));
      const char* pc = (const char*)(pcv & ~255ull);
      dma1<L_W2X, 0>(pc, bi * 1024 + l16, lbase);
      dma1<L_W2X + 1024, 32768>(pc, bi * 1024 + l16, lbase);
      dma1<L_W2X + 2048, 65536>(pc, min(bi * 1024 + l16, 2 * 1024 - 16), lbase);
    }
    HSTAMP(1);
  }

  if (w >= 4) {
    const u32 voff = hw * 1024 + lane * 16;
    const char* w1f = ws + OFF_W1 + j * 3 * UNIT;
    const char* w2 = ws + OFF_W2 + cb * W2B;
    const char* w3 = ws + OFF_W3 + j * 7 * UNIT;
    const char* w4 = ws + OFF_W4 + cb * 8 * UNIT;
#define DMA(src, slot) dma7((src), voff, lbase + (slot) * UNIT + hw * 1024)
#define DMAP(src, slot) dma7p((src), voff, lbase + (slot) * UNIT + hw * 1024)
#define DMAN(which, slot) dma7n(w1 + (which) * 896, voffn, lbase + (slot) * UNIT + hw * 1024)
    v4u wr[28];
    unsigned long long tag = 0;
    auto hlayer = [&](auto LC) AI {
      constexpr int L = LC.value;
      Retile R;
      const int job = hw >= 2 ? b * 2 + hw - 2 : (hw == 0 && b < 224 ? 512 + b : -1);
      float c0 = 0.f, c1 = 0.f, c2 = 0.f, c3 = 0.f;
      auto colpart = [&](int slot, auto K0, auto K1) AI {
        constexpr int k0 = K0.value, n = K1.value - K0.value;
        const int ib = opqv(slot * UNIT + opqv(w1lane));
        v4u fr[n];
#pragma unroll
        for (int ks = 0; ks < n; ++ks) fr[ks] = tr_frag(LD, ib + (k0 + ks) * 1024, 256);
        SB();
        const u32 one = 0x3f803f80u;
#pragma unroll
        for (int ks = 0; ks < n; ++ks) { c0 = dot2(fr[ks].x, one, c0); c1 = dot2(fr[ks].y, one, c1); c2 = dot2(fr[ks].z, one, c2); c3 = dot2(fr[ks].w, one, c3); }
        SB();
      };
      auto colfin = [&](int which) AI {
        const float cs = xsum((c0 + c1) + (c2 + c3));
        if (lane < 32) *(LAS float*)(LD + L_CS1 + which * 128 + lane * 4) = cs;
      };
      v4u csa[7];
      auto export_img = [&](int slot, int which) AI {
        const int ib = opqv(slot * UNIT + opqv(w1lane) + g * 1024);
        const int eo = opq(OFF_W1 + (j * 3 + which) * UNIT + g * 1024);
        st16(rs, l16, eo, tr_frag(LD, ib, 256));
        if (g < 12) st16(rs, l16, eo + 16384, tr_frag(LD, ib + 16384, 256));
      };
      if constexpr (L == 0) {
        if (hw == 1 && lane < 2) *(LAS v4u*)(LD + L_ARR + l16) = v4u{0u, 0u, 0u, 0u};
        HSTAMP(2);
#define VMW(n, n3) if (hw == 3) { VMCNT(n3); } else { VMCNT(n); }
        VMW(4, 7); BAR();
        HSTAMP(3);
        KP(4); VP(3);
        if (hw == 1) {
          PIN(ep);
          tag = ep + 1ull;
          if (b < NZ && lane == 0) __hip_atomic_store((unsigned long long*)(ws + INIT_OFF + b * 128), tag, __ATOMIC_RELAXED, __HIP_MEMORY_SCOPE_AGENT);
        }
        if (hw == 2) {
          const int wb = opqv(L_KVF + (4 * h + tq_) * 56 + tc8 * 8);
          const v4u f0 = tr_frag(LD, wb, 8 * 56);
          v4u f1 = tr_frag(LD, wb + 16 * 56, 8 * 56);
          if (h) { f1.z = 0u; f1.w = 0u; }
          *(LAS v4u*)(LD + L_WPHI + l16) = f0;
          *(LAS v4u*)(LD + L_WPHI + 1024 + l16) = f1;
        }
        if (hw == 0) colpart(4, IC<0>{}, IC<8>{});
        VMW(4, 7); BAR();
        HSTAMP(4);
        KP(5); VP(4);
        if (job >= 0) retile_prep(A, job, lane, L_STAGE + hw * STG, R);
        if (hw == 1) colpart(0, IC<0>{}, IC<8>{});
        VMW(4, 7); BAR();
        HSTAMP(5);
        KP(6); VP(5); VP(6);
        if (hw == 0) colpart(4, IC<8>{}, IC<16>{});
        if (hw == 1) colpart(0, IC<8>{}, IC<12>{});
        VMCNT(5); BAR();
        HSTAMP(6);
        QP(0); QP(1); QP(2); QP(3);
        if (hw == 0) colpart(4, IC<16>{}, IC<20>{});
        if (hw == 1) colpart(0, IC<12>{}, IC<16>{});
        VMCNT(7); BAR();
        HSTAMP(7);
        QP(4); QP(5); QP(6);
        if (hw == 0) colpart(4, IC<20>{}, IC<24>{});
        if (hw == 1) colpart(0, IC<16>{}, IC<20>{});
        VMCNT(7); BAR();
        HSTAMP(8);
        if (hw == 0) { colpart(4, IC<24>{}, IC<28>{}); colfin(0); }
        if (hw == 1) { colpart(0, IC<20>{}, IC<28>{}); colfin(1); }
        if (hw == 2) export_img(4, 1);
        if (hw == 3) export_img(0, 2);
        HSTAMP(9);
        BAR();
        HSTAMP(10);
        if (job >= 0) retile_load(R, lbase + L_STAGE + hw * STG);
      } else {
        wait_arr(LD, 0, 4);
        DMA(w1f + 2 * UNIT, 0);
        VMCNT(0); BAR();
      }
      {
        const int wb = opqv(L_WPHI + opqv(l16));
        const v4u wf0 = lds16(LD, wb), wf1 = lds16(LD, wb + 1024);
        if (L == 0) HSTAMP(11);
        BAR();
        if (L == 0) HSTAMP(12);
        __builtin_amdgcn_s_setprio(1);
        const int lz = opqv(lane);
        const int xk = opqv(L_W2X + hw * 1024 + lz * 16);
        const v4u kf0 = lds16(LD, xk), kf1 = lds16(LD, xk + 4096);
        const float rstd = *(LAS float*)(LD + L_KVF + hw * 256 + lz * 4);
        f32x16 p = {};
        p = mfma(wf0, kf0, p);
        p = mfma(wf1, kf1, p);
        v4u o0, o1;
        half_softmax<1>(p, h, rstd, o0, o1);
        const int pb = opqv(L_PHK + (hw * 32 + (lz & 31)) * PHK_STRIDE + 8 * (lz >> 5) + 64);
        *(LAS v2u*)(LD + pb) = v2u{o0.x, o0.y}; *(LAS v2u*)(LD + pb + 16) = v2u{o0.z, o0.w};
        *(LAS v2u*)(LD + pb + 32) = v2u{o1.x, o1.y}; *(LAS v2u*)(LD + pb + 48) = v2u{o1.z, o1.w};
        __builtin_amdgcn_s_setprio(0);
      }
      if (L == 0) HSTAMP(13);
      if constexpr (L == 0) {
        if (hw == 1) {
          u32 spins = 0;
          while (!__all(__hip_atomic_load((unsigned long long*)(ws + INIT_OFF + min(lane, NZ - 1) * 128), __ATOMIC_RELAXED, __HIP_MEMORY_SCOPE_AGENT) == tag)) {
            __builtin_amdgcn_s_sleep(1);
            if (++spins > 400000u) break;
          }
        }
        if (job >= 0) { VMCNT(7); } else { VMCNT(0); }
      } else VMCNT(0);
      if (L == 0) HSTAMP(14);
      BAR();
      if (hw < 2) {
        __builtin_amdgcn_s_setprio(2);
        f32x16 kp = {};
        const int lz = opqv(lane);
        const int g16 = (lz >> 4) & 1, q = (lz & 15) >> 2, p = lz & 3, hz = lz >> 5;
        const int ab0 = opqv(L_PHK + (8 * hz + q) * PHK_STRIDE + (g16 * 16 + 4 * p) * 2 + hw * 64);
        const int bb0 = opqv(L_V + (8 * hz + q) * V_STRIDE + (g16 * 16 + 4 * p) * 2);
        v4u ap[8], bv[8];
#pragma unroll
        for (int s = 0; s < 8; ++s) { ap[s] = tr_frag(LD, ab0 + 16 * s * PHK_STRIDE, 4 * PHK_STRIDE); bv[s] = tr_frag(LD, bb0 + 16 * s * V_STRIDE, 4 * V_STRIDE); }
        SB();
#pragma unroll
        for (int s = 0; s < 8; ++s) kp = mfma(ap[s], bv[s], kp);
        const int ko = opq(OFF_KVP + L * KVB + (j * 16 + g) * 4096 + hw * 2048);
        st16(rs, l16, ko, pack8(kp, 0));
        st16(rs, l16, ko + 1024, pack8(kp, 1));
        wave_signal(ws, C_KV + L * 16 + j, lane);
        __builtin_amdgcn_s_setprio(0);
      }
      if constexpr (L == 0) {
        HSTAMP(15);
        if (hw == 3) {
          colpart(1, IC<0>{}, IC<28>{}); colfin(2);
          arrive_lds(LD, 1, lane);
        }
        if (hw == 1) { export_img(1, 0); wave_signal(ws, C_W1 + j, lane); }
        if (job >= 0) { VMCNT(0); retile_store(job, R, rs, LD, lane); }
        HSTAMP(16);
        if (job >= 0) wave_signal(ws, R.sig, lane);
        if (hw >= 2) signal_cnt(ws, C_W1 + j);
        HSTAMP(17);
        BAR();
        HSTAMP(18);
        u32* fw = cnt_ptr(ws, (lane & 1) ? C_W3 + j : C_W2 + (cb >> 1));
        kv_reduce(rs, LD, L, j, w, lane);
        HSTAMP(19);
        BAR();
        HSTAMP(20);
        wait_flags(ws, fw, __hip_atomic_load(fw, __ATOMIC_RELAXED, __HIP_MEMORY_SCOPE_AGENT), (lane & 1) ? 28u : 16u);
        HSTAMP(21);
        DMA(w2, 2); DMA(w2 + UNIT, 3); dma2(w2 + 2 * UNIT, voff, lbase + L_W2X + hw * 1024);
#pragma unroll
        for (int i = 0; i < 7; ++i) csa[i] = ld16(rs, min(lane, 55) * 16, OFF_CS3 + (j * 28 + hw * 7 + i) * 896);
        DMA(w3, 0);
        HSTAMP(22);
        VMCNT(14); HSTAMP(23); BAR();
        HSTAMP(24);
        DMA(w3 + UNIT, 1);
      } else {
        DMA(w2, 2); DMA(w2 + UNIT, 3); dma2(w2 + 2 * UNIT, voff, lbase + L_W2X + hw * 1024);
        DMA(w3, 0);
        BAR();
        kv_reduce(rs, LD, L, j, w, lane);
        BAR();
        DMA(w3 + UNIT, 1);
      }
      BAR();
      if constexpr (L == 0) {
        f32x4 c4 = __builtin_bit_cast(f32x4, csa[0]);
#pragma unroll
        for (int i = 1; i < 7; ++i) c4 += __builtin_bit_cast(f32x4, csa[i]);
        if (lane < 56) *(LAS f32x4*)(LD + L_KVF + hw * 896 + l16) = c4;
      }
      DMA(w3 + 2 * UNIT, 2);
      VMCNT(7); BAR();
      const int wo4 = opq(OFF_W4 + cb * 8 * UNIT + 4 * UNIT + hw * UNIT);
#define WR(lo, hi) _Pragma("unroll") for (int i = lo; i < hi; ++i) wr[i] = ld16(rs, l16, wo4 + i * 1024)
      if constexpr (L == 0) {
        const int t4 = min(hw * 64 + lane, 223) * 4;
        *(LAS float*)(LD + L_CS3 + t4) = (*(LAS float*)(LD + L_KVF + t4) + *(LAS float*)(LD + L_KVF + 896 + t4)) +
                                         (*(LAS float*)(LD + L_KVF + 1792 + t4) + *(LAS float*)(LD + L_KVF + 2688 + t4));
        wait_cnt(ws, C_W4 + (cb >> 1), 56);
        wait_cnt(ws, C_W1 + j, 48);
        WR(0, 28);
      }
      DMA(w3 + 3 * UNIT, 3);
      VMCNT(7); BAR();
      DMAP(w3 + 4 * UNIT, 0);
      DMAP(w4 + UNIT, 4);
      VMCNT(14); BAR();
      DMAP(w3 + 5 * UNIT, 1);
      VMCNT(14); BAR();
      DMAP(w3 + 6 * UNIT, 2);
      VMCNT(7); BAR();
      DMAP(w4 + 2 * UNIT, 3);
      VMCNT(7); BAR();
      {
        u32* fp = cnt_ptr(ws, C_H + (L * 64 + 2 * rb + (lane & 1)) * 8);
        const int ho = opq(OFF_H + L * HB + 2 * rb * 112 * 1024 + hw * 7168);
        v4u hx[28];
        auto ldh = [&](auto I) AI { constexpr int i = I.value; hx[i] = ld16(rs, l16, ho + (i & 1) * 112 * 1024 + p4_koff(p4_oh(i >> 1))); };
        auto poll = [&](int lv) AI { return __hip_atomic_load(fp + 32 * lv, __ATOMIC_RELAXED, __HIP_MEMORY_SCOPE_AGENT); };
        DMAP(w4, 0);
        wait_flags(ws, fp, poll(0));
        static_for<8>([&](auto I) AI { ldh(I); });
        BAR();
        DMA(w4 + 3 * UNIT, 1);
        u32 f = poll(1);
        f32x16 acc[4] = {};
        auto ord = [](int i) constexpr { return i < 20 ? i : (i < 24 ? 20 + 2 * (i - 20) : 21 + 2 * (i - 24)); };
        const int pb0 = opqv(p4_part(0, hw) + opqv(l16)), pb2 = opqv(p4_part(2, hw) + opqv(l16));
        static_for<28>([&](auto I) AI {
          constexpr int i = I.value, k = ord(i), t = k >> 1, tq = k & 1;
          if constexpr (i == H1) {
            wait_flags(ws, fp + 32, f);
            VMCNT(0); arrive_lds(LD, 4, lane);
            static_for<6>([&](auto K) AI { ldh(IC<8 + K.value>{}); });
          }
          if constexpr (i == H3 - 2) f = poll(3);
          if constexpr (i == H3) { wait_flags(ws, fp + 96, f); f = poll(4); }
          if constexpr (i == 24) wait_arr(LD, 2, 4 * L + 4);
          acc[tq] = mfma(wr[2 * t], hx[k], acc[tq]);
          acc[2 + tq] = mfma(wr[2 * t + 1], hx[k], acc[2 + tq]);
          if constexpr (i >= H3 && i < H3 + 3) { ldh(IC<14 + 2 * (i - H3)>{}); ldh(IC<15 + 2 * (i - H3)>{}); }
          if constexpr (i == H4) wait_flags(ws, fp + 128, f);
          if constexpr (i >= H4 && i < H4 + 4) { ldh(IC<20 + 2 * (i - H4)>{}); ldh(IC<21 + 2 * (i - H4)>{}); }
          if constexpr (i >= 24) {
            constexpr int qd = i - 24;
            *(LAS f32x4*)(LD + pb0 + qd * 1024) = f32x4{acc[0][4 * qd], acc[0][4 * qd + 1], acc[0][4 * qd + 2], acc[0][4 * qd + 3]};
            *(LAS f32x4*)(LD + pb2 + qd * 1024) = f32x4{acc[2][4 * qd], acc[2][4 * qd + 1], acc[2][4 * qd + 2], acc[2][4 * qd + 3]};
          }
          SB();
        });
        wait_arr(LD, 3, 4 * L + 4);
        put16(acc[1], LD, opqv(p4_part(1, hw) + opqv(l16)));
        put16(acc[3], LD, opqv(p4_part(3, hw) + opqv(l16)));
        BAR();
      }
      if (L == 0) { DMA(w1f + UNIT, 3); DMA(w1f, 1); }
    };
    hlayer(IC<0>{});
    hlayer(IC<1>{});
    if (b == 0 && hw == 1 && lane == 0) __hip_atomic_store((unsigned long long*)(ws + EPOCH_OFF), tag, __ATOMIC_RELAXED, __HIP_MEMORY_SCOPE_AGENT);
    return;
  }

  const int tt = 4 * g + w;
  const int tt2 = 2 * rb + (w & 1);
  const int kh = w >> 1;
  const rsrc_t rx0 = __builtin_amdgcn_make_buffer_rsrc((void*)A.x0, 0, XB, 0x00020000);
  v4u res[2];
#define XWV 14
  constexpr int XW = XWV;

  auto clayer = [&](auto LC) AI {
    constexpr int L = LC.value;
    v4u x[28];
    v2u st[8];
    float mean, rstd;
    v4u pq[4];
    STAMP(0);
    auto phase1 = [&](auto FIRST) AI {
      constexpr bool first = FIRST.value;
      const int xo = opq(first ? tt * 32 * (D * 2) : OFF_X2 + tt * 28 * 1024);
      auto ldx = [&](int ks) AI { return ld16(rs, l16, xo + ks * 1024); };
      const int so_ = opq(OFF_ST2 + tt * 4096);
      const int sv_ = (opqv(lane) & 31) * 8 + h * 2048;
      v4u gx[28];
      const int stg = L_STAGE + w * STG;
      const int gv = (lane >> 4) * (D * 2) + (lane & 15) * 16, gv3 = (lane >> 3) * (D * 2) + (lane & 7) * 16;
      const int gl = stg + (lane >> 4) * 272 + (lane & 15) * 16, gl3 = stg + (lane >> 3) * 272 + (lane & 7) * 16;
      const int srd = opqv(stg + (opqv(lane) & 31) * 272 + h * 16);
      auto ldg = [&](int i) AI {
        return i < 24 ? __builtin_amdgcn_raw_buffer_load_b128(rx0, gv, xo + (i & 7) * 4 * (D * 2) + (i >> 3) * 256, 0)
                      : __builtin_amdgcn_raw_buffer_load_b128(rx0, gv3, xo + (i & 7) * 8 * (D * 2) + 768, 0);
      };
      auto stage = [&](int c) AI {
#pragma unroll
        for (int i = 0; i < (c < 3 ? 8 : 4); ++i) *(LAS v4u*)(LD + (c < 3 ? gl + i * 4 * 272 : gl3 + i * 8 * 272)) = gx[8 * c + i];
#pragma unroll
        for (int i = 0; i < (c < 3 ? 8 : 4); ++i) x[8 * c + i] = lds16(LD, srd + i * 32);
      };
      if constexpr (first) {
#pragma unroll
        for (int i = 0; i < 16; ++i) gx[i] = ldg(i);
        stage(0);
      } else {
#pragma unroll
        for (int ks = 0; ks < XW; ++ks) x[ks] = ldx(ks);
      }
      USTAMP(18);
      BAR();
      USTAMP(19);
      f32x16 ak = {}, av = {}, p = {};
      const int sbk = first ? opqv(opqv(w1lane) + 4 * UNIT) : opqv(opqv(l16) + 3 * UNIT), sbv = first ? opqv(w1lane) : opqv(l16);
      const int csb = opqv(L_CS1 + 16 * (opqv(lane) >> 5)), wb = opqv(L_WPHI + opqv(l16));
      const int xk = opqv(L_W2X + w * 1024 + opqv(l16));
      f32x4 cs[4]; v4u kf[2], wf[2];
      float nmean, s1 = 0.f, s2 = 0.f, q1 = 0.f, q2 = 0.f;
      auto isk = [](int i) constexpr { return i < LAG || (i < 56 - LAG && ((i - LAG) & 1) == 0); };
      auto kst = [](int i) constexpr { return i < LAG ? i : (i < 56 - LAG ? ((((i - LAG) & 1) == 0) ? LAG + (i - LAG) / 2 : (i - LAG) / 2) : i - 28); };
      auto rdw = [&](int base, int ks) AI { if constexpr (first) return tr_frag(LD, base + ks * 1024, 256); else return lds16(LD, base + ks * 1024); };
      pipe<56, first ? 2 : 1>([&](auto I) AI { return rdw(isk(I.value) ? sbk : sbv, kst(I.value)); },
               [&](auto I) AI {
                 constexpr int i = I.value;
                 if constexpr (first && (i == 6 || i == 14 || i == 22 || i == 30 || i == 38 || i == 46)) { USTAMP(40 + i / 8 * 2); RAWBAR(); USTAMP(41 + i / 8 * 2); }
                 if constexpr (i == 54) { USTAMP(20); BAR(); USTAMP(21); }
               },
               [&](auto I, v4u a, auto nx) AI {
                 constexpr int i = I.value, ks = kst(i);
                 if constexpr (i == 54) { p = mfma(wf[0], kf[0], p); p = mfma(wf[1], kf[1], p); }
                 if constexpr (isk(i)) ak = mfma(a, x[ks], ak); else av = mfma(a, x[ks], av);
                 nx();
                 if constexpr (!first && isk(i) && ks + XW < 28) x[ks + XW] = ldx(ks + XW);
                 if constexpr (first) {
                   if constexpr (i < 12) gx[16 + i] = ldg(16 + i);
                   if constexpr (isk(i) && ks % 8 == 6) stage(ks / 8 + 1);
                   if constexpr (isk(i)) {
                     const u32 one = 0x3f803f80u;
                     s1 = dot2(x[ks].x, one, s1); q1 = dot2(x[ks].x, x[ks].x, q1); s2 = dot2(x[ks].y, one, s2); q2 = dot2(x[ks].y, x[ks].y, q2);
                     s1 = dot2(x[ks].z, one, s1); q1 = dot2(x[ks].z, x[ks].z, q1); s2 = dot2(x[ks].w, one, s2); q2 = dot2(x[ks].w, x[ks].w, q2);
                   }
                   if constexpr (i == 51) { ln_finish(s1 + s2, q1 + q2, mean, rstd); nmean = -mean; }
                 } else {
                   if constexpr (!isk(i) && ks >= 4 && ks < 12) st[ks - 4] = ld8(rs, sv_ + (ks - 4) * 256, so_);
                   if constexpr (i == 45) { ln_stats(st, mean, rstd); nmean = -mean; }
                 }
                 if constexpr (i >= 46 && i < 50) cs[i - 46] = *(LAS f32x4*)(LD + csb + 32 * (i - 46));
                 if constexpr (i == 50) wf[0] = lds16(LD, wb);
                 if constexpr (i == 51) wf[1] = lds16(LD, wb + 1024);
                 if constexpr (i == 52 || i == 53) {
#pragma unroll
                   for (int qd = 2 * (i - 52); qd < 2 * (i - 52) + 2; ++qd) {
                     const float y0 = fmaf(nmean, cs[qd][0], ak[4 * qd]), y1 = fmaf(nmean, cs[qd][1], ak[4 * qd + 1]);
                     const float y2 = fmaf(nmean, cs[qd][2], ak[4 * qd + 2]), y3 = fmaf(nmean, cs[qd][3], ak[4 * qd + 3]);
                     kf[qd >> 1][2 * (qd & 1)] = pk2(y0, y1);
                     kf[qd >> 1][2 * (qd & 1) + 1] = pk2(y2, y3);
                   }
                   *(LAS v4u*)(LD + xk + (i - 52) * 4096) = kf[i - 52];
                   if constexpr (i == 52) *(LAS float*)(LD + L_KVF + w * 256 + opqv(lane) * 4) = rstd;
                 }
                 if constexpr (i == 55) {
#pragma unroll
                   for (int qd = 0; qd < 4; ++qd) cs[qd] = *(LAS f32x4*)(LD + csb + 128 + 32 * qd);
                 }
               });
      STAMP(1);
      const int lz = opqv(lane);
      const int trow = w * 32 + (lz & 31);
      const int pb = opqv(L_PHK + trow * PHK_STRIDE + 8 * (lz >> 5)), vb = opqv(L_V + trow * V_STRIDE + 8 * (lz >> 5));
      half_softmax<0>(p, h, rstd, pq[0], pq[1]);
#pragma unroll
      for (int s = 0; s < 2; ++s) {
        *(LAS v2u*)(LD + pb + 32 * s) = v2u{pq[s].x, pq[s].y};
        *(LAS v2u*)(LD + pb + 32 * s + 16) = v2u{pq[s].z, pq[s].w};
      }
      SB();
#pragma unroll
      for (int r = 0; r < 16; ++r) av[r] = fmaf(nmean, cs[r >> 2][r & 3], av[r]) * rstd;
      if (h) { av[12] = 1.f; av[13] = 0.f; av[14] = 0.f; av[15] = 0.f; }
#pragma unroll
      for (int qd = 0; qd < 4; ++qd) *(LAS v2u*)(LD + vb + 16 * qd) = v2u{pk2(av[4 * qd], av[4 * qd + 1]), pk2(av[4 * qd + 2], av[4 * qd + 3])};
      STAMP(2);
      BAR();
      if constexpr (first) {
#pragma unroll
        for (int ro = 0; ro < 2; ++ro) {
          const int oi = min(kh * 4 + ro * 2 + h, 6);
          res[ro] = __builtin_amdgcn_raw_buffer_load_b128(rx0, tok * (D * 2) + (7 * cb + oi) * 16, tt2 * 32 * (D * 2), 0);
        }
      }
      f32x16 aq = {};
      const int sbq = first ? opqv(opqv(w1lane) + 1 * UNIT) : opqv(opqv(l16) + 1 * UNIT);
      pipe<28, first ? 2 : 1>([&](auto I) AI { return rdw(sbq, I.value); }, [&](auto) AI {},
               [&](auto I, v4u a, auto nx) AI { aq = mfma(a, x[I.value], aq); nx(); });
      if constexpr (first) wait_arr(LD, 1, 1);
#pragma unroll
      for (int qd = 0; qd < 4; ++qd) cs[qd] = *(LAS f32x4*)(LD + csb + 256 + 32 * qd);
#pragma unroll
      for (int r = 0; r < 16; ++r) aq[r] = fmaf(nmean, cs[r >> 2][r & 3], aq[r]);
      f32x16 p2 = {};
      const int wb2 = opqv(L_WPHI + opqv(l16));
      p2 = mfma(lds16(LD, wb2), pack8(aq, 0), p2);
      p2 = mfma(lds16(LD, wb2 + 1024), pack8(aq, 1), p2);
      half_softmax<0>(p2, h, rstd, pq[0], pq[1]);
      half_softmax<1>(p2, h, rstd, pq[2], pq[3]);
    };
    phase1(std::integral_constant<bool, L == 0>{});
    STAMP(3);
    if (w == 0) wait_cnt(ws, C_KV + L * 16 + j, 32);
    BAR();
    STAMP(4);
    kv_reduce(rs, LD, L, j, w, lane);
    BAR();
    STAMP(5);
    {
      f32x16 nm = {};
      const int kb = opqv(L_KVF + opqv(l16));
      v4u kf[4];
#pragma unroll
      for (int s = 0; s < 4; ++s) kf[s] = lds16(LD, kb + s * 1024);
#pragma unroll
      for (int s = 0; s < 4; ++s) nm = mfma(kf[s], pq[s], nm);
      auto r = __builtin_amdgcn_permlane32_swap(__float_as_uint(nm[12]), __float_as_uint(nm[12]), false, false);
      const float den = __uint_as_float(r[1]) + 1e-6f;
      const float rd = __builtin_amdgcn_rcpf(den);
#pragma unroll
      for (int i = 0; i < 16; ++i) nm[i] *= rd;
      const int oo = opq(OFF_O + L * OB + (tt * 32 + 2 * j) * 1024);
      st16(rs, l16, oo, pack8(nm, 0));
      st16(rs, l16, oo + 1024, pack8(nm, 1));
    }
    wave_signal(ws, C_O + (L * 64 + tt) * 2 + (j >> 3), lane);
    STAMP(6);
    wait_cnt(ws, C_O + (L * 64 + tt2) * 2 + kh, 8, true);
    STAMP(7);
    {
      const int xo = opq(OFF_O + L * OB + (tt2 * 32 + 16 * kh) * 1024);
#pragma unroll
      for (int t = 0; t < (XW < 16 ? XW : 16); ++t) x[t] = ld16(rs, l16, xo + t * 1024);
      if (L == 0) RAWBAR();
      f32x16 a0 = {}, a1 = {};
      const int sb2 = opqv(2 * UNIT + kh * 2048 + opqv(l16)), sbx = opqv(L_W2X + kh * 2048 + opqv(l16));
      pipe<32>([&](auto I) AI { constexpr int i = I.value, t = i >> 1; return lds16(LD, (t < 14 ? sb2 + t * 4096 : sbx + (t - 14) * 4096) + (i & 1) * 1024); },
               [&](auto I) AI { if constexpr (I.value == 14) RAWBAR();   },
               [&](auto I, v4u a, auto nx) AI {
                 constexpr int i = I.value;
                 if constexpr (i & 1) a1 = mfma(a, x[i >> 1], a1); else a0 = mfma(a, x[i >> 1], a0);
                 nx();
                 if constexpr (!(i & 1) && (i >> 1) + XW < 16) x[(i >> 1) + XW] = ld16(rs, l16, xo + ((i >> 1) + XW) * 1024);
               });
      const int xb = opqv(L_SCR + w * 4096 + opqv(l16)), xb2 = opqv(L_SCR + (w ^ 2) * 4096 + opqv(l16));
      f32x16 fin;
      if (kh) {
#pragma unroll
        for (int qd = 0; qd < 4; ++qd) *(LAS f32x4*)(LD + xb + qd * 1024) = f32x4{a0[4 * qd], a0[4 * qd + 1], a0[4 * qd + 2], a0[4 * qd + 3]};
        fin = a1;
      } else {
#pragma unroll
        for (int qd = 0; qd < 4; ++qd) *(LAS f32x4*)(LD + xb + qd * 1024) = f32x4{a1[4 * qd], a1[4 * qd + 1], a1[4 * qd + 2], a1[4 * qd + 3]};
        fin = a0;
      }
      BAR();
#pragma unroll
      for (int qd = 0; qd < 4; ++qd) {
        const f32x4 v = *(LAS f32x4*)(LD + xb2 + qd * 1024);
#pragma unroll
        for (int i = 0; i < 4; ++i) fin[4 * qd + i] += v[i];
      }
      finish_tile(fin, res, rs, opq(OFF_X1 + L * XB), opq(OFF_ST1 + L * STB), tt2, cb, kh, lane, false, A.out);
    }
    wave_signal(ws, C_X1 + L * 64 + tt2, lane);
    STAMP(8);
    wait_cnt(ws, C_X1 + L * 64 + tt, 16, true);
    STAMP(9);
    {
      const int xo = opq(OFF_X1 + L * XB + tt * 28 * 1024);
      const int so_ = opq(OFF_ST1 + L * STB + tt * 4096);
      const int sv_ = (opqv(lane) & 31) * 8 + h * 2048;
      constexpr int XW3 = 8;
#pragma unroll
      for (int ks = 0; ks < XW3; ++ks) x[ks] = ld16(rs, l16, xo + ks * 1024);
      const int ho = opq(OFF_H + L * HB + (tt * 112 + 7 * j) * 1024);
      const int lvo = opq(C_H + (L * 64 + tt) * 8);
      u32* fp = cnt_ptr(ws, C_H + (L * 64 + tt2) * 8);
      u32* fp12 = fp + 32 + 32 * (lane & 1);
      u32 f;
      float nmean, gs, irs;
      const int csb = opqv(L_CS3 + 16 * (opqv(lane) >> 5));
      const int sbA = opqv(l16), sbB = opqv(opqv(l16) + 2 * UNIT);
      f32x4 Gcg[2], Gcu[2]; float Gt[8], Gyu[8]; u32 Go[4];
      auto glu = [&](const f32x16& a, auto N_, auto S_) AI {
        constexpr int n = N_.value, s = S_.value;
        if constexpr (s == 1) Gcu[0] = *(LAS f32x4*)(LD + csb + n * 128 + 32);
        if constexpr (s == 4) Gcg[1] = *(LAS f32x4*)(LD + csb + n * 128 + 64);
        if constexpr (s == 9) Gcu[1] = *(LAS f32x4*)(LD + csb + n * 128 + 96);
        if constexpr (s == 12 && n < 6) Gcg[0] = *(LAS f32x4*)(LD + csb + (n + 1) * 128);
        static_for<8>([&](auto V_) AI {
          constexpr int v = V_.value, k = s - (1 + 4 * (v >> 1) + (v & 1)), hf = v >> 2, i = v & 3;
          if constexpr (k == 0) { Gt[v] = fmaf(nmean, Gcg[hf][i], a[8 * hf + i]); PIN(Gt[v]); }
          if constexpr (k == 1) { Gt[v] = Gt[v] * gs; PIN(Gt[v]); }
          if constexpr (k == 2) { Gt[v] = __builtin_amdgcn_exp2f(Gt[v]); PIN(Gt[v]); }
          if constexpr (k == 3) { Gt[v] = fmaf(Gt[v], irs, irs); PIN(Gt[v]); }
          if constexpr (k == 4) { Gt[v] = __builtin_amdgcn_rcpf(Gt[v]); PIN(Gt[v]); }
          if constexpr (k == 5) { Gyu[v] = fmaf(nmean, Gcu[hf][i], a[8 * hf + 4 + i]); PIN(Gyu[v]); }
          if constexpr (k == 6) { Gt[v] = Gt[v] * Gyu[v]; PIN(Gt[v]); }
          if constexpr (k == 7 && (v & 1)) { Go[v >> 1] = pk2(Gt[v - 1], Gt[v]); PIN(Go[v >> 1]); }
        });
        if constexpr (s == 21) st16(rs, l16, ho + n * 1024, v4u{Go[0], Go[1], Go[2], Go[3]});
      };
      const int ho2 = opq(OFF_H + L * HB + tt2 * 112 * 1024 + kh * 7168);
      v4u hx[28];
      auto ldh = [&](auto T) AI { hx[T.value] = ld16(rs, l16, ho2 + p4_koff(p4_oc(T.value))); };
      f32x16 acc[7];
      constexpr int PB = 7 * PK;
      pipe<196>(
          [&](auto I) AI { constexpr int i = I.value, u = i / 28, sl = u & 3; return lds16(LD, (sl < 2 ? sbA : sbB) + (sl & 1) * UNIT + (i % 28) * 1024); },
          [&](auto I) AI { if constexpr (I.value > 28 && I.value % 28 == 6) { VSTAMP(30 + I.value / 28); RAWBAR(); WSTAMP(22 + I.value / 28); } },
          [&](auto I, v4u a, auto nx) AI {
            constexpr int i = I.value;
            if constexpr (i < PB) {
              constexpr int n = i % 7, ks = i / 7;
              if constexpr (ks == 0) { f32x16 z = {}; acc[n] = mfma(a, x[ks], z); } else acc[n] = mfma(a, x[ks], acc[n]);
              nx();
              constexpr int K2 = 28 - XW3 - PK, fx = ks < K2 ? XW3 + 2 * ks + (n ? 1 : 0) : XW3 + K2 + ks;
              if constexpr (n == 0 || (n == 3 && ks < K2)) x[fx] = ld16(rs, l16, xo + fx * 1024);
              if constexpr (n >= 5 && ks < 4) st[2 * ks + n - 5] = ld8(rs, sv_ + (2 * ks + n - 5) * 256, so_);
              if constexpr (i == 46) {
                ln_stats(st, mean, rstd);
                nmean = -mean; gs = -LOG2E * rstd; irs = __builtin_amdgcn_rcpf(rstd);
                PIN(nmean); PIN(gs); PIN(irs);
              }
            } else {
              constexpr int r = i - PB, n = r / MK, s = r % MK;
              acc[n] = mfma(a, x[PK + s], acc[n]);
              nx();
              if constexpr (r == 8) Gcg[0] = *(LAS f32x4*)(LD + csb);
              if constexpr (n >= 2 && s + MK < 22) glu(acc[n - 2], IC<n - 2>{}, IC<s + MK>{});
              if constexpr (n >= 1) glu(acc[n - 1], IC<n - 1>{}, IC<s>{});
              if constexpr (n >= 3 && s == 25 - MK) { if constexpr (n == 3) VMCNT(1); else VMCNT(2); signal_cnt(ws, lvo + n - 3); }
              if constexpr (i == 194) f = poll_issue(fp12);
            }
          });
      STAMP(10);
      arrive_lds(LD, 2, lane);
      static_for<22>([&](auto S_) AI {
        if constexpr (S_.value + MK < 22) glu(acc[5], IC<5>{}, IC<S_.value + MK>{});
        glu(acc[6], IC<6>{}, S_);
        if constexpr (S_.value == 10) { VMCNT(3); signal_cnt(ws, lvo + 4); }
      });
      WSTAMP(55);
      VMCNT(3);
      { u32 f2 = f; PIN(f2); wait_flags(ws, fp12, f2); }
      WSTAMP(56);
      f = poll_issue(fp + 96);
      static_for<10>([&](auto T) AI { ldh(T); });
      WSTAMP(57); VMCNT(13); WSTAMP(58); signal_cnt(ws, lvo + 5);
      wait_arr(LD, 4, 4 * L + 4);
      STAMP(11);
      const int wo_ = opqv(kh * 3072 + opqv(l16)), wp_ = opqv(kh * 1024 + 1024 + opqv(l16));
      const int wo2 = opqv(wo_ + 3 * UNIT), wp2 = opqv(wp_ + 3 * UNIT);
      v4u wq[56];
      auto ldw = [&](auto K) AI {
        constexpr int k = K.value, t = k >> 1, wb = p4_wbase(t);
        if constexpr (wb >= 3 * UNIT) wq[k] = lds16(LD, ((k & 1) ? wp2 : wo2) + wb - 3 * UNIT);
        else wq[k] = lds16(LD, ((k & 1) ? wp_ : wo_) + wb);
      };
      static_for<4>([&](auto K) AI { ldw(K); });
      VMCNT(12); WSTAMP(59); signal_cnt(ws, lvo + 6);
      VMCNT(12);
      { u32 f2 = f; PIN(f2); wait_flags(ws, fp + 96, f2); }
      WSTAMP(60); ldh(IC<10>{}); ldh(IC<11>{});
      f32x16 fin = {}, oth = {};
      static_for<28>([&](auto T) AI {
        constexpr int t = T.value;
        if constexpr (t == PT5) f = __hip_atomic_load(fp + 160, __ATOMIC_RELAXED, __HIP_MEMORY_SCOPE_AGENT);
        if constexpr (t == 12) {
          STAMP(12);
          arrive_lds(LD, 3, lane);
          wait_flags(ws, fp + 160, f);
          f = __hip_atomic_load(fp + 192, __ATOMIC_RELAXED, __HIP_MEMORY_SCOPE_AGENT);
          static_for<8>([&](auto K) AI { ldh(IC<12 + K.value>{}); });
          STAMP(13);
          wait_flags(ws, fp + 192, f);
          static_for<8>([&](auto K) AI { ldh(IC<20 + K.value>{}); });
          STAMP(22);
        }
        if constexpr (t + 2 < 28) { ldw(IC<2 * t + 4>{}); ldw(IC<2 * t + 5>{}); }
        oth = mfma(wq[2 * t + 1], hx[t], oth);
        fin = mfma(wq[2 * t], hx[t], fin);
        SB();
      });
      STAMP(14);
      wait_arr(LD, 3, 4 * L + 4);
      put16(oth, LD, opqv(L_SCR + w * 4096 + opqv(l16)));
      BAR();
#pragma unroll
      for (int c = 0; c < 4; ++c) add16(fin, LD, opqv(p4_part(w, c) + opqv(l16)));
      add16(fin, LD, opqv(L_SCR + (w ^ 2) * 4096 + opqv(l16)));
      STAMP(15);
      finish_tile(fin, res, rs, OFF_X2, OFF_ST2, tt2, cb, kh, lane, L == 1, A.out);
    }
    if (L == 0) {
      wave_signal(ws, C_X2 + tt2, lane);
      arrive_lds(LD, 0, lane);
      STAMP(16);
      wait_cnt(ws, C_X2 + tt, 16, true);
    }
    STAMP(17);
  };
  clayer(IC<0>{});
  clayer(IC<1>{});
}

extern "C" void kernel_launch(void* const* d_in, const int* in_sizes, int n_in, void* d_out, int out_size, void* d_ws,
                              size_t ws_size, hipStream_t stream) {
  Args a;
  a.x0 = (const u16*)d_in[0]; a.wqkv = (const u16*)d_in[1]; a.wphi = (const u16*)d_in[2]; a.wo = (const u16*)d_in[3];
  a.wg = (const u16*)d_in[5]; a.wu = (const u16*)d_in[6]; a.wd = (const u16*)d_in[7];
  a.ws = (char*)d_ws; a.out = (u16*)d_out;
  (void)hipFuncSetAttribute((const void*)k_main, hipFuncAttributeMaxDynamicSharedMemorySize, LDS_BYTES);
  k_main<<<256, 512, LDS_BYTES, stream>>>(a);
}
```

```cpp
#include <hip/hip_runtime.h>
#include <stdint.h>
#include <utility>
#include <type_traits>

typedef unsigned short u16;
typedef unsigned int u32;
typedef u32 v4u __attribute__((ext_vector_type(4)));
typedef u32 v2u __attribute__((ext_vector_type(2)));
typedef float f32x2 __attribute__((ext_vector_type(2)));
typedef float f32x4 __attribute__((ext_vector_type(4)));
typedef float f32x16 __attribute__((ext_vector_type(16)));
typedef __bf16 bf16x2 __attribute__((ext_vector_type(2)));
typedef __bf16 bf16x8 __attribute__((ext_vector_type(8)));
typedef short s16x4 __attribute__((ext_vector_type(4)));
#define LAS __attribute__((address_space(3)))
#define DI __device__ __forceinline__
#define AI __attribute__((always_inline))
template <int V> using IC = std::integral_constant<int, V>;

constexpr int S = 2048, D = 448, FF = 1792;
constexpr int UNIT = 28672;

constexpr int C_KV = 0;
constexpr int C_W2 = 32;
constexpr int C_W4 = 36;
constexpr int C_W3 = 40;
constexpr int C_ERR = 60;
constexpr int C_X2 = 64;
constexpr int C_X1 = 128;
constexpr int C_O = 256;
constexpr int C_H = 512;
constexpr int C_W1 = 1536;
constexpr int NZ = 25;
constexpr int EPOCH_OFF = NZ * 64 * 128;
constexpr int INIT_OFF = EPOCH_OFF + 256;
constexpr int CTL_BYTES = EPOCH_OFF + 4096;
constexpr int W2B = 65536;
constexpr int OFF_W1 = CTL_BYTES;
constexpr int OFF_W2 = OFF_W1 + 16 * 3 * UNIT;
constexpr int OFF_W3 = OFF_W2 + 8 * W2B;
constexpr int OFF_W4 = OFF_W3 + 16 * 7 * UNIT;
constexpr int OFF_CS3 = OFF_W4 + 8 * 8 * UNIT;
constexpr int XB = S * D * 2;
constexpr int OB = S * 512 * 2;
constexpr int OFF_X2 = OFF_CS3 + 16 * 28 * 896;
constexpr int OFF_X1 = OFF_X2 + XB;
constexpr int OFF_O = OFF_X1 + 2 * XB;
constexpr int STB = S * 16 * 8;
constexpr int OFF_ST2 = OFF_O + 2 * OB;
constexpr int OFF_ST1 = OFF_ST2 + STB;
constexpr int KVB = 16 * 16 * 4096;
constexpr int OFF_KVP = OFF_ST1 + 2 * STB;
constexpr int HB = S * FF * 2;
constexpr int OFF_H = OFF_KVP + 2 * KVB;
constexpr int WS_USED = OFF_H + 2 * HB;

constexpr int L_SCR = 4 * UNIT;
constexpr int PHK_STRIDE = 136, V_STRIDE = 72;
constexpr int L_PHK = L_SCR;
constexpr int L_V = L_SCR + 128 * PHK_STRIDE;
constexpr int L_KVF = L_SCR + 28672;
constexpr int L_W2X = L_KVF + 4096;
constexpr int L_CONST = 155648;
constexpr int L_CS1 = L_CONST, L_CS3 = L_CONST + 384, L_WPHI = L_CONST + 1280, L_ARR = L_CONST + 3328;
constexpr int L_STAGE = 2 * UNIT;
constexpr int STG = 8704;
constexpr int LDS_BYTES = 163840;

constexpr float LOG2E = 1.4426950408889634f;
#define PKV 12
constexpr int PK = PKV, MK = 28 - PK;
constexpr int LAG = 4;

DI u32 pk2(float a, float b) { f32x2 v = {a, b}; return __builtin_bit_cast(u32, __builtin_convertvector(v, bf16x2)); }
DI float bflo(u32 v) { return __uint_as_float(v << 16); }
DI float bfhi(u32 v) { return __uint_as_float(v & 0xffff0000u); }
DI f32x16 mfma(v4u a, v4u b, f32x16 c) {
  f32x16 d = __builtin_amdgcn_mfma_f32_32x32x16_bf16(__builtin_bit_cast(bf16x8, a), __builtin_bit_cast(bf16x8, b), c, 0, 0, 0);
  asm volatile("" : "+v"(d));
  __builtin_amdgcn_sched_barrier(0);
  return d;
}
DI float xsum(float v) {
  auto r = __builtin_amdgcn_permlane32_swap(__float_as_uint(v), __float_as_uint(v), false, false);
  return __uint_as_float(r[0]) + __uint_as_float(r[1]);
}
DI float xmax(float v) {
  auto r = __builtin_amdgcn_permlane32_swap(__float_as_uint(v), __float_as_uint(v), false, false);
  return fmaxf(__uint_as_float(r[0]), __uint_as_float(r[1]));
}
DI float xmin(float v) {
  auto r = __builtin_amdgcn_permlane32_swap(__float_as_uint(v), __float_as_uint(v), false, false);
  return fminf(__uint_as_float(r[0]), __uint_as_float(r[1]));
}
#define BAR() asm volatile("s_waitcnt lgkmcnt(0)\n\ts_barrier" ::: "memory")
#define RAWBAR() asm volatile("s_barrier" ::: "memory")
#define VMCNT(n) asm volatile("s_waitcnt vmcnt(" #n ")" ::: "memory")
#define SB() __builtin_amdgcn_sched_barrier(0)
#define PIN(x) asm volatile("" : "+v"(x))

typedef __amdgpu_buffer_rsrc_t rsrc_t;
DI v4u ld16(rsrc_t rs, int voff, int soff) { return __builtin_amdgcn_raw_buffer_load_b128(rs, voff, soff, 16); }
DI v2u ld8(rsrc_t rs, int voff, int soff) { return __builtin_amdgcn_raw_buffer_load_b64(rs, voff, soff, 16); }
DI u32 ld4(rsrc_t rs, int voff, int soff) { return __builtin_amdgcn_raw_buffer_load_b32(rs, voff, soff, 16); }
DI void st16(rsrc_t rs, int voff, int soff, v4u v) {
  __builtin_amdgcn_raw_buffer_store_b128(v, rs, voff, soff, 16);
  asm volatile("s_nop 1" ::"v"(v) : "memory");
}
DI void st8(rsrc_t rs, int voff, int soff, v2u v) { __builtin_amdgcn_raw_buffer_store_b64(v, rs, voff, soff, 16); }
DI void st4(rsrc_t rs, int voff, int soff, u32 v) { __builtin_amdgcn_raw_buffer_store_b32(v, rs, voff, soff, 16); }
DI int opq(int v) { asm volatile("" : "+s"(v)); return v; }
DI int opqv(int v) { asm volatile("" : "+v"(v)); return v; }
DI v4u lds16(LAS char* l, int off) { return *(LAS v4u*)(l + off); }
DI float dot2(u32 a, u32 b, float c) {
  return __builtin_amdgcn_fdot2_f32_bf16(__builtin_bit_cast(bf16x2, a), __builtin_bit_cast(bf16x2, b), c, false);
}
DI v4u tr_frag(LAS char* L, int base, int step) {
  v2u lo = __builtin_bit_cast(v2u, __builtin_amdgcn_ds_read_tr16_b64_v4i16((LAS s16x4*)(L + base)));
  v2u hi = __builtin_bit_cast(v2u, __builtin_amdgcn_ds_read_tr16_b64_v4i16((LAS s16x4*)(L + base + step)));
  return v4u{lo.x, lo.y, hi.x, hi.y};
}
DI float sum8(v4u o) {
  const u32 one = 0x3f803f80u;
  return dot2(o.x, one, dot2(o.y, one, 0.f)) + dot2(o.z, one, dot2(o.w, one, 0.f));
}

DI u32* cnt_ptr(char* ws, int idx) { return (u32*)(ws + idx * 128); }
#define POLL_D0 16
#define LATE_ARR
#define WOV 0
#define WOS 20
#define H2 6
#define H3 12
#define CP1 12
#define CP5 0
#define CT5 11
#define PT5 8
#define POLL_IV 1
DI bool spin_more(char* ws, u32& spins) {
  u32* e = cnt_ptr(ws, C_ERR);
  __builtin_amdgcn_s_sleep(POLL_IV);
  ++spins;
  if ((spins & 255u) == 0u) {
    if (__builtin_amdgcn_readfirstlane(__hip_atomic_load(e, __ATOMIC_RELAXED, __HIP_MEMORY_SCOPE_AGENT)) != 0) return false;
    if (spins > 400000u) { __hip_atomic_store(e, 1u, __ATOMIC_RELAXED, __HIP_MEMORY_SCOPE_AGENT); return false; }
  }
  return true;
}
DI void wait_cnt(char* ws, int idx, u32 target, bool delay = false) {
  u32* c = cnt_ptr(ws, idx);
  u32 spins = 0;
  if (delay && POLL_D0) __builtin_amdgcn_s_sleep(POLL_D0);
  while ((u32)__builtin_amdgcn_readfirstlane(__hip_atomic_load(c, __ATOMIC_RELAXED, __HIP_MEMORY_SCOPE_AGENT)) < target)
    if (!spin_more(ws, spins)) break;
  __builtin_amdgcn_fence(__ATOMIC_ACQUIRE, "wavefront");
  asm volatile("" ::: "memory");
}
DI void wait_flags(char* ws, u32* fp, u32 f, u32 target = 16u) {
  if (__builtin_expect(!__all(f >= target), 0)) {
    u32 spins = 0;
    while (spin_more(ws, spins))
      if (__all(__hip_atomic_load(fp, __ATOMIC_RELAXED, __HIP_MEMORY_SCOPE_AGENT) >= target)) break;
  }
  __builtin_amdgcn_fence(__ATOMIC_ACQUIRE, "wavefront");
  asm volatile("" ::: "memory");
}
DI u32 poll_issue(const u32* p) { u32 f; asm volatile("global_load_dword %0, %1, off sc1" : "=v"(f) : "v"(p) : "memory"); return f; }
DI void signal_cnt(char* ws, int idx) {
  unsigned long long sv;
  asm volatile("s_mov_b64 %0, exec\n\ts_mov_b64 exec, 1\n\tglobal_atomic_add %1, %2, %3\n\ts_mov_b64 exec, %0"
               : "=&s"(sv) : "v"(idx * 128), "v"(1), "s"(ws) : "memory");
}
DI void wave_signal(char* ws, int idx, int lane) { VMCNT(0); signal_cnt(ws, idx); }
DI void arrive_lds(LAS char* LD, int slot, int lane) {
  if (lane == 0) __hip_atomic_fetch_add((LAS u32*)(LD + L_ARR + slot * 4), 1u, __ATOMIC_RELAXED, __HIP_MEMORY_SCOPE_WORKGROUP);
}
DI void wait_arr(LAS char* LD, int slot, u32 target) {
  while ((u32)__builtin_amdgcn_readfirstlane(__hip_atomic_load((LAS u32*)(LD + L_ARR + slot * 4), __ATOMIC_RELAXED, __HIP_MEMORY_SCOPE_WORKGROUP)) < target)
    __builtin_amdgcn_s_sleep(1);
}

#define STAMP(i)
#define HSTAMP(i)
#define VSTAMP(i)
#define WSTAMP(i)
#define USTAMP(i)

#define DMA_STEP(GSTEP) \
      "s_add_u32 m0, m0, 0x1000\n\t" \
      "v_add_u32 %1, " GSTEP ", %1\n\t" \
      "global_load_lds_dwordx4 %1, %2 sc1\n\t"
#define DMA_FN(NAME, BODY) \
DI void NAME(const char* gsrc, u32 voff, u32 lds_dst) { \
  u32 keep; \
  asm volatile("s_mov_b32 %0, m0\n\ts_mov_b32 m0, %3\n\ts_nop 0\n\tglobal_load_lds_dwordx4 %1, %2 sc1\n\t" BODY "s_mov_b32 m0, %0" \
               : "=&s"(keep), "+v"(voff) : "s"(gsrc), "s"(lds_dst) : "memory", "scc"); }
DMA_FN(dma7, DMA_STEP("0x1000") DMA_STEP("0x1000") DMA_STEP("0x1000") DMA_STEP("0x1000") DMA_STEP("0x1000") DMA_STEP("0x1000"))
DMA_FN(dma2, DMA_STEP("0x1000"))
#define PACE 1
#define STR2(x) #x
#define STR(x) STR2(x)
#define PSTEP "s_sleep " STR(PACE) "\n\t" DMA_STEP("0x1000")
DMA_FN(dma7p, PSTEP PSTEP PSTEP PSTEP PSTEP PSTEP)
DMA_FN(dma7n, DMA_STEP("0x2a000") DMA_STEP("0x2a000") DMA_STEP("0x2a000") DMA_STEP("0x2a000") DMA_STEP("0x2a000") DMA_STEP("0x2a000"))

template <int LO, int GO>
DI void dma1(const char* gsrc, u32 voff, u32 lb) {
  u32 keep, tmp;
  asm volatile("s_mov_b32 %0, m0\n\ts_add_u32 m0, %4, %5\n\tv_add_u32 %1, %6, %2\n\tglobal_load_lds_dwordx4 %1, %3 sc1\n\ts_mov_b32 m0, %0"
               : "=&s"(keep), "=&v"(tmp) : "v"(voff), "s"(gsrc), "s"(lb), "i"(LO), "i"(GO) : "memory", "scc");
}

template <typename F, int... I>
DI void static_for_impl(F&& f, std::integer_sequence<int, I...>) { (f(IC<I>{}), ...); }
template <int N, typename F>
DI void static_for(F&& f) { static_for_impl(f, std::make_integer_sequence<int, N>{}); }

constexpr int PF = 6;
#define WEV 2
template <int N, int RPS = 1, typename RD, typename PRE, typename ST>
DI void pipe(RD rd, PRE pre, ST step) {
  v4u buf[PF];
  static_for<PF>([&](auto I) AI { buf[I.value] = rd(I); });
  SB();
  static_for<N>([&](auto I) AI {
    constexpr int i = I.value;
    pre(I);
    if constexpr (WEV > 1 && i % WEV == 0 && i + PF <= N) __builtin_amdgcn_s_waitcnt(0xC07F | (((PF - WEV) * RPS) << 8));
    const v4u a = buf[i % PF];
    step(I, a, [&]() AI { if constexpr (i + PF < N) buf[i % PF] = rd(IC<i + PF>{}); SB(); });
    SB();
  });
}

DI void ln_finish(float s, float q, float& mean, float& rstd) {
  s = xsum(s); q = xsum(q);
  mean = s * (1.f / 448.f);
  const float var = fmaxf(q * (1.f / 448.f) - mean * mean, 0.f);
  rstd = __builtin_amdgcn_rsqf(var + 1e-5f);
}
DI void ln_stats(const v2u (&st)[8], float& mean, float& rstd) {
  f32x2 a = __builtin_bit_cast(f32x2, st[0]), b = __builtin_bit_cast(f32x2, st[1]);
#pragma unroll
  for (int i = 2; i < 8; i += 2) { a += __builtin_bit_cast(f32x2, st[i]); b += __builtin_bit_cast(f32x2, st[i + 1]); }
  a += b;
  ln_finish(a[0], a[1], mean, rstd);
}
DI v4u pack8(const f32x16& a, int s) {
  v4u r = {pk2(a[8 * s], a[8 * s + 1]), pk2(a[8 * s + 2], a[8 * s + 3]), pk2(a[8 * s + 4], a[8 * s + 5]), pk2(a[8 * s + 6], a[8 * s + 7])};
  return r;
}
template <int SG>
DI void half_softmax(const f32x16& p, int h, float rstd, v4u& o0, v4u& o1) {
  auto mm = [](float a, float b) AI { return SG ? fminf(a, b) : fmaxf(a, b); };
  float m0 = mm(mm(p[0], p[1]), p[2]), m1 = mm(mm(p[3], p[4]), p[5]), m2 = mm(mm(p[6], p[7]), p[8]), m3 = mm(mm(p[9], p[10]), p[11]);
  float m4 = mm(mm(mm(p[12], p[13]), p[14]), p[15]);
  m4 = h ? p[0] : m4;
  float mx = mm(mm(mm(m0, m1), m2), mm(m3, m4));
  mx = SG ? xmin(mx) : xmax(mx);
  const float c = rstd * LOG2E;
  const float cc = SG ? -c : c, off = -mx * cc;
  f32x16 e;
  float s0 = 0.f, s1 = 0.f;
#pragma unroll
  for (int r = 0; r < 16; ++r) {
    float v = __builtin_amdgcn_exp2f(fmaf(p[r], cc, off));
    if (r >= 12) v = h ? 0.f : v;
    e[r] = v;
    if (r & 1) s1 += v; else s0 += v;
  }
  const float rs_ = __builtin_amdgcn_rcpf(xsum(s0 + s1));
#pragma unroll
  for (int r = 0; r < 16; ++r) e[r] *= rs_;
  o0 = pack8(e, 0); o1 = pack8(e, 1);
  PIN(o0); PIN(o1);
  SB();
}

DI void finish_tile(const f32x16& acc, v4u (&res)[2], rsrc_t rs, int xoff, int stoff, int tt, int cb, int nt, int lane, bool last, u16* out) {
  lane = opqv(lane);
  const int h = lane >> 5, tok = lane & 31;
#pragma unroll
  for (int ro = 0; ro < 2; ++ro) {
    v4u r = res[ro];
    v4u o;
    o.x = pk2(bflo(r.x) + acc[8 * ro + 0], bfhi(r.x) + acc[8 * ro + 1]);
    o.y = pk2(bflo(r.y) + acc[8 * ro + 2], bfhi(r.y) + acc[8 * ro + 3]);
    o.z = pk2(bflo(r.z) + acc[8 * ro + 4], bfhi(r.z) + acc[8 * ro + 5]);
    o.w = pk2(bflo(r.w) + acc[8 * ro + 6], bfhi(r.w) + acc[8 * ro + 7]);
    res[ro] = o;
    const int oi = nt * 4 + ro * 2 + h;
    if (oi < 7) {
      const int go = 7 * cb + oi;
      if (last) *(v4u*)(out + (size_t)(tt * 32 + tok) * D + go * 8) = o;
      else st16(rs, ((go >> 1) * 64 + tok + 32 * (go & 1)) * 16, xoff + tt * 28 * 1024, o);
    }
  }
  if (!last) {
    const bool v1 = !(nt == 1 && h == 1);
    const u32 one = 0x3f803f80u;
    float s0 = dot2(res[0].x, one, 0.f), s1 = dot2(res[1].x, one, 0.f), q0 = dot2(res[0].x, res[0].x, 0.f), q1 = dot2(res[1].x, res[1].x, 0.f);
    s0 = dot2(res[0].y, one, s0); s1 = dot2(res[1].y, one, s1); q0 = dot2(res[0].y, res[0].y, q0); q1 = dot2(res[1].y, res[1].y, q1);
    s0 = dot2(res[0].z, one, s0); s1 = dot2(res[1].z, one, s1); q0 = dot2(res[0].z, res[0].z, q0); q1 = dot2(res[1].z, res[1].z, q1);
    s0 = dot2(res[0].w, one, s0); s1 = dot2(res[1].w, one, s1); q0 = dot2(res[0].w, res[0].w, q0); q1 = dot2(res[1].w, res[1].w, q1);
    const float sum = xsum(s0 + (v1 ? s1 : 0.f));
    const float sq = xsum(q0 + (v1 ? q1 : 0.f));
    if (h == 0) st8(rs, tok * 8, stoff + ((tt * 8 + cb) * 2 + nt) * 256, v2u{__float_as_uint(sum), __float_as_uint(sq)});
  }
}

DI void kv_reduce(rsrc_t rs, LAS char* LD, int L, int j, int w, int lane) {
  const int ko = opq(OFF_KVP + L * KVB + j * 16 * 4096 + (w >> 1) * 1024 + (w & 1) * 512);
  const int lz = opqv(lane);
  const int vo = (lz >> 5) * 4096 + (lz & 31) * 16;
  v4u t[8];
#pragma unroll
  for (int i = 0; i < 8; ++i) t[i] = ld16(rs, vo, ko + i * 8192);
  float s[8] = {};
  const u32 lo1 = (u32)opq(0x00003f80), hi1 = (u32)opq(0x3f800000);
#pragma unroll
  for (int i = 0; i < 8; ++i) {
#pragma unroll
    for (int c = 0; c < 4; ++c) { s[2 * c] = dot2(t[i][c], lo1, s[2 * c]); s[2 * c + 1] = dot2(t[i][c], hi1, s[2 * c + 1]); }
  }
#pragma unroll
  for (int c = 0; c < 8; ++c) s[c] = xsum(s[c]);
  if (lz < 32) *(LAS v4u*)(LD + L_KVF + (w >> 1) * 1024 + (w & 1) * 512 + lz * 16) = v4u{pk2(s[0], s[1]), pk2(s[2], s[3]), pk2(s[4], s[5]), pk2(s[6], s[7])};
}

constexpr int p4_oh(int t) { return 4 * t; }
constexpr int p4_oc(int t) { return 56 + 2 * t; }
constexpr int p4_koff(int o) { return (7 * (o & 15) + (o >> 4)) * 1024; }
constexpr int p4_wbase(int t) { return (t < 7 ? 0 : t < 14 ? L_SCR : t < 21 ? 3 * UNIT : UNIT) + (t % 7) * 4096; }
DI int p4_part(int a, int hw) {
  const int i = ((a >> 1) | ((a & 1) << 1)) * 4 + hw;
  return i < 7 ? 2 * UNIT + i * 4096 : i < 10 ? L_KVF + (i - 7) * 4096 : (i - 10) * 4096;
}
DI void put16(const f32x16& acc, LAS char* LD, int off) {
#pragma unroll
  for (int qd = 0; qd < 4; ++qd) *(LAS f32x4*)(LD + off + qd * 1024) = f32x4{acc[4 * qd], acc[4 * qd + 1], acc[4 * qd + 2], acc[4 * qd + 3]};
}
DI void add16(f32x16& acc, LAS char* LD, int off) {
#pragma unroll
  for (int qd = 0; qd < 4; ++qd) {
    const f32x4 v = *(LAS f32x4*)(LD + off + qd * 1024);
#pragma unroll
    for (int i = 0; i < 4; ++i) acc[4 * qd + i] += v[i];
  }
}

struct Args {
  const u16* x0; const u16* wqkv; const u16* wphi; const u16* wo; const u16* wg; const u16* wu; const u16* wd;
  char* ws; u16* out;
};

struct Retile { const v4u* pa[7]; int rd0, rd1, step, o0, o1, ocb, sig; };
DI void retile_prep(const Args& A, int job, int lane, int stage, Retile& R) {
  const int h = lane >> 5, q = (lane & 15) >> 2, c8 = ((lane >> 4) & 1) * 4 + (lane & 3);
  const int hh = c8 & 1, quad = c8 >> 1;
  if (job < 64 || job >= 512) {
    const bool iswo = job < 64;
    const int a0 = iswo ? job >> 2 : (job - 512) >> 2, cg = job & 3;
    const u16* src = (iswo ? A.wo + (size_t)a0 * 28 * D : A.wd + (size_t)a0 * 32 * D) + 112 * cg;
    const int nrow = iswo ? 28 : 32;
#pragma unroll
    for (int i = 0; i < 7; ++i) {
      const int p = lane + 64 * i, r = p / 14, c = p % 14;
      R.pa[i] = (const v4u*)(src + (size_t)(r < nrow ? r : 0) * D + c * 8);
    }
    const int rb = stage + ((iswo ? 4 : 8) * h + q) * 224 + 8 * (quad & 1);
    const int oi = (quad >> 1) * 2 + hh;
    R.rd0 = rb + 16 * oi; R.rd1 = rb + 16 * min(4 + oi, 6); R.step = (iswo ? 8 : 4) * 224;
    int ob[2];
#pragma unroll
    for (int k2 = 0; k2 < 2; ++k2) {
      const int ks = a0 * 2 + k2, o_ = 16 * (ks % 7) + ks / 7;
      int fr;
      if (o_ < 56) fr = 112 + ((o_ & 3) * 14 + (o_ >> 2)) * 2;
      else fr = (o_ - 56) * 2;
      ob[k2] = iswo ? OFF_W2 + cg * 2 * W2B + ((ks & 15) * 2 + (ks >> 4)) * 2048 : OFF_W4 + cg * 16 * UNIT + fr * 1024;
    }
    R.o0 = opq(ob[0]); R.o1 = opq(ob[1]); R.ocb = opq(iswo ? W2B : 8 * UNIT); R.sig = opq((iswo ? C_W2 : C_W4) + cg);
  } else {
    const int a = job - 64, j = a / 28, ks = a % 28;
#pragma unroll
    for (int i = 0; i < 7; ++i) {
      const int p = lane + 64 * i, r = p / 28, c = p % 28;
      R.pa[i] = (const v4u*)((c < 14 ? A.wg : A.wu) + (size_t)(ks * 16 + r) * FF + 112 * j + (c < 14 ? c : c - 14) * 8);
    }
    R.rd0 = stage + (8 * h + q) * 448 + (quad & 1) * 224 + (8 * hh + 4 * (quad >> 1)) * 2; R.rd1 = 0; R.step = 0;
    R.o0 = opq(OFF_W3 + j * 7 * UNIT + (ks < PK ? ks * 7 : 6 * PK + ks) * 1024); R.o1 = opq(ks < PK ? 1024 : MK * 1024);
    R.ocb = opq(OFF_CS3 + (j * 28 + ks) * 896); R.sig = opq(C_W3 + j);
  }
#pragma unroll
  for (int i = 0; i < 7; ++i) PIN(R.pa[i]);
  PIN(R.rd0); PIN(R.rd1);
}
DI void retile_load(const Retile& R, u32 lds_dst) {
#pragma unroll
  for (int i = 0; i < 7; ++i) {
    u32 keep;
    asm volatile("s_mov_b32 %0, m0\n\ts_add_u32 m0, %2, %3\n\ts_nop 0\n\tglobal_load_lds_dwordx4 %1, off\n\ts_mov_b32 m0, %0"
                 : "=&s"(keep) : "v"(R.pa[i]), "s"(lds_dst), "i"(i * 1024) : "memory", "scc");
  }
}
DI void retile_store(int job, const Retile& R, rsrc_t rs, LAS char* LD, int lane) {
  const int h = lane >> 5;
  if (job < 64 || job >= 512) {
    const bool pad = (lane & 0x14) == 0x14;
    const bool kpad = job < 64 && h;
    v4u o[8];
#pragma unroll
    for (int i = 0; i < 8; ++i) {
      const int ad = ((i & 2) ? R.rd1 : R.rd0) + (i >> 2) * 112 + (i & 1) * 16 * 224;
      const v2u lo = __builtin_bit_cast(v2u, __builtin_amdgcn_ds_read_tr16_b64_v4i16((LAS s16x4*)(LD + ad)));
      const v2u hi = __builtin_bit_cast(v2u, __builtin_amdgcn_ds_read_tr16_b64_v4i16((LAS s16x4*)(LD + ad + R.step)));
      o[i] = v4u{lo.x, lo.y, hi.x, hi.y};
    }
#pragma unroll
    for (int i = 0; i < 8; ++i) {
      if ((i & 2) && pad) o[i] = v4u{0u, 0u, 0u, 0u};
      if ((i & 1) && kpad) { o[i].z = 0u; o[i].w = 0u; }
      st16(rs, lane * 16, ((i & 1) ? R.o1 : R.o0) + (i >> 2) * R.ocb + ((i >> 1) & 1) * 1024, o[i]);
    }
  } else {
    v4u o[7];
#pragma unroll
    for (int n = 0; n < 7; ++n) o[n] = tr_frag(LD, R.rd0 + n * 32, 4 * 448);
    float cs[7];
#pragma unroll
    for (int n = 0; n < 7; ++n) {
      st16(rs, lane * 16, R.o0 + n * R.o1, o[n]);
      cs[n] = xsum(sum8(o[n]));
    }
#pragma unroll
    for (int n2 = 0; n2 < 4; ++n2) {
      const float v = h ? cs[n2 < 3 ? 2 * n2 + 1 : 6] : cs[2 * n2];
      if (n2 < 3 || !h) st4(rs, lane * 4, R.ocb + n2 * 256, __float_as_uint(v));
    }
  }
}

__global__ __launch_bounds__(512, 2) void k_main(const Args A) {
  extern __shared__ __attribute__((aligned(16))) char smem[];
  LAS char* LD = (LAS char*)smem;
  char* ws = A.ws;
  const int b = blockIdx.x, xcd = b & 7, bi = b >> 3;
#define MAPV 1
  const int g = (xcd >> 2) * 8 + (bi >> 2), j = (xcd & 3) * 4 + (bi & 3);
  const int rb = 2 * g + (j >> 3), cb = j & 7;
  const int lane = threadIdx.x & 63, w = __builtin_amdgcn_readfirstlane(threadIdx.x >> 6);
  const int h = lane >> 5, tok = lane & 31;
  const rsrc_t rs = __builtin_amdgcn_make_buffer_rsrc((void*)ws, 0, WS_USED, 0x00020000);
  const u32 lbase = (u32)(size_t)smem;
  const int l16 = lane * 16;
  const int tq_ = (lane & 15) >> 2, tc8 = ((lane >> 4) & 1) * 4 + (lane & 3);
  const int w1lane = (8 * h + tq_) * 64 + min(tc8 + (j & 1), 7) * 8;

  const int hw = w - 4;
  const u32 voffn = (hw * 16 + (lane >> 2)) * (3 * D * 2) + (lane & 3) * 16;
  const char* w1 = (const char*)A.wqkv + 56 * j - (j & 1) * 8;
  unsigned long long ep;
  const u32 lb = lbase + hw * 1024;
#define KP(p) dma1<4 * UNIT + (p) * 4096, 896 + (p) * 0x2a000>(w1, voffn, lb)
#define VP(p) dma1<(p) * 4096, 1792 + (p) * 0x2a000>(w1, voffn, lb)
#define QP(p) dma1<UNIT + (p) * 4096, (p) * 0x2a000>(w1, voffn, lb)
  if (opq(w) >= 4) {
    HSTAMP(0);
    if (hw == 1 && b < NZ) {
      asm volatile("global_load_dwordx2 %0, %1, %2 sc1" : "=v"(ep) : "v"(EPOCH_OFF), "s"(ws) : "memory");
      st4(rs, lane * 128, b * 8192, 0u);
    }
    if (hw == 2) {
      dma1<L_KVF, 0>((const char*)A.wphi + j * 1568, l16, lbase);
      dma1<L_KVF + 1024, 0>((const char*)A.wphi + j * 1568, min(1024 + l16, 1552), lbase);
    }
    KP(0); KP(1); VP(0); KP(2); VP(1); KP(3); VP(2);
    if (hw == 3) {
      unsigned long long pcv;
      asm volatile("s_getpc_b64 %0" : "=s"(pcv));
      const char* pc = (const char*)(pcv & ~255ull);
      dma1<L_W2X, 0>(pc, bi * 1024 + l16, lbase);
      dma1<L_W2X + 1024, 32768>(pc, bi * 1024 + l16, lbase);
      dma1<L_W2X + 2048, 65536>(pc, min(bi * 1024 + l16, 2 * 1024 - 16), lbase);
    }
    HSTAMP(1);
  }

  if (w >= 4) {
    const u32 voff = hw * 1024 + lane * 16;
    const char* w1f = ws + OFF_W1 + j * 3 * UNIT;
    const char* w2 = ws + OFF_W2 + cb * W2B;
    const char* w3 = ws + OFF_W3 + j * 7 * UNIT;
    const char* w4 = ws + OFF_W4 + cb * 8 * UNIT;
#define DMA(src, slot) dma7((src), voff, lbase + (slot) * UNIT + hw * 1024)
#define DMAP(src, slot) dma7p((src), voff, lbase + (slot) * UNIT + hw * 1024)
#define DMAN(which, slot) dma7n(w1 + (which) * 896, voffn, lbase + (slot) * UNIT + hw * 1024)
    v4u wr[28];
    unsigned long long tag = 0;
    auto hlayer = [&](auto LC) AI {
      constexpr int L = LC.value;
      Retile R;
      const int job = hw >= 2 ? b * 2 + hw - 2 : (hw == 0 && b < 224 ? 512 + b : -1);
      float c0 = 0.f, c1 = 0.f, c2 = 0.f, c3 = 0.f;
      auto colpart = [&](int slot, auto K0, auto K1) AI {
        constexpr int k0 = K0.value, n = K1.value - K0.value;
        const int ib = opqv(slot * UNIT + opqv(w1lane));
        v4u fr[n];
#pragma unroll
        for (int ks = 0; ks < n; ++ks) fr[ks] = tr_frag(LD, ib + (k0 + ks) * 1024, 256);
        SB();
        const u32 one = 0x3f803f80u;
#pragma unroll
        for (int ks = 0; ks < n; ++ks) { c0 = dot2(fr[ks].x, one, c0); c1 = dot2(fr[ks].y, one, c1); c2 = dot2(fr[ks].z, one, c2); c3 = dot2(fr[ks].w, one, c3); }
        SB();
      };
      auto colfin = [&](int which) AI {
        const float cs = xsum((c0 + c1) + (c2 + c3));
        if (lane < 32) *(LAS float*)(LD + L_CS1 + which * 128 + lane * 4) = cs;
      };
      v4u csa[7];
      auto export_img = [&](int slot, int which) AI {
        const int ib = opqv(slot * UNIT + opqv(w1lane) + g * 1024);
        const int eo = opq(OFF_W1 + (j * 3 + which) * UNIT + g * 1024);
        st16(rs, l16, eo, tr_frag(LD, ib, 256));
        if (g < 12) st16(rs, l16, eo + 16384, tr_frag(LD, ib + 16384, 256));
      };
      if constexpr (L == 0) {
        if (hw == 1 && lane < 2) *(LAS v4u*)(LD + L_ARR + l16) = v4u{0u, 0u, 0u, 0u};
        HSTAMP(2);
#define VMW(n, n3) if (hw == 3) { VMCNT(n3); } else { VMCNT(n); }
        VMW(4, 7); BAR();
        HSTAMP(3);
        KP(4); VP(3);
        if (hw == 1 && b < NZ) {
          PIN(ep);
          tag = ep + 1ull;
          if (lane == 0) __hip_atomic_store((unsigned long long*)(ws + INIT_OFF + b * 128), tag, __ATOMIC_RELAXED, __HIP_MEMORY_SCOPE_AGENT);
        }
        if (hw == 2) {
          const int wb = opqv(L_KVF + (4 * h + tq_) * 56 + tc8 * 8);
          const v4u f0 = tr_frag(LD, wb, 8 * 56);
          v4u f1 = tr_frag(LD, wb + 16 * 56, 8 * 56);
          if (h) { f1.z = 0u; f1.w = 0u; }
          *(LAS v4u*)(LD + L_WPHI + l16) = f0;
          *(LAS v4u*)(LD + L_WPHI + 1024 + l16) = f1;
        }
        if (hw == 0) colpart(4, IC<0>{}, IC<8>{});
        VMW(4, 7); BAR();
        HSTAMP(4);
        KP(5); VP(4);
        if (job >= 0) retile_prep(A, job, lane, L_STAGE + hw * STG, R);
        if (hw == 1) colpart(0, IC<0>{}, IC<8>{});
        VMW(4, 7); BAR();
        HSTAMP(5);
        KP(6); VP(5); VP(6);
        if (hw == 0) colpart(4, IC<8>{}, IC<16>{});
        if (hw == 1) colpart(0, IC<8>{}, IC<12>{});
        VMCNT(5); BAR();
        HSTAMP(6);
        QP(0); QP(1); QP(2); QP(3);
        if (hw == 0) colpart(4, IC<16>{}, IC<20>{});
        if (hw == 1) colpart(0, IC<12>{}, IC<16>{});
        VMCNT(7); BAR();
        HSTAMP(7);
        QP(4); QP(5); QP(6);
        if (hw == 0) colpart(4, IC<20>{}, IC<24>{});
        if (hw == 1) colpart(0, IC<16>{}, IC<20>{});
        VMCNT(7); BAR();
        HSTAMP(8);
        if (hw == 0) { colpart(4, IC<24>{}, IC<28>{}); colfin(0); }
        if (hw == 1) { colpart(0, IC<20>{}, IC<28>{}); colfin(1); }
        if (hw == 2) export_img(4, 1);
        if (hw == 3) export_img(0, 2);
        HSTAMP(9);
        BAR();
        HSTAMP(10);
        if (job >= 0) retile_load(R, lbase + L_STAGE + hw * STG);
      } else {
        wait_arr(LD, 0, 4);
        DMA(w1f + 2 * UNIT, 0);
        VMCNT(0); BAR();
      }
      {
        const int wb = opqv(L_WPHI + opqv(l16));
        const v4u wf0 = lds16(LD, wb), wf1 = lds16(LD, wb + 1024);
        if (L == 0) HSTAMP(11);
        BAR();
        if (L == 0) HSTAMP(12);
        __builtin_amdgcn_s_setprio(1);
        const int lz = opqv(lane);
        const int xk = opqv(L_W2X + hw * 1024 + lz * 16);
        const v4u kf0 = lds16(LD, xk), kf1 = lds16(LD, xk + 4096);
        const float rstd = *(LAS float*)(LD + L_KVF + hw * 256 + lz * 4);
        f32x16 p = {};
        p = mfma(wf0, kf0, p);
        p = mfma(wf1, kf1, p);
        v4u o0, o1;
        half_softmax<1>(p, h, rstd, o0, o1);
        const int pb = opqv(L_PHK + (hw * 32 + (lz & 31)) * PHK_STRIDE + 8 * (lz >> 5) + 64);
        *(LAS v2u*)(LD + pb) = v2u{o0.x, o0.y}; *(LAS v2u*)(LD + pb + 16) = v2u{o0.z, o0.w};
        *(LAS v2u*)(LD + pb + 32) = v2u{o1.x, o1.y}; *(LAS v2u*)(LD + pb + 48) = v2u{o1.z, o1.w};
        __builtin_amdgcn_s_setprio(0);
      }
      if (L == 0) HSTAMP(13);
      if constexpr (L == 0) {
        if (job >= 0) { VMCNT(7); } else { VMCNT(0); }
      } else VMCNT(0);
      if (L == 0) HSTAMP(14);
      BAR();
      if (hw < 2) {
        __builtin_amdgcn_s_setprio(2);
        f32x16 kp = {};
        const int lz = opqv(lane);
        const int g16 = (lz >> 4) & 1, q = (lz & 15) >> 2, p = lz & 3, hz = lz >> 5;
        const int ab0 = opqv(L_PHK + (8 * hz + q) * PHK_STRIDE + (g16 * 16 + 4 * p) * 2 + hw * 64);
        const int bb0 = opqv(L_V + (8 * hz + q) * V_STRIDE + (g16 * 16 + 4 * p) * 2);
        v4u ap[8], bv[8];
#pragma unroll
        for (int s = 0; s < 8; ++s) { ap[s] = tr_frag(LD, ab0 + 16 * s * PHK_STRIDE, 4 * PHK_STRIDE); bv[s] = tr_frag(LD, bb0 + 16 * s * V_STRIDE, 4 * V_STRIDE); }
        SB();
#pragma unroll
        for (int s = 0; s < 8; ++s) kp = mfma(ap[s], bv[s], kp);
        const int ko = opq(OFF_KVP + L * KVB + (j * 16 + g) * 4096 + hw * 2048);
        st16(rs, l16, ko, pack8(kp, 0));
        st16(rs, l16, ko + 1024, pack8(kp, 1));
        wave_signal(ws, C_KV + L * 16 + j, lane);
        __builtin_amdgcn_s_setprio(0);
      }
      if constexpr (L == 0) {
        HSTAMP(15);
        if (hw == 3) {
          colpart(1, IC<0>{}, IC<28>{}); colfin(2);
          arrive_lds(LD, 1, lane);
        }
        if (hw == 1) { export_img(1, 0); wave_signal(ws, C_W1 + j, lane); }
        if (job >= 0) { VMCNT(0); retile_store(job, R, rs, LD, lane); }
        HSTAMP(16);
        if (job >= 0) wave_signal(ws, R.sig, lane);
        if (hw >= 2) signal_cnt(ws, C_W1 + j);
        HSTAMP(17);
        BAR();
        HSTAMP(18);
        u32* fw = cnt_ptr(ws, (lane & 1) ? C_W3 + j : C_W2 + (cb >> 1));
        kv_reduce(rs, LD, L, j, w, lane);
        HSTAMP(19);
        BAR();
        HSTAMP(20);
        wait_flags(ws, fw, __hip_atomic_load(fw, __ATOMIC_RELAXED, __HIP_MEMORY_SCOPE_AGENT), (lane & 1) ? 28u : 16u);
        HSTAMP(21);
        DMA(w2, 2); DMA(w2 + UNIT, 3); dma2(w2 + 2 * UNIT, voff, lbase + L_W2X + hw * 1024);
#pragma unroll
        for (int i = 0; i < 7; ++i) csa[i] = ld16(rs, min(lane, 55) * 16, OFF_CS3 + (j * 28 + hw * 7 + i) * 896);
        DMA(w3, 0);
        HSTAMP(22);
        VMCNT(14); HSTAMP(23); BAR();
        HSTAMP(24);
        DMA(w3 + UNIT, 1);
      } else {
        DMA(w2, 2); DMA(w2 + UNIT, 3); dma2(w2 + 2 * UNIT, voff, lbase + L_W2X + hw * 1024);
        DMA(w3, 0);
        BAR();
        kv_reduce(rs, LD, L, j, w, lane);
        BAR();
        DMA(w3 + UNIT, 1);
      }
      BAR();
      if constexpr (L == 0) {
        f32x4 c4 = __builtin_bit_cast(f32x4, csa[0]);
#pragma unroll
        for (int i = 1; i < 7; ++i) c4 += __builtin_bit_cast(f32x4, csa[i]);
        if (lane < 56) *(LAS f32x4*)(LD + L_KVF + hw * 896 + l16) = c4;
      }
      DMA(w3 + 2 * UNIT, 2);
      VMCNT(7); BAR();
      const int wo4 = opq(OFF_W4 + cb * 8 * UNIT + 4 * UNIT + hw * UNIT);
#define WR(lo, hi) _Pragma("unroll") for (int i = lo; i < hi; ++i) wr[i] = ld16(rs, l16, wo4 + i * 1024)
      if constexpr (L == 0) {
        const int t4 = min(hw * 64 + lane, 223) * 4;
        *(LAS float*)(LD + L_CS3 + t4) = (*(LAS float*)(LD + L_KVF + t4) + *(LAS float*)(LD + L_KVF + 896 + t4)) +
                                         (*(LAS float*)(LD + L_KVF + 1792 + t4) + *(LAS float*)(LD + L_KVF + 2688 + t4));
        wait_cnt(ws, C_W4 + (cb >> 1), 56);
        wait_cnt(ws, C_W1 + j, 48);
        WR(0, 28);
      }
      DMA(w3 + 3 * UNIT, 3);
      VMCNT(7); BAR();
      DMAP(w3 + 4 * UNIT, 0);
      DMAP(w4 + UNIT, 4);
      VMCNT(14); BAR();
      DMAP(w3 + 5 * UNIT, 1);
      VMCNT(14); BAR();
      DMAP(w3 + 6 * UNIT, 2);
      VMCNT(7); BAR();
      DMAP(w4 + 2 * UNIT, 3);
      VMCNT(7); BAR();
      {
        u32* fp = cnt_ptr(ws, C_H + (L * 64 + 2 * rb + (lane & 1)) * 8);
        const int ho = opq(OFF_H + L * HB + 2 * rb * 112 * 1024 + hw * 7168);
        v4u hx[28];
        auto ldh = [&](auto I) AI { constexpr int i = I.value; hx[i] = ld16(rs, l16, ho + (i & 1) * 112 * 1024 + p4_koff(p4_oh(i >> 1))); };
        auto poll = [&](int lv) AI { return __hip_atomic_load(fp + 32 * lv, __ATOMIC_RELAXED, __HIP_MEMORY_SCOPE_AGENT); };
        DMAP(w4, 0);
        wait_flags(ws, fp, poll(0));
        static_for<8>([&](auto I) AI { ldh(I); });
        BAR();
        u32 f = poll_issue(fp + 32);
        DMA(w4 + 3 * UNIT, 1);
        VMCNT(7); PIN(f);
        wait_flags(ws, fp + 32, f);
        static_for<6>([&](auto I) AI { ldh(IC<8 + I.value>{}); });
        f32x16 acc[4] = {};
        auto ord = [](int i) constexpr { return i < 20 ? i : (i < 24 ? 20 + 2 * (i - 20) : 21 + 2 * (i - 24)); };
        const int pb0 = opqv(p4_part(0, hw) + opqv(l16)), pb2 = opqv(p4_part(2, hw) + opqv(l16));
        static_for<28>([&](auto I) AI {
          constexpr int i = I.value, k = ord(i), t = k >> 1, tq = k & 1;
          if constexpr (i == 0) f = poll(2);
          if constexpr (i == H2) {
            wait_flags(ws, fp + 64, f);
            VMCNT(2); arrive_lds(LD, 4, lane);
          }
          if constexpr (i == H3 - 3) f = poll(3);
          if constexpr (i == H3) wait_flags(ws, fp + 96, f);
          if constexpr (i == 24) wait_arr(LD, 2, 4 * L + 4);
          acc[tq] = mfma(wr[2 * t], hx[k], acc[tq]);
          acc[2 + tq] = mfma(wr[2 * t + 1], hx[k], acc[2 + tq]);
          if constexpr (i < 2) ldh(IC<14 + i>{});
          if constexpr (i >= H2 && i < H2 + 4) { ldh(IC<16 + 2 * (i - H2)>{}); ldh(IC<17 + 2 * (i - H2)>{}); }
          if constexpr (i >= H3 && i < H3 + 2) { ldh(IC<24 + 2 * (i - H3)>{}); ldh(IC<25 + 2 * (i - H3)>{}); }
          if constexpr (i >= 24) {
            constexpr int qd = i - 24;
            *(LAS f32x4*)(LD + pb0 + qd * 1024) = f32x4{acc[0][4 * qd], acc[0][4 * qd + 1], acc[0][4 * qd + 2], acc[0][4 * qd + 3]};
            *(LAS f32x4*)(LD + pb2 + qd * 1024) = f32x4{acc[2][4 * qd], acc[2][4 * qd + 1], acc[2][4 * qd + 2], acc[2][4 * qd + 3]};
          }
          SB();
        });
        wait_arr(LD, 3, 4 * L + 4);
        put16(acc[1], LD, opqv(p4_part(1, hw) + opqv(l16)));
        put16(acc[3], LD, opqv(p4_part(3, hw) + opqv(l16)));
        BAR();
      }
      if (L == 0) { DMA(w1f + UNIT, 3); DMA(w1f, 1); }
    };
    hlayer(IC<0>{});
    hlayer(IC<1>{});
    if (b == 0 && hw == 1 && lane == 0) __hip_atomic_store((unsigned long long*)(ws + EPOCH_OFF), tag, __ATOMIC_RELAXED, __HIP_MEMORY_SCOPE_AGENT);
    return;
  }

  const int tt = 4 * g + w;
  const int tt2 = 2 * rb + (w & 1);
  const int kh = w >> 1;
  const rsrc_t rx0 = __builtin_amdgcn_make_buffer_rsrc((void*)A.x0, 0, XB, 0x00020000);
  v4u res[2];
#define XWV 14
  constexpr int XW = XWV;

  auto clayer = [&](auto LC) AI {
    constexpr int L = LC.value;
    v4u x[28];
    v2u st[8];
    float mean, rstd;
    v4u pq[4];
    STAMP(0);
    auto phase1 = [&](auto FIRST) AI {
      constexpr bool first = FIRST.value;
      const int xo = opq(first ? tt * 32 * (D * 2) : OFF_X2 + tt * 28 * 1024);
      auto ldx = [&](int ks) AI { return ld16(rs, l16, xo + ks * 1024); };
      const int so_ = opq(OFF_ST2 + tt * 4096);
      const int sv_ = (opqv(lane) & 31) * 8 + h * 2048;
      v4u gx[28];
      const int stg = L_STAGE + w * STG;
      const int gv = (lane >> 4) * (D * 2) + (lane & 15) * 16, gv3 = (lane >> 3) * (D * 2) + (lane & 7) * 16;
      const int gl = stg + (lane >> 4) * 272 + (lane & 15) * 16, gl3 = stg + (lane >> 3) * 272 + (lane & 7) * 16;
      const int srd = opqv(stg + (opqv(lane) & 31) * 272 + h * 16);
      auto ldg = [&](int i) AI {
        return i < 24 ? __builtin_amdgcn_raw_buffer_load_b128(rx0, gv, xo + (i & 7) * 4 * (D * 2) + (i >> 3) * 256, 0)
                      : __builtin_amdgcn_raw_buffer_load_b128(rx0, gv3, xo + (i & 7) * 8 * (D * 2) + 768, 0);
      };
      auto stage = [&](int c) AI {
#pragma unroll
        for (int i = 0; i < (c < 3 ? 8 : 4); ++i) *(LAS v4u*)(LD + (c < 3 ? gl + i * 4 * 272 : gl3 + i * 8 * 272)) = gx[8 * c + i];
#pragma unroll
        for (int i = 0; i < (c < 3 ? 8 : 4); ++i) x[8 * c + i] = lds16(LD, srd + i * 32);
      };
      unsigned long long iv = 0;
      unsigned long long* initp = (unsigned long long*)(ws + (w == 3 && lane < NZ ? INIT_OFF + lane * 128 : EPOCH_OFF));
      auto init_ok = [&](unsigned long long v) AI {
        const unsigned long long e = ((unsigned long long)(u32)__builtin_amdgcn_readlane((int)(v >> 32), 63) << 32) | (u32)__builtin_amdgcn_readlane((int)v, 63);
        return __all(lane >= NZ || v == e + 1ull);
      };
      if constexpr (first) {
#pragma unroll
        for (int i = 0; i < 16; ++i) gx[i] = ldg(i);
        stage(0);
      } else {
#pragma unroll
        for (int ks = 0; ks < XW; ++ks) x[ks] = ldx(ks);
      }
      USTAMP(18);
      BAR();
      USTAMP(19);
      f32x16 ak = {}, av = {}, p = {};
      const int sbk = first ? opqv(opqv(w1lane) + 4 * UNIT) : opqv(opqv(l16) + 3 * UNIT), sbv = first ? opqv(w1lane) : opqv(l16);
      const int csb = opqv(L_CS1 + 16 * (opqv(lane) >> 5)), wb = opqv(L_WPHI + opqv(l16));
      const int xk = opqv(L_W2X + w * 1024 + opqv(l16));
      f32x4 cs[4]; v4u kf[2], wf[2];
      float nmean, s1 = 0.f, s2 = 0.f, q1 = 0.f, q2 = 0.f;
      auto isk = [](int i) constexpr { return i < LAG || (i < 56 - LAG && ((i - LAG) & 1) == 0); };
      auto kst = [](int i) constexpr { return i < LAG ? i : (i < 56 - LAG ? ((((i - LAG) & 1) == 0) ? LAG + (i - LAG) / 2 : (i - LAG) / 2) : i - 28); };
      auto rdw = [&](int base, int ks) AI { if constexpr (first) return tr_frag(LD, base + ks * 1024, 256); else return lds16(LD, base + ks * 1024); };
      pipe<56, first ? 2 : 1>([&](auto I) AI { return rdw(isk(I.value) ? sbk : sbv, kst(I.value)); },
               [&](auto I) AI {
                 constexpr int i = I.value;
                 if constexpr (first && (i == 6 || i == 14 || i == 22 || i == 30 || i == 38 || i == 46)) { USTAMP(40 + i / 8 * 2); RAWBAR(); USTAMP(41 + i / 8 * 2); }
                 if constexpr (i == 54) { USTAMP(20); BAR(); USTAMP(21); }
               },
               [&](auto I, v4u a, auto nx) AI {
                 constexpr int i = I.value, ks = kst(i);
                 if constexpr (i == 54) { p = mfma(wf[0], kf[0], p); p = mfma(wf[1], kf[1], p); }
                 if constexpr (isk(i)) ak = mfma(a, x[ks], ak); else av = mfma(a, x[ks], av);
                 nx();
                 if constexpr (!first && isk(i) && ks + XW < 28) x[ks + XW] = ldx(ks + XW);
                 if constexpr (first) {
                   if constexpr (i < 12) gx[16 + i] = ldg(16 + i);
                   if constexpr (isk(i) && ks % 8 == 6) stage(ks / 8 + 1);
                   if constexpr (isk(i)) {
                     const u32 one = 0x3f803f80u;
                     s1 = dot2(x[ks].x, one, s1); q1 = dot2(x[ks].x, x[ks].x, q1); s2 = dot2(x[ks].y, one, s2); q2 = dot2(x[ks].y, x[ks].y, q2);
                     s1 = dot2(x[ks].z, one, s1); q1 = dot2(x[ks].z, x[ks].z, q1); s2 = dot2(x[ks].w, one, s2); q2 = dot2(x[ks].w, x[ks].w, q2);
                   }
                   if constexpr (i == 45) iv = __hip_atomic_load(initp, __ATOMIC_RELAXED, __HIP_MEMORY_SCOPE_AGENT);
                   if constexpr (i == 51) { ln_finish(s1 + s2, q1 + q2, mean, rstd); nmean = -mean; }
                 } else {
                   if constexpr (!isk(i) && ks >= 4 && ks < 12) st[ks - 4] = ld8(rs, sv_ + (ks - 4) * 256, so_);
                   if constexpr (i == 45) { ln_stats(st, mean, rstd); nmean = -mean; }
                 }
                 if constexpr (i >= 46 && i < 50) cs[i - 46] = *(LAS f32x4*)(LD + csb + 32 * (i - 46));
                 if constexpr (i == 50) wf[0] = lds16(LD, wb);
                 if constexpr (i == 51) wf[1] = lds16(LD, wb + 1024);
                 if constexpr (i == 52 || i == 53) {
#pragma unroll
                   for (int qd = 2 * (i - 52); qd < 2 * (i - 52) + 2; ++qd) {
                     const float y0 = fmaf(nmean, cs[qd][0], ak[4 * qd]), y1 = fmaf(nmean, cs[qd][1], ak[4 * qd + 1]);
                     const float y2 = fmaf(nmean, cs[qd][2], ak[4 * qd + 2]), y3 = fmaf(nmean, cs[qd][3], ak[4 * qd + 3]);
                     kf[qd >> 1][2 * (qd & 1)] = pk2(y0, y1);
                     kf[qd >> 1][2 * (qd & 1) + 1] = pk2(y2, y3);
                   }
                   *(LAS v4u*)(LD + xk + (i - 52) * 4096) = kf[i - 52];
                   if constexpr (i == 52) *(LAS float*)(LD + L_KVF + w * 256 + opqv(lane) * 4) = rstd;
                 }
                 if constexpr (i == 55) {
#pragma unroll
                   for (int qd = 0; qd < 4; ++qd) cs[qd] = *(LAS f32x4*)(LD + csb + 128 + 32 * qd);
                 }
               });
      STAMP(1);
      const int lz = opqv(lane);
      const int trow = w * 32 + (lz & 31);
      const int pb = opqv(L_PHK + trow * PHK_STRIDE + 8 * (lz >> 5)), vb = opqv(L_V + trow * V_STRIDE + 8 * (lz >> 5));
      half_softmax<0>(p, h, rstd, pq[0], pq[1]);
#pragma unroll
      for (int s = 0; s < 2; ++s) {
        *(LAS v2u*)(LD + pb + 32 * s) = v2u{pq[s].x, pq[s].y};
        *(LAS v2u*)(LD + pb + 32 * s + 16) = v2u{pq[s].z, pq[s].w};
      }
      SB();
#pragma unroll
      for (int r = 0; r < 16; ++r) av[r] = fmaf(nmean, cs[r >> 2][r & 3], av[r]) * rstd;
      if (h) { av[12] = 1.f; av[13] = 0.f; av[14] = 0.f; av[15] = 0.f; }
#pragma unroll
      for (int qd = 0; qd < 4; ++qd) *(LAS v2u*)(LD + vb + 16 * qd) = v2u{pk2(av[4 * qd], av[4 * qd + 1]), pk2(av[4 * qd + 2], av[4 * qd + 3])};
      if constexpr (first) {
        if (w == 3 && !init_ok(iv)) {
          u32 spins = 0;
          while (!init_ok(__hip_atomic_load(initp, __ATOMIC_RELAXED, __HIP_MEMORY_SCOPE_AGENT))) {
            __builtin_amdgcn_s_sleep(1);
            if (++spins > 400000u) break;
          }
        }
      }
      STAMP(2);
      BAR();
      if constexpr (first) {
#pragma unroll
        for (int ro = 0; ro < 2; ++ro) {
          const int oi = min(kh * 4 + ro * 2 + h, 6);
          res[ro] = __builtin_amdgcn_raw_buffer_load_b128(rx0, tok * (D * 2) + (7 * cb + oi) * 16, tt2 * 32 * (D * 2), 0);
        }
      }
      f32x16 aq = {};
      const int sbq = first ? opqv(opqv(w1lane) + 1 * UNIT) : opqv(opqv(l16) + 1 * UNIT);
      pipe<28, first ? 2 : 1>([&](auto I) AI { return rdw(sbq, I.value); }, [&](auto) AI {},
               [&](auto I, v4u a, auto nx) AI { aq = mfma(a, x[I.value], aq); nx(); });
      if constexpr (first) wait_arr(LD, 1, 1);
#pragma unroll
      for (int qd = 0; qd < 4; ++qd) cs[qd] = *(LAS f32x4*)(LD + csb + 256 + 32 * qd);
#pragma unroll
      for (int r = 0; r < 16; ++r) aq[r] = fmaf(nmean, cs[r >> 2][r & 3], aq[r]);
      f32x16 p2 = {};
      const int wb2 = opqv(L_WPHI + opqv(l16));
      p2 = mfma(lds16(LD, wb2), pack8(aq, 0), p2);
      p2 = mfma(lds16(LD, wb2 + 1024), pack8(aq, 1), p2);
      half_softmax<0>(p2, h, rstd, pq[0], pq[1]);
      half_softmax<1>(p2, h, rstd, pq[2], pq[3]);
    };
    phase1(std::integral_constant<bool, L == 0>{});
    STAMP(3);
    if (w == 0) wait_cnt(ws, C_KV + L * 16 + j, 32);
    BAR();
    STAMP(4);
    kv_reduce(rs, LD, L, j, w, lane);
    BAR();
    STAMP(5);
    {
      f32x16 nm = {};
      const int kb = opqv(L_KVF + opqv(l16));
      v4u kf[4];
#pragma unroll
      for (int s = 0; s < 4; ++s) kf[s] = lds16(LD, kb + s * 1024);
#pragma unroll
      for (int s = 0; s < 4; ++s) nm = mfma(kf[s], pq[s], nm);
      auto r = __builtin_amdgcn_permlane32_swap(__float_as_uint(nm[12]), __float_as_uint(nm[12]), false, false);
      const float den = __uint_as_float(r[1]) + 1e-6f;
      const float rd = __builtin_amdgcn_rcpf(den);
#pragma unroll
      for (int i = 0; i < 16; ++i) nm[i] *= rd;
      const int oo = opq(OFF_O + L * OB + (tt * 32 + 2 * j) * 1024);
      st16(rs, l16, oo, pack8(nm, 0));
      st16(rs, l16, oo + 1024, pack8(nm, 1));
    }
    wave_signal(ws, C_O + (L * 64 + tt) * 2 + (j >> 3), lane);
    STAMP(6);
    wait_cnt(ws, C_O + (L * 64 + tt2) * 2 + kh, 8, true);
    STAMP(7);
    {
      const int xo = opq(OFF_O + L * OB + (tt2 * 32 + 16 * kh) * 1024);
#pragma unroll
      for (int t = 0; t < (XW < 16 ? XW : 16); ++t) x[t] = ld16(rs, l16, xo + t * 1024);
      if (L == 0) RAWBAR();
      f32x16 a0 = {}, a1 = {};
      const int sb2 = opqv(2 * UNIT + kh * 2048 + opqv(l16)), sbx = opqv(L_W2X + kh * 2048 + opqv(l16));
      pipe<32>([&](auto I) AI { constexpr int i = I.value, t = i >> 1; return lds16(LD, (t < 14 ? sb2 + t * 4096 : sbx + (t - 14) * 4096) + (i & 1) * 1024); },
               [&](auto I) AI { if constexpr (I.value == 14) RAWBAR();   },
               [&](auto I, v4u a, auto nx) AI {
                 constexpr int i = I.value;
                 if constexpr (i & 1) a1 = mfma(a, x[i >> 1], a1); else a0 = mfma(a, x[i >> 1], a0);
                 nx();
                 if constexpr (!(i & 1) && (i >> 1) + XW < 16) x[(i >> 1) + XW] = ld16(rs, l16, xo + ((i >> 1) + XW) * 1024);
               });
      const int xb = opqv(L_SCR + w * 4096 + opqv(l16)), xb2 = opqv(L_SCR + (w ^ 2) * 4096 + opqv(l16));
      f32x16 fin;
      if (kh) {
#pragma unroll
        for (int qd = 0; qd < 4; ++qd) *(LAS f32x4*)(LD + xb + qd * 1024) = f32x4{a0[4 * qd], a0[4 * qd + 1], a0[4 * qd + 2], a0[4 * qd + 3]};
        fin = a1;
      } else {
#pragma unroll
        for (int qd = 0; qd < 4; ++qd) *(LAS f32x4*)(LD + xb + qd * 1024) = f32x4{a1[4 * qd], a1[4 * qd + 1], a1[4 * qd + 2], a1[4 * qd + 3]};
        fin = a0;
      }
      BAR();
#pragma unroll
      for (int qd = 0; qd < 4; ++qd) {
        const f32x4 v = *(LAS f32x4*)(LD + xb2 + qd * 1024);
#pragma unroll
        for (int i = 0; i < 4; ++i) fin[4 * qd + i] += v[i];
      }
      finish_tile(fin, res, rs, opq(OFF_X1 + L * XB), opq(OFF_ST1 + L * STB), tt2, cb, kh, lane, false, A.out);
    }
    wave_signal(ws, C_X1 + L * 64 + tt2, lane);
    STAMP(8);
    wait_cnt(ws, C_X1 + L * 64 + tt, 16, true);
    STAMP(9);
    {
      const int xo = opq(OFF_X1 + L * XB + tt * 28 * 1024);
      const int so_ = opq(OFF_ST1 + L * STB + tt * 4096);
      const int sv_ = (opqv(lane) & 31) * 8 + h * 2048;
      constexpr int XW3 = 8;
#pragma unroll
      for (int ks = 0; ks < XW3; ++ks) x[ks] = ld16(rs, l16, xo + ks * 1024);
      const int ho = opq(OFF_H + L * HB + (tt * 112 + 7 * j) * 1024);
      const int lvo = opq(C_H + (L * 64 + tt) * 8);
      u32* fp = cnt_ptr(ws, C_H + (L * 64 + tt2) * 8);
      auto poll = [&](int lv) AI { return __hip_atomic_load(fp + 32 * lv, __ATOMIC_RELAXED, __HIP_MEMORY_SCOPE_AGENT); };
      u32 f;
      float nmean, gs, irs;
      const int csb = opqv(L_CS3 + 16 * (opqv(lane) >> 5));
      const int sbA = opqv(l16), sbB = opqv(opqv(l16) + 2 * UNIT);
      f32x4 Gcg[2], Gcu[2]; float Gt[8], Gyu[8]; u32 Go[4];
      auto glu = [&](const f32x16& a, auto N_, auto S_) AI {
        constexpr int n = N_.value, s = S_.value;
        if constexpr (s == 1) Gcu[0] = *(LAS f32x4*)(LD + csb + n * 128 + 32);
        if constexpr (s == 4) Gcg[1] = *(LAS f32x4*)(LD + csb + n * 128 + 64);
        if constexpr (s == 9) Gcu[1] = *(LAS f32x4*)(LD + csb + n * 128 + 96);
        if constexpr (s == 12 && n < 6) Gcg[0] = *(LAS f32x4*)(LD + csb + (n + 1) * 128);
        static_for<8>([&](auto V_) AI {
          constexpr int v = V_.value, k = s - (1 + 4 * (v >> 1) + (v & 1)), hf = v >> 2, i = v & 3;
          if constexpr (k == 0) { Gt[v] = fmaf(nmean, Gcg[hf][i], a[8 * hf + i]); PIN(Gt[v]); }
          if constexpr (k == 1) { Gt[v] = Gt[v] * gs; PIN(Gt[v]); }
          if constexpr (k == 2) { Gt[v] = __builtin_amdgcn_exp2f(Gt[v]); PIN(Gt[v]); }
          if constexpr (k == 3) { Gt[v] = fmaf(Gt[v], irs, irs); PIN(Gt[v]); }
          if constexpr (k == 4) { Gt[v] = __builtin_amdgcn_rcpf(Gt[v]); PIN(Gt[v]); }
          if constexpr (k == 5) { Gyu[v] = fmaf(nmean, Gcu[hf][i], a[8 * hf + 4 + i]); PIN(Gyu[v]); }
          if constexpr (k == 6) { Gt[v] = Gt[v] * Gyu[v]; PIN(Gt[v]); }
          if constexpr (k == 7 && (v & 1)) { Go[v >> 1] = pk2(Gt[v - 1], Gt[v]); PIN(Go[v >> 1]); }
        });
        if constexpr (s == 21) st16(rs, l16, ho + n * 1024, v4u{Go[0], Go[1], Go[2], Go[3]});
      };
      const int ho2 = opq(OFF_H + L * HB + tt2 * 112 * 1024 + kh * 7168);
      v4u hx[28];
      auto ldh = [&](auto T) AI { hx[T.value] = ld16(rs, l16, ho2 + p4_koff(p4_oc(T.value))); };
      f32x16 acc[7];
      constexpr int PB = 7 * PK;
      pipe<196>(
          [&](auto I) AI { constexpr int i = I.value, u = i / 28, sl = u & 3; return lds16(LD, (sl < 2 ? sbA : sbB) + (sl & 1) * UNIT + (i % 28) * 1024); },
          [&](auto I) AI { if constexpr (I.value > 28 && I.value % 28 == 6) { VSTAMP(30 + I.value / 28); RAWBAR(); WSTAMP(22 + I.value / 28); } },
          [&](auto I, v4u a, auto nx) AI {
            constexpr int i = I.value;
            if constexpr (i < PB) {
              constexpr int n = i % 7, ks = i / 7;
              if constexpr (ks == 0) { f32x16 z = {}; acc[n] = mfma(a, x[ks], z); } else acc[n] = mfma(a, x[ks], acc[n]);
              nx();
              constexpr int K2 = 28 - XW3 - PK, fx = ks < K2 ? XW3 + 2 * ks + (n ? 1 : 0) : XW3 + K2 + ks;
              if constexpr (n == 0 || (n == 3 && ks < K2)) x[fx] = ld16(rs, l16, xo + fx * 1024);
              if constexpr (n >= 5 && ks < 4) st[2 * ks + n - 5] = ld8(rs, sv_ + (2 * ks + n - 5) * 256, so_);
              if constexpr (i == 46) {
                ln_stats(st, mean, rstd);
                nmean = -mean; gs = -LOG2E * rstd; irs = __builtin_amdgcn_rcpf(rstd);
                PIN(nmean); PIN(gs); PIN(irs);
              }
            } else {
              constexpr int r = i - PB, n = r / MK, s = r % MK;
              acc[n] = mfma(a, x[PK + s], acc[n]);
              nx();
              if constexpr (r == 8) Gcg[0] = *(LAS f32x4*)(LD + csb);
              if constexpr (n >= 2 && s + MK < 22) glu(acc[n - 2], IC<n - 2>{}, IC<s + MK>{});
              if constexpr (n >= 1) glu(acc[n - 1], IC<n - 1>{}, IC<s>{});
              if constexpr (n >= 3 && s == 20 - MK) { VMCNT(0); signal_cnt(ws, lvo + n - 3); }
            }
          });
      STAMP(10);
      arrive_lds(LD, 2, lane);
      static_for<22>([&](auto S_) AI {
        if constexpr (S_.value + MK < 22) glu(acc[5], IC<5>{}, IC<S_.value + MK>{});
        glu(acc[6], IC<6>{}, S_);
        if constexpr (S_.value == 4) { VMCNT(0); signal_cnt(ws, lvo + 4); }
        if constexpr (S_.value == CP1) f = poll_issue(fp + 96);
      });
      STAMP(11);
      const int wo_ = opqv(kh * 3072 + opqv(l16)), wp_ = opqv(kh * 1024 + 1024 + opqv(l16));
      const int wo2 = opqv(wo_ + 3 * UNIT), wp2 = opqv(wp_ + 3 * UNIT);
      v4u wq[56];
      auto ldw = [&](auto K) AI {
        constexpr int k = K.value, t = k >> 1, wb = p4_wbase(t);
        if constexpr (wb >= 3 * UNIT) wq[k] = lds16(LD, ((k & 1) ? wp2 : wo2) + wb - 3 * UNIT);
        else wq[k] = lds16(LD, ((k & 1) ? wp_ : wo_) + wb);
      };
      VMCNT(1); signal_cnt(ws, lvo + 5);
      PIN(f);
      const u32 f2 = poll(4);
      if (__all(f >= 16u)) {
        static_for<4>([&](auto T) AI { ldh(T); });
        VMCNT(6); signal_cnt(ws, lvo + 6);
      } else {
        VMCNT(2); signal_cnt(ws, lvo + 6);
        wait_flags(ws, fp + 96, f);
        static_for<4>([&](auto T) AI { ldh(T); });
      }
      wait_flags(ws, fp + 128, f2);
      static_for<8>([&](auto T) AI { ldh(IC<4 + T.value>{}); });
      wait_arr(LD, 4, 4 * L + 4);
      static_for<4>([&](auto K) AI { ldw(K); });
      STAMP(12);
      f32x16 fin = {}, oth = {};
      static_for<28>([&](auto T) AI {
        constexpr int t = T.value;
        if constexpr (t == CP5) f = poll(5);
        if constexpr (t == CT5) {
          wait_flags(ws, fp + 160, f);
          f = poll(6);
          static_for<8>([&](auto K) AI { ldh(IC<12 + K.value>{}); });
          STAMP(13);
        }
        if constexpr (t == 12) {
          wait_flags(ws, fp + 192, f);
          static_for<8>([&](auto K) AI { ldh(IC<20 + K.value>{}); });
          STAMP(22);
        }
        if constexpr (t == 12) arrive_lds(LD, 3, lane);
        if constexpr (t + 2 < 28) { ldw(IC<2 * t + 4>{}); ldw(IC<2 * t + 5>{}); }
        oth = mfma(wq[2 * t + 1], hx[t], oth);
        fin = mfma(wq[2 * t], hx[t], fin);
        SB();
      });
      STAMP(14);
      wait_arr(LD, 3, 4 * L + 4);
      put16(oth, LD, opqv(L_SCR + w * 4096 + opqv(l16)));
      BAR();
#pragma unroll
      for (int c = 0; c < 4; ++c) add16(fin, LD, opqv(p4_part(w, c) + opqv(l16)));
      add16(fin, LD, opqv(L_SCR + (w ^ 2) * 4096 + opqv(l16)));
      STAMP(15);
      finish_tile(fin, res, rs, OFF_X2, OFF_ST2, tt2, cb, kh, lane, L == 1, A.out);
    }
    if (L == 0) {
      wave_signal(ws, C_X2 + tt2, lane);
      arrive_lds(LD, 0, lane);
      STAMP(16);
      wait_cnt(ws, C_X2 + tt, 16, true);
    }
    STAMP(17);
  };
  clayer(IC<0>{});
  clayer(IC<1>{});
}

extern "C" void kernel_launch(void* const* d_in, const int* in_sizes, int n_in, void* d_out, int out_size, void* d_ws,
                              size_t ws_size, hipStream_t stream) {
  Args a;
  a.x0 = (const u16*)d_in[0]; a.wqkv = (const u16*)d_in[1]; a.wphi = (const u16*)d_in[2]; a.wo = (const u16*)d_in[3];
  a.wg = (const u16*)d_in[5]; a.wu = (const u16*)d_in[6]; a.wd = (const u16*)d_in[7];
  a.ws = (char*)d_ws; a.out = (u16*)d_out;
  (void)hipFuncSetAttribute((const void*)k_main, hipFuncAttributeMaxDynamicSharedMemorySize, LDS_BYTES);
  k_main<<<256, 512, LDS_BYTES, stream>>>(a);
}
```

```cpp
#include <hip/hip_runtime.h>
#include <stdint.h>
#include <utility>
#include <type_traits>

typedef unsigned short u16;
typedef unsigned int u32;
typedef u32 v4u __attribute__((ext_vector_type(4)));
typedef u32 v2u __attribute__((ext_vector_type(2)));
typedef float f32x2 __attribute__((ext_vector_type(2)));
typedef float f32x4 __attribute__((ext_vector_type(4)));
typedef float f32x16 __attribute__((ext_vector_type(16)));
typedef __bf16 bf16x2 __attribute__((ext_vector_type(2)));
typedef __bf16 bf16x8 __attribute__((ext_vector_type(8)));
typedef short s16x4 __attribute__((ext_vector_type(4)));
#define LAS __attribute__((address_space(3)))
#define DI __device__ __forceinline__
#define AI __attribute__((always_inline))
template <int V> using IC = std::integral_constant<int, V>;

constexpr int S = 2048, D = 448, FF = 1792;
constexpr int UNIT = 28672;

constexpr int C_KV = 0;
constexpr int C_W2 = 32;
constexpr int C_W4 = 36;
constexpr int C_W3 = 40;
constexpr int C_ERR = 60;
constexpr int C_X2 = 64;
constexpr int C_X1 = 128;
constexpr int C_O = 256;
constexpr int C_H = 512;
constexpr int C_W1 = 1536;
constexpr int NZ = 25;
constexpr int EPOCH_OFF = NZ * 64 * 128;
constexpr int INIT_OFF = EPOCH_OFF + 256;
constexpr int CTL_BYTES = EPOCH_OFF + 4096;
constexpr int W2B = 65536;
constexpr int OFF_W1 = CTL_BYTES;
constexpr int OFF_W2 = OFF_W1 + 16 * 3 * UNIT;
constexpr int OFF_W3 = OFF_W2 + 8 * W2B;
constexpr int OFF_W4 = OFF_W3 + 16 * 7 * UNIT;
constexpr int OFF_CS3 = OFF_W4 + 8 * 8 * UNIT;
constexpr int XB = S * D * 2;
constexpr int OB = S * 512 * 2;
constexpr int OFF_X2 = OFF_CS3 + 16 * 28 * 896;
constexpr int OFF_X1 = OFF_X2 + XB;
constexpr int OFF_O = OFF_X1 + 2 * XB;
constexpr int STB = S * 16 * 8;
constexpr int OFF_ST2 = OFF_O + 2 * OB;
constexpr int OFF_ST1 = OFF_ST2 + STB;
constexpr int KVB = 16 * 16 * 4096;
constexpr int OFF_KVP = OFF_ST1 + 2 * STB;
constexpr int HB = S * FF * 2;
constexpr int OFF_H = OFF_KVP + 2 * KVB;
constexpr int WS_USED = OFF_H + 2 * HB;

constexpr int L_SCR = 4 * UNIT;
constexpr int PHK_STRIDE = 136, V_STRIDE = 72;
constexpr int L_PHK = L_SCR;
constexpr int L_V = L_SCR + 128 * PHK_STRIDE;
constexpr int L_KVF = L_SCR + 28672;
constexpr int L_W2X = L_KVF + 4096;
constexpr int L_CONST = 155648;
constexpr int L_CS1 = L_CONST, L_CS3 = L_CONST + 384, L_WPHI = L_CONST + 1280, L_ARR = L_CONST + 3328;
constexpr int L_STAGE = 2 * UNIT;
constexpr int STG = 8704;
constexpr int LDS_BYTES = 163840;

constexpr float LOG2E = 1.4426950408889634f;
#define PKV 12
constexpr int PK = PKV, MK = 28 - PK;
constexpr int LAG = 4;

DI u32 pk2(float a, float b) { f32x2 v = {a, b}; return __builtin_bit_cast(u32, __builtin_convertvector(v, bf16x2)); }
DI float bflo(u32 v) { return __uint_as_float(v << 16); }
DI float bfhi(u32 v) { return __uint_as_float(v & 0xffff0000u); }
DI f32x16 mfma(v4u a, v4u b, f32x16 c) {
  f32x16 d = __builtin_amdgcn_mfma_f32_32x32x16_bf16(__builtin_bit_cast(bf16x8, a), __builtin_bit_cast(bf16x8, b), c, 0, 0, 0);
  asm volatile("" : "+v"(d));
  __builtin_amdgcn_sched_barrier(0);
  return d;
}
DI float xsum(float v) {
  auto r = __builtin_amdgcn_permlane32_swap(__float_as_uint(v), __float_as_uint(v), false, false);
  return __uint_as_float(r[0]) + __uint_as_float(r[1]);
}
DI float xmax(float v) {
  auto r = __builtin_amdgcn_permlane32_swap(__float_as_uint(v), __float_as_uint(v), false, false);
  return fmaxf(__uint_as_float(r[0]), __uint_as_float(r[1]));
}
DI float xmin(float v) {
  auto r = __builtin_amdgcn_permlane32_swap(__float_as_uint(v), __float_as_uint(v), false, false);
  return fminf(__uint_as_float(r[0]), __uint_as_float(r[1]));
}
#define BAR() asm volatile("s_waitcnt lgkmcnt(0)\n\ts_barrier" ::: "memory")
#define RAWBAR() asm volatile("s_barrier" ::: "memory")
#define VMCNT(n) asm volatile("s_waitcnt vmcnt(" #n ")" ::: "memory")
#define SB() __builtin_amdgcn_sched_barrier(0)
#define PIN(x) asm volatile("" : "+v"(x))

typedef __amdgpu_buffer_rsrc_t rsrc_t;
DI v4u ld16(rsrc_t rs, int voff, int soff) { return __builtin_amdgcn_raw_buffer_load_b128(rs, voff, soff, 16); }
DI v2u ld8(rsrc_t rs, int voff, int soff) { return __builtin_amdgcn_raw_buffer_load_b64(rs, voff, soff, 16); }
DI u32 ld4(rsrc_t rs, int voff, int soff) { return __builtin_amdgcn_raw_buffer_load_b32(rs, voff, soff, 16); }
DI void st16(rsrc_t rs, int voff, int soff, v4u v) {
  __builtin_amdgcn_raw_buffer_store_b128(v, rs, voff, soff, 16);
  asm volatile("s_nop 1" ::"v"(v) : "memory");
}
DI void st8(rsrc_t rs, int voff, int soff, v2u v) { __builtin_amdgcn_raw_buffer_store_b64(v, rs, voff, soff, 16); }
DI void st4(rsrc_t rs, int voff, int soff, u32 v) { __builtin_amdgcn_raw_buffer_store_b32(v, rs, voff, soff, 16); }
DI int opq(int v) { asm volatile("" : "+s"(v)); return v; }
DI int opqv(int v) { asm volatile("" : "+v"(v)); return v; }
DI v4u lds16(LAS char* l, int off) { return *(LAS v4u*)(l + off); }
DI float dot2(u32 a, u32 b, float c) {
  return __builtin_amdgcn_fdot2_f32_bf16(__builtin_bit_cast(bf16x2, a), __builtin_bit_cast(bf16x2, b), c, false);
}
DI v4u tr_frag(LAS char* L, int base, int step) {
  v2u lo = __builtin_bit_cast(v2u, __builtin_amdgcn_ds_read_tr16_b64_v4i16((LAS s16x4*)(L + base)));
  v2u hi = __builtin_bit_cast(v2u, __builtin_amdgcn_ds_read_tr16_b64_v4i16((LAS s16x4*)(L + base + step)));
  return v4u{lo.x, lo.y, hi.x, hi.y};
}
DI float sum8(v4u o) {
  const u32 one = 0x3f803f80u;
  return dot2(o.x, one, dot2(o.y, one, 0.f)) + dot2(o.z, one, dot2(o.w, one, 0.f));
}

DI u32* cnt_ptr(char* ws, int idx) { return (u32*)(ws + idx * 128); }
#define POLL_D0 16
#define LATE_ARR
#define WOV 0
#define WOS 20
#define H2 6
#define H3 12
#define CP1 12
#define CP5 0
#define CT5 11
#define PT5 8
#define OUTAUX -1
#define POLL_IV 1
DI bool spin_more(char* ws, u32& spins) {
  u32* e = cnt_ptr(ws, C_ERR);
  __builtin_amdgcn_s_sleep(POLL_IV);
  ++spins;
  if ((spins & 255u) == 0u) {
    if (__builtin_amdgcn_readfirstlane(__hip_atomic_load(e, __ATOMIC_RELAXED, __HIP_MEMORY_SCOPE_AGENT)) != 0) return false;
    if (spins > 400000u) { __hip_atomic_store(e, 1u, __ATOMIC_RELAXED, __HIP_MEMORY_SCOPE_AGENT); return false; }
  }
  return true;
}
DI void wait_cnt(char* ws, int idx, u32 target, bool delay = false) {
  u32* c = cnt_ptr(ws, idx);
  u32 spins = 0;
  if (delay && POLL_D0) __builtin_amdgcn_s_sleep(POLL_D0);
  while ((u32)__builtin_amdgcn_readfirstlane(__hip_atomic_load(c, __ATOMIC_RELAXED, __HIP_MEMORY_SCOPE_AGENT)) < target)
    if (!spin_more(ws, spins)) break;
  __builtin_amdgcn_fence(__ATOMIC_ACQUIRE, "wavefront");
  asm volatile("" ::: "memory");
}
DI void wait_flags(char* ws, u32* fp, u32 f, u32 target = 16u) {
  if (__builtin_expect(!__all(f >= target), 0)) {
    u32 spins = 0;
    while (spin_more(ws, spins))
      if (__all(__hip_atomic_load(fp, __ATOMIC_RELAXED, __HIP_MEMORY_SCOPE_AGENT) >= target)) break;
  }
  __builtin_amdgcn_fence(__ATOMIC_ACQUIRE, "wavefront");
  asm volatile("" ::: "memory");
}
DI u32 poll_issue(const u32* p) { u32 f; asm volatile("global_load_dword %0, %1, off sc1" : "=v"(f) : "v"(p) : "memory"); return f; }
DI void signal_cnt(char* ws, int idx) {
  unsigned long long sv;
  asm volatile("s_mov_b64 %0, exec\n\ts_mov_b64 exec, 1\n\tglobal_atomic_add %1, %2, %3\n\ts_mov_b64 exec, %0"
               : "=&s"(sv) : "v"(idx * 128), "v"(1), "s"(ws) : "memory");
}
DI void wave_signal(char* ws, int idx, int lane) { VMCNT(0); signal_cnt(ws, idx); }
DI void arrive_lds(LAS char* LD, int slot, int lane) {
  if (lane == 0) __hip_atomic_fetch_add((LAS u32*)(LD + L_ARR + slot * 4), 1u, __ATOMIC_RELAXED, __HIP_MEMORY_SCOPE_WORKGROUP);
}
DI void wait_arr(LAS char* LD, int slot, u32 target) {
  while ((u32)__builtin_amdgcn_readfirstlane(__hip_atomic_load((LAS u32*)(LD + L_ARR + slot * 4), __ATOMIC_RELAXED, __HIP_MEMORY_SCOPE_WORKGROUP)) < target)
    __builtin_amdgcn_s_sleep(1);
}

#define STAMP(i)
#define HSTAMP(i)
#define VSTAMP(i)
#define WSTAMP(i)
#define USTAMP(i)

#define DMA_STEP(GSTEP) \
      "s_add_u32 m0, m0, 0x1000\n\t" \
      "v_add_u32 %1, " GSTEP ", %1\n\t" \
      "global_load_lds_dwordx4 %1, %2 sc1\n\t"
#define DMA_FN(NAME, BODY) \
DI void NAME(const char* gsrc, u32 voff, u32 lds_dst) { \
  u32 keep; \
  asm volatile("s_mov_b32 %0, m0\n\ts_mov_b32 m0, %3\n\ts_nop 0\n\tglobal_load_lds_dwordx4 %1, %2 sc1\n\t" BODY "s_mov_b32 m0, %0" \
               : "=&s"(keep), "+v"(voff) : "s"(gsrc), "s"(lds_dst) : "memory", "scc"); }
DMA_FN(dma7, DMA_STEP("0x1000") DMA_STEP("0x1000") DMA_STEP("0x1000") DMA_STEP("0x1000") DMA_STEP("0x1000") DMA_STEP("0x1000"))
DMA_FN(dma2, DMA_STEP("0x1000"))
#define PACE 1
#define STR2(x) #x
#define STR(x) STR2(x)
#define PSTEP "s_sleep " STR(PACE) "\n\t" DMA_STEP("0x1000")
DMA_FN(dma7p, PSTEP PSTEP PSTEP PSTEP PSTEP PSTEP)
DMA_FN(dma7n, DMA_STEP("0x2a000") DMA_STEP("0x2a000") DMA_STEP("0x2a000") DMA_STEP("0x2a000") DMA_STEP("0x2a000") DMA_STEP("0x2a000"))

template <int LO, int GO>
DI void dma1(const char* gsrc, u32 voff, u32 lb) {
  u32 keep, tmp;
  asm volatile("s_mov_b32 %0, m0\n\ts_add_u32 m0, %4, %5\n\tv_add_u32 %1, %6, %2\n\tglobal_load_lds_dwordx4 %1, %3 sc1\n\ts_mov_b32 m0, %0"
               : "=&s"(keep), "=&v"(tmp) : "v"(voff), "s"(gsrc), "s"(lb), "i"(LO), "i"(GO) : "memory", "scc");
}

template <typename F, int... I>
DI void static_for_impl(F&& f, std::integer_sequence<int, I...>) { (f(IC<I>{}), ...); }
template <int N, typename F>
DI void static_for(F&& f) { static_for_impl(f, std::make_integer_sequence<int, N>{}); }

constexpr int PF = 6;
#define WEV 2
template <int N, int RPS = 1, typename RD, typename PRE, typename ST>
DI void pipe(RD rd, PRE pre, ST step) {
  v4u buf[PF];
  static_for<PF>([&](auto I) AI { buf[I.value] = rd(I); });
  SB();
  static_for<N>([&](auto I) AI {
    constexpr int i = I.value;
    pre(I);
    if constexpr (WEV > 1 && i % WEV == 0 && i + PF <= N) __builtin_amdgcn_s_waitcnt(0xC07F | (((PF - WEV) * RPS) << 8));
    const v4u a = buf[i % PF];
    step(I, a, [&]() AI { if constexpr (i + PF < N) buf[i % PF] = rd(IC<i + PF>{}); SB(); });
    SB();
  });
}

DI void ln_finish(float s, float q, float& mean, float& rstd) {
  s = xsum(s); q = xsum(q);
  mean = s * (1.f / 448.f);
  const float var = fmaxf(q * (1.f / 448.f) - mean * mean, 0.f);
  rstd = __builtin_amdgcn_rsqf(var + 1e-5f);
}
DI void ln_stats(const v2u (&st)[8], float& mean, float& rstd) {
  f32x2 a = __builtin_bit_cast(f32x2, st[0]), b = __builtin_bit_cast(f32x2, st[1]);
#pragma unroll
  for (int i = 2; i < 8; i += 2) { a += __builtin_bit_cast(f32x2, st[i]); b += __builtin_bit_cast(f32x2, st[i + 1]); }
  a += b;
  ln_finish(a[0], a[1], mean, rstd);
}
DI v4u pack8(const f32x16& a, int s) {
  v4u r = {pk2(a[8 * s], a[8 * s + 1]), pk2(a[8 * s + 2], a[8 * s + 3]), pk2(a[8 * s + 4], a[8 * s + 5]), pk2(a[8 * s + 6], a[8 * s + 7])};
  return r;
}
template <int SG>
DI void half_softmax(const f32x16& p, int h, float rstd, v4u& o0, v4u& o1) {
  auto mm = [](float a, float b) AI { return SG ? fminf(a, b) : fmaxf(a, b); };
  float m0 = mm(mm(p[0], p[1]), p[2]), m1 = mm(mm(p[3], p[4]), p[5]), m2 = mm(mm(p[6], p[7]), p[8]), m3 = mm(mm(p[9], p[10]), p[11]);
  float m4 = mm(mm(mm(p[12], p[13]), p[14]), p[15]);
  m4 = h ? p[0] : m4;
  float mx = mm(mm(mm(m0, m1), m2), mm(m3, m4));
  mx = SG ? xmin(mx) : xmax(mx);
  const float c = rstd * LOG2E;
  const float cc = SG ? -c : c, off = -mx * cc;
  f32x16 e;
  float s0 = 0.f, s1 = 0.f;
#pragma unroll
  for (int r = 0; r < 16; ++r) {
    float v = __builtin_amdgcn_exp2f(fmaf(p[r], cc, off));
    if (r >= 12) v = h ? 0.f : v;
    e[r] = v;
    if (r & 1) s1 += v; else s0 += v;
  }
  const float rs_ = __builtin_amdgcn_rcpf(xsum(s0 + s1));
#pragma unroll
  for (int r = 0; r < 16; ++r) e[r] *= rs_;
  o0 = pack8(e, 0); o1 = pack8(e, 1);
  PIN(o0); PIN(o1);
  SB();
}

DI void finish_tile(const f32x16& acc, v4u (&res)[2], rsrc_t rs, int xoff, int stoff, int tt, int cb, int nt, int lane, bool last, u16* out) {
  lane = opqv(lane);
  const int h = lane >> 5, tok = lane & 31;
#pragma unroll
  for (int ro = 0; ro < 2; ++ro) {
    v4u r = res[ro];
    v4u o;
    o.x = pk2(bflo(r.x) + acc[8 * ro + 0], bfhi(r.x) + acc[8 * ro + 1]);
    o.y = pk2(bflo(r.y) + acc[8 * ro + 2], bfhi(r.y) + acc[8 * ro + 3]);
    o.z = pk2(bflo(r.z) + acc[8 * ro + 4], bfhi(r.z) + acc[8 * ro + 5]);
    o.w = pk2(bflo(r.w) + acc[8 * ro + 6], bfhi(r.w) + acc[8 * ro + 7]);
    res[ro] = o;
    const int oi = nt * 4 + ro * 2 + h;
    if (oi < 7) {
      const int go = 7 * cb + oi;
      if (last) {
        *(v4u*)(out + (size_t)(tt * 32 + tok) * D + go * 8) = o;
      }
      else st16(rs, ((go >> 1) * 64 + tok + 32 * (go & 1)) * 16, xoff + tt * 28 * 1024, o);
    }
  }
  if (!last) {
    const bool v1 = !(nt == 1 && h == 1);
    const u32 one = 0x3f803f80u;
    float s0 = dot2(res[0].x, one, 0.f), s1 = dot2(res[1].x, one, 0.f), q0 = dot2(res[0].x, res[0].x, 0.f), q1 = dot2(res[1].x, res[1].x, 0.f);
    s0 = dot2(res[0].y, one, s0); s1 = dot2(res[1].y, one, s1); q0 = dot2(res[0].y, res[0].y, q0); q1 = dot2(res[1].y, res[1].y, q1);
    s0 = dot2(res[0].z, one, s0); s1 = dot2(res[1].z, one, s1); q0 = dot2(res[0].z, res[0].z, q0); q1 = dot2(res[1].z, res[1].z, q1);
    s0 = dot2(res[0].w, one, s0); s1 = dot2(res[1].w, one, s1); q0 = dot2(res[0].w, res[0].w, q0); q1 = dot2(res[1].w, res[1].w, q1);
    const float sum = xsum(s0 + (v1 ? s1 : 0.f));
    const float sq = xsum(q0 + (v1 ? q1 : 0.f));
    if (h == 0) st8(rs, tok * 8, stoff + ((tt * 8 + cb) * 2 + nt) * 256, v2u{__float_as_uint(sum), __float_as_uint(sq)});
  }
}

DI void kv_reduce(rsrc_t rs, LAS char* LD, int L, int j, int w, int lane) {
  const int ko = opq(OFF_KVP + L * KVB + j * 16 * 4096 + (w >> 1) * 1024 + (w & 1) * 512);
  const int lz = opqv(lane);
  const int vo = (lz >> 5) * 4096 + (lz & 31) * 16;
  v4u t[8];
#pragma unroll
  for (int i = 0; i < 8; ++i) t[i] = ld16(rs, vo, ko + i * 8192);
  float s[8] = {};
  const u32 lo1 = (u32)opq(0x00003f80), hi1 = (u32)opq(0x3f800000);
#pragma unroll
  for (int i = 0; i < 8; ++i) {
#pragma unroll
    for (int c = 0; c < 4; ++c) { s[2 * c] = dot2(t[i][c], lo1, s[2 * c]); s[2 * c + 1] = dot2(t[i][c], hi1, s[2 * c + 1]); }
  }
#pragma unroll
  for (int c = 0; c < 8; ++c) s[c] = xsum(s[c]);
  if (lz < 32) *(LAS v4u*)(LD + L_KVF + (w >> 1) * 1024 + (w & 1) * 512 + lz * 16) = v4u{pk2(s[0], s[1]), pk2(s[2], s[3]), pk2(s[4], s[5]), pk2(s[6], s[7])};
}

constexpr int p4_oh(int t) { return 4 * t; }
constexpr int p4_oc(int t) { return 56 + 2 * t; }
constexpr int p4_koff(int o) { return (7 * (o & 15) + (o >> 4)) * 1024; }
constexpr int p4_wbase(int t) { return (t < 7 ? 0 : t < 14 ? L_SCR : t < 21 ? 3 * UNIT : UNIT) + (t % 7) * 4096; }
DI int p4_part(int a, int hw) {
  const int i = ((a >> 1) | ((a & 1) << 1)) * 4 + hw;
  return i < 7 ? 2 * UNIT + i * 4096 : i < 10 ? L_KVF + (i - 7) * 4096 : (i - 10) * 4096;
}
DI void put16(const f32x16& acc, LAS char* LD, int off) {
#pragma unroll
  for (int qd = 0; qd < 4; ++qd) *(LAS f32x4*)(LD + off + qd * 1024) = f32x4{acc[4 * qd], acc[4 * qd + 1], acc[4 * qd + 2], acc[4 * qd + 3]};
}
DI void add16(f32x16& acc, LAS char* LD, int off) {
#pragma unroll
  for (int qd = 0; qd < 4; ++qd) {
    const f32x4 v = *(LAS f32x4*)(LD + off + qd * 1024);
#pragma unroll
    for (int i = 0; i < 4; ++i) acc[4 * qd + i] += v[i];
  }
}

struct Args {
  const u16* x0; const u16* wqkv; const u16* wphi; const u16* wo; const u16* wg; const u16* wu; const u16* wd;
  char* ws; u16* out;
};

struct Retile { const v4u* pa[7]; int rd0, rd1, step, o0, o1, ocb, sig; };
DI void retile_prep(const Args& A, int job, int lane, int stage, Retile& R) {
  const int h = lane >> 5, q = (lane & 15) >> 2, c8 = ((lane >> 4) & 1) * 4 + (lane & 3);
  const int hh = c8 & 1, quad = c8 >> 1;
  if (job < 64 || job >= 512) {
    const bool iswo = job < 64;
    const int a0 = iswo ? job >> 2 : (job - 512) >> 2, cg = job & 3;
    const u16* src = (iswo ? A.wo + (size_t)a0 * 28 * D : A.wd + (size_t)a0 * 32 * D) + 112 * cg;
    const int nrow = iswo ? 28 : 32;
#pragma unroll
    for (int i = 0; i < 7; ++i) {
      const int p = lane + 64 * i, r = p / 14, c = p % 14;
      R.pa[i] = (const v4u*)(src + (size_t)(r < nrow ? r : 0) * D + c * 8);
    }
    const int rb = stage + ((iswo ? 4 : 8) * h + q) * 224 + 8 * (quad & 1);
    const int oi = (quad >> 1) * 2 + hh;
    R.rd0 = rb + 16 * oi; R.rd1 = rb + 16 * min(4 + oi, 6); R.step = (iswo ? 8 : 4) * 224;
    int ob[2];
#pragma unroll
    for (int k2 = 0; k2 < 2; ++k2) {
      const int ks = a0 * 2 + k2, o_ = 16 * (ks % 7) + ks / 7;
      int fr;
      if (o_ < 56) fr = 112 + ((o_ & 3) * 14 + (o_ >> 2)) * 2;
      else fr = (o_ - 56) * 2;
      ob[k2] = iswo ? OFF_W2 + cg * 2 * W2B + ((ks & 15) * 2 + (ks >> 4)) * 2048 : OFF_W4 + cg * 16 * UNIT + fr * 1024;
    }
    R.o0 = opq(ob[0]); R.o1 = opq(ob[1]); R.ocb = opq(iswo ? W2B : 8 * UNIT); R.sig = opq((iswo ? C_W2 : C_W4) + cg);
  } else {
    const int a = job - 64, j = a / 28, ks = a % 28;
#pragma unroll
    for (int i = 0; i < 7; ++i) {
      const int p = lane + 64 * i, r = p / 28, c = p % 28;
      R.pa[i] = (const v4u*)((c < 14 ? A.wg : A.wu) + (size_t)(ks * 16 + r) * FF + 112 * j + (c < 14 ? c : c - 14) * 8);
    }
    R.rd0 = stage + (8 * h + q) * 448 + (quad & 1) * 224 + (8 * hh + 4 * (quad >> 1)) * 2; R.rd1 = 0; R.step = 0;
    R.o0 = opq(OFF_W3 + j * 7 * UNIT + (ks < PK ? ks * 7 : 6 * PK + ks) * 1024); R.o1 = opq(ks < PK ? 1024 : MK * 1024);
    R.ocb = opq(OFF_CS3 + (j * 28 + ks) * 896); R.sig = opq(C_W3 + j);
  }
#pragma unroll
  for (int i = 0; i < 7; ++i) PIN(R.pa[i]);
  PIN(R.rd0); PIN(R.rd1);
}
DI void retile_load(const Retile& R, u32 lds_dst) {
#pragma unroll
  for (int i = 0; i < 7; ++i) {
    u32 keep;
    asm volatile("s_mov_b32 %0, m0\n\ts_add_u32 m0, %2, %3\n\ts_nop 0\n\tglobal_load_lds_dwordx4 %1, off\n\ts_mov_b32 m0, %0"
                 : "=&s"(keep) : "v"(R.pa[i]), "s"(lds_dst), "i"(i * 1024) : "memory", "scc");
  }
}
DI void retile_store(int job, const Retile& R, rsrc_t rs, LAS char* LD, int lane) {
  const int h = lane >> 5;
  if (job < 64 || job >= 512) {
    const bool pad = (lane & 0x14) == 0x14;
    const bool kpad = job < 64 && h;
    v4u o[8];
#pragma unroll
    for (int i = 0; i < 8; ++i) {
      const int ad = ((i & 2) ? R.rd1 : R.rd0) + (i >> 2) * 112 + (i & 1) * 16 * 224;
      const v2u lo = __builtin_bit_cast(v2u, __builtin_amdgcn_ds_read_tr16_b64_v4i16((LAS s16x4*)(LD + ad)));
      const v2u hi = __builtin_bit_cast(v2u, __builtin_amdgcn_ds_read_tr16_b64_v4i16((LAS s16x4*)(LD + ad + R.step)));
      o[i] = v4u{lo.x, lo.y, hi.x, hi.y};
    }
#pragma unroll
    for (int i = 0; i < 8; ++i) {
      if ((i & 2) && pad) o[i] = v4u{0u, 0u, 0u, 0u};
      if ((i & 1) && kpad) { o[i].z = 0u; o[i].w = 0u; }
      st16(rs, lane * 16, ((i & 1) ? R.o1 : R.o0) + (i >> 2) * R.ocb + ((i >> 1) & 1) * 1024, o[i]);
    }
  } else {
    v4u o[7];
#pragma unroll
    for (int n = 0; n < 7; ++n) o[n] = tr_frag(LD, R.rd0 + n * 32, 4 * 448);
    float cs[7];
#pragma unroll
    for (int n = 0; n < 7; ++n) {
      st16(rs, lane * 16, R.o0 + n * R.o1, o[n]);
      cs[n] = xsum(sum8(o[n]));
    }
#pragma unroll
    for (int n2 = 0; n2 < 4; ++n2) {
      const float v = h ? cs[n2 < 3 ? 2 * n2 + 1 : 6] : cs[2 * n2];
      if (n2 < 3 || !h) st4(rs, lane * 4, R.ocb + n2 * 256, __float_as_uint(v));
    }
  }
}

__global__ __launch_bounds__(512, 2) void k_main(const Args A) {
  extern __shared__ __attribute__((aligned(16))) char smem[];
  LAS char* LD = (LAS char*)smem;
  char* ws = A.ws;
  const int b = blockIdx.x, xcd = b & 7, bi = b >> 3;
  const int g = (xcd >> 2) * 8 + (bi >> 2), j = (xcd & 3) * 4 + (bi & 3);
  const int rb = 2 * g + (j >> 3), cb = j & 7;
  const int lane = threadIdx.x & 63, w = __builtin_amdgcn_readfirstlane(threadIdx.x >> 6);
  const int h = lane >> 5, tok = lane & 31;
  const rsrc_t rs = __builtin_amdgcn_make_buffer_rsrc((void*)ws, 0, WS_USED, 0x00020000);
  const u32 lbase = (u32)(size_t)smem;
  const int l16 = lane * 16;
  const int tq_ = (lane & 15) >> 2, tc8 = ((lane >> 4) & 1) * 4 + (lane & 3);
  const int w1lane = (8 * h + tq_) * 64 + min(tc8 + (j & 1), 7) * 8;

  const int hw = w - 4;
  const u32 voffn = (hw * 16 + (lane >> 2)) * (3 * D * 2) + (lane & 3) * 16;
  const char* w1 = (const char*)A.wqkv + 56 * j - (j & 1) * 8;
  unsigned long long ep;
  const u32 lb = lbase + hw * 1024;
#define KP(p) dma1<4 * UNIT + (p) * 4096, 896 + (p) * 0x2a000>(w1, voffn, lb)
#define VP(p) dma1<(p) * 4096, 1792 + (p) * 0x2a000>(w1, voffn, lb)
#define QP(p) dma1<UNIT + (p) * 4096, (p) * 0x2a000>(w1, voffn, lb)
  if (opq(w) >= 4) {
    HSTAMP(0);
    if (hw == 1 && b < NZ) {
      asm volatile("global_load_dwordx2 %0, %1, %2 sc1" : "=v"(ep) : "v"(EPOCH_OFF), "s"(ws) : "memory");
      st4(rs, lane * 128, b * 8192, 0u);
    }
    if (hw == 2) {
      dma1<L_KVF, 0>((const char*)A.wphi + j * 1568, l16, lbase);
      dma1<L_KVF + 1024, 0>((const char*)A.wphi + j * 1568, min(1024 + l16, 1552), lbase);
    }
    KP(0); KP(1); VP(0); KP(2); VP(1); KP(3); VP(2);
    if (hw == 3) {
      unsigned long long pcv;
      asm volatile("s_getpc_b64 %0" : "=s"(pcv));
      const char* pc = (const char*)(pcv & ~255ull);
      dma1<L_W2X, 0>(pc, bi * 1024 + l16, lbase);
      dma1<L_W2X + 1024, 32768>(pc, bi * 1024 + l16, lbase);
    }
    HSTAMP(1);
  }

  if (w >= 4) {
    const u32 voff = hw * 1024 + lane * 16;
    const char* w1f = ws + OFF_W1 + j * 3 * UNIT;
    const char* w2 = ws + OFF_W2 + cb * W2B;
    const char* w3 = ws + OFF_W3 + j * 7 * UNIT;
    const char* w4 = ws + OFF_W4 + cb * 8 * UNIT;
#define DMA(src, slot) dma7((src), voff, lbase + (slot) * UNIT + hw * 1024)
#define DMAP(src, slot) dma7p((src), voff, lbase + (slot) * UNIT + hw * 1024)
#define DMAN(which, slot) dma7n(w1 + (which) * 896, voffn, lbase + (slot) * UNIT + hw * 1024)
    v4u wr[28];
    unsigned long long tag = 0;
    auto hlayer = [&](auto LC) AI {
      constexpr int L = LC.value;
      Retile R;
      const int job = hw >= 2 ? b * 2 + hw - 2 : (hw == 0 && b < 224 ? 512 + b : -1);
      float c0 = 0.f, c1 = 0.f, c2 = 0.f, c3 = 0.f;
      auto colpart = [&](int slot, auto K0, auto K1) AI {
        constexpr int k0 = K0.value, n = K1.value - K0.value;
        const int ib = opqv(slot * UNIT + opqv(w1lane));
        v4u fr[n];
#pragma unroll
        for (int ks = 0; ks < n; ++ks) fr[ks] = tr_frag(LD, ib + (k0 + ks) * 1024, 256);
        SB();
        const u32 one = 0x3f803f80u;
#pragma unroll
        for (int ks = 0; ks < n; ++ks) { c0 = dot2(fr[ks].x, one, c0); c1 = dot2(fr[ks].y, one, c1); c2 = dot2(fr[ks].z, one, c2); c3 = dot2(fr[ks].w, one, c3); }
        SB();
      };
      auto colfin = [&](int which) AI {
        const float cs = xsum((c0 + c1) + (c2 + c3));
        if (lane < 32) *(LAS float*)(LD + L_CS1 + which * 128 + lane * 4) = cs;
      };
      v4u csa[7];
      auto export_img = [&](int slot, int which) AI {
        const int ib = opqv(slot * UNIT + opqv(w1lane) + g * 1024);
        const int eo = opq(OFF_W1 + (j * 3 + which) * UNIT + g * 1024);
        st16(rs, l16, eo, tr_frag(LD, ib, 256));
        if (g < 12) st16(rs, l16, eo + 16384, tr_frag(LD, ib + 16384, 256));
      };
      if constexpr (L == 0) {
        if (hw == 1 && lane < 2) *(LAS v4u*)(LD + L_ARR + l16) = v4u{0u, 0u, 0u, 0u};
        HSTAMP(2);
#define VMW(n, n3) if (hw == 3) { VMCNT(n3); } else { VMCNT(n); }
        VMW(4, 6); BAR();
        HSTAMP(3);
        KP(4); VP(3);
        if (hw == 1 && b < NZ) {
          PIN(ep);
          tag = ep + 1ull;
          if (lane == 0) __hip_atomic_store((unsigned long long*)(ws + INIT_OFF + b * 128), tag, __ATOMIC_RELAXED, __HIP_MEMORY_SCOPE_AGENT);
        }
        if (hw == 2) {
          const int wb = opqv(L_KVF + (4 * h + tq_) * 56 + tc8 * 8);
          const v4u f0 = tr_frag(LD, wb, 8 * 56);
          v4u f1 = tr_frag(LD, wb + 16 * 56, 8 * 56);
          if (h) { f1.z = 0u; f1.w = 0u; }
          *(LAS v4u*)(LD + L_WPHI + l16) = f0;
          *(LAS v4u*)(LD + L_WPHI + 1024 + l16) = f1;
        }
        if (hw == 0) colpart(4, IC<0>{}, IC<8>{});
        VMW(4, 6); BAR();
        HSTAMP(4);
        KP(5); VP(4);
        if (job >= 0) retile_prep(A, job, lane, L_STAGE + hw * STG, R);
        if (hw == 1) colpart(0, IC<0>{}, IC<8>{});
        VMW(4, 6); BAR();
        HSTAMP(5);
        KP(6); VP(5); VP(6);
        if (hw == 0) colpart(4, IC<8>{}, IC<16>{});
        if (hw == 1) colpart(0, IC<8>{}, IC<12>{});
        VMCNT(5); BAR();
        HSTAMP(6);
        QP(0); QP(1); QP(2); QP(3);
        if (hw == 0) colpart(4, IC<16>{}, IC<20>{});
        if (hw == 1) colpart(0, IC<12>{}, IC<16>{});
        VMCNT(7); BAR();
        HSTAMP(7);
        QP(4); QP(5); QP(6);
        if (hw == 0) colpart(4, IC<20>{}, IC<24>{});
        if (hw == 1) colpart(0, IC<16>{}, IC<20>{});
        VMCNT(7); BAR();
        HSTAMP(8);
        if (hw == 0) { colpart(4, IC<24>{}, IC<28>{}); colfin(0); }
        if (hw == 1) { colpart(0, IC<20>{}, IC<28>{}); colfin(1); }
        if (hw == 2) export_img(4, 1);
        if (hw == 3) export_img(0, 2);
        HSTAMP(9);
        BAR();
        HSTAMP(10);
        if (job >= 0) retile_load(R, lbase + L_STAGE + hw * STG);
      } else {
        wait_arr(LD, 0, 4);
        DMA(w1f + 2 * UNIT, 0);
        VMCNT(0); BAR();
      }
      {
        const int wb = opqv(L_WPHI + opqv(l16));
        const v4u wf0 = lds16(LD, wb), wf1 = lds16(LD, wb + 1024);
        if (L == 0) HSTAMP(11);
        BAR();
        if (L == 0) HSTAMP(12);
        __builtin_amdgcn_s_setprio(1);
        const int lz = opqv(lane);
        const int xk = opqv(L_W2X + hw * 1024 + lz * 16);
        const v4u kf0 = lds16(LD, xk), kf1 = lds16(LD, xk + 4096);
        const float rstd = *(LAS float*)(LD + L_KVF + hw * 256 + lz * 4);
        f32x16 p = {};
        p = mfma(wf0, kf0, p);
        p = mfma(wf1, kf1, p);
        v4u o0, o1;
        half_softmax<1>(p, h, rstd, o0, o1);
        const int pb = opqv(L_PHK + (hw * 32 + (lz & 31)) * PHK_STRIDE + 8 * (lz >> 5) + 64);
        *(LAS v2u*)(LD + pb) = v2u{o0.x, o0.y}; *(LAS v2u*)(LD + pb + 16) = v2u{o0.z, o0.w};
        *(LAS v2u*)(LD + pb + 32) = v2u{o1.x, o1.y}; *(LAS v2u*)(LD + pb + 48) = v2u{o1.z, o1.w};
        __builtin_amdgcn_s_setprio(0);
      }
      if (L == 0) HSTAMP(13);
      if constexpr (L == 0) {
        if (job >= 0) { VMCNT(7); } else { VMCNT(0); }
      } else VMCNT(0);
      if (L == 0) HSTAMP(14);
      BAR();
      if (hw < 2) {
        __builtin_amdgcn_s_setprio(2);
        f32x16 kp = {};
        const int lz = opqv(lane);
        const int g16 = (lz >> 4) & 1, q = (lz & 15) >> 2, p = lz & 3, hz = lz >> 5;
        const int ab0 = opqv(L_PHK + (8 * hz + q) * PHK_STRIDE + (g16 * 16 + 4 * p) * 2 + hw * 64);
        const int bb0 = opqv(L_V + (8 * hz + q) * V_STRIDE + (g16 * 16 + 4 * p) * 2);
        v4u ap[8], bv[8];
#pragma unroll
        for (int s = 0; s < 8; ++s) { ap[s] = tr_frag(LD, ab0 + 16 * s * PHK_STRIDE, 4 * PHK_STRIDE); bv[s] = tr_frag(LD, bb0 + 16 * s * V_STRIDE, 4 * V_STRIDE); }
        SB();
#pragma unroll
        for (int s = 0; s < 8; ++s) kp = mfma(ap[s], bv[s], kp);
        const int ko = opq(OFF_KVP + L * KVB + (j * 16 + g) * 4096 + hw * 2048);
        st16(rs, l16, ko, pack8(kp, 0));
        st16(rs, l16, ko + 1024, pack8(kp, 1));
        wave_signal(ws, C_KV + L * 16 + j, lane);
        __builtin_amdgcn_s_setprio(0);
      }
      if constexpr (L == 0) {
        HSTAMP(15);
        if (hw == 3) {
          colpart(1, IC<0>{}, IC<28>{}); colfin(2);
          arrive_lds(LD, 1, lane);
        }
        if (hw == 1) { export_img(1, 0); wave_signal(ws, C_W1 + j, lane); }
        if (job >= 0) { VMCNT(0); retile_store(job, R, rs, LD, lane); }
        HSTAMP(16);
        if (job >= 0) wave_signal(ws, R.sig, lane);
        if (hw >= 2) signal_cnt(ws, C_W1 + j);
        HSTAMP(17);
        BAR();
        HSTAMP(18);
        u32* fw = cnt_ptr(ws, (lane & 1) ? C_W3 + j : C_W2 + (cb >> 1));
        kv_reduce(rs, LD, L, j, w, lane);
        HSTAMP(19);
        BAR();
        HSTAMP(20);
        wait_flags(ws, fw, __hip_atomic_load(fw, __ATOMIC_RELAXED, __HIP_MEMORY_SCOPE_AGENT), (lane & 1) ? 28u : 16u);
        HSTAMP(21);
        DMA(w2, 2); DMA(w2 + UNIT, 3); dma2(w2 + 2 * UNIT, voff, lbase + L_W2X + hw * 1024);
#pragma unroll
        for (int i = 0; i < 7; ++i) csa[i] = ld16(rs, min(lane, 55) * 16, OFF_CS3 + (j * 28 + hw * 7 + i) * 896);
        DMA(w3, 0);
        HSTAMP(22);
        VMCNT(14); HSTAMP(23); BAR();
        HSTAMP(24);
        DMA(w3 + UNIT, 1);
      } else {
        DMA(w2, 2); DMA(w2 + UNIT, 3); dma2(w2 + 2 * UNIT, voff, lbase + L_W2X + hw * 1024);
        DMA(w3, 0);
        BAR();
        kv_reduce(rs, LD, L, j, w, lane);
        BAR();
        DMA(w3 + UNIT, 1);
      }
      BAR();
      if constexpr (L == 0) {
        f32x4 c4 = __builtin_bit_cast(f32x4, csa[0]);
#pragma unroll
        for (int i = 1; i < 7; ++i) c4 += __builtin_bit_cast(f32x4, csa[i]);
        if (lane < 56) *(LAS f32x4*)(LD + L_KVF + hw * 896 + l16) = c4;
      }
      DMA(w3 + 2 * UNIT, 2);
      VMCNT(7); BAR();
      const int wo4 = opq(OFF_W4 + cb * 8 * UNIT + 4 * UNIT + hw * UNIT);
#define WR(lo, hi) _Pragma("unroll") for (int i = lo; i < hi; ++i) wr[i] = ld16(rs, l16, wo4 + i * 1024)
      if constexpr (L == 0) {
        const int t4 = min(hw * 64 + lane, 223) * 4;
        *(LAS float*)(LD + L_CS3 + t4) = (*(LAS float*)(LD + L_KVF + t4) + *(LAS float*)(LD + L_KVF + 896 + t4)) +
                                         (*(LAS float*)(LD + L_KVF + 1792 + t4) + *(LAS float*)(LD + L_KVF + 2688 + t4));
        wait_cnt(ws, C_W4 + (cb >> 1), 56);
        wait_cnt(ws, C_W1 + j, 48);
        WR(0, 28);
      }
      DMA(w3 + 3 * UNIT, 3);
      VMCNT(7); BAR();
      DMAP(w3 + 4 * UNIT, 0);
      DMAP(w4 + UNIT, 4);
      VMCNT(14); BAR();
      DMAP(w3 + 5 * UNIT, 1);
      VMCNT(14); BAR();
      DMAP(w3 + 6 * UNIT, 2);
      VMCNT(7); BAR();
      DMAP(w4 + 2 * UNIT, 3);
      VMCNT(7); BAR();
      {
        u32* fp = cnt_ptr(ws, C_H + (L * 64 + 2 * rb + (lane & 1)) * 8);
        const int ho = opq(OFF_H + L * HB + 2 * rb * 112 * 1024 + hw * 7168);
        v4u hx[28];
        auto ldh = [&](auto I) AI { constexpr int i = I.value; hx[i] = ld16(rs, l16, ho + (i & 1) * 112 * 1024 + p4_koff(p4_oh(i >> 1))); };
        auto poll = [&](int lv) AI { return __hip_atomic_load(fp + 32 * lv, __ATOMIC_RELAXED, __HIP_MEMORY_SCOPE_AGENT); };
        DMAP(w4, 0);
        wait_flags(ws, fp, poll(0));
        static_for<8>([&](auto I) AI { ldh(I); });
        BAR();
        u32 f = poll_issue(fp + 32);
        DMA(w4 + 3 * UNIT, 1);
        VMCNT(7); PIN(f);
        wait_flags(ws, fp + 32, f);
        static_for<6>([&](auto I) AI { ldh(IC<8 + I.value>{}); });
        f32x16 acc[4] = {};
        auto ord = [](int i) constexpr { return i < 20 ? i : (i < 24 ? 20 + 2 * (i - 20) : 21 + 2 * (i - 24)); };
        const int pb0 = opqv(p4_part(0, hw) + opqv(l16)), pb2 = opqv(p4_part(2, hw) + opqv(l16));
        static_for<28>([&](auto I) AI {
          constexpr int i = I.value, k = ord(i), t = k >> 1, tq = k & 1;
          if constexpr (i == 0) f = poll(2);
          if constexpr (i == H2) {
            wait_flags(ws, fp + 64, f);
            VMCNT(2); arrive_lds(LD, 4, lane);
          }
          if constexpr (i == H3 - 3) f = poll(3);
          if constexpr (i == H3) wait_flags(ws, fp + 96, f);
          if constexpr (i == 24) wait_arr(LD, 2, 4 * L + 4);
          acc[tq] = mfma(wr[2 * t], hx[k], acc[tq]);
          acc[2 + tq] = mfma(wr[2 * t + 1], hx[k], acc[2 + tq]);
          if constexpr (i < 2) ldh(IC<14 + i>{});
          if constexpr (i >= H2 && i < H2 + 4) { ldh(IC<16 + 2 * (i - H2)>{}); ldh(IC<17 + 2 * (i - H2)>{}); }
          if constexpr (i >= H3 && i < H3 + 2) { ldh(IC<24 + 2 * (i - H3)>{}); ldh(IC<25 + 2 * (i - H3)>{}); }
          if constexpr (i >= 24) {
            constexpr int qd = i - 24;
            *(LAS f32x4*)(LD + pb0 + qd * 1024) = f32x4{acc[0][4 * qd], acc[0][4 * qd + 1], acc[0][4 * qd + 2], acc[0][4 * qd + 3]};
            *(LAS f32x4*)(LD + pb2 + qd * 1024) = f32x4{acc[2][4 * qd], acc[2][4 * qd + 1], acc[2][4 * qd + 2], acc[2][4 * qd + 3]};
          }
          SB();
        });
        wait_arr(LD, 3, 4 * L + 4);
        put16(acc[1], LD, opqv(p4_part(1, hw) + opqv(l16)));
        put16(acc[3], LD, opqv(p4_part(3, hw) + opqv(l16)));
        BAR();
      }
      if (L == 0) { DMA(w1f + UNIT, 3); DMA(w1f, 1); }
    };
    hlayer(IC<0>{});
    hlayer(IC<1>{});
    if (b == 0 && hw == 1 && lane == 0) __hip_atomic_store((unsigned long long*)(ws + EPOCH_OFF), tag, __ATOMIC_RELAXED, __HIP_MEMORY_SCOPE_AGENT);
    return;
  }

  const int tt = 4 * g + w;
  const int tt2 = 2 * rb + (w & 1);
  const int kh = w >> 1;
  const rsrc_t rx0 = __builtin_amdgcn_make_buffer_rsrc((void*)A.x0, 0, XB, 0x00020000);
  v4u res[2];
#define XWV 14
  constexpr int XW = XWV;

  auto clayer = [&](auto LC) AI {
    constexpr int L = LC.value;
    v4u x[28];
    v2u st[8];
    float mean, rstd;
    v4u pq[4];
    STAMP(0);
    auto phase1 = [&](auto FIRST) AI {
      constexpr bool first = FIRST.value;
      const int xo = opq(first ? tt * 32 * (D * 2) : OFF_X2 + tt * 28 * 1024);
      auto ldx = [&](int ks) AI { return ld16(rs, l16, xo + ks * 1024); };
      const int so_ = opq(OFF_ST2 + tt * 4096);
      const int sv_ = (opqv(lane) & 31) * 8 + h * 2048;
      v4u gx[28];
      const int stg = L_STAGE + w * STG;
      const int gv = (lane >> 4) * (D * 2) + (lane & 15) * 16, gv3 = (lane >> 3) * (D * 2) + (lane & 7) * 16;
      const int gl = stg + (lane >> 4) * 272 + (lane & 15) * 16, gl3 = stg + (lane >> 3) * 272 + (lane & 7) * 16;
      const int srd = opqv(stg + (opqv(lane) & 31) * 272 + h * 16);
      auto ldg = [&](int i) AI {
        return i < 24 ? __builtin_amdgcn_raw_buffer_load_b128(rx0, gv, xo + (i & 7) * 4 * (D * 2) + (i >> 3) * 256, 0)
                      : __builtin_amdgcn_raw_buffer_load_b128(rx0, gv3, xo + (i & 7) * 8 * (D * 2) + 768, 0);
      };
      auto stage = [&](int c) AI {
#pragma unroll
        for (int i = 0; i < (c < 3 ? 8 : 4); ++i) *(LAS v4u*)(LD + (c < 3 ? gl + i * 4 * 272 : gl3 + i * 8 * 272)) = gx[8 * c + i];
#pragma unroll
        for (int i = 0; i < (c < 3 ? 8 : 4); ++i) x[8 * c + i] = lds16(LD, srd + i * 32);
      };
      unsigned long long iv = 0;
      unsigned long long* initp = (unsigned long long*)(ws + (w == 3 && lane < NZ ? INIT_OFF + lane * 128 : EPOCH_OFF));
      auto init_ok = [&](unsigned long long v) AI {
        const unsigned long long e = ((unsigned long long)(u32)__builtin_amdgcn_readlane((int)(v >> 32), 63) << 32) | (u32)__builtin_amdgcn_readlane((int)v, 63);
        return __all(lane >= NZ || v == e + 1ull);
      };
      if constexpr (first) {
#pragma unroll
        for (int i = 0; i < 16; ++i) gx[i] = ldg(i);
        stage(0);
      } else {
#pragma unroll
        for (int ks = 0; ks < XW; ++ks) x[ks] = ldx(ks);
      }
      USTAMP(18);
      BAR();
      USTAMP(19);
      f32x16 ak = {}, av = {}, p = {};
      const int sbk = first ? opqv(opqv(w1lane) + 4 * UNIT) : opqv(opqv(l16) + 3 * UNIT), sbv = first ? opqv(w1lane) : opqv(l16);
      const int csb = opqv(L_CS1 + 16 * (opqv(lane) >> 5)), wb = opqv(L_WPHI + opqv(l16));
      const int xk = opqv(L_W2X + w * 1024 + opqv(l16));
      f32x4 cs[4]; v4u kf[2], wf[2];
      float nmean, s1 = 0.f, s2 = 0.f, q1 = 0.f, q2 = 0.f;
      auto isk = [](int i) constexpr { return i < LAG || (i < 56 - LAG && ((i - LAG) & 1) == 0); };
      auto kst = [](int i) constexpr { return i < LAG ? i : (i < 56 - LAG ? ((((i - LAG) & 1) == 0) ? LAG + (i - LAG) / 2 : (i - LAG) / 2) : i - 28); };
      auto rdw = [&](int base, int ks) AI { if constexpr (first) return tr_frag(LD, base + ks * 1024, 256); else return lds16(LD, base + ks * 1024); };
      pipe<56, first ? 2 : 1>([&](auto I) AI { return rdw(isk(I.value) ? sbk : sbv, kst(I.value)); },
               [&](auto I) AI {
                 constexpr int i = I.value;
                 if constexpr (first && (i == 6 || i == 14 || i == 22 || i == 30 || i == 38 || i == 46)) { USTAMP(40 + i / 8 * 2); RAWBAR(); USTAMP(41 + i / 8 * 2); }
                 if constexpr (i == 54) { USTAMP(20); BAR(); USTAMP(21); }
               },
               [&](auto I, v4u a, auto nx) AI {
                 constexpr int i = I.value, ks = kst(i);
                 if constexpr (i == 54) { p = mfma(wf[0], kf[0], p); p = mfma(wf[1], kf[1], p); }
                 if constexpr (isk(i)) ak = mfma(a, x[ks], ak); else av = mfma(a, x[ks], av);
                 nx();
                 if constexpr (!first && isk(i) && ks + XW < 28) x[ks + XW] = ldx(ks + XW);
                 if constexpr (first) {
                   if constexpr (i < 12) gx[16 + i] = ldg(16 + i);
                   if constexpr (isk(i) && ks % 8 == 6) stage(ks / 8 + 1);
                   if constexpr (isk(i)) {
                     const u32 one = 0x3f803f80u;
                     s1 = dot2(x[ks].x, one, s1); q1 = dot2(x[ks].x, x[ks].x, q1); s2 = dot2(x[ks].y, one, s2); q2 = dot2(x[ks].y, x[ks].y, q2);
                     s1 = dot2(x[ks].z, one, s1); q1 = dot2(x[ks].z, x[ks].z, q1); s2 = dot2(x[ks].w, one, s2); q2 = dot2(x[ks].w, x[ks].w, q2);
                   }
                   if constexpr (i == 45) iv = __hip_atomic_load(initp, __ATOMIC_RELAXED, __HIP_MEMORY_SCOPE_AGENT);
                   if constexpr (i == 51) { ln_finish(s1 + s2, q1 + q2, mean, rstd); nmean = -mean; }
                 } else {
                   if constexpr (!isk(i) && ks >= 4 && ks < 12) st[ks - 4] = ld8(rs, sv_ + (ks - 4) * 256, so_);
                   if constexpr (i == 45) { ln_stats(st, mean, rstd); nmean = -mean; }
                 }
                 if constexpr (i >= 46 && i < 50) cs[i - 46] = *(LAS f32x4*)(LD + csb + 32 * (i - 46));
                 if constexpr (i == 50) wf[0] = lds16(LD, wb);
                 if constexpr (i == 51) wf[1] = lds16(LD, wb + 1024);
                 if constexpr (i == 52 || i == 53) {
#pragma unroll
                   for (int qd = 2 * (i - 52); qd < 2 * (i - 52) + 2; ++qd) {
                     const float y0 = fmaf(nmean, cs[qd][0], ak[4 * qd]), y1 = fmaf(nmean, cs[qd][1], ak[4 * qd + 1]);
                     const float y2 = fmaf(nmean, cs[qd][2], ak[4 * qd + 2]), y3 = fmaf(nmean, cs[qd][3], ak[4 * qd + 3]);
                     kf[qd >> 1][2 * (qd & 1)] = pk2(y0, y1);
                     kf[qd >> 1][2 * (qd & 1) + 1] = pk2(y2, y3);
                   }
                   *(LAS v4u*)(LD + xk + (i - 52) * 4096) = kf[i - 52];
                   if constexpr (i == 52) *(LAS float*)(LD + L_KVF + w * 256 + opqv(lane) * 4) = rstd;
                 }
                 if constexpr (i == 55) {
#pragma unroll
                   for (int qd = 0; qd < 4; ++qd) cs[qd] = *(LAS f32x4*)(LD + csb + 128 + 32 * qd);
                 }
               });
      STAMP(1);
      const int lz = opqv(lane);
      const int trow = w * 32 + (lz & 31);
      const int pb = opqv(L_PHK + trow * PHK_STRIDE + 8 * (lz >> 5)), vb = opqv(L_V + trow * V_STRIDE + 8 * (lz >> 5));
      half_softmax<0>(p, h, rstd, pq[0], pq[1]);
#pragma unroll
      for (int s = 0; s < 2; ++s) {
        *(LAS v2u*)(LD + pb + 32 * s) = v2u{pq[s].x, pq[s].y};
        *(LAS v2u*)(LD + pb + 32 * s + 16) = v2u{pq[s].z, pq[s].w};
      }
      SB();
#pragma unroll
      for (int r = 0; r < 16; ++r) av[r] = fmaf(nmean, cs[r >> 2][r & 3], av[r]) * rstd;
      if (h) { av[12] = 1.f; av[13] = 0.f; av[14] = 0.f; av[15] = 0.f; }
#pragma unroll
      for (int qd = 0; qd < 4; ++qd) *(LAS v2u*)(LD + vb + 16 * qd) = v2u{pk2(av[4 * qd], av[4 * qd + 1]), pk2(av[4 * qd + 2], av[4 * qd + 3])};
      if constexpr (first) {
        if (w == 3 && !init_ok(iv)) {
          u32 spins = 0;
          while (!init_ok(__hip_atomic_load(initp, __ATOMIC_RELAXED, __HIP_MEMORY_SCOPE_AGENT))) {
            __builtin_amdgcn_s_sleep(1);
            if (++spins > 400000u) break;
          }
        }
      }
      STAMP(2);
      BAR();
      if constexpr (first) {
#pragma unroll
        for (int ro = 0; ro < 2; ++ro) {
          const int oi = min(kh * 4 + ro * 2 + h, 6);
          res[ro] = __builtin_amdgcn_raw_buffer_load_b128(rx0, tok * (D * 2) + (7 * cb + oi) * 16, tt2 * 32 * (D * 2), 0);
        }
      }
      f32x16 aq = {};
      const int sbq = first ? opqv(opqv(w1lane) + 1 * UNIT) : opqv(opqv(l16) + 1 * UNIT);
      pipe<28, first ? 2 : 1>([&](auto I) AI { return rdw(sbq, I.value); }, [&](auto) AI {},
               [&](auto I, v4u a, auto nx) AI { aq = mfma(a, x[I.value], aq); nx(); });
      if constexpr (first) wait_arr(LD, 1, 1);
#pragma unroll
      for (int qd = 0; qd < 4; ++qd) cs[qd] = *(LAS f32x4*)(LD + csb + 256 + 32 * qd);
#pragma unroll
      for (int r = 0; r < 16; ++r) aq[r] = fmaf(nmean, cs[r >> 2][r & 3], aq[r]);
      f32x16 p2 = {};
      const int wb2 = opqv(L_WPHI + opqv(l16));
      p2 = mfma(lds16(LD, wb2), pack8(aq, 0), p2);
      p2 = mfma(lds16(LD, wb2 + 1024), pack8(aq, 1), p2);
      half_softmax<0>(p2, h, rstd, pq[0], pq[1]);
      half_softmax<1>(p2, h, rstd, pq[2], pq[3]);
    };
    phase1(std::integral_constant<bool, L == 0>{});
    STAMP(3);
    if (w == 0) wait_cnt(ws, C_KV + L * 16 + j, 32);
    BAR();
    STAMP(4);
    kv_reduce(rs, LD, L, j, w, lane);
    BAR();
    STAMP(5);
    {
      f32x16 nm = {};
      const int kb = opqv(L_KVF + opqv(l16));
      v4u kf[4];
#pragma unroll
      for (int s = 0; s < 4; ++s) kf[s] = lds16(LD, kb + s * 1024);
#pragma unroll
      for (int s = 0; s < 4; ++s) nm = mfma(kf[s], pq[s], nm);
      auto r = __builtin_amdgcn_permlane32_swap(__float_as_uint(nm[12]), __float_as_uint(nm[12]), false, false);
      const float den = __uint_as_float(r[1]) + 1e-6f;
      const float rd = __builtin_amdgcn_rcpf(den);
#pragma unroll
      for (int i = 0; i < 16; ++i) nm[i] *= rd;
      const int oo = opq(OFF_O + L * OB + (tt * 32 + 2 * j) * 1024);
      st16(rs, l16, oo, pack8(nm, 0));
      st16(rs, l16, oo + 1024, pack8(nm, 1));
    }
    wave_signal(ws, C_O + (L * 64 + tt) * 2 + (j >> 3), lane);
    STAMP(6);
    wait_cnt(ws, C_O + (L * 64 + tt2) * 2 + kh, 8, true);
    STAMP(7);
    {
      const int xo = opq(OFF_O + L * OB + (tt2 * 32 + 16 * kh) * 1024);
#pragma unroll
      for (int t = 0; t < (XW < 16 ? XW : 16); ++t) x[t] = ld16(rs, l16, xo + t * 1024);
      if (L == 0) RAWBAR();
      f32x16 a0 = {}, a1 = {};
      const int sb2 = opqv(2 * UNIT + kh * 2048 + opqv(l16)), sbx = opqv(L_W2X + kh * 2048 + opqv(l16));
      pipe<32>([&](auto I) AI { constexpr int i = I.value, t = i >> 1; return lds16(LD, (t < 14 ? sb2 + t * 4096 : sbx + (t - 14) * 4096) + (i & 1) * 1024); },
               [&](auto I) AI { if constexpr (I.value == 14) RAWBAR();   },
               [&](auto I, v4u a, auto nx) AI {
                 constexpr int i = I.value;
                 if constexpr (i & 1) a1 = mfma(a, x[i >> 1], a1); else a0 = mfma(a, x[i >> 1], a0);
                 nx();
                 if constexpr (!(i & 1) && (i >> 1) + XW < 16) x[(i >> 1) + XW] = ld16(rs, l16, xo + ((i >> 1) + XW) * 1024);
               });
      const int xb = opqv(L_SCR + w * 4096 + opqv(l16)), xb2 = opqv(L_SCR + (w ^ 2) * 4096 + opqv(l16));
      f32x16 fin;
      if (kh) {
#pragma unroll
        for (int qd = 0; qd < 4; ++qd) *(LAS f32x4*)(LD + xb + qd * 1024) = f32x4{a0[4 * qd], a0[4 * qd + 1], a0[4 * qd + 2], a0[4 * qd + 3]};
        fin = a1;
      } else {
#pragma unroll
        for (int qd = 0; qd < 4; ++qd) *(LAS f32x4*)(LD + xb + qd * 1024) = f32x4{a1[4 * qd], a1[4 * qd + 1], a1[4 * qd + 2], a1[4 * qd + 3]};
        fin = a0;
      }
      BAR();
#pragma unroll
      for (int qd = 0; qd < 4; ++qd) {
        const f32x4 v = *(LAS f32x4*)(LD + xb2 + qd * 1024);
#pragma unroll
        for (int i = 0; i < 4; ++i) fin[4 * qd + i] += v[i];
      }
      finish_tile(fin, res, rs, opq(OFF_X1 + L * XB), opq(OFF_ST1 + L * STB), tt2, cb, kh, lane, false, A.out);
    }
    wave_signal(ws, C_X1 + L * 64 + tt2, lane);
    STAMP(8);
    wait_cnt(ws, C_X1 + L * 64 + tt, 16, true);
    STAMP(9);
    {
      const int xo = opq(OFF_X1 + L * XB + tt * 28 * 1024);
      const int so_ = opq(OFF_ST1 + L * STB + tt * 4096);
      const int sv_ = (opqv(lane) & 31) * 8 + h * 2048;
      constexpr int XW3 = 8;
#pragma unroll
      for (int ks = 0; ks < XW3; ++ks) x[ks] = ld16(rs, l16, xo + ks * 1024);
      const int ho = opq(OFF_H + L * HB + (tt * 112 + 7 * j) * 1024);
      const int lvo = opq(C_H + (L * 64 + tt) * 8);
      u32* fp = cnt_ptr(ws, C_H + (L * 64 + tt2) * 8);
      auto poll = [&](int lv) AI { return __hip_atomic_load(fp + 32 * lv, __ATOMIC_RELAXED, __HIP_MEMORY_SCOPE_AGENT); };
      u32 f;
      float nmean, gs, irs;
      const int csb = opqv(L_CS3 + 16 * (opqv(lane) >> 5));
      const int sbA = opqv(l16), sbB = opqv(opqv(l16) + 2 * UNIT);
      f32x4 Gcg[2], Gcu[2]; float Gt[8], Gyu[8]; u32 Go[4];
      auto glu = [&](const f32x16& a, auto N_, auto S_) AI {
        constexpr int n = N_.value, s = S_.value;
        if constexpr (s == 1) Gcu[0] = *(LAS f32x4*)(LD + csb + n * 128 + 32);
        if constexpr (s == 4) Gcg[1] = *(LAS f32x4*)(LD + csb + n * 128 + 64);
        if constexpr (s == 9) Gcu[1] = *(LAS f32x4*)(LD + csb + n * 128 + 96);
        if constexpr (s == 12 && n < 6) Gcg[0] = *(LAS f32x4*)(LD + csb + (n + 1) * 128);
        static_for<8>([&](auto V_) AI {
          constexpr int v = V_.value, k = s - (1 + 4 * (v >> 1) + (v & 1)), hf = v >> 2, i = v & 3;
          if constexpr (k == 0) { Gt[v] = fmaf(nmean, Gcg[hf][i], a[8 * hf + i]); PIN(Gt[v]); }
          if constexpr (k == 1) { Gt[v] = Gt[v] * gs; PIN(Gt[v]); }
          if constexpr (k == 2) { Gt[v] = __builtin_amdgcn_exp2f(Gt[v]); PIN(Gt[v]); }
          if constexpr (k == 3) { Gt[v] = fmaf(Gt[v], irs, irs); PIN(Gt[v]); }
          if constexpr (k == 4) { Gt[v] = __builtin_amdgcn_rcpf(Gt[v]); PIN(Gt[v]); }
          if constexpr (k == 5) { Gyu[v] = fmaf(nmean, Gcu[hf][i], a[8 * hf + 4 + i]); PIN(Gyu[v]); }
          if constexpr (k == 6) { Gt[v] = Gt[v] * Gyu[v]; PIN(Gt[v]); }
          if constexpr (k == 7 && (v & 1)) { Go[v >> 1] = pk2(Gt[v - 1], Gt[v]); PIN(Go[v >> 1]); }
        });
        if constexpr (s == 21) st16(rs, l16, ho + n * 1024, v4u{Go[0], Go[1], Go[2], Go[3]});
      };
      const int ho2 = opq(OFF_H + L * HB + tt2 * 112 * 1024 + kh * 7168);
      v4u hx[28];
      auto ldh = [&](auto T) AI { hx[T.value] = ld16(rs, l16, ho2 + p4_koff(p4_oc(T.value))); };
      f32x16 acc[7];
      constexpr int PB = 7 * PK;
      pipe<196>(
          [&](auto I) AI { constexpr int i = I.value, u = i / 28, sl = u & 3; return lds16(LD, (sl < 2 ? sbA : sbB) + (sl & 1) * UNIT + (i % 28) * 1024); },
          [&](auto I) AI { if constexpr (I.value > 28 && I.value % 28 == 6) { VSTAMP(30 + I.value / 28); RAWBAR(); WSTAMP(22 + I.value / 28); } },
          [&](auto I, v4u a, auto nx) AI {
            constexpr int i = I.value;
            if constexpr (i < PB) {
              constexpr int n = i % 7, ks = i / 7;
              if constexpr (ks == 0) { f32x16 z = {}; acc[n] = mfma(a, x[ks], z); } else acc[n] = mfma(a, x[ks], acc[n]);
              nx();
              constexpr int K2 = 28 - XW3 - PK, fx = ks < K2 ? XW3 + 2 * ks + (n ? 1 : 0) : XW3 + K2 + ks;
              if constexpr (n == 0 || (n == 3 && ks < K2)) x[fx] = ld16(rs, l16, xo + fx * 1024);
              if constexpr (n >= 5 && ks < 4) st[2 * ks + n - 5] = ld8(rs, sv_ + (2 * ks + n - 5) * 256, so_);
              if constexpr (i == 46) {
                ln_stats(st, mean, rstd);
                nmean = -mean; gs = -LOG2E * rstd; irs = __builtin_amdgcn_rcpf(rstd);
                PIN(nmean); PIN(gs); PIN(irs);
              }
            } else {
              constexpr int r = i - PB, n = r / MK, s = r % MK;
              acc[n] = mfma(a, x[PK + s], acc[n]);
              nx();
              if constexpr (r == 8) Gcg[0] = *(LAS f32x4*)(LD + csb);
              if constexpr (n >= 2 && s + MK < 22) glu(acc[n - 2], IC<n - 2>{}, IC<s + MK>{});
              if constexpr (n >= 1) glu(acc[n - 1], IC<n - 1>{}, IC<s>{});
              if constexpr (n >= 3 && s == 20 - MK) { VMCNT(0); signal_cnt(ws, lvo + n - 3); }
            }
          });
      STAMP(10);
      arrive_lds(LD, 2, lane);
      static_for<22>([&](auto S_) AI {
        if constexpr (S_.value + MK < 22) glu(acc[5], IC<5>{}, IC<S_.value + MK>{});
        glu(acc[6], IC<6>{}, S_);
        if constexpr (S_.value == 4) { VMCNT(0); signal_cnt(ws, lvo + 4); }
        if constexpr (S_.value == CP1) f = poll_issue(fp + 96);
      });
      STAMP(11);
      const int wo_ = opqv(kh * 3072 + opqv(l16)), wp_ = opqv(kh * 1024 + 1024 + opqv(l16));
      const int wo2 = opqv(wo_ + 3 * UNIT), wp2 = opqv(wp_ + 3 * UNIT);
      v4u wq[56];
      auto ldw = [&](auto K) AI {
        constexpr int k = K.value, t = k >> 1, wb = p4_wbase(t);
        if constexpr (wb >= 3 * UNIT) wq[k] = lds16(LD, ((k & 1) ? wp2 : wo2) + wb - 3 * UNIT);
        else wq[k] = lds16(LD, ((k & 1) ? wp_ : wo_) + wb);
      };
      VMCNT(1); signal_cnt(ws, lvo + 5);
      PIN(f);
      const u32 f2 = poll(4);
      if (__all(f >= 16u)) {
        static_for<4>([&](auto T) AI { ldh(T); });
        VMCNT(6); signal_cnt(ws, lvo + 6);
      } else {
        VMCNT(2); signal_cnt(ws, lvo + 6);
        wait_flags(ws, fp + 96, f);
        static_for<4>([&](auto T) AI { ldh(T); });
      }
      wait_flags(ws, fp + 128, f2);
      static_for<8>([&](auto T) AI { ldh(IC<4 + T.value>{}); });
      wait_arr(LD, 4, 4 * L + 4);
      static_for<4>([&](auto K) AI { ldw(K); });
      STAMP(12);
      f32x16 fin = {}, oth = {};
      static_for<28>([&](auto T) AI {
        constexpr int t = T.value;
        if constexpr (t == CP5) f = poll(5);
        if constexpr (t == CT5) {
          wait_flags(ws, fp + 160, f);
          f = poll(6);
          static_for<8>([&](auto K) AI { ldh(IC<12 + K.value>{}); });
          STAMP(13);
        }
        if constexpr (t == 12) {
          wait_flags(ws, fp + 192, f);
          static_for<8>([&](auto K) AI { ldh(IC<20 + K.value>{}); });
          STAMP(22);
        }
        if constexpr (t == 12) arrive_lds(LD, 3, lane);
        if constexpr (t + 2 < 28) { ldw(IC<2 * t + 4>{}); ldw(IC<2 * t + 5>{}); }
        oth = mfma(wq[2 * t + 1], hx[t], oth);
        fin = mfma(wq[2 * t], hx[t], fin);
        SB();
      });
      STAMP(14);
      wait_arr(LD, 3, 4 * L + 4);
      put16(oth, LD, opqv(L_SCR + w * 4096 + opqv(l16)));
      BAR();
#pragma unroll
      for (int c = 0; c < 4; ++c) add16(fin, LD, opqv(p4_part(w, c) + opqv(l16)));
      add16(fin, LD, opqv(L_SCR + (w ^ 2) * 4096 + opqv(l16)));
      STAMP(15);
      finish_tile(fin, res, rs, OFF_X2, OFF_ST2, tt2, cb, kh, lane, L == 1, A.out);
    }
    if (L == 0) {
      wave_signal(ws, C_X2 + tt2, lane);
      arrive_lds(LD, 0, lane);
      STAMP(16);
      wait_cnt(ws, C_X2 + tt, 16, true);
    }
    STAMP(17);
  };
  clayer(IC<0>{});
  clayer(IC<1>{});
}

extern "C" void kernel_launch(void* const* d_in, const int* in_sizes, int n_in, void* d_out, int out_size, void* d_ws,
                              size_t ws_size, hipStream_t stream) {
  Args a;
  a.x0 = (const u16*)d_in[0]; a.wqkv = (const u16*)d_in[1]; a.wphi = (const u16*)d_in[2]; a.wo = (const u16*)d_in[3];
  a.wg = (const u16*)d_in[5]; a.wu = (const u16*)d_in[6]; a.wd = (const u16*)d_in[7];
  a.ws = (char*)d_ws; a.out = (u16*)d_out;
  (void)hipFuncSetAttribute((const void*)k_main, hipFuncAttributeMaxDynamicSharedMemorySize, LDS_BYTES);
  k_main<<<256, 512, LDS_BYTES, stream>>>(a);
}
```

```cpp
#include <hip/hip_runtime.h>
#include <stdint.h>
#include <utility>
#include <type_traits>

typedef unsigned short u16;
typedef unsigned int u32;
typedef u32 v4u __attribute__((ext_vector_type(4)));
typedef u32 v2u __attribute__((ext_vector_type(2)));
typedef float f32x2 __attribute__((ext_vector_type(2)));
typedef float f32x4 __attribute__((ext_vector_type(4)));
typedef float f32x16 __attribute__((ext_vector_type(16)));
typedef __bf16 bf16x2 __attribute__((ext_vector_type(2)));
typedef __bf16 bf16x8 __attribute__((ext_vector_type(8)));
typedef short s16x4 __attribute__((ext_vector_type(4)));
#define LAS __attribute__((address_space(3)))
#define DI __device__ __forceinline__
#define AI __attribute__((always_inline))
template <int V> using IC = std::integral_constant<int, V>;

constexpr int S = 2048, D = 448, FF = 1792;
constexpr int UNIT = 28672;

constexpr int C_KV = 0;
constexpr int C_W2 = 32;
constexpr int C_W4 = 36;
constexpr int C_W3 = 40;
constexpr int C_ERR = 60;
constexpr int C_X2 = 64;
constexpr int C_X1 = 128;
constexpr int C_O = 256;
constexpr int C_H = 512;
constexpr int C_W1 = 1536;
constexpr int NZ = 25;
constexpr int EPOCH_OFF = NZ * 64 * 128;
constexpr int INIT_OFF = EPOCH_OFF + 256;
constexpr int CTL_BYTES = EPOCH_OFF + 4096;
constexpr int W2B = 65536;
constexpr int OFF_W1 = CTL_BYTES;
constexpr int OFF_W2 = OFF_W1 + 16 * 3 * UNIT;
constexpr int OFF_W3 = OFF_W2 + 8 * W2B;
constexpr int OFF_W4 = OFF_W3 + 16 * 7 * UNIT;
constexpr int OFF_CS3 = OFF_W4 + 8 * 8 * UNIT;
constexpr int XB = S * D * 2;
constexpr int OB = S * 512 * 2;
constexpr int OFF_X2 = OFF_CS3 + 16 * 28 * 896;
constexpr int OFF_X1 = OFF_X2 + XB;
constexpr int OFF_O = OFF_X1 + 2 * XB;
constexpr int STB = S * 16 * 8;
constexpr int OFF_ST2 = OFF_O + 2 * OB;
constexpr int OFF_ST1 = OFF_ST2 + STB;
constexpr int KVB = 16 * 16 * 4096;
constexpr int OFF_KVP = OFF_ST1 + 2 * STB;
constexpr int HB = S * FF * 2;
constexpr int OFF_H = OFF_KVP + 2 * KVB;
constexpr int WS_USED = OFF_H + 2 * HB;

constexpr int L_SCR = 4 * UNIT;
constexpr int PHK_STRIDE = 136, V_STRIDE = 72;
constexpr int L_PHK = L_SCR;
constexpr int L_V = L_SCR + 128 * PHK_STRIDE;
constexpr int L_KVF = L_SCR + 28672;
constexpr int L_W2X = L_KVF + 4096;
constexpr int L_CONST = 155648;
constexpr int L_CS1 = L_CONST, L_CS3 = L_CONST + 384, L_WPHI = L_CONST + 1280, L_ARR = L_CONST + 3328;
constexpr int L_STAGE = 2 * UNIT;
constexpr int STG = 8704;
constexpr int LDS_BYTES = 163840;

constexpr float LOG2E = 1.4426950408889634f;
#define PKV 12
constexpr int PK = PKV, MK = 28 - PK;
constexpr int LAG = 4;

DI u32 pk2(float a, float b) { f32x2 v = {a, b}; return __builtin_bit_cast(u32, __builtin_convertvector(v, bf16x2)); }
DI float bflo(u32 v) { return __uint_as_float(v << 16); }
DI float bfhi(u32 v) { return __uint_as_float(v & 0xffff0000u); }
DI f32x16 mfma(v4u a, v4u b, f32x16 c) {
  f32x16 d = __builtin_amdgcn_mfma_f32_32x32x16_bf16(__builtin_bit_cast(bf16x8, a), __builtin_bit_cast(bf16x8, b), c, 0, 0, 0);
  asm volatile("" : "+v"(d));
  __builtin_amdgcn_sched_barrier(0);
  return d;
}
DI float xsum(float v) {
  auto r = __builtin_amdgcn_permlane32_swap(__float_as_uint(v), __float_as_uint(v), false, false);
  return __uint_as_float(r[0]) + __uint_as_float(r[1]);
}
DI float xmax(float v) {
  auto r = __builtin_amdgcn_permlane32_swap(__float_as_uint(v), __float_as_uint(v), false, false);
  return fmaxf(__uint_as_float(r[0]), __uint_as_float(r[1]));
}
DI float xmin(float v) {
  auto r = __builtin_amdgcn_permlane32_swap(__float_as_uint(v), __float_as_uint(v), false, false);
  return fminf(__uint_as_float(r[0]), __uint_as_float(r[1]));
}
#define BAR() asm volatile("s_waitcnt lgkmcnt(0)\n\ts_barrier" ::: "memory")
#define RAWBAR() asm volatile("s_barrier" ::: "memory")
#define VMCNT(n) asm volatile("s_waitcnt vmcnt(" #n ")" ::: "memory")
#define SB() __builtin_amdgcn_sched_barrier(0)
#define PIN(x) asm volatile("" : "+v"(x))

typedef __amdgpu_buffer_rsrc_t rsrc_t;
DI v4u ld16(rsrc_t rs, int voff, int soff) { return __builtin_amdgcn_raw_buffer_load_b128(rs, voff, soff, 16); }
DI v2u ld8(rsrc_t rs, int voff, int soff) { return __builtin_amdgcn_raw_buffer_load_b64(rs, voff, soff, 16); }
DI u32 ld4(rsrc_t rs, int voff, int soff) { return __builtin_amdgcn_raw_buffer_load_b32(rs, voff, soff, 16); }
DI void st16(rsrc_t rs, int voff, int soff, v4u v) {
  __builtin_amdgcn_raw_buffer_store_b128(v, rs, voff, soff, 16);
  asm volatile("s_nop 1" ::"v"(v) : "memory");
}
DI void st8(rsrc_t rs, int voff, int soff, v2u v) { __builtin_amdgcn_raw_buffer_store_b64(v, rs, voff, soff, 16); }
DI void st4(rsrc_t rs, int voff, int soff, u32 v) { __builtin_amdgcn_raw_buffer_store_b32(v, rs, voff, soff, 16); }
DI int opq(int v) { asm volatile("" : "+s"(v)); return v; }
DI int opqv(int v) { asm volatile("" : "+v"(v)); return v; }
DI v4u lds16(LAS char* l, int off) { return *(LAS v4u*)(l + off); }
DI float dot2(u32 a, u32 b, float c) {
  return __builtin_amdgcn_fdot2_f32_bf16(__builtin_bit_cast(bf16x2, a), __builtin_bit_cast(bf16x2, b), c, false);
}
DI v4u tr_frag(LAS char* L, int base, int step) {
  v2u lo = __builtin_bit_cast(v2u, __builtin_amdgcn_ds_read_tr16_b64_v4i16((LAS s16x4*)(L + base)));
  v2u hi = __builtin_bit_cast(v2u, __builtin_amdgcn_ds_read_tr16_b64_v4i16((LAS s16x4*)(L + base + step)));
  return v4u{lo.x, lo.y, hi.x, hi.y};
}
DI float sum8(v4u o) {
  const u32 one = 0x3f803f80u;
  return dot2(o.x, one, dot2(o.y, one, 0.f)) + dot2(o.z, one, dot2(o.w, one, 0.f));
}

DI u32* cnt_ptr(char* ws, int idx) { return (u32*)(ws + idx * 128); }
#define POLL_D0 16
#define LATE_ARR
#define WOV 0
#define WOS 20
#define H2 6
#define H3 12
#define CP1 12
#define CP5 0
#define CT5 11
#define PT5 8
#define WRO 0
#define POLL_IV 1
DI bool spin_more(char* ws, u32& spins) {
  u32* e = cnt_ptr(ws, C_ERR);
  __builtin_amdgcn_s_sleep(POLL_IV);
  ++spins;
  if ((spins & 255u) == 0u) {
    if (__builtin_amdgcn_readfirstlane(__hip_atomic_load(e, __ATOMIC_RELAXED, __HIP_MEMORY_SCOPE_AGENT)) != 0) return false;
    if (spins > 400000u) { __hip_atomic_store(e, 1u, __ATOMIC_RELAXED, __HIP_MEMORY_SCOPE_AGENT); return false; }
  }
  return true;
}
DI void wait_cnt(char* ws, int idx, u32 target, bool delay = false) {
  u32* c = cnt_ptr(ws, idx);
  u32 spins = 0;
  if (delay && POLL_D0) __builtin_amdgcn_s_sleep(POLL_D0);
  while ((u32)__builtin_amdgcn_readfirstlane(__hip_atomic_load(c, __ATOMIC_RELAXED, __HIP_MEMORY_SCOPE_AGENT)) < target)
    if (!spin_more(ws, spins)) break;
  __builtin_amdgcn_fence(__ATOMIC_ACQUIRE, "wavefront");
  asm volatile("" ::: "memory");
}
DI void wait_flags(char* ws, u32* fp, u32 f, u32 target = 16u) {
  if (__builtin_expect(!__all(f >= target), 0)) {
    u32 spins = 0;
    while (spin_more(ws, spins))
      if (__all(__hip_atomic_load(fp, __ATOMIC_RELAXED, __HIP_MEMORY_SCOPE_AGENT) >= target)) break;
  }
  __builtin_amdgcn_fence(__ATOMIC_ACQUIRE, "wavefront");
  asm volatile("" ::: "memory");
}
DI u32 poll_issue(const u32* p) { u32 f; asm volatile("global_load_dword %0, %1, off sc1" : "=v"(f) : "v"(p) : "memory"); return f; }
DI void signal_cnt(char* ws, int idx) {
  unsigned long long sv;
  asm volatile("s_mov_b64 %0, exec\n\ts_mov_b64 exec, 1\n\tglobal_atomic_add %1, %2, %3\n\ts_mov_b64 exec, %0"
               : "=&s"(sv) : "v"(idx * 128), "v"(1), "s"(ws) : "memory");
}
DI void wave_signal(char* ws, int idx, int lane) { VMCNT(0); signal_cnt(ws, idx); }
DI void arrive_lds(LAS char* LD, int slot, int lane) {
  if (lane == 0) __hip_atomic_fetch_add((LAS u32*)(LD + L_ARR + slot * 4), 1u, __ATOMIC_RELAXED, __HIP_MEMORY_SCOPE_WORKGROUP);
}
DI void wait_arr(LAS char* LD, int slot, u32 target) {
  while ((u32)__builtin_amdgcn_readfirstlane(__hip_atomic_load((LAS u32*)(LD + L_ARR + slot * 4), __ATOMIC_RELAXED, __HIP_MEMORY_SCOPE_WORKGROUP)) < target)
    __builtin_amdgcn_s_sleep(1);
}

#define STAMP(i)
#define HSTAMP(i)
#define VSTAMP(i)
#define WSTAMP(i)
#define USTAMP(i)

#define DMA_STEP(GSTEP) \
      "s_add_u32 m0, m0, 0x1000\n\t" \
      "v_add_u32 %1, " GSTEP ", %1\n\t" \
      "global_load_lds_dwordx4 %1, %2 sc1\n\t"
#define DMA_FN(NAME, BODY) \
DI void NAME(const char* gsrc, u32 voff, u32 lds_dst) { \
  u32 keep; \
  asm volatile("s_mov_b32 %0, m0\n\ts_mov_b32 m0, %3\n\ts_nop 0\n\tglobal_load_lds_dwordx4 %1, %2 sc1\n\t" BODY "s_mov_b32 m0, %0" \
               : "=&s"(keep), "+v"(voff) : "s"(gsrc), "s"(lds_dst) : "memory", "scc"); }
DMA_FN(dma7, DMA_STEP("0x1000") DMA_STEP("0x1000") DMA_STEP("0x1000") DMA_STEP("0x1000") DMA_STEP("0x1000") DMA_STEP("0x1000"))
DMA_FN(dma2, DMA_STEP("0x1000"))
#define PACE 1
#define STR2(x) #x
#define STR(x) STR2(x)
#define PSTEP "s_sleep " STR(PACE) "\n\t" DMA_STEP("0x1000")
DMA_FN(dma7p, PSTEP PSTEP PSTEP PSTEP PSTEP PSTEP)
DMA_FN(dma7n, DMA_STEP("0x2a000") DMA_STEP("0x2a000") DMA_STEP("0x2a000") DMA_STEP("0x2a000") DMA_STEP("0x2a000") DMA_STEP("0x2a000"))

template <int LO, int GO>
DI void dma1(const char* gsrc, u32 voff, u32 lb) {
  u32 keep, tmp;
  asm volatile("s_mov_b32 %0, m0\n\ts_add_u32 m0, %4, %5\n\tv_add_u32 %1, %6, %2\n\tglobal_load_lds_dwordx4 %1, %3 sc1\n\ts_mov_b32 m0, %0"
               : "=&s"(keep), "=&v"(tmp) : "v"(voff), "s"(gsrc), "s"(lb), "i"(LO), "i"(GO) : "memory", "scc");
}

template <typename F, int... I>
DI void static_for_impl(F&& f, std::integer_sequence<int, I...>) { (f(IC<I>{}), ...); }
template <int N, typename F>
DI void static_for(F&& f) { static_for_impl(f, std::make_integer_sequence<int, N>{}); }

constexpr int PF = 6;
#define WEV 2
template <int N, int RPS = 1, typename RD, typename PRE, typename ST>
DI void pipe(RD rd, PRE pre, ST step) {
  v4u buf[PF];
  static_for<PF>([&](auto I) AI { buf[I.value] = rd(I); });
  SB();
  static_for<N>([&](auto I) AI {
    constexpr int i = I.value;
    pre(I);
    if constexpr (WEV > 1 && i % WEV == 0 && i + PF <= N) __builtin_amdgcn_s_waitcnt(0xC07F | (((PF - WEV) * RPS) << 8));
    const v4u a = buf[i % PF];
    step(I, a, [&]() AI { if constexpr (i + PF < N) buf[i % PF] = rd(IC<i + PF>{}); SB(); });
    SB();
  });
}

DI void ln_finish(float s, float q, float& mean, float& rstd) {
  s = xsum(s); q = xsum(q);
  mean = s * (1.f / 448.f);
  const float var = fmaxf(q * (1.f / 448.f) - mean * mean, 0.f);
  rstd = __builtin_amdgcn_rsqf(var + 1e-5f);
}
DI void ln_stats(const v2u (&st)[8], float& mean, float& rstd) {
  f32x2 a = __builtin_bit_cast(f32x2, st[0]), b = __builtin_bit_cast(f32x2, st[1]);
#pragma unroll
  for (int i = 2; i < 8; i += 2) { a += __builtin_bit_cast(f32x2, st[i]); b += __builtin_bit_cast(f32x2, st[i + 1]); }
  a += b;
  ln_finish(a[0], a[1], mean, rstd);
}
DI v4u pack8(const f32x16& a, int s) {
  v4u r = {pk2(a[8 * s], a[8 * s + 1]), pk2(a[8 * s + 2], a[8 * s + 3]), pk2(a[8 * s + 4], a[8 * s + 5]), pk2(a[8 * s + 6], a[8 * s + 7])};
  return r;
}
template <int SG>
DI void half_softmax(const f32x16& p, int h, float rstd, v4u& o0, v4u& o1) {
  auto mm = [](float a, float b) AI { return SG ? fminf(a, b) : fmaxf(a, b); };
  float m0 = mm(mm(p[0], p[1]), p[2]), m1 = mm(mm(p[3], p[4]), p[5]), m2 = mm(mm(p[6], p[7]), p[8]), m3 = mm(mm(p[9], p[10]), p[11]);
  float m4 = mm(mm(mm(p[12], p[13]), p[14]), p[15]);
  m4 = h ? p[0] : m4;
  float mx = mm(mm(mm(m0, m1), m2), mm(m3, m4));
  mx = SG ? xmin(mx) : xmax(mx);
  const float c = rstd * LOG2E;
  const float cc = SG ? -c : c, off = -mx * cc;
  f32x16 e;
  float s0 = 0.f, s1 = 0.f;
#pragma unroll
  for (int r = 0; r < 16; ++r) {
    float v = __builtin_amdgcn_exp2f(fmaf(p[r], cc, off));
    if (r >= 12) v = h ? 0.f : v;
    e[r] = v;
    if (r & 1) s1 += v; else s0 += v;
  }
  const float rs_ = __builtin_amdgcn_rcpf(xsum(s0 + s1));
#pragma unroll
  for (int r = 0; r < 16; ++r) e[r] *= rs_;
  o0 = pack8(e, 0); o1 = pack8(e, 1);
  PIN(o0); PIN(o1);
  SB();
}

DI void finish_tile(const f32x16& acc, v4u (&res)[2], rsrc_t rs, int xoff, int stoff, int tt, int cb, int nt, int lane, bool last, u16* out) {
  lane = opqv(lane);
  const int h = lane >> 5, tok = lane & 31;
#pragma unroll
  for (int ro = 0; ro < 2; ++ro) {
    v4u r = res[ro];
    v4u o;
    o.x = pk2(bflo(r.x) + acc[8 * ro + 0], bfhi(r.x) + acc[8 * ro + 1]);
    o.y = pk2(bflo(r.y) + acc[8 * ro + 2], bfhi(r.y) + acc[8 * ro + 3]);
    o.z = pk2(bflo(r.z) + acc[8 * ro + 4], bfhi(r.z) + acc[8 * ro + 5]);
    o.w = pk2(bflo(r.w) + acc[8 * ro + 6], bfhi(r.w) + acc[8 * ro + 7]);
    res[ro] = o;
    const int oi = nt * 4 + ro * 2 + h;
    if (oi < 7) {
      const int go = 7 * cb + oi;
      if (last) *(v4u*)(out + (size_t)(tt * 32 + tok) * D + go * 8) = o;
      else st16(rs, ((go >> 1) * 64 + tok + 32 * (go & 1)) * 16, xoff + tt * 28 * 1024, o);
    }
  }
  if (!last) {
    const bool v1 = !(nt == 1 && h == 1);
    const u32 one = 0x3f803f80u;
    float s0 = dot2(res[0].x, one, 0.f), s1 = dot2(res[1].x, one, 0.f), q0 = dot2(res[0].x, res[0].x, 0.f), q1 = dot2(res[1].x, res[1].x, 0.f);
    s0 = dot2(res[0].y, one, s0); s1 = dot2(res[1].y, one, s1); q0 = dot2(res[0].y, res[0].y, q0); q1 = dot2(res[1].y, res[1].y, q1);
    s0 = dot2(res[0].z, one, s0); s1 = dot2(res[1].z, one, s1); q0 = dot2(res[0].z, res[0].z, q0); q1 = dot2(res[1].z, res[1].z, q1);
    s0 = dot2(res[0].w, one, s0); s1 = dot2(res[1].w, one, s1); q0 = dot2(res[0].w, res[0].w, q0); q1 = dot2(res[1].w, res[1].w, q1);
    const float sum = xsum(s0 + (v1 ? s1 : 0.f));
    const float sq = xsum(q0 + (v1 ? q1 : 0.f));
    if (h == 0) st8(rs, tok * 8, stoff + ((tt * 8 + cb) * 2 + nt) * 256, v2u{__float_as_uint(sum), __float_as_uint(sq)});
  }
}

DI void kv_reduce(rsrc_t rs, LAS char* LD, int L, int j, int w, int lane) {
  const int ko = opq(OFF_KVP + L * KVB + j * 16 * 4096 + (w >> 1) * 1024 + (w & 1) * 512);
  const int lz = opqv(lane);
  const int vo = (lz >> 5) * 4096 + (lz & 31) * 16;
  v4u t[8];
#pragma unroll
  for (int i = 0; i < 8; ++i) t[i] = ld16(rs, vo, ko + i * 8192);
  float s[8] = {};
  const u32 lo1 = (u32)opq(0x00003f80), hi1 = (u32)opq(0x3f800000);
#pragma unroll
  for (int i = 0; i < 8; ++i) {
#pragma unroll
    for (int c = 0; c < 4; ++c) { s[2 * c] = dot2(t[i][c], lo1, s[2 * c]); s[2 * c + 1] = dot2(t[i][c], hi1, s[2 * c + 1]); }
  }
#pragma unroll
  for (int c = 0; c < 8; ++c) s[c] = xsum(s[c]);
  if (lz < 32) *(LAS v4u*)(LD + L_KVF + (w >> 1) * 1024 + (w & 1) * 512 + lz * 16) = v4u{pk2(s[0], s[1]), pk2(s[2], s[3]), pk2(s[4], s[5]), pk2(s[6], s[7])};
}

constexpr int p4_oh(int t) { return 4 * t; }
constexpr int p4_oc(int t) { return 56 + 2 * t; }
constexpr int p4_koff(int o) { return (7 * (o & 15) + (o >> 4)) * 1024; }
constexpr int p4_wbase(int t) { return (t < 7 ? 0 : t < 14 ? L_SCR : t < 21 ? 3 * UNIT : UNIT) + (t % 7) * 4096; }
DI int p4_part(int a, int hw) {
  const int i = ((a >> 1) | ((a & 1) << 1)) * 4 + hw;
  return i < 7 ? 2 * UNIT + i * 4096 : i < 10 ? L_KVF + (i - 7) * 4096 : (i - 10) * 4096;
}
DI void put16(const f32x16& acc, LAS char* LD, int off) {
#pragma unroll
  for (int qd = 0; qd < 4; ++qd) *(LAS f32x4*)(LD + off + qd * 1024) = f32x4{acc[4 * qd], acc[4 * qd + 1], acc[4 * qd + 2], acc[4 * qd + 3]};
}
DI void add16(f32x16& acc, LAS char* LD, int off) {
#pragma unroll
  for (int qd = 0; qd < 4; ++qd) {
    const f32x4 v = *(LAS f32x4*)(LD + off + qd * 1024);
#pragma unroll
    for (int i = 0; i < 4; ++i) acc[4 * qd + i] += v[i];
  }
}

struct Args {
  const u16* x0; const u16* wqkv; const u16* wphi; const u16* wo; const u16* wg; const u16* wu; const u16* wd;
  char* ws; u16* out;
};

struct Retile { const v4u* pa[7]; int rd0, rd1, step, o0, o1, ocb, sig; };
DI void retile_prep(const Args& A, int job, int lane, int stage, Retile& R) {
  const int h = lane >> 5, q = (lane & 15) >> 2, c8 = ((lane >> 4) & 1) * 4 + (lane & 3);
  const int hh = c8 & 1, quad = c8 >> 1;
  if (job < 64 || job >= 512) {
    const bool iswo = job < 64;
    const int a0 = iswo ? job >> 2 : (job - 512) >> 2, cg = job & 3;
    const u16* src = (iswo ? A.wo + (size_t)a0 * 28 * D : A.wd + (size_t)a0 * 32 * D) + 112 * cg;
    const int nrow = iswo ? 28 : 32;
#pragma unroll
    for (int i = 0; i < 7; ++i) {
      const int p = lane + 64 * i, r = p / 14, c = p % 14;
      R.pa[i] = (const v4u*)(src + (size_t)(r < nrow ? r : 0) * D + c * 8);
    }
    const int rb = stage + ((iswo ? 4 : 8) * h + q) * 224 + 8 * (quad & 1);
    const int oi = (quad >> 1) * 2 + hh;
    R.rd0 = rb + 16 * oi; R.rd1 = rb + 16 * min(4 + oi, 6); R.step = (iswo ? 8 : 4) * 224;
    int ob[2];
#pragma unroll
    for (int k2 = 0; k2 < 2; ++k2) {
      const int ks = a0 * 2 + k2, o_ = 16 * (ks % 7) + ks / 7;
      int fr;
      if (o_ < 56) fr = 112 + ((o_ & 3) * 14 + (o_ >> 2)) * 2;
      else fr = (o_ - 56) * 2;
      ob[k2] = iswo ? OFF_W2 + cg * 2 * W2B + ((ks & 15) * 2 + (ks >> 4)) * 2048 : OFF_W4 + cg * 16 * UNIT + fr * 1024;
    }
    R.o0 = opq(ob[0]); R.o1 = opq(ob[1]); R.ocb = opq(iswo ? W2B : 8 * UNIT); R.sig = opq((iswo ? C_W2 : C_W4) + cg);
  } else {
    const int a = job - 64, j = a / 28, ks = a % 28;
#pragma unroll
    for (int i = 0; i < 7; ++i) {
      const int p = lane + 64 * i, r = p / 28, c = p % 28;
      R.pa[i] = (const v4u*)((c < 14 ? A.wg : A.wu) + (size_t)(ks * 16 + r) * FF + 112 * j + (c < 14 ? c : c - 14) * 8);
    }
    R.rd0 = stage + (8 * h + q) * 448 + (quad & 1) * 224 + (8 * hh + 4 * (quad >> 1)) * 2; R.rd1 = 0; R.step = 0;
    R.o0 = opq(OFF_W3 + j * 7 * UNIT + (ks < PK ? ks * 7 : 6 * PK + ks) * 1024); R.o1 = opq(ks < PK ? 1024 : MK * 1024);
    R.ocb = opq(OFF_CS3 + (j * 28 + ks) * 896); R.sig = opq(C_W3 + j);
  }
#pragma unroll
  for (int i = 0; i < 7; ++i) PIN(R.pa[i]);
  PIN(R.rd0); PIN(R.rd1);
}
DI void retile_load(const Retile& R, u32 lds_dst) {
#pragma unroll
  for (int i = 0; i < 7; ++i) {
    u32 keep;
    asm volatile("s_mov_b32 %0, m0\n\ts_add_u32 m0, %2, %3\n\ts_nop 0\n\tglobal_load_lds_dwordx4 %1, off\n\ts_mov_b32 m0, %0"
                 : "=&s"(keep) : "v"(R.pa[i]), "s"(lds_dst), "i"(i * 1024) : "memory", "scc");
  }
}
DI void retile_store(int job, const Retile& R, rsrc_t rs, LAS char* LD, int lane) {
  const int h = lane >> 5;
  if (job < 64 || job >= 512) {
    const bool pad = (lane & 0x14) == 0x14;
    const bool kpad = job < 64 && h;
    v4u o[8];
#pragma unroll
    for (int i = 0; i < 8; ++i) {
      const int ad = ((i & 2) ? R.rd1 : R.rd0) + (i >> 2) * 112 + (i & 1) * 16 * 224;
      const v2u lo = __builtin_bit_cast(v2u, __builtin_amdgcn_ds_read_tr16_b64_v4i16((LAS s16x4*)(LD + ad)));
      const v2u hi = __builtin_bit_cast(v2u, __builtin_amdgcn_ds_read_tr16_b64_v4i16((LAS s16x4*)(LD + ad + R.step)));
      o[i] = v4u{lo.x, lo.y, hi.x, hi.y};
    }
#pragma unroll
    for (int i = 0; i < 8; ++i) {
      if ((i & 2) && pad) o[i] = v4u{0u, 0u, 0u, 0u};
      if ((i & 1) && kpad) { o[i].z = 0u; o[i].w = 0u; }
      st16(rs, lane * 16, ((i & 1) ? R.o1 : R.o0) + (i >> 2) * R.ocb + ((i >> 1) & 1) * 1024, o[i]);
    }
  } else {
    v4u o[7];
#pragma unroll
    for (int n = 0; n < 7; ++n) o[n] = tr_frag(LD, R.rd0 + n * 32, 4 * 448);
    float cs[7];
#pragma unroll
    for (int n = 0; n < 7; ++n) {
      st16(rs, lane * 16, R.o0 + n * R.o1, o[n]);
      cs[n] = xsum(sum8(o[n]));
    }
#pragma unroll
    for (int n2 = 0; n2 < 4; ++n2) {
      const float v = h ? cs[n2 < 3 ? 2 * n2 + 1 : 6] : cs[2 * n2];
      if (n2 < 3 || !h) st4(rs, lane * 4, R.ocb + n2 * 256, __float_as_uint(v));
    }
  }
}

__global__ __launch_bounds__(512, 2) void k_main(const Args A) {
  extern __shared__ __attribute__((aligned(16))) char smem[];
  LAS char* LD = (LAS char*)smem;
  char* ws = A.ws;
  const int b = blockIdx.x, xcd = b & 7, bi = b >> 3;
  const int g = (xcd >> 2) * 8 + (bi >> 2), j = (xcd & 3) * 4 + (bi & 3);
  const int rb = 2 * g + (j >> 3), cb = j & 7;
  const int lane = threadIdx.x & 63, w = __builtin_amdgcn_readfirstlane(threadIdx.x >> 6);
  const int h = lane >> 5, tok = lane & 31;
  const rsrc_t rs = __builtin_amdgcn_make_buffer_rsrc((void*)ws, 0, WS_USED, 0x00020000);
  const u32 lbase = (u32)(size_t)smem;
  const int l16 = lane * 16;
  const int tq_ = (lane & 15) >> 2, tc8 = ((lane >> 4) & 1) * 4 + (lane & 3);
  const int w1lane = (8 * h + tq_) * 64 + min(tc8 + (j & 1), 7) * 8;

  const int hw = w - 4;
  const u32 voffn = (hw * 16 + (lane >> 2)) * (3 * D * 2) + (lane & 3) * 16;
  const char* w1 = (const char*)A.wqkv + 56 * j - (j & 1) * 8;
  unsigned long long ep;
  const u32 lb = lbase + hw * 1024;
#define KP(p) dma1<4 * UNIT + (p) * 4096, 896 + (p) * 0x2a000>(w1, voffn, lb)
#define VP(p) dma1<(p) * 4096, 1792 + (p) * 0x2a000>(w1, voffn, lb)
#define QP(p) dma1<UNIT + (p) * 4096, (p) * 0x2a000>(w1, voffn, lb)
  if (opq(w) >= 4) {
    HSTAMP(0);
    if (hw == 1 && b < NZ) {
      asm volatile("global_load_dwordx2 %0, %1, %2 sc1" : "=v"(ep) : "v"(EPOCH_OFF), "s"(ws) : "memory");
      st4(rs, lane * 128, b * 8192, 0u);
    }
    if (hw == 2) {
      dma1<L_KVF, 0>((const char*)A.wphi + j * 1568, l16, lbase);
      dma1<L_KVF + 1024, 0>((const char*)A.wphi + j * 1568, min(1024 + l16, 1552), lbase);
    }
    KP(0); KP(1); VP(0); KP(2); VP(1); KP(3); VP(2);
    if (hw == 3) {
      unsigned long long pcv;
      asm volatile("s_getpc_b64 %0" : "=s"(pcv));
      const char* pc = (const char*)(pcv & ~255ull);
      dma1<L_W2X, 0>(pc, bi * 1024 + l16, lbase);
      dma1<L_W2X + 1024, 32768>(pc, bi * 1024 + l16, lbase);
    }
    HSTAMP(1);
  }

  if (w >= 4) {
    const u32 voff = hw * 1024 + lane * 16;
    const char* w1f = ws + OFF_W1 + j * 3 * UNIT;
    const char* w2 = ws + OFF_W2 + cb * W2B;
    const char* w3 = ws + OFF_W3 + j * 7 * UNIT;
    const char* w4 = ws + OFF_W4 + cb * 8 * UNIT;
#define DMA(src, slot) dma7((src), voff, lbase + (slot) * UNIT + hw * 1024)
#define DMAP(src, slot) dma7p((src), voff, lbase + (slot) * UNIT + hw * 1024)
#define DMAN(which, slot) dma7n(w1 + (which) * 896, voffn, lbase + (slot) * UNIT + hw * 1024)
    v4u wr[28];
    unsigned long long tag = 0;
    auto hlayer = [&](auto LC) AI {
      constexpr int L = LC.value;
      Retile R;
      const int job = hw >= 2 ? b * 2 + hw - 2 : (hw == 0 && b < 224 ? 512 + b : -1);
      float c0 = 0.f, c1 = 0.f, c2 = 0.f, c3 = 0.f;
      auto colpart = [&](int slot, auto K0, auto K1) AI {
        constexpr int k0 = K0.value, n = K1.value - K0.value;
        const int ib = opqv(slot * UNIT + opqv(w1lane));
        v4u fr[n];
#pragma unroll
        for (int ks = 0; ks < n; ++ks) fr[ks] = tr_frag(LD, ib + (k0 + ks) * 1024, 256);
        SB();
        const u32 one = 0x3f803f80u;
#pragma unroll
        for (int ks = 0; ks < n; ++ks) { c0 = dot2(fr[ks].x, one, c0); c1 = dot2(fr[ks].y, one, c1); c2 = dot2(fr[ks].z, one, c2); c3 = dot2(fr[ks].w, one, c3); }
        SB();
      };
      auto colfin = [&](int which) AI {
        const float cs = xsum((c0 + c1) + (c2 + c3));
        if (lane < 32) *(LAS float*)(LD + L_CS1 + which * 128 + lane * 4) = cs;
      };
      v4u csa[7];
      auto export_img = [&](int slot, int which) AI {
        const int ib = opqv(slot * UNIT + opqv(w1lane) + g * 1024);
        const int eo = opq(OFF_W1 + (j * 3 + which) * UNIT + g * 1024);
        st16(rs, l16, eo, tr_frag(LD, ib, 256));
        if (g < 12) st16(rs, l16, eo + 16384, tr_frag(LD, ib + 16384, 256));
      };
      if constexpr (L == 0) {
        if (hw == 1 && lane < 2) *(LAS v4u*)(LD + L_ARR + l16) = v4u{0u, 0u, 0u, 0u};
        HSTAMP(2);
#define VMW(n, n3) if (hw == 3) { VMCNT(n3); } else { VMCNT(n); }
        VMW(4, 6); BAR();
        HSTAMP(3);
        KP(4); VP(3);
        if (hw == 1 && b < NZ) {
          PIN(ep);
          tag = ep + 1ull;
          if (lane == 0) __hip_atomic_store((unsigned long long*)(ws + INIT_OFF + b * 128), tag, __ATOMIC_RELAXED, __HIP_MEMORY_SCOPE_AGENT);
        }
        if (hw == 2) {
          const int wb = opqv(L_KVF + (4 * h + tq_) * 56 + tc8 * 8);
          const v4u f0 = tr_frag(LD, wb, 8 * 56);
          v4u f1 = tr_frag(LD, wb + 16 * 56, 8 * 56);
          if (h) { f1.z = 0u; f1.w = 0u; }
          *(LAS v4u*)(LD + L_WPHI + l16) = f0;
          *(LAS v4u*)(LD + L_WPHI + 1024 + l16) = f1;
        }
        if (hw == 0) colpart(4, IC<0>{}, IC<8>{});
        VMW(4, 6); BAR();
        HSTAMP(4);
        KP(5); VP(4);
        if (job >= 0) retile_prep(A, job, lane, L_STAGE + hw * STG, R);
        if (hw == 1) colpart(0, IC<0>{}, IC<8>{});
        VMW(4, 6); BAR();
        HSTAMP(5);
        KP(6); VP(5); VP(6);
        if (hw == 0) colpart(4, IC<8>{}, IC<16>{});
        if (hw == 1) colpart(0, IC<8>{}, IC<12>{});
        VMCNT(5); BAR();
        HSTAMP(6);
        QP(0); QP(1); QP(2); QP(3);
        if (hw == 0) colpart(4, IC<16>{}, IC<20>{});
        if (hw == 1) colpart(0, IC<12>{}, IC<16>{});
        VMCNT(7); BAR();
        HSTAMP(7);
        QP(4); QP(5); QP(6);
        if (hw == 0) colpart(4, IC<20>{}, IC<24>{});
        if (hw == 1) colpart(0, IC<16>{}, IC<20>{});
        VMCNT(7); BAR();
        HSTAMP(8);
        if (hw == 0) { colpart(4, IC<24>{}, IC<28>{}); colfin(0); }
        if (hw == 1) { colpart(0, IC<20>{}, IC<28>{}); colfin(1); }
        if (hw == 2) export_img(4, 1);
        if (hw == 3) export_img(0, 2);
        HSTAMP(9);
        BAR();
        HSTAMP(10);
        if (job >= 0) retile_load(R, lbase + L_STAGE + hw * STG);
      } else {
        wait_arr(LD, 0, 4);
        DMA(w1f + 2 * UNIT, 0);
        VMCNT(0); BAR();
      }
      {
        const int wb = opqv(L_WPHI + opqv(l16));
        const v4u wf0 = lds16(LD, wb), wf1 = lds16(LD, wb + 1024);
        if (L == 0) HSTAMP(11);
        BAR();
        if (L == 0) HSTAMP(12);
        __builtin_amdgcn_s_setprio(1);
        const int lz = opqv(lane);
        const int xk = opqv(L_W2X + hw * 1024 + lz * 16);
        const v4u kf0 = lds16(LD, xk), kf1 = lds16(LD, xk + 4096);
        const float rstd = *(LAS float*)(LD + L_KVF + hw * 256 + lz * 4);
        f32x16 p = {};
        p = mfma(wf0, kf0, p);
        p = mfma(wf1, kf1, p);
        v4u o0, o1;
        half_softmax<1>(p, h, rstd, o0, o1);
        const int pb = opqv(L_PHK + (hw * 32 + (lz & 31)) * PHK_STRIDE + 8 * (lz >> 5) + 64);
        *(LAS v2u*)(LD + pb) = v2u{o0.x, o0.y}; *(LAS v2u*)(LD + pb + 16) = v2u{o0.z, o0.w};
        *(LAS v2u*)(LD + pb + 32) = v2u{o1.x, o1.y}; *(LAS v2u*)(LD + pb + 48) = v2u{o1.z, o1.w};
        __builtin_amdgcn_s_setprio(0);
      }
      if (L == 0) HSTAMP(13);
      if constexpr (L == 0) {
        if (job >= 0) { VMCNT(7); } else { VMCNT(0); }
      } else VMCNT(0);
      if (L == 0) HSTAMP(14);
      BAR();
      if (hw < 2) {
        __builtin_amdgcn_s_setprio(2);
        f32x16 kp = {};
        const int lz = opqv(lane);
        const int g16 = (lz >> 4) & 1, q = (lz & 15) >> 2, p = lz & 3, hz = lz >> 5;
        const int ab0 = opqv(L_PHK + (8 * hz + q) * PHK_STRIDE + (g16 * 16 + 4 * p) * 2 + hw * 64);
        const int bb0 = opqv(L_V + (8 * hz + q) * V_STRIDE + (g16 * 16 + 4 * p) * 2);
        v4u ap[8], bv[8];
#pragma unroll
        for (int s = 0; s < 8; ++s) { ap[s] = tr_frag(LD, ab0 + 16 * s * PHK_STRIDE, 4 * PHK_STRIDE); bv[s] = tr_frag(LD, bb0 + 16 * s * V_STRIDE, 4 * V_STRIDE); }
        SB();
#pragma unroll
        for (int s = 0; s < 8; ++s) kp = mfma(ap[s], bv[s], kp);
        const int ko = opq(OFF_KVP + L * KVB + (j * 16 + g) * 4096 + hw * 2048);
        st16(rs, l16, ko, pack8(kp, 0));
        st16(rs, l16, ko + 1024, pack8(kp, 1));
        wave_signal(ws, C_KV + L * 16 + j, lane);
        __builtin_amdgcn_s_setprio(0);
      }
      if constexpr (L == 0) {
        HSTAMP(15);
        if (hw == 3) {
          colpart(1, IC<0>{}, IC<28>{}); colfin(2);
          arrive_lds(LD, 1, lane);
        }
        if (hw == 1) { export_img(1, 0); wave_signal(ws, C_W1 + j, lane); }
        if (job >= 0) { VMCNT(0); retile_store(job, R, rs, LD, lane); }
        HSTAMP(16);
        if (job >= 0) wave_signal(ws, R.sig, lane);
        if (hw >= 2) signal_cnt(ws, C_W1 + j);
        HSTAMP(17);
        BAR();
        HSTAMP(18);
        u32* fw = cnt_ptr(ws, (lane & 3) == 0 ? C_W2 + (cb >> 1) : (lane & 3) == 1 ? C_W3 + j : C_W4 + (cb >> 1));
        kv_reduce(rs, LD, L, j, w, lane);
        HSTAMP(19);
        BAR();
        HSTAMP(20);
        wait_flags(ws, fw, __hip_atomic_load(fw, __ATOMIC_RELAXED, __HIP_MEMORY_SCOPE_AGENT), (lane & 3) == 0 ? 16u : (lane & 3) == 1 ? 28u : 56u);
        HSTAMP(21);
        DMA(w2, 2); DMA(w2 + UNIT, 3); dma2(w2 + 2 * UNIT, voff, lbase + L_W2X + hw * 1024);
#pragma unroll
        for (int i = 0; i < 7; ++i) csa[i] = ld16(rs, min(lane, 55) * 16, OFF_CS3 + (j * 28 + hw * 7 + i) * 896);
        DMA(w3, 0);
        HSTAMP(22);
        VMCNT(14); HSTAMP(23); BAR();
        HSTAMP(24);
        DMA(w3 + UNIT, 1);
      } else {
        DMA(w2, 2); DMA(w2 + UNIT, 3); dma2(w2 + 2 * UNIT, voff, lbase + L_W2X + hw * 1024);
        DMA(w3, 0);
        BAR();
        kv_reduce(rs, LD, L, j, w, lane);
        BAR();
        DMA(w3 + UNIT, 1);
      }
      BAR();
      if constexpr (L == 0) {
        f32x4 c4 = __builtin_bit_cast(f32x4, csa[0]);
#pragma unroll
        for (int i = 1; i < 7; ++i) c4 += __builtin_bit_cast(f32x4, csa[i]);
        if (lane < 56) *(LAS f32x4*)(LD + L_KVF + hw * 896 + l16) = c4;
      }
      DMA(w3 + 2 * UNIT, 2);
      VMCNT(7); BAR();
      const int wo4 = opq(OFF_W4 + cb * 8 * UNIT + 4 * UNIT + hw * UNIT);
#define WR(lo, hi) _Pragma("unroll") for (int i = lo; i < hi; ++i) wr[i] = ld16(rs, l16, wo4 + i * 1024)
      if constexpr (L == 0) {
        const int t4 = min(hw * 64 + lane, 223) * 4;
        *(LAS float*)(LD + L_CS3 + t4) = (*(LAS float*)(LD + L_KVF + t4) + *(LAS float*)(LD + L_KVF + 896 + t4)) +
                                         (*(LAS float*)(LD + L_KVF + 1792 + t4) + *(LAS float*)(LD + L_KVF + 2688 + t4));
        WR(0, 28);
      }
      DMA(w3 + 3 * UNIT, 3);
      VMCNT(7);
      BAR();
      DMAP(w3 + 4 * UNIT, 0);
      DMAP(w4 + UNIT, 4);
      VMCNT(14); BAR();
      DMAP(w3 + 5 * UNIT, 1);
      if (L == 0) wait_cnt(ws, C_W1 + j, 48);
      VMCNT(14); BAR();
      DMAP(w3 + 6 * UNIT, 2);
      VMCNT(7); BAR();
      DMAP(w4 + 2 * UNIT, 3);
      VMCNT(7); BAR();
      {
        u32* fp = cnt_ptr(ws, C_H + (L * 64 + 2 * rb + (lane & 1)) * 8);
        const int ho = opq(OFF_H + L * HB + 2 * rb * 112 * 1024 + hw * 7168);
        v4u hx[28];
        auto ldh = [&](auto I) AI { constexpr int i = I.value; hx[i] = ld16(rs, l16, ho + (i & 1) * 112 * 1024 + p4_koff(p4_oh(i >> 1))); };
        auto poll = [&](int lv) AI { return __hip_atomic_load(fp + 32 * lv, __ATOMIC_RELAXED, __HIP_MEMORY_SCOPE_AGENT); };
        DMAP(w4, 0);
        wait_flags(ws, fp, poll(0));
        static_for<8>([&](auto I) AI { ldh(I); });
        BAR();
        u32 f = poll_issue(fp + 32);
        DMA(w4 + 3 * UNIT, 1);
        VMCNT(7); PIN(f);
        wait_flags(ws, fp + 32, f);
        static_for<6>([&](auto I) AI { ldh(IC<8 + I.value>{}); });
        f32x16 acc[4] = {};
        auto ord = [](int i) constexpr { return i < 20 ? i : (i < 24 ? 20 + 2 * (i - 20) : 21 + 2 * (i - 24)); };
        const int pb0 = opqv(p4_part(0, hw) + opqv(l16)), pb2 = opqv(p4_part(2, hw) + opqv(l16));
        static_for<28>([&](auto I) AI {
          constexpr int i = I.value, k = ord(i), t = k >> 1, tq = k & 1;
          if constexpr (i == 0) f = poll(2);
          if constexpr (i == H2) {
            wait_flags(ws, fp + 64, f);
            VMCNT(2); arrive_lds(LD, 4, lane);
          }
          if constexpr (i == H3 - 3) f = poll(3);
          if constexpr (i == H3) wait_flags(ws, fp + 96, f);
          if constexpr (i == 24) wait_arr(LD, 2, 4 * L + 4);
          acc[tq] = mfma(wr[2 * t], hx[k], acc[tq]);
          acc[2 + tq] = mfma(wr[2 * t + 1], hx[k], acc[2 + tq]);
          if constexpr (i < 2) ldh(IC<14 + i>{});
          if constexpr (i >= H2 && i < H2 + 4) { ldh(IC<16 + 2 * (i - H2)>{}); ldh(IC<17 + 2 * (i - H2)>{}); }
          if constexpr (i >= H3 && i < H3 + 2) { ldh(IC<24 + 2 * (i - H3)>{}); ldh(IC<25 + 2 * (i - H3)>{}); }
          if constexpr (i >= 24) {
            constexpr int qd = i - 24;
            *(LAS f32x4*)(LD + pb0 + qd * 1024) = f32x4{acc[0][4 * qd], acc[0][4 * qd + 1], acc[0][4 * qd + 2], acc[0][4 * qd + 3]};
            *(LAS f32x4*)(LD + pb2 + qd * 1024) = f32x4{acc[2][4 * qd], acc[2][4 * qd + 1], acc[2][4 * qd + 2], acc[2][4 * qd + 3]};
          }
          SB();
        });
        wait_arr(LD, 3, 4 * L + 4);
        put16(acc[1], LD, opqv(p4_part(1, hw) + opqv(l16)));
        put16(acc[3], LD, opqv(p4_part(3, hw) + opqv(l16)));
        BAR();
      }
      if (L == 0) { DMA(w1f + UNIT, 3); DMA(w1f, 1); }
    };
    hlayer(IC<0>{});
    hlayer(IC<1>{});
    if (b == 0 && hw == 1 && lane == 0) __hip_atomic_store((unsigned long long*)(ws + EPOCH_OFF), tag, __ATOMIC_RELAXED, __HIP_MEMORY_SCOPE_AGENT);
    return;
  }

  const int tt = 4 * g + w;
  const int tt2 = 2 * rb + (w & 1);
  const int kh = w >> 1;
  const rsrc_t rx0 = __builtin_amdgcn_make_buffer_rsrc((void*)A.x0, 0, XB, 0x00020000);
  v4u res[2];
#define XWV 14
  constexpr int XW = XWV;

  auto clayer = [&](auto LC) AI {
    constexpr int L = LC.value;
    v4u x[28];
    v2u st[8];
    float mean, rstd;
    v4u pq[4];
    STAMP(0);
    auto phase1 = [&](auto FIRST) AI {
      constexpr bool first = FIRST.value;
      const int xo = opq(first ? tt * 32 * (D * 2) : OFF_X2 + tt * 28 * 1024);
      auto ldx = [&](int ks) AI { return ld16(rs, l16, xo + ks * 1024); };
      const int so_ = opq(OFF_ST2 + tt * 4096);
      const int sv_ = (opqv(lane) & 31) * 8 + h * 2048;
      v4u gx[28];
      const int stg = L_STAGE + w * STG;
      const int gv = (lane >> 4) * (D * 2) + (lane & 15) * 16, gv3 = (lane >> 3) * (D * 2) + (lane & 7) * 16;
      const int gl = stg + (lane >> 4) * 272 + (lane & 15) * 16, gl3 = stg + (lane >> 3) * 272 + (lane & 7) * 16;
      const int srd = opqv(stg + (opqv(lane) & 31) * 272 + h * 16);
      auto ldg = [&](int i) AI {
        return i < 24 ? __builtin_amdgcn_raw_buffer_load_b128(rx0, gv, xo + (i & 7) * 4 * (D * 2) + (i >> 3) * 256, 0)
                      : __builtin_amdgcn_raw_buffer_load_b128(rx0, gv3, xo + (i & 7) * 8 * (D * 2) + 768, 0);
      };
      auto stage = [&](int c) AI {
#pragma unroll
        for (int i = 0; i < (c < 3 ? 8 : 4); ++i) *(LAS v4u*)(LD + (c < 3 ? gl + i * 4 * 272 : gl3 + i * 8 * 272)) = gx[8 * c + i];
#pragma unroll
        for (int i = 0; i < (c < 3 ? 8 : 4); ++i) x[8 * c + i] = lds16(LD, srd + i * 32);
      };
      unsigned long long iv = 0;
      unsigned long long* initp = (unsigned long long*)(ws + (w == 3 && lane < NZ ? INIT_OFF + lane * 128 : EPOCH_OFF));
      auto init_ok = [&](unsigned long long v) AI {
        const unsigned long long e = ((unsigned long long)(u32)__builtin_amdgcn_readlane((int)(v >> 32), 63) << 32) | (u32)__builtin_amdgcn_readlane((int)v, 63);
        return __all(lane >= NZ || v == e + 1ull);
      };
      if constexpr (first) {
#pragma unroll
        for (int i = 0; i < 16; ++i) gx[i] = ldg(i);
        stage(0);
      } else {
#pragma unroll
        for (int ks = 0; ks < XW; ++ks) x[ks] = ldx(ks);
      }
      USTAMP(18);
      BAR();
      USTAMP(19);
      f32x16 ak = {}, av = {}, p = {};
      const int sbk = first ? opqv(opqv(w1lane) + 4 * UNIT) : opqv(opqv(l16) + 3 * UNIT), sbv = first ? opqv(w1lane) : opqv(l16);
      const int csb = opqv(L_CS1 + 16 * (opqv(lane) >> 5)), wb = opqv(L_WPHI + opqv(l16));
      const int xk = opqv(L_W2X + w * 1024 + opqv(l16));
      f32x4 cs[4]; v4u kf[2], wf[2];
      float nmean, s1 = 0.f, s2 = 0.f, q1 = 0.f, q2 = 0.f;
      auto isk = [](int i) constexpr { return i < LAG || (i < 56 - LAG && ((i - LAG) & 1) == 0); };
      auto kst = [](int i) constexpr { return i < LAG ? i : (i < 56 - LAG ? ((((i - LAG) & 1) == 0) ? LAG + (i - LAG) / 2 : (i - LAG) / 2) : i - 28); };
      auto rdw = [&](int base, int ks) AI { if constexpr (first) return tr_frag(LD, base + ks * 1024, 256); else return lds16(LD, base + ks * 1024); };
      pipe<56, first ? 2 : 1>([&](auto I) AI { return rdw(isk(I.value) ? sbk : sbv, kst(I.value)); },
               [&](auto I) AI {
                 constexpr int i = I.value;
                 if constexpr (first && (i == 6 || i == 14 || i == 22 || i == 30 || i == 38 || i == 46)) { USTAMP(40 + i / 8 * 2); RAWBAR(); USTAMP(41 + i / 8 * 2); }
                 if constexpr (i == 54) { USTAMP(20); BAR(); USTAMP(21); }
               },
               [&](auto I, v4u a, auto nx) AI {
                 constexpr int i = I.value, ks = kst(i);
                 if constexpr (i == 54) { p = mfma(wf[0], kf[0], p); p = mfma(wf[1], kf[1], p); }
                 if constexpr (isk(i)) ak = mfma(a, x[ks], ak); else av = mfma(a, x[ks], av);
                 nx();
                 if constexpr (!first && isk(i) && ks + XW < 28) x[ks + XW] = ldx(ks + XW);
                 if constexpr (first) {
                   if constexpr (i < 12) gx[16 + i] = ldg(16 + i);
                   if constexpr (isk(i) && ks % 8 == 6) stage(ks / 8 + 1);
                   if constexpr (isk(i)) {
                     const u32 one = 0x3f803f80u;
                     s1 = dot2(x[ks].x, one, s1); q1 = dot2(x[ks].x, x[ks].x, q1); s2 = dot2(x[ks].y, one, s2); q2 = dot2(x[ks].y, x[ks].y, q2);
                     s1 = dot2(x[ks].z, one, s1); q1 = dot2(x[ks].z, x[ks].z, q1); s2 = dot2(x[ks].w, one, s2); q2 = dot2(x[ks].w, x[ks].w, q2);
                   }
                   if constexpr (i == 45) iv = __hip_atomic_load(initp, __ATOMIC_RELAXED, __HIP_MEMORY_SCOPE_AGENT);
                   if constexpr (i == 51) { ln_finish(s1 + s2, q1 + q2, mean, rstd); nmean = -mean; }
                 } else {
                   if constexpr (!isk(i) && ks >= 4 && ks < 12) st[ks - 4] = ld8(rs, sv_ + (ks - 4) * 256, so_);
                   if constexpr (i == 45) { ln_stats(st, mean, rstd); nmean = -mean; }
                 }
                 if constexpr (i >= 46 && i < 50) cs[i - 46] = *(LAS f32x4*)(LD + csb + 32 * (i - 46));
                 if constexpr (i == 50) wf[0] = lds16(LD, wb);
                 if constexpr (i == 51) wf[1] = lds16(LD, wb + 1024);
                 if constexpr (i == 52 || i == 53) {
#pragma unroll
                   for (int qd = 2 * (i - 52); qd < 2 * (i - 52) + 2; ++qd) {
                     const float y0 = fmaf(nmean, cs[qd][0], ak[4 * qd]), y1 = fmaf(nmean, cs[qd][1], ak[4 * qd + 1]);
                     const float y2 = fmaf(nmean, cs[qd][2], ak[4 * qd + 2]), y3 = fmaf(nmean, cs[qd][3], ak[4 * qd + 3]);
                     kf[qd >> 1][2 * (qd & 1)] = pk2(y0, y1);
                     kf[qd >> 1][2 * (qd & 1) + 1] = pk2(y2, y3);
                   }
                   *(LAS v4u*)(LD + xk + (i - 52) * 4096) = kf[i - 52];
                   if constexpr (i == 52) *(LAS float*)(LD + L_KVF + w * 256 + opqv(lane) * 4) = rstd;
                 }
                 if constexpr (i == 55) {
#pragma unroll
                   for (int qd = 0; qd < 4; ++qd) cs[qd] = *(LAS f32x4*)(LD + csb + 128 + 32 * qd);
                 }
               });
      STAMP(1);
      const int lz = opqv(lane);
      const int trow = w * 32 + (lz & 31);
      const int pb = opqv(L_PHK + trow * PHK_STRIDE + 8 * (lz >> 5)), vb = opqv(L_V + trow * V_STRIDE + 8 * (lz >> 5));
      half_softmax<0>(p, h, rstd, pq[0], pq[1]);
#pragma unroll
      for (int s = 0; s < 2; ++s) {
        *(LAS v2u*)(LD + pb + 32 * s) = v2u{pq[s].x, pq[s].y};
        *(LAS v2u*)(LD + pb + 32 * s + 16) = v2u{pq[s].z, pq[s].w};
      }
      SB();
#pragma unroll
      for (int r = 0; r < 16; ++r) av[r] = fmaf(nmean, cs[r >> 2][r & 3], av[r]) * rstd;
      if (h) { av[12] = 1.f; av[13] = 0.f; av[14] = 0.f; av[15] = 0.f; }
#pragma unroll
      for (int qd = 0; qd < 4; ++qd) *(LAS v2u*)(LD + vb + 16 * qd) = v2u{pk2(av[4 * qd], av[4 * qd + 1]), pk2(av[4 * qd + 2], av[4 * qd + 3])};
      if constexpr (first) {
        if (w == 3 && !init_ok(iv)) {
          u32 spins = 0;
          while (!init_ok(__hip_atomic_load(initp, __ATOMIC_RELAXED, __HIP_MEMORY_SCOPE_AGENT))) {
            __builtin_amdgcn_s_sleep(1);
            if (++spins > 400000u) break;
          }
        }
      }
      STAMP(2);
      BAR();
      if constexpr (first) {
#pragma unroll
        for (int ro = 0; ro < 2; ++ro) {
          const int oi = min(kh * 4 + ro * 2 + h, 6);
          res[ro] = __builtin_amdgcn_raw_buffer_load_b128(rx0, tok * (D * 2) + (7 * cb + oi) * 16, tt2 * 32 * (D * 2), 0);
        }
      }
      f32x16 aq = {};
      const int sbq = first ? opqv(opqv(w1lane) + 1 * UNIT) : opqv(opqv(l16) + 1 * UNIT);
      pipe<28, first ? 2 : 1>([&](auto I) AI { return rdw(sbq, I.value); }, [&](auto) AI {},
               [&](auto I, v4u a, auto nx) AI { aq = mfma(a, x[I.value], aq); nx(); });
      if constexpr (first) wait_arr(LD, 1, 1);
#pragma unroll
      for (int qd = 0; qd < 4; ++qd) cs[qd] = *(LAS f32x4*)(LD + csb + 256 + 32 * qd);
#pragma unroll
      for (int r = 0; r < 16; ++r) aq[r] = fmaf(nmean, cs[r >> 2][r & 3], aq[r]);
      f32x16 p2 = {};
      const int wb2 = opqv(L_WPHI + opqv(l16));
      p2 = mfma(lds16(LD, wb2), pack8(aq, 0), p2);
      p2 = mfma(lds16(LD, wb2 + 1024), pack8(aq, 1), p2);
      half_softmax<0>(p2, h, rstd, pq[0], pq[1]);
      half_softmax<1>(p2, h, rstd, pq[2], pq[3]);
    };
    phase1(std::integral_constant<bool, L == 0>{});
    STAMP(3);
    if (w == 0) wait_cnt(ws, C_KV + L * 16 + j, 32);
    BAR();
    STAMP(4);
    kv_reduce(rs, LD, L, j, w, lane);
    BAR();
    STAMP(5);
    {
      f32x16 nm = {};
      const int kb = opqv(L_KVF + opqv(l16));
      v4u kf[4];
#pragma unroll
      for (int s = 0; s < 4; ++s) kf[s] = lds16(LD, kb + s * 1024);
#pragma unroll
      for (int s = 0; s < 4; ++s) nm = mfma(kf[s], pq[s], nm);
      auto r = __builtin_amdgcn_permlane32_swap(__float_as_uint(nm[12]), __float_as_uint(nm[12]), false, false);
      const float den = __uint_as_float(r[1]) + 1e-6f;
      const float rd = __builtin_amdgcn_rcpf(den);
#pragma unroll
      for (int i = 0; i < 16; ++i) nm[i] *= rd;
      const int oo = opq(OFF_O + L * OB + (tt * 32 + 2 * j) * 1024);
      st16(rs, l16, oo, pack8(nm, 0));
      st16(rs, l16, oo + 1024, pack8(nm, 1));
    }
    wave_signal(ws, C_O + (L * 64 + tt) * 2 + (j >> 3), lane);
    STAMP(6);
    wait_cnt(ws, C_O + (L * 64 + tt2) * 2 + kh, 8, true);
    STAMP(7);
    {
      const int xo = opq(OFF_O + L * OB + (tt2 * 32 + 16 * kh) * 1024);
#pragma unroll
      for (int t = 0; t < (XW < 16 ? XW : 16); ++t) x[t] = ld16(rs, l16, xo + t * 1024);
      if (L == 0) RAWBAR();
      f32x16 a0 = {}, a1 = {};
      const int sb2 = opqv(2 * UNIT + kh * 2048 + opqv(l16)), sbx = opqv(L_W2X + kh * 2048 + opqv(l16));
      pipe<32>([&](auto I) AI { constexpr int i = I.value, t = i >> 1; return lds16(LD, (t < 14 ? sb2 + t * 4096 : sbx + (t - 14) * 4096) + (i & 1) * 1024); },
               [&](auto I) AI { if constexpr (I.value == 14) RAWBAR();   },
               [&](auto I, v4u a, auto nx) AI {
                 constexpr int i = I.value;
                 if constexpr (i & 1) a1 = mfma(a, x[i >> 1], a1); else a0 = mfma(a, x[i >> 1], a0);
                 nx();
                 if constexpr (!(i & 1) && (i >> 1) + XW < 16) x[(i >> 1) + XW] = ld16(rs, l16, xo + ((i >> 1) + XW) * 1024);
               });
      const int xb = opqv(L_SCR + w * 4096 + opqv(l16)), xb2 = opqv(L_SCR + (w ^ 2) * 4096 + opqv(l16));
      f32x16 fin;
      if (kh) {
#pragma unroll
        for (int qd = 0; qd < 4; ++qd) *(LAS f32x4*)(LD + xb + qd * 1024) = f32x4{a0[4 * qd], a0[4 * qd + 1], a0[4 * qd + 2], a0[4 * qd + 3]};
        fin = a1;
      } else {
#pragma unroll
        for (int qd = 0; qd < 4; ++qd) *(LAS f32x4*)(LD + xb + qd * 1024) = f32x4{a1[4 * qd], a1[4 * qd + 1], a1[4 * qd + 2], a1[4 * qd + 3]};
        fin = a0;
      }
      BAR();
#pragma unroll
      for (int qd = 0; qd < 4; ++qd) {
        const f32x4 v = *(LAS f32x4*)(LD + xb2 + qd * 1024);
#pragma unroll
        for (int i = 0; i < 4; ++i) fin[4 * qd + i] += v[i];
      }
      finish_tile(fin, res, rs, opq(OFF_X1 + L * XB), opq(OFF_ST1 + L * STB), tt2, cb, kh, lane, false, A.out);
    }
    wave_signal(ws, C_X1 + L * 64 + tt2, lane);
    STAMP(8);
    wait_cnt(ws, C_X1 + L * 64 + tt, 16, true);
    STAMP(9);
    {
      const int xo = opq(OFF_X1 + L * XB + tt * 28 * 1024);
      const int so_ = opq(OFF_ST1 + L * STB + tt * 4096);
      const int sv_ = (opqv(lane) & 31) * 8 + h * 2048;
      constexpr int XW3 = 8;
#pragma unroll
      for (int ks = 0; ks < XW3; ++ks) x[ks] = ld16(rs, l16, xo + ks * 1024);
      const int ho = opq(OFF_H + L * HB + (tt * 112 + 7 * j) * 1024);
      const int lvo = opq(C_H + (L * 64 + tt) * 8);
      u32* fp = cnt_ptr(ws, C_H + (L * 64 + tt2) * 8);
      auto poll = [&](int lv) AI { return __hip_atomic_load(fp + 32 * lv, __ATOMIC_RELAXED, __HIP_MEMORY_SCOPE_AGENT); };
      u32 f;
      float nmean, gs, irs;
      const int csb = opqv(L_CS3 + 16 * (opqv(lane) >> 5));
      const int sbA = opqv(l16), sbB = opqv(opqv(l16) + 2 * UNIT);
      f32x4 Gcg[2], Gcu[2]; float Gt[8], Gyu[8]; u32 Go[4];
      auto glu = [&](const f32x16& a, auto N_, auto S_) AI {
        constexpr int n = N_.value, s = S_.value;
        if constexpr (s == 1) Gcu[0] = *(LAS f32x4*)(LD + csb + n * 128 + 32);
        if constexpr (s == 4) Gcg[1] = *(LAS f32x4*)(LD + csb + n * 128 + 64);
        if constexpr (s == 9) Gcu[1] = *(LAS f32x4*)(LD + csb + n * 128 + 96);
        if constexpr (s == 12 && n < 6) Gcg[0] = *(LAS f32x4*)(LD + csb + (n + 1) * 128);
        static_for<8>([&](auto V_) AI {
          constexpr int v = V_.value, k = s - (1 + 4 * (v >> 1) + (v & 1)), hf = v >> 2, i = v & 3;
          if constexpr (k == 0) { Gt[v] = fmaf(nmean, Gcg[hf][i], a[8 * hf + i]); PIN(Gt[v]); }
          if constexpr (k == 1) { Gt[v] = Gt[v] * gs; PIN(Gt[v]); }
          if constexpr (k == 2) { Gt[v] = __builtin_amdgcn_exp2f(Gt[v]); PIN(Gt[v]); }
          if constexpr (k == 3) { Gt[v] = fmaf(Gt[v], irs, irs); PIN(Gt[v]); }
          if constexpr (k == 4) { Gt[v] = __builtin_amdgcn_rcpf(Gt[v]); PIN(Gt[v]); }
          if constexpr (k == 5) { Gyu[v] = fmaf(nmean, Gcu[hf][i], a[8 * hf + 4 + i]); PIN(Gyu[v]); }
          if constexpr (k == 6) { Gt[v] = Gt[v] * Gyu[v]; PIN(Gt[v]); }
          if constexpr (k == 7 && (v & 1)) { Go[v >> 1] = pk2(Gt[v - 1], Gt[v]); PIN(Go[v >> 1]); }
        });
        if constexpr (s == 21) st16(rs, l16, ho + n * 1024, v4u{Go[0], Go[1], Go[2], Go[3]});
      };
      const int ho2 = opq(OFF_H + L * HB + tt2 * 112 * 1024 + kh * 7168);
      v4u hx[28];
      auto ldh = [&](auto T) AI { hx[T.value] = ld16(rs, l16, ho2 + p4_koff(p4_oc(T.value))); };
      f32x16 acc[7];
      constexpr int PB = 7 * PK;
      pipe<196>(
          [&](auto I) AI { constexpr int i = I.value, u = i / 28, sl = u & 3; return lds16(LD, (sl < 2 ? sbA : sbB) + (sl & 1) * UNIT + (i % 28) * 1024); },
          [&](auto I) AI { if constexpr (I.value > 28 && I.value % 28 == 6) { VSTAMP(30 + I.value / 28); RAWBAR(); WSTAMP(22 + I.value / 28); } },
          [&](auto I, v4u a, auto nx) AI {
            constexpr int i = I.value;
            if constexpr (i < PB) {
              constexpr int n = i % 7, ks = i / 7;
              if constexpr (ks == 0) { f32x16 z = {}; acc[n] = mfma(a, x[ks], z); } else acc[n] = mfma(a, x[ks], acc[n]);
              nx();
              constexpr int K2 = 28 - XW3 - PK, fx = ks < K2 ? XW3 + 2 * ks + (n ? 1 : 0) : XW3 + K2 + ks;
              if constexpr (n == 0 || (n == 3 && ks < K2)) x[fx] = ld16(rs, l16, xo + fx * 1024);
              if constexpr (n >= 5 && ks < 4) st[2 * ks + n - 5] = ld8(rs, sv_ + (2 * ks + n - 5) * 256, so_);
              if constexpr (i == 46) {
                ln_stats(st, mean, rstd);
                nmean = -mean; gs = -LOG2E * rstd; irs = __builtin_amdgcn_rcpf(rstd);
                PIN(nmean); PIN(gs); PIN(irs);
              }
            } else {
              constexpr int r = i - PB, n = r / MK, s = r % MK;
              acc[n] = mfma(a, x[PK + s], acc[n]);
              nx();
              if constexpr (r == 8) Gcg[0] = *(LAS f32x4*)(LD + csb);
              if constexpr (n >= 2 && s + MK < 22) glu(acc[n - 2], IC<n - 2>{}, IC<s + MK>{});
              if constexpr (n >= 1) glu(acc[n - 1], IC<n - 1>{}, IC<s>{});
              if constexpr (n >= 3 && s == 20 - MK) { VMCNT(0); signal_cnt(ws, lvo + n - 3); }
            }
          });
      STAMP(10);
      arrive_lds(LD, 2, lane);
      static_for<22>([&](auto S_) AI {
        if constexpr (S_.value + MK < 22) glu(acc[5], IC<5>{}, IC<S_.value + MK>{});
        glu(acc[6], IC<6>{}, S_);
        if constexpr (S_.value == 4) { VMCNT(0); signal_cnt(ws, lvo + 4); }
        if constexpr (S_.value == CP1) f = poll_issue(fp + 96);
      });
      STAMP(11);
      const int wo_ = opqv(kh * 3072 + opqv(l16)), wp_ = opqv(kh * 1024 + 1024 + opqv(l16));
      const int wo2 = opqv(wo_ + 3 * UNIT), wp2 = opqv(wp_ + 3 * UNIT);
      v4u wq[56];
      auto ldw = [&](auto K) AI {
        constexpr int k = K.value, t = k >> 1, wb = p4_wbase(t);
        if constexpr (wb >= 3 * UNIT) wq[k] = lds16(LD, ((k & 1) ? wp2 : wo2) + wb - 3 * UNIT);
        else wq[k] = lds16(LD, ((k & 1) ? wp_ : wo_) + wb);
      };
      VMCNT(1); signal_cnt(ws, lvo + 5);
      PIN(f);
      const u32 f2 = poll(4);
      if (__all(f >= 16u)) {
        static_for<4>([&](auto T) AI { ldh(T); });
        VMCNT(6); signal_cnt(ws, lvo + 6);
      } else {
        VMCNT(2); signal_cnt(ws, lvo + 6);
        wait_flags(ws, fp + 96, f);
        static_for<4>([&](auto T) AI { ldh(T); });
      }
      wait_flags(ws, fp + 128, f2);
      static_for<8>([&](auto T) AI { ldh(IC<4 + T.value>{}); });
      wait_arr(LD, 4, 4 * L + 4);
      static_for<4>([&](auto K) AI { ldw(K); });
      STAMP(12);
      f32x16 fin = {}, oth = {};
      static_for<28>([&](auto T) AI {
        constexpr int t = T.value;
        if constexpr (t == CP5) f = poll(5);
        if constexpr (t == CT5) {
          wait_flags(ws, fp + 160, f);
          f = poll(6);
          static_for<8>([&](auto K) AI { ldh(IC<12 + K.value>{}); });
          STAMP(13);
        }
        if constexpr (t == 12) {
          wait_flags(ws, fp + 192, f);
          static_for<8>([&](auto K) AI { ldh(IC<20 + K.value>{}); });
          STAMP(22);
        }
        if constexpr (t == 12) arrive_lds(LD, 3, lane);
        if constexpr (t + 2 < 28) { ldw(IC<2 * t + 4>{}); ldw(IC<2 * t + 5>{}); }
        oth = mfma(wq[2 * t + 1], hx[t], oth);
        fin = mfma(wq[2 * t], hx[t], fin);
        SB();
      });
      STAMP(14);
      wait_arr(LD, 3, 4 * L + 4);
      put16(oth, LD, opqv(L_SCR + w * 4096 + opqv(l16)));
      BAR();
#pragma unroll
      for (int c = 0; c < 4; ++c) add16(fin, LD, opqv(p4_part(w, c) + opqv(l16)));
      add16(fin, LD, opqv(L_SCR + (w ^ 2) * 4096 + opqv(l16)));
      STAMP(15);
      finish_tile(fin, res, rs, OFF_X2, OFF_ST2, tt2, cb, kh, lane, L == 1, A.out);
    }
    if (L == 0) {
      wave_signal(ws, C_X2 + tt2, lane);
      arrive_lds(LD, 0, lane);
      STAMP(16);
      wait_cnt(ws, C_X2 + tt, 16, true);
    }
    STAMP(17);
  };
  clayer(IC<0>{});
  clayer(IC<1>{});
}

extern "C" void kernel_launch(void* const* d_in, const int* in_sizes, int n_in, void* d_out, int out_size, void* d_ws,
                              size_t ws_size, hipStream_t stream) {
  Args a;
  a.x0 = (const u16*)d_in[0]; a.wqkv = (const u16*)d_in[1]; a.wphi = (const u16*)d_in[2]; a.wo = (const u16*)d_in[3];
  a.wg = (const u16*)d_in[5]; a.wu = (const u16*)d_in[6]; a.wd = (const u16*)d_in[7];
  a.ws = (char*)d_ws; a.out = (u16*)d_out;
  (void)hipFuncSetAttribute((const void*)k_main, hipFuncAttributeMaxDynamicSharedMemorySize, LDS_BYTES);
  k_main<<<256, 512, LDS_BYTES, stream>>>(a);
}
```

```cpp
#include <hip/hip_runtime.h>
#include <stdint.h>
#include <utility>
#include <type_traits>

typedef unsigned short u16;
typedef unsigned int u32;
typedef u32 v4u __attribute__((ext_vector_type(4)));
typedef u32 v2u __attribute__((ext_vector_type(2)));
typedef float f32x2 __attribute__((ext_vector_type(2)));
typedef float f32x4 __attribute__((ext_vector_type(4)));
typedef float f32x16 __attribute__((ext_vector_type(16)));
typedef __bf16 bf16x2 __attribute__((ext_vector_type(2)));
typedef __bf16 bf16x8 __attribute__((ext_vector_type(8)));
typedef short s16x4 __attribute__((ext_vector_type(4)));
#define LAS __attribute__((address_space(3)))
#define DI __device__ __forceinline__
#define AI __attribute__((always_inline))
template <int V> using IC = std::integral_constant<int, V>;

constexpr int S = 2048, D = 448, FF = 1792;
constexpr int UNIT = 28672;

constexpr int C_KV = 0;
constexpr int C_W2 = 32;
constexpr int C_W4 = 36;
constexpr int C_W3 = 40;
constexpr int C_ERR = 60;
constexpr int C_X2 = 64;
constexpr int C_X1 = 128;
constexpr int C_O = 256;
constexpr int C_H = 512;
constexpr int C_W1 = 1536;
#define CSTV 1152
constexpr int CST = CSTV, CW = CST / 4;
constexpr int NZ = 25;
constexpr int EPOCH_OFF = NZ * 64 * CST;
constexpr int INIT_OFF = EPOCH_OFF + 256;
constexpr int CTL_BYTES = EPOCH_OFF + 4096;
constexpr int W2B = 65536;
constexpr int OFF_W1 = CTL_BYTES;
constexpr int OFF_W2 = OFF_W1 + 16 * 3 * UNIT;
constexpr int OFF_W3 = OFF_W2 + 8 * W2B;
constexpr int OFF_W4 = OFF_W3 + 16 * 7 * UNIT;
constexpr int OFF_CS3 = OFF_W4 + 8 * 8 * UNIT;
constexpr int XB = S * D * 2;
constexpr int OB = S * 512 * 2;
constexpr int OFF_X2 = OFF_CS3 + 16 * 28 * 896;
constexpr int OFF_X1 = OFF_X2 + XB;
constexpr int OFF_O = OFF_X1 + 2 * XB;
constexpr int STB = S * 16 * 8;
constexpr int OFF_ST2 = OFF_O + 2 * OB;
constexpr int OFF_ST1 = OFF_ST2 + STB;
constexpr int KVB = 16 * 16 * 4096;
constexpr int OFF_KVP = OFF_ST1 + 2 * STB;
constexpr int HB = S * FF * 2;
constexpr int OFF_H = OFF_KVP + 2 * KVB;
constexpr int WS_USED = OFF_H + 2 * HB;

constexpr int L_SCR = 4 * UNIT;
constexpr int PHK_STRIDE = 136, V_STRIDE = 72;
constexpr int L_PHK = L_SCR;
constexpr int L_V = L_SCR + 128 * PHK_STRIDE;
constexpr int L_KVF = L_SCR + 28672;
constexpr int L_W2X = L_KVF + 4096;
constexpr int L_CONST = 155648;
constexpr int L_CS1 = L_CONST, L_CS3 = L_CONST + 384, L_WPHI = L_CONST + 1280, L_ARR = L_CONST + 3328;
constexpr int L_STAGE = 2 * UNIT;
constexpr int STG = 8704;
constexpr int LDS_BYTES = 163840;

constexpr float LOG2E = 1.4426950408889634f;
#define PKV 12
constexpr int PK = PKV, MK = 28 - PK;
constexpr int LAG = 4;

DI u32 pk2(float a, float b) { f32x2 v = {a, b}; return __builtin_bit_cast(u32, __builtin_convertvector(v, bf16x2)); }
DI float bflo(u32 v) { return __uint_as_float(v << 16); }
DI float bfhi(u32 v) { return __uint_as_float(v & 0xffff0000u); }
DI f32x16 mfma(v4u a, v4u b, f32x16 c) {
  f32x16 d = __builtin_amdgcn_mfma_f32_32x32x16_bf16(__builtin_bit_cast(bf16x8, a), __builtin_bit_cast(bf16x8, b), c, 0, 0, 0);
  asm volatile("" : "+v"(d));
  __builtin_amdgcn_sched_barrier(0);
  return d;
}
DI float xsum(float v) {
  auto r = __builtin_amdgcn_permlane32_swap(__float_as_uint(v), __float_as_uint(v), false, false);
  return __uint_as_float(r[0]) + __uint_as_float(r[1]);
}
DI float xmax(float v) {
  auto r = __builtin_amdgcn_permlane32_swap(__float_as_uint(v), __float_as_uint(v), false, false);
  return fmaxf(__uint_as_float(r[0]), __uint_as_float(r[1]));
}
DI float xmin(float v) {
  auto r = __builtin_amdgcn_permlane32_swap(__float_as_uint(v), __float_as_uint(v), false, false);
  return fminf(__uint_as_float(r[0]), __uint_as_float(r[1]));
}
#define BAR() asm volatile("s_waitcnt lgkmcnt(0)\n\ts_barrier" ::: "memory")
#define RAWBAR() asm volatile("s_barrier" ::: "memory")
#define VMCNT(n) asm volatile("s_waitcnt vmcnt(" #n ")" ::: "memory")
#define SB() __builtin_amdgcn_sched_barrier(0)
#define PIN(x) asm volatile("" : "+v"(x))

typedef __amdgpu_buffer_rsrc_t rsrc_t;
DI v4u ld16(rsrc_t rs, int voff, int soff) { return __builtin_amdgcn_raw_buffer_load_b128(rs, voff, soff, 16); }
DI v2u ld8(rsrc_t rs, int voff, int soff) { return __builtin_amdgcn_raw_buffer_load_b64(rs, voff, soff, 16); }
DI u32 ld4(rsrc_t rs, int voff, int soff) { return __builtin_amdgcn_raw_buffer_load_b32(rs, voff, soff, 16); }
DI void st16(rsrc_t rs, int voff, int soff, v4u v) {
  __builtin_amdgcn_raw_buffer_store_b128(v, rs, voff, soff, 16);
  asm volatile("s_nop 1" ::"v"(v) : "memory");
}
DI void st8(rsrc_t rs, int voff, int soff, v2u v) { __builtin_amdgcn_raw_buffer_store_b64(v, rs, voff, soff, 16); }
DI void st4(rsrc_t rs, int voff, int soff, u32 v) { __builtin_amdgcn_raw_buffer_store_b32(v, rs, voff, soff, 16); }
DI int opq(int v) { asm volatile("" : "+s"(v)); return v; }
DI int opqv(int v) { asm volatile("" : "+v"(v)); return v; }
DI v4u lds16(LAS char* l, int off) { return *(LAS v4u*)(l + off); }
DI float dot2(u32 a, u32 b, float c) {
  return __builtin_amdgcn_fdot2_f32_bf16(__builtin_bit_cast(bf16x2, a), __builtin_bit_cast(bf16x2, b), c, false);
}
DI v4u tr_frag(LAS char* L, int base, int step) {
  v2u lo = __builtin_bit_cast(v2u, __builtin_amdgcn_ds_read_tr16_b64_v4i16((LAS s16x4*)(L + base)));
  v2u hi = __builtin_bit_cast(v2u, __builtin_amdgcn_ds_read_tr16_b64_v4i16((LAS s16x4*)(L + base + step)));
  return v4u{lo.x, lo.y, hi.x, hi.y};
}
DI float sum8(v4u o) {
  const u32 one = 0x3f803f80u;
  return dot2(o.x, one, dot2(o.y, one, 0.f)) + dot2(o.z, one, dot2(o.w, one, 0.f));
}

DI u32* cnt_ptr(char* ws, int idx) { return (u32*)(ws + idx * CST); }
#define POLL_D0 16
#define LATE_ARR
#define WOV 0
#define WOS 20
#define H2 6
#define H3 12
#define CP1 12
#define CP5 0
#define CT5 11
#define PT5 8
#define POLL_IV 1
DI bool spin_more(char* ws, u32& spins) {
  u32* e = cnt_ptr(ws, C_ERR);
  __builtin_amdgcn_s_sleep(POLL_IV);
  ++spins;
  if ((spins & 255u) == 0u) {
    if (__builtin_amdgcn_readfirstlane(__hip_atomic_load(e, __ATOMIC_RELAXED, __HIP_MEMORY_SCOPE_AGENT)) != 0) return false;
    if (spins > 400000u) { __hip_atomic_store(e, 1u, __ATOMIC_RELAXED, __HIP_MEMORY_SCOPE_AGENT); return false; }
  }
  return true;
}
DI void wait_cnt(char* ws, int idx, u32 target, bool delay = false) {
  u32* c = cnt_ptr(ws, idx);
  u32 spins = 0;
  if (delay && POLL_D0) __builtin_amdgcn_s_sleep(POLL_D0);
  while ((u32)__builtin_amdgcn_readfirstlane(__hip_atomic_load(c, __ATOMIC_RELAXED, __HIP_MEMORY_SCOPE_AGENT)) < target)
    if (!spin_more(ws, spins)) break;
  __builtin_amdgcn_fence(__ATOMIC_ACQUIRE, "wavefront");
  asm volatile("" ::: "memory");
}
DI void wait_flags(char* ws, u32* fp, u32 f, u32 target = 16u) {
  if (__builtin_expect(!__all(f >= target), 0)) {
    u32 spins = 0;
    while (spin_more(ws, spins))
      if (__all(__hip_atomic_load(fp, __ATOMIC_RELAXED, __HIP_MEMORY_SCOPE_AGENT) >= target)) break;
  }
  __builtin_amdgcn_fence(__ATOMIC_ACQUIRE, "wavefront");
  asm volatile("" ::: "memory");
}
DI u32 poll_issue(const u32* p) { u32 f; asm volatile("global_load_dword %0, %1, off sc1" : "=v"(f) : "v"(p) : "memory"); return f; }
DI void signal_cnt(char* ws, int idx) {
  unsigned long long sv;
  asm volatile("s_mov_b64 %0, exec\n\ts_mov_b64 exec, 1\n\tglobal_atomic_add %1, %2, %3\n\ts_mov_b64 exec, %0"
               : "=&s"(sv) : "v"(idx * CST), "v"(1), "s"(ws) : "memory");
}
DI void wave_signal(char* ws, int idx, int lane) { VMCNT(0); signal_cnt(ws, idx); }
DI void arrive_lds(LAS char* LD, int slot, int lane) {
  if (lane == 0) __hip_atomic_fetch_add((LAS u32*)(LD + L_ARR + slot * 4), 1u, __ATOMIC_RELAXED, __HIP_MEMORY_SCOPE_WORKGROUP);
}
DI void wait_arr(LAS char* LD, int slot, u32 target) {
  while ((u32)__builtin_amdgcn_readfirstlane(__hip_atomic_load((LAS u32*)(LD + L_ARR + slot * 4), __ATOMIC_RELAXED, __HIP_MEMORY_SCOPE_WORKGROUP)) < target)
    __builtin_amdgcn_s_sleep(1);
}

#define STAMP(i)
#define HSTAMP(i)
#define VSTAMP(i)
#define WSTAMP(i)
#define USTAMP(i)

#define DMA_STEP(GSTEP) \
      "s_add_u32 m0, m0, 0x1000\n\t" \
      "v_add_u32 %1, " GSTEP ", %1\n\t" \
      "global_load_lds_dwordx4 %1, %2 sc1\n\t"
#define DMA_FN(NAME, BODY) \
DI void NAME(const char* gsrc, u32 voff, u32 lds_dst) { \
  u32 keep; \
  asm volatile("s_mov_b32 %0, m0\n\ts_mov_b32 m0, %3\n\ts_nop 0\n\tglobal_load_lds_dwordx4 %1, %2 sc1\n\t" BODY "s_mov_b32 m0, %0" \
               : "=&s"(keep), "+v"(voff) : "s"(gsrc), "s"(lds_dst) : "memory", "scc"); }
DMA_FN(dma7, DMA_STEP("0x1000") DMA_STEP("0x1000") DMA_STEP("0x1000") DMA_STEP("0x1000") DMA_STEP("0x1000") DMA_STEP("0x1000"))
DMA_FN(dma2, DMA_STEP("0x1000"))
#define PACE 1
#define STR2(x) #x
#define STR(x) STR2(x)
#define PSTEP "s_sleep " STR(PACE) "\n\t" DMA_STEP("0x1000")
DMA_FN(dma7p, PSTEP PSTEP PSTEP PSTEP PSTEP PSTEP)
DMA_FN(dma7n, DMA_STEP("0x2a000") DMA_STEP("0x2a000") DMA_STEP("0x2a000") DMA_STEP("0x2a000") DMA_STEP("0x2a000") DMA_STEP("0x2a000"))

template <int LO, int GO>
DI void dma1(const char* gsrc, u32 voff, u32 lb) {
  u32 keep, tmp;
  asm volatile("s_mov_b32 %0, m0\n\ts_add_u32 m0, %4, %5\n\tv_add_u32 %1, %6, %2\n\tglobal_load_lds_dwordx4 %1, %3 sc1\n\ts_mov_b32 m0, %0"
               : "=&s"(keep), "=&v"(tmp) : "v"(voff), "s"(gsrc), "s"(lb), "i"(LO), "i"(GO) : "memory", "scc");
}

template <typename F, int... I>
DI void static_for_impl(F&& f, std::integer_sequence<int, I...>) { (f(IC<I>{}), ...); }
template <int N, typename F>
DI void static_for(F&& f) { static_for_impl(f, std::make_integer_sequence<int, N>{}); }

constexpr int PF = 6;
#define WEV 2
template <int N, int RPS = 1, typename RD, typename PRE, typename ST>
DI void pipe(RD rd, PRE pre, ST step) {
  v4u buf[PF];
  static_for<PF>([&](auto I) AI { buf[I.value] = rd(I); });
  SB();
  static_for<N>([&](auto I) AI {
    constexpr int i = I.value;
    pre(I);
    if constexpr (WEV > 1 && i % WEV == 0 && i + PF <= N) __builtin_amdgcn_s_waitcnt(0xC07F | (((PF - WEV) * RPS) << 8));
    const v4u a = buf[i % PF];
    step(I, a, [&]() AI { if constexpr (i + PF < N) buf[i % PF] = rd(IC<i + PF>{}); SB(); });
    SB();
  });
}

DI void ln_finish(float s, float q, float& mean, float& rstd) {
  s = xsum(s); q = xsum(q);
  mean = s * (1.f / 448.f);
  const float var = fmaxf(q * (1.f / 448.f) - mean * mean, 0.f);
  rstd = __builtin_amdgcn_rsqf(var + 1e-5f);
}
DI void ln_stats(const v2u (&st)[8], float& mean, float& rstd) {
  f32x2 a = __builtin_bit_cast(f32x2, st[0]), b = __builtin_bit_cast(f32x2, st[1]);
#pragma unroll
  for (int i = 2; i < 8; i += 2) { a += __builtin_bit_cast(f32x2, st[i]); b += __builtin_bit_cast(f32x2, st[i + 1]); }
  a += b;
  ln_finish(a[0], a[1], mean, rstd);
}
DI v4u pack8(const f32x16& a, int s) {
  v4u r = {pk2(a[8 * s], a[8 * s + 1]), pk2(a[8 * s + 2], a[8 * s + 3]), pk2(a[8 * s + 4], a[8 * s + 5]), pk2(a[8 * s + 6], a[8 * s + 7])};
  return r;
}
template <int SG>
DI void half_softmax(const f32x16& p, int h, float rstd, v4u& o0, v4u& o1) {
  auto mm = [](float a, float b) AI { return SG ? fminf(a, b) : fmaxf(a, b); };
  float m0 = mm(mm(p[0], p[1]), p[2]), m1 = mm(mm(p[3], p[4]), p[5]), m2 = mm(mm(p[6], p[7]), p[8]), m3 = mm(mm(p[9], p[10]), p[11]);
  float m4 = mm(mm(mm(p[12], p[13]), p[14]), p[15]);
  m4 = h ? p[0] : m4;
  float mx = mm(mm(mm(m0, m1), m2), mm(m3, m4));
  mx = SG ? xmin(mx) : xmax(mx);
  const float c = rstd * LOG2E;
  const float cc = SG ? -c : c, off = -mx * cc;
  f32x16 e;
  float s0 = 0.f, s1 = 0.f;
#pragma unroll
  for (int r = 0; r < 16; ++r) {
    float v = __builtin_amdgcn_exp2f(fmaf(p[r], cc, off));
    if (r >= 12) v = h ? 0.f : v;
    e[r] = v;
    if (r & 1) s1 += v; else s0 += v;
  }
  const float rs_ = __builtin_amdgcn_rcpf(xsum(s0 + s1));
#pragma unroll
  for (int r = 0; r < 16; ++r) e[r] *= rs_;
  o0 = pack8(e, 0); o1 = pack8(e, 1);
  PIN(o0); PIN(o1);
  SB();
}

DI void finish_tile(const f32x16& acc, v4u (&res)[2], rsrc_t rs, int xoff, int stoff, int tt, int cb, int nt, int lane, bool last, u16* out) {
  lane = opqv(lane);
  const int h = lane >> 5, tok = lane & 31;
#pragma unroll
  for (int ro = 0; ro < 2; ++ro) {
    v4u r = res[ro];
    v4u o;
    o.x = pk2(bflo(r.x) + acc[8 * ro + 0], bfhi(r.x) + acc[8 * ro + 1]);
    o.y = pk2(bflo(r.y) + acc[8 * ro + 2], bfhi(r.y) + acc[8 * ro + 3]);
    o.z = pk2(bflo(r.z) + acc[8 * ro + 4], bfhi(r.z) + acc[8 * ro + 5]);
    o.w = pk2(bflo(r.w) + acc[8 * ro + 6], bfhi(r.w) + acc[8 * ro + 7]);
    res[ro] = o;
    const int oi = nt * 4 + ro * 2 + h;
    if (oi < 7) {
      const int go = 7 * cb + oi;
      if (last) *(v4u*)(out + (size_t)(tt * 32 + tok) * D + go * 8) = o;
      else st16(rs, ((go >> 1) * 64 + tok + 32 * (go & 1)) * 16, xoff + tt * 28 * 1024, o);
    }
  }
  if (!last) {
    const bool v1 = !(nt == 1 && h == 1);
    const u32 one = 0x3f803f80u;
    float s0 = dot2(res[0].x, one, 0.f), s1 = dot2(res[1].x, one, 0.f), q0 = dot2(res[0].x, res[0].x, 0.f), q1 = dot2(res[1].x, res[1].x, 0.f);
    s0 = dot2(res[0].y, one, s0); s1 = dot2(res[1].y, one, s1); q0 = dot2(res[0].y, res[0].y, q0); q1 = dot2(res[1].y, res[1].y, q1);
    s0 = dot2(res[0].z, one, s0); s1 = dot2(res[1].z, one, s1); q0 = dot2(res[0].z, res[0].z, q0); q1 = dot2(res[1].z, res[1].z, q1);
    s0 = dot2(res[0].w, one, s0); s1 = dot2(res[1].w, one, s1); q0 = dot2(res[0].w, res[0].w, q0); q1 = dot2(res[1].w, res[1].w, q1);
    const float sum = xsum(s0 + (v1 ? s1 : 0.f));
    const float sq = xsum(q0 + (v1 ? q1 : 0.f));
    if (h == 0) st8(rs, tok * 8, stoff + ((tt * 8 + cb) * 2 + nt) * 256, v2u{__float_as_uint(sum), __float_as_uint(sq)});
  }
}

DI void kv_reduce(rsrc_t rs, LAS char* LD, int L, int j, int w, int lane) {
  const int ko = opq(OFF_KVP + L * KVB + j * 16 * 4096 + (w >> 1) * 1024 + (w & 1) * 512);
  const int lz = opqv(lane);
  const int vo = (lz >> 5) * 4096 + (lz & 31) * 16;
  v4u t[8];
#pragma unroll
  for (int i = 0; i < 8; ++i) t[i] = ld16(rs, vo, ko + i * 8192);
  float s[8] = {};
  const u32 lo1 = (u32)opq(0x00003f80), hi1 = (u32)opq(0x3f800000);
#pragma unroll
  for (int i = 0; i < 8; ++i) {
#pragma unroll
    for (int c = 0; c < 4; ++c) { s[2 * c] = dot2(t[i][c], lo1, s[2 * c]); s[2 * c + 1] = dot2(t[i][c], hi1, s[2 * c + 1]); }
  }
#pragma unroll
  for (int c = 0; c < 8; ++c) s[c] = xsum(s[c]);
  if (lz < 32) *(LAS v4u*)(LD + L_KVF + (w >> 1) * 1024 + (w & 1) * 512 + lz * 16) = v4u{pk2(s[0], s[1]), pk2(s[2], s[3]), pk2(s[4], s[5]), pk2(s[6], s[7])};
}

constexpr int p4_oh(int t) { return 4 * t; }
constexpr int p4_oc(int t) { return 56 + 2 * t; }
constexpr int p4_koff(int o) { return (7 * (o & 15) + (o >> 4)) * 1024; }
constexpr int p4_wbase(int t) { return (t < 7 ? 0 : t < 14 ? L_SCR : t < 21 ? 3 * UNIT : UNIT) + (t % 7) * 4096; }
DI int p4_part(int a, int hw) {
  const int i = ((a >> 1) | ((a & 1) << 1)) * 4 + hw;
  return i < 7 ? 2 * UNIT + i * 4096 : i < 10 ? L_KVF + (i - 7) * 4096 : (i - 10) * 4096;
}
DI void put16(const f32x16& acc, LAS char* LD, int off) {
#pragma unroll
  for (int qd = 0; qd < 4; ++qd) *(LAS f32x4*)(LD + off + qd * 1024) = f32x4{acc[4 * qd], acc[4 * qd + 1], acc[4 * qd + 2], acc[4 * qd + 3]};
}
DI void add16(f32x16& acc, LAS char* LD, int off) {
#pragma unroll
  for (int qd = 0; qd < 4; ++qd) {
    const f32x4 v = *(LAS f32x4*)(LD + off + qd * 1024);
#pragma unroll
    for (int i = 0; i < 4; ++i) acc[4 * qd + i] += v[i];
  }
}

struct Args {
  const u16* x0; const u16* wqkv; const u16* wphi; const u16* wo; const u16* wg; const u16* wu; const u16* wd;
  char* ws; u16* out;
};

struct Retile { const v4u* pa[7]; int rd0, rd1, step, o0, o1, ocb, sig; };
DI void retile_prep(const Args& A, int job, int lane, int stage, Retile& R) {
  const int h = lane >> 5, q = (lane & 15) >> 2, c8 = ((lane >> 4) & 1) * 4 + (lane & 3);
  const int hh = c8 & 1, quad = c8 >> 1;
  if (job < 64 || job >= 512) {
    const bool iswo = job < 64;
    const int a0 = iswo ? job >> 2 : (job - 512) >> 2, cg = job & 3;
    const u16* src = (iswo ? A.wo + (size_t)a0 * 28 * D : A.wd + (size_t)a0 * 32 * D) + 112 * cg;
    const int nrow = iswo ? 28 : 32;
#pragma unroll
    for (int i = 0; i < 7; ++i) {
      const int p = lane + 64 * i, r = p / 14, c = p % 14;
      R.pa[i] = (const v4u*)(src + (size_t)(r < nrow ? r : 0) * D + c * 8);
    }
    const int rb = stage + ((iswo ? 4 : 8) * h + q) * 224 + 8 * (quad & 1);
    const int oi = (quad >> 1) * 2 + hh;
    R.rd0 = rb + 16 * oi; R.rd1 = rb + 16 * min(4 + oi, 6); R.step = (iswo ? 8 : 4) * 224;
    int ob[2];
#pragma unroll
    for (int k2 = 0; k2 < 2; ++k2) {
      const int ks = a0 * 2 + k2, o_ = 16 * (ks % 7) + ks / 7;
      int fr;
      if (o_ < 56) fr = 112 + ((o_ & 3) * 14 + (o_ >> 2)) * 2;
      else fr = (o_ - 56) * 2;
      ob[k2] = iswo ? OFF_W2 + cg * 2 * W2B + ((ks & 15) * 2 + (ks >> 4)) * 2048 : OFF_W4 + cg * 16 * UNIT + fr * 1024;
    }
    R.o0 = opq(ob[0]); R.o1 = opq(ob[1]); R.ocb = opq(iswo ? W2B : 8 * UNIT); R.sig = opq((iswo ? C_W2 : C_W4) + cg);
  } else {
    const int a = job - 64, j = a / 28, ks = a % 28;
#pragma unroll
    for (int i = 0; i < 7; ++i) {
      const int p = lane + 64 * i, r = p / 28, c = p % 28;
      R.pa[i] = (const v4u*)((c < 14 ? A.wg : A.wu) + (size_t)(ks * 16 + r) * FF + 112 * j + (c < 14 ? c : c - 14) * 8);
    }
    R.rd0 = stage + (8 * h + q) * 448 + (quad & 1) * 224 + (8 * hh + 4 * (quad >> 1)) * 2; R.rd1 = 0; R.step = 0;
    R.o0 = opq(OFF_W3 + j * 7 * UNIT + (ks < PK ? ks * 7 : 6 * PK + ks) * 1024); R.o1 = opq(ks < PK ? 1024 : MK * 1024);
    R.ocb = opq(OFF_CS3 + (j * 28 + ks) * 896); R.sig = opq(C_W3 + j);
  }
#pragma unroll
  for (int i = 0; i < 7; ++i) PIN(R.pa[i]);
  PIN(R.rd0); PIN(R.rd1);
}
DI void retile_load(const Retile& R, u32 lds_dst) {
#pragma unroll
  for (int i = 0; i < 7; ++i) {
    u32 keep;
    asm volatile("s_mov_b32 %0, m0\n\ts_add_u32 m0, %2, %3\n\ts_nop 0\n\tglobal_load_lds_dwordx4 %1, off\n\ts_mov_b32 m0, %0"
                 : "=&s"(keep) : "v"(R.pa[i]), "s"(lds_dst), "i"(i * 1024) : "memory", "scc");
  }
}
DI void retile_store(int job, const Retile& R, rsrc_t rs, LAS char* LD, int lane) {
  const int h = lane >> 5;
  if (job < 64 || job >= 512) {
    const bool pad = (lane & 0x14) == 0x14;
    const bool kpad = job < 64 && h;
    v4u o[8];
#pragma unroll
    for (int i = 0; i < 8; ++i) {
      const int ad = ((i & 2) ? R.rd1 : R.rd0) + (i >> 2) * 112 + (i & 1) * 16 * 224;
      const v2u lo = __builtin_bit_cast(v2u, __builtin_amdgcn_ds_read_tr16_b64_v4i16((LAS s16x4*)(LD + ad)));
      const v2u hi = __builtin_bit_cast(v2u, __builtin_amdgcn_ds_read_tr16_b64_v4i16((LAS s16x4*)(LD + ad + R.step)));
      o[i] = v4u{lo.x, lo.y, hi.x, hi.y};
    }
#pragma unroll
    for (int i = 0; i < 8; ++i) {
      if ((i & 2) && pad) o[i] = v4u{0u, 0u, 0u, 0u};
      if ((i & 1) && kpad) { o[i].z = 0u; o[i].w = 0u; }
      st16(rs, lane * 16, ((i & 1) ? R.o1 : R.o0) + (i >> 2) * R.ocb + ((i >> 1) & 1) * 1024, o[i]);
    }
  } else {
    v4u o[7];
#pragma unroll
    for (int n = 0; n < 7; ++n) o[n] = tr_frag(LD, R.rd0 + n * 32, 4 * 448);
    float cs[7];
#pragma unroll
    for (int n = 0; n < 7; ++n) {
      st16(rs, lane * 16, R.o0 + n * R.o1, o[n]);
      cs[n] = xsum(sum8(o[n]));
    }
#pragma unroll
    for (int n2 = 0; n2 < 4; ++n2) {
      const float v = h ? cs[n2 < 3 ? 2 * n2 + 1 : 6] : cs[2 * n2];
      if (n2 < 3 || !h) st4(rs, lane * 4, R.ocb + n2 * 256, __float_as_uint(v));
    }
  }
}

__global__ __launch_bounds__(512, 2) void k_main(const Args A) {
  extern __shared__ __attribute__((aligned(16))) char smem[];
  LAS char* LD = (LAS char*)smem;
  char* ws = A.ws;
  const int b = blockIdx.x, xcd = b & 7, bi = b >> 3;
  const int g = (xcd >> 2) * 8 + (bi >> 2), j = (xcd & 3) * 4 + (bi & 3);
  const int rb = 2 * g + (j >> 3), cb = j & 7;
  const int lane = threadIdx.x & 63, w = __builtin_amdgcn_readfirstlane(threadIdx.x >> 6);
  const int h = lane >> 5, tok = lane & 31;
  const rsrc_t rs = __builtin_amdgcn_make_buffer_rsrc((void*)ws, 0, WS_USED, 0x00020000);
  const u32 lbase = (u32)(size_t)smem;
  const int l16 = lane * 16;
  const int tq_ = (lane & 15) >> 2, tc8 = ((lane >> 4) & 1) * 4 + (lane & 3);
  const int w1lane = (8 * h + tq_) * 64 + min(tc8 + (j & 1), 7) * 8;

  const int hw = w - 4;
  const u32 voffn = (hw * 16 + (lane >> 2)) * (3 * D * 2) + (lane & 3) * 16;
  const char* w1 = (const char*)A.wqkv + 56 * j - (j & 1) * 8;
  unsigned long long ep;
  const u32 lb = lbase + hw * 1024;
#define KP(p) dma1<4 * UNIT + (p) * 4096, 896 + (p) * 0x2a000>(w1, voffn, lb)
#define VP(p) dma1<(p) * 4096, 1792 + (p) * 0x2a000>(w1, voffn, lb)
#define QP(p) dma1<UNIT + (p) * 4096, (p) * 0x2a000>(w1, voffn, lb)
  if (opq(w) >= 4) {
    HSTAMP(0);
    if (hw == 1 && b < NZ) {
      asm volatile("global_load_dwordx2 %0, %1, %2 sc1" : "=v"(ep) : "v"(EPOCH_OFF), "s"(ws) : "memory");
      st4(rs, lane * CST, b * 64 * CST, 0u);
    }
    if (hw == 2) {
      dma1<L_KVF, 0>((const char*)A.wphi + j * 1568, l16, lbase);
      dma1<L_KVF + 1024, 0>((const char*)A.wphi + j * 1568, min(1024 + l16, 1552), lbase);
    }
    KP(0); KP(1); VP(0); KP(2); VP(1); KP(3); VP(2);
    if (hw == 3) {
      unsigned long long pcv;
      asm volatile("s_getpc_b64 %0" : "=s"(pcv));
      const char* pc = (const char*)(pcv & ~255ull);
      dma1<L_W2X, 0>(pc, bi * 1024 + l16, lbase);
      dma1<L_W2X + 1024, 32768>(pc, bi * 1024 + l16, lbase);
    }
    HSTAMP(1);
  }

  if (w >= 4) {
    const u32 voff = hw * 1024 + lane * 16;
    const char* w1f = ws + OFF_W1 + j * 3 * UNIT;
    const char* w2 = ws + OFF_W2 + cb * W2B;
    const char* w3 = ws + OFF_W3 + j * 7 * UNIT;
    const char* w4 = ws + OFF_W4 + cb * 8 * UNIT;
#define DMA(src, slot) dma7((src), voff, lbase + (slot) * UNIT + hw * 1024)
#define DMAP(src, slot) dma7p((src), voff, lbase + (slot) * UNIT + hw * 1024)
#define DMAN(which, slot) dma7n(w1 + (which) * 896, voffn, lbase + (slot) * UNIT + hw * 1024)
    v4u wr[28];
    unsigned long long tag = 0;
    auto hlayer = [&](auto LC) AI {
      constexpr int L = LC.value;
      Retile R;
      const int job = hw >= 2 ? b * 2 + hw - 2 : (hw == 0 && b < 224 ? 512 + b : -1);
      float c0 = 0.f, c1 = 0.f, c2 = 0.f, c3 = 0.f;
      auto colpart = [&](int slot, auto K0, auto K1) AI {
        constexpr int k0 = K0.value, n = K1.value - K0.value;
        const int ib = opqv(slot * UNIT + opqv(w1lane));
        v4u fr[n];
#pragma unroll
        for (int ks = 0; ks < n; ++ks) fr[ks] = tr_frag(LD, ib + (k0 + ks) * 1024, 256);
        SB();
        const u32 one = 0x3f803f80u;
#pragma unroll
        for (int ks = 0; ks < n; ++ks) { c0 = dot2(fr[ks].x, one, c0); c1 = dot2(fr[ks].y, one, c1); c2 = dot2(fr[ks].z, one, c2); c3 = dot2(fr[ks].w, one, c3); }
        SB();
      };
      auto colfin = [&](int which) AI {
        const float cs = xsum((c0 + c1) + (c2 + c3));
        if (lane < 32) *(LAS float*)(LD + L_CS1 + which * 128 + lane * 4) = cs;
      };
      v4u csa[7];
      auto export_img = [&](int slot, int which) AI {
        const int ib = opqv(slot * UNIT + opqv(w1lane) + g * 1024);
        const int eo = opq(OFF_W1 + (j * 3 + which) * UNIT + g * 1024);
        st16(rs, l16, eo, tr_frag(LD, ib, 256));
        if (g < 12) st16(rs, l16, eo + 16384, tr_frag(LD, ib + 16384, 256));
      };
      if constexpr (L == 0) {
        if (hw == 1 && lane < 2) *(LAS v4u*)(LD + L_ARR + l16) = v4u{0u, 0u, 0u, 0u};
        HSTAMP(2);
#define VMW(n, n3) if (hw == 3) { VMCNT(n3); } else { VMCNT(n); }
        VMW(4, 6); BAR();
        HSTAMP(3);
        KP(4); VP(3);
        if (hw == 1 && b < NZ) {
          PIN(ep);
          tag = ep + 1ull;
          if (lane == 0) __hip_atomic_store((unsigned long long*)(ws + INIT_OFF + b * 128), tag, __ATOMIC_RELAXED, __HIP_MEMORY_SCOPE_AGENT);
        }
        if (hw == 2) {
          const int wb = opqv(L_KVF + (4 * h + tq_) * 56 + tc8 * 8);
          const v4u f0 = tr_frag(LD, wb, 8 * 56);
          v4u f1 = tr_frag(LD, wb + 16 * 56, 8 * 56);
          if (h) { f1.z = 0u; f1.w = 0u; }
          *(LAS v4u*)(LD + L_WPHI + l16) = f0;
          *(LAS v4u*)(LD + L_WPHI + 1024 + l16) = f1;
        }
        if (hw == 0) colpart(4, IC<0>{}, IC<8>{});
        VMW(4, 6); BAR();
        HSTAMP(4);
        KP(5); VP(4);
        if (job >= 0) retile_prep(A, job, lane, L_STAGE + hw * STG, R);
        if (hw == 1) colpart(0, IC<0>{}, IC<8>{});
        VMW(4, 6); BAR();
        HSTAMP(5);
        KP(6); VP(5); VP(6);
        if (hw == 0) colpart(4, IC<8>{}, IC<16>{});
        if (hw == 1) colpart(0, IC<8>{}, IC<12>{});
        VMCNT(5); BAR();
        HSTAMP(6);
        QP(0); QP(1); QP(2); QP(3);
        if (hw == 0) colpart(4, IC<16>{}, IC<20>{});
        if (hw == 1) colpart(0, IC<12>{}, IC<16>{});
        VMCNT(7); BAR();
        HSTAMP(7);
        QP(4); QP(5); QP(6);
        if (hw == 0) colpart(4, IC<20>{}, IC<24>{});
        if (hw == 1) colpart(0, IC<16>{}, IC<20>{});
        VMCNT(7); BAR();
        HSTAMP(8);
        if (hw == 0) { colpart(4, IC<24>{}, IC<28>{}); colfin(0); }
        if (hw == 1) { colpart(0, IC<20>{}, IC<28>{}); colfin(1); }
        if (hw == 2) export_img(4, 1);
        if (hw == 3) export_img(0, 2);
        HSTAMP(9);
        BAR();
        HSTAMP(10);
        if (job >= 0) retile_load(R, lbase + L_STAGE + hw * STG);
      } else {
        wait_arr(LD, 0, 4);
        DMA(w1f + 2 * UNIT, 0);
        VMCNT(0); BAR();
      }
      {
        const int wb = opqv(L_WPHI + opqv(l16));
        const v4u wf0 = lds16(LD, wb), wf1 = lds16(LD, wb + 1024);
        if (L == 0) HSTAMP(11);
        BAR();
        if (L == 0) HSTAMP(12);
        __builtin_amdgcn_s_setprio(1);
        const int lz = opqv(lane);
        const int xk = opqv(L_W2X + hw * 1024 + lz * 16);
        const v4u kf0 = lds16(LD, xk), kf1 = lds16(LD, xk + 4096);
        const float rstd = *(LAS float*)(LD + L_KVF + hw * 256 + lz * 4);
        f32x16 p = {};
        p = mfma(wf0, kf0, p);
        p = mfma(wf1, kf1, p);
        v4u o0, o1;
        half_softmax<1>(p, h, rstd, o0, o1);
        const int pb = opqv(L_PHK + (hw * 32 + (lz & 31)) * PHK_STRIDE + 8 * (lz >> 5) + 64);
        *(LAS v2u*)(LD + pb) = v2u{o0.x, o0.y}; *(LAS v2u*)(LD + pb + 16) = v2u{o0.z, o0.w};
        *(LAS v2u*)(LD + pb + 32) = v2u{o1.x, o1.y}; *(LAS v2u*)(LD + pb + 48) = v2u{o1.z, o1.w};
        __builtin_amdgcn_s_setprio(0);
      }
      if (L == 0) HSTAMP(13);
      if constexpr (L == 0) {
        if (job >= 0) { VMCNT(7); } else { VMCNT(0); }
      } else VMCNT(0);
      if (L == 0) HSTAMP(14);
      BAR();
      if (hw < 2) {
        __builtin_amdgcn_s_setprio(2);
        f32x16 kp = {};
        const int lz = opqv(lane);
        const int g16 = (lz >> 4) & 1, q = (lz & 15) >> 2, p = lz & 3, hz = lz >> 5;
        const int ab0 = opqv(L_PHK + (8 * hz + q) * PHK_STRIDE + (g16 * 16 + 4 * p) * 2 + hw * 64);
        const int bb0 = opqv(L_V + (8 * hz + q) * V_STRIDE + (g16 * 16 + 4 * p) * 2);
        v4u ap[8], bv[8];
#pragma unroll
        for (int s = 0; s < 8; ++s) { ap[s] = tr_frag(LD, ab0 + 16 * s * PHK_STRIDE, 4 * PHK_STRIDE); bv[s] = tr_frag(LD, bb0 + 16 * s * V_STRIDE, 4 * V_STRIDE); }
        SB();
#pragma unroll
        for (int s = 0; s < 8; ++s) kp = mfma(ap[s], bv[s], kp);
        const int ko = opq(OFF_KVP + L * KVB + (j * 16 + g) * 4096 + hw * 2048);
        st16(rs, l16, ko, pack8(kp, 0));
        st16(rs, l16, ko + 1024, pack8(kp, 1));
        wave_signal(ws, C_KV + L * 16 + j, lane);
        __builtin_amdgcn_s_setprio(0);
      }
      if constexpr (L == 0) {
        HSTAMP(15);
        if (hw == 3) {
          colpart(1, IC<0>{}, IC<28>{}); colfin(2);
          arrive_lds(LD, 1, lane);
        }
        if (hw == 1) { export_img(1, 0); wave_signal(ws, C_W1 + j, lane); }
        if (job >= 0) { VMCNT(0); retile_store(job, R, rs, LD, lane); }
        HSTAMP(16);
        if (job >= 0) wave_signal(ws, R.sig, lane);
        if (hw >= 2) signal_cnt(ws, C_W1 + j);
        HSTAMP(17);
        BAR();
        HSTAMP(18);
        u32* fw = cnt_ptr(ws, (lane & 3) == 0 ? C_W2 + (cb >> 1) : (lane & 3) == 1 ? C_W3 + j : C_W4 + (cb >> 1));
        kv_reduce(rs, LD, L, j, w, lane);
        HSTAMP(19);
        BAR();
        HSTAMP(20);
        wait_flags(ws, fw, __hip_atomic_load(fw, __ATOMIC_RELAXED, __HIP_MEMORY_SCOPE_AGENT), (lane & 3) == 0 ? 16u : (lane & 3) == 1 ? 28u : 56u);
        HSTAMP(21);
        DMA(w2, 2); DMA(w2 + UNIT, 3); dma2(w2 + 2 * UNIT, voff, lbase + L_W2X + hw * 1024);
#pragma unroll
        for (int i = 0; i < 7; ++i) csa[i] = ld16(rs, min(lane, 55) * 16, OFF_CS3 + (j * 28 + hw * 7 + i) * 896);
        DMA(w3, 0);
        HSTAMP(22);
        VMCNT(14); HSTAMP(23); BAR();
        HSTAMP(24);
        DMA(w3 + UNIT, 1);
      } else {
        DMA(w2, 2); DMA(w2 + UNIT, 3); dma2(w2 + 2 * UNIT, voff, lbase + L_W2X + hw * 1024);
        DMA(w3, 0);
        BAR();
        kv_reduce(rs, LD, L, j, w, lane);
        BAR();
        DMA(w3 + UNIT, 1);
      }
      BAR();
      if constexpr (L == 0) {
        f32x4 c4 = __builtin_bit_cast(f32x4, csa[0]);
#pragma unroll
        for (int i = 1; i < 7; ++i) c4 += __builtin_bit_cast(f32x4, csa[i]);
        if (lane < 56) *(LAS f32x4*)(LD + L_KVF + hw * 896 + l16) = c4;
      }
      DMA(w3 + 2 * UNIT, 2);
      VMCNT(7); BAR();
      const int wo4 = opq(OFF_W4 + cb * 8 * UNIT + 4 * UNIT + hw * UNIT);
#define WR(lo, hi) _Pragma("unroll") for (int i = lo; i < hi; ++i) wr[i] = ld16(rs, l16, wo4 + i * 1024)
      if constexpr (L == 0) {
        const int t4 = min(hw * 64 + lane, 223) * 4;
        *(LAS float*)(LD + L_CS3 + t4) = (*(LAS float*)(LD + L_KVF + t4) + *(LAS float*)(LD + L_KVF + 896 + t4)) +
                                         (*(LAS float*)(LD + L_KVF + 1792 + t4) + *(LAS float*)(LD + L_KVF + 2688 + t4));
        WR(0, 28);
      }
      DMA(w3 + 3 * UNIT, 3);
      VMCNT(7); BAR();
      DMAP(w3 + 4 * UNIT, 0);
      DMAP(w4 + UNIT, 4);
      VMCNT(14); BAR();
      DMAP(w3 + 5 * UNIT, 1);
      if (L == 0) wait_cnt(ws, C_W1 + j, 48);
      VMCNT(14); BAR();
      DMAP(w3 + 6 * UNIT, 2);
      VMCNT(7); BAR();
      DMAP(w4 + 2 * UNIT, 3);
      VMCNT(7); BAR();
      {
        u32* fp = cnt_ptr(ws, C_H + (L * 64 + 2 * rb + (lane & 1)) * 8);
        const int ho = opq(OFF_H + L * HB + 2 * rb * 112 * 1024 + hw * 7168);
        v4u hx[28];
        auto ldh = [&](auto I) AI { constexpr int i = I.value; hx[i] = ld16(rs, l16, ho + (i & 1) * 112 * 1024 + p4_koff(p4_oh(i >> 1))); };
        auto poll = [&](int lv) AI { return __hip_atomic_load(fp + CW * lv, __ATOMIC_RELAXED, __HIP_MEMORY_SCOPE_AGENT); };
        DMAP(w4, 0);
        wait_flags(ws, fp, poll(0));
        static_for<8>([&](auto I) AI { ldh(I); });
        BAR();
        u32 f = poll_issue(fp + 1 * CW);
        DMA(w4 + 3 * UNIT, 1);
        VMCNT(7); PIN(f);
        wait_flags(ws, fp + 1 * CW, f);
        static_for<6>([&](auto I) AI { ldh(IC<8 + I.value>{}); });
        f32x16 acc[4] = {};
        auto ord = [](int i) constexpr { return i < 20 ? i : (i < 24 ? 20 + 2 * (i - 20) : 21 + 2 * (i - 24)); };
        const int pb0 = opqv(p4_part(0, hw) + opqv(l16)), pb2 = opqv(p4_part(2, hw) + opqv(l16));
        static_for<28>([&](auto I) AI {
          constexpr int i = I.value, k = ord(i), t = k >> 1, tq = k & 1;
          if constexpr (i == 0) f = poll(2);
          if constexpr (i == H2) {
            wait_flags(ws, fp + 2 * CW, f);
            VMCNT(2); arrive_lds(LD, 4, lane);
          }
          if constexpr (i == H3 - 3) f = poll(3);
          if constexpr (i == H3) wait_flags(ws, fp + 3 * CW, f);
          if constexpr (i == 24) wait_arr(LD, 2, 4 * L + 4);
          acc[tq] = mfma(wr[2 * t], hx[k], acc[tq]);
          acc[2 + tq] = mfma(wr[2 * t + 1], hx[k], acc[2 + tq]);
          if constexpr (i < 2) ldh(IC<14 + i>{});
          if constexpr (i >= H2 && i < H2 + 4) { ldh(IC<16 + 2 * (i - H2)>{}); ldh(IC<17 + 2 * (i - H2)>{}); }
          if constexpr (i >= H3 && i < H3 + 2) { ldh(IC<24 + 2 * (i - H3)>{}); ldh(IC<25 + 2 * (i - H3)>{}); }
          if constexpr (i >= 24) {
            constexpr int qd = i - 24;
            *(LAS f32x4*)(LD + pb0 + qd * 1024) = f32x4{acc[0][4 * qd], acc[0][4 * qd + 1], acc[0][4 * qd + 2], acc[0][4 * qd + 3]};
            *(LAS f32x4*)(LD + pb2 + qd * 1024) = f32x4{acc[2][4 * qd], acc[2][4 * qd + 1], acc[2][4 * qd + 2], acc[2][4 * qd + 3]};
          }
          SB();
        });
        wait_arr(LD, 3, 4 * L + 4);
        put16(acc[1], LD, opqv(p4_part(1, hw) + opqv(l16)));
        put16(acc[3], LD, opqv(p4_part(3, hw) + opqv(l16)));
        BAR();
      }
      if (L == 0) { DMA(w1f + UNIT, 3); DMA(w1f, 1); }
    };
    hlayer(IC<0>{});
    hlayer(IC<1>{});
    if (b == 0 && hw == 1 && lane == 0) __hip_atomic_store((unsigned long long*)(ws + EPOCH_OFF), tag, __ATOMIC_RELAXED, __HIP_MEMORY_SCOPE_AGENT);
    return;
  }

  const int tt = 4 * g + w;
  const int tt2 = 2 * rb + (w & 1);
  const int kh = w >> 1;
  const rsrc_t rx0 = __builtin_amdgcn_make_buffer_rsrc((void*)A.x0, 0, XB, 0x00020000);
  v4u res[2];
#define XWV 14
  constexpr int XW = XWV;

  auto clayer = [&](auto LC) AI {
    constexpr int L = LC.value;
    v4u x[28];
    v2u st[8];
    float mean, rstd;
    v4u pq[4];
    STAMP(0);
    auto phase1 = [&](auto FIRST) AI {
      constexpr bool first = FIRST.value;
      const int xo = opq(first ? tt * 32 * (D * 2) : OFF_X2 + tt * 28 * 1024);
      auto ldx = [&](int ks) AI { return ld16(rs, l16, xo + ks * 1024); };
      const int so_ = opq(OFF_ST2 + tt * 4096);
      const int sv_ = (opqv(lane) & 31) * 8 + h * 2048;
      v4u gx[28];
      const int stg = L_STAGE + w * STG;
      const int gv = (lane >> 4) * (D * 2) + (lane & 15) * 16, gv3 = (lane >> 3) * (D * 2) + (lane & 7) * 16;
      const int gl = stg + (lane >> 4) * 272 + (lane & 15) * 16, gl3 = stg + (lane >> 3) * 272 + (lane & 7) * 16;
      const int srd = opqv(stg + (opqv(lane) & 31) * 272 + h * 16);
      auto ldg = [&](int i) AI {
        return i < 24 ? __builtin_amdgcn_raw_buffer_load_b128(rx0, gv, xo + (i & 7) * 4 * (D * 2) + (i >> 3) * 256, 0)
                      : __builtin_amdgcn_raw_buffer_load_b128(rx0, gv3, xo + (i & 7) * 8 * (D * 2) + 768, 0);
      };
      auto stage = [&](int c) AI {
#pragma unroll
        for (int i = 0; i < (c < 3 ? 8 : 4); ++i) *(LAS v4u*)(LD + (c < 3 ? gl + i * 4 * 272 : gl3 + i * 8 * 272)) = gx[8 * c + i];
#pragma unroll
        for (int i = 0; i < (c < 3 ? 8 : 4); ++i) x[8 * c + i] = lds16(LD, srd + i * 32);
      };
      unsigned long long iv = 0;
      unsigned long long* initp = (unsigned long long*)(ws + (w == 3 && lane < NZ ? INIT_OFF + lane * 128 : EPOCH_OFF));
      auto init_ok = [&](unsigned long long v) AI {
        const unsigned long long e = ((unsigned long long)(u32)__builtin_amdgcn_readlane((int)(v >> 32), 63) << 32) | (u32)__builtin_amdgcn_readlane((int)v, 63);
        return __all(lane >= NZ || v == e + 1ull);
      };
      if constexpr (first) {
#pragma unroll
        for (int i = 0; i < 16; ++i) gx[i] = ldg(i);
        stage(0);
      } else {
#pragma unroll
        for (int ks = 0; ks < XW; ++ks) x[ks] = ldx(ks);
      }
      USTAMP(18);
      BAR();
      USTAMP(19);
      f32x16 ak = {}, av = {}, p = {};
      const int sbk = first ? opqv(opqv(w1lane) + 4 * UNIT) : opqv(opqv(l16) + 3 * UNIT), sbv = first ? opqv(w1lane) : opqv(l16);
      const int csb = opqv(L_CS1 + 16 * (opqv(lane) >> 5)), wb = opqv(L_WPHI + opqv(l16));
      const int xk = opqv(L_W2X + w * 1024 + opqv(l16));
      f32x4 cs[4]; v4u kf[2], wf[2];
      float nmean, s1 = 0.f, s2 = 0.f, q1 = 0.f, q2 = 0.f;
      auto isk = [](int i) constexpr { return i < LAG || (i < 56 - LAG && ((i - LAG) & 1) == 0); };
      auto kst = [](int i) constexpr { return i < LAG ? i : (i < 56 - LAG ? ((((i - LAG) & 1) == 0) ? LAG + (i - LAG) / 2 : (i - LAG) / 2) : i - 28); };
      auto rdw = [&](int base, int ks) AI { if constexpr (first) return tr_frag(LD, base + ks * 1024, 256); else return lds16(LD, base + ks * 1024); };
      pipe<56, first ? 2 : 1>([&](auto I) AI { return rdw(isk(I.value) ? sbk : sbv, kst(I.value)); },
               [&](auto I) AI {
                 constexpr int i = I.value;
                 if constexpr (first && (i == 6 || i == 14 || i == 22 || i == 30 || i == 38 || i == 46)) { USTAMP(40 + i / 8 * 2); RAWBAR(); USTAMP(41 + i / 8 * 2); }
                 if constexpr (i == 54) { USTAMP(20); BAR(); USTAMP(21); }
               },
               [&](auto I, v4u a, auto nx) AI {
                 constexpr int i = I.value, ks = kst(i);
                 if constexpr (i == 54) { p = mfma(wf[0], kf[0], p); p = mfma(wf[1], kf[1], p); }
                 if constexpr (isk(i)) ak = mfma(a, x[ks], ak); else av = mfma(a, x[ks], av);
                 nx();
                 if constexpr (!first && isk(i) && ks + XW < 28) x[ks + XW] = ldx(ks + XW);
                 if constexpr (first) {
                   if constexpr (i < 12) gx[16 + i] = ldg(16 + i);
                   if constexpr (isk(i) && ks % 8 == 6) stage(ks / 8 + 1);
                   if constexpr (isk(i)) {
                     const u32 one = 0x3f803f80u;
                     s1 = dot2(x[ks].x, one, s1); q1 = dot2(x[ks].x, x[ks].x, q1); s2 = dot2(x[ks].y, one, s2); q2 = dot2(x[ks].y, x[ks].y, q2);
                     s1 = dot2(x[ks].z, one, s1); q1 = dot2(x[ks].z, x[ks].z, q1); s2 = dot2(x[ks].w, one, s2); q2 = dot2(x[ks].w, x[ks].w, q2);
                   }
                   if constexpr (i == 45) iv = __hip_atomic_load(initp, __ATOMIC_RELAXED, __HIP_MEMORY_SCOPE_AGENT);
                   if constexpr (i == 51) { ln_finish(s1 + s2, q1 + q2, mean, rstd); nmean = -mean; }
                 } else {
                   if constexpr (!isk(i) && ks >= 4 && ks < 12) st[ks - 4] = ld8(rs, sv_ + (ks - 4) * 256, so_);
                   if constexpr (i == 45) { ln_stats(st, mean, rstd); nmean = -mean; }
                 }
                 if constexpr (i >= 46 && i < 50) cs[i - 46] = *(LAS f32x4*)(LD + csb + 32 * (i - 46));
                 if constexpr (i == 50) wf[0] = lds16(LD, wb);
                 if constexpr (i == 51) wf[1] = lds16(LD, wb + 1024);
                 if constexpr (i == 52 || i == 53) {
#pragma unroll
                   for (int qd = 2 * (i - 52); qd < 2 * (i - 52) + 2; ++qd) {
                     const float y0 = fmaf(nmean, cs[qd][0], ak[4 * qd]), y1 = fmaf(nmean, cs[qd][1], ak[4 * qd + 1]);
                     const float y2 = fmaf(nmean, cs[qd][2], ak[4 * qd + 2]), y3 = fmaf(nmean, cs[qd][3], ak[4 * qd + 3]);
                     kf[qd >> 1][2 * (qd & 1)] = pk2(y0, y1);
                     kf[qd >> 1][2 * (qd & 1) + 1] = pk2(y2, y3);
                   }
                   *(LAS v4u*)(LD + xk + (i - 52) * 4096) = kf[i - 52];
                   if constexpr (i == 52) *(LAS float*)(LD + L_KVF + w * 256 + opqv(lane) * 4) = rstd;
                 }
                 if constexpr (i == 55) {
#pragma unroll
                   for (int qd = 0; qd < 4; ++qd) cs[qd] = *(LAS f32x4*)(LD + csb + 128 + 32 * qd);
                 }
               });
      STAMP(1);
      const int lz = opqv(lane);
      const int trow = w * 32 + (lz & 31);
      const int pb = opqv(L_PHK + trow * PHK_STRIDE + 8 * (lz >> 5)), vb = opqv(L_V + trow * V_STRIDE + 8 * (lz >> 5));
      half_softmax<0>(p, h, rstd, pq[0], pq[1]);
#pragma unroll
      for (int s = 0; s < 2; ++s) {
        *(LAS v2u*)(LD + pb + 32 * s) = v2u{pq[s].x, pq[s].y};
        *(LAS v2u*)(LD + pb + 32 * s + 16) = v2u{pq[s].z, pq[s].w};
      }
      SB();
#pragma unroll
      for (int r = 0; r < 16; ++r) av[r] = fmaf(nmean, cs[r >> 2][r & 3], av[r]) * rstd;
      if (h) { av[12] = 1.f; av[13] = 0.f; av[14] = 0.f; av[15] = 0.f; }
#pragma unroll
      for (int qd = 0; qd < 4; ++qd) *(LAS v2u*)(LD + vb + 16 * qd) = v2u{pk2(av[4 * qd], av[4 * qd + 1]), pk2(av[4 * qd + 2], av[4 * qd + 3])};
      if constexpr (first) {
        if (w == 3 && !init_ok(iv)) {
          u32 spins = 0;
          while (!init_ok(__hip_atomic_load(initp, __ATOMIC_RELAXED, __HIP_MEMORY_SCOPE_AGENT))) {
            __builtin_amdgcn_s_sleep(1);
            if (++spins > 400000u) break;
          }
        }
      }
      STAMP(2);
      BAR();
      if constexpr (first) {
#pragma unroll
        for (int ro = 0; ro < 2; ++ro) {
          const int oi = min(kh * 4 + ro * 2 + h, 6);
          res[ro] = __builtin_amdgcn_raw_buffer_load_b128(rx0, tok * (D * 2) + (7 * cb + oi) * 16, tt2 * 32 * (D * 2), 0);
        }
      }
      f32x16 aq = {};
      const int sbq = first ? opqv(opqv(w1lane) + 1 * UNIT) : opqv(opqv(l16) + 1 * UNIT);
      pipe<28, first ? 2 : 1>([&](auto I) AI { return rdw(sbq, I.value); }, [&](auto) AI {},
               [&](auto I, v4u a, auto nx) AI { aq = mfma(a, x[I.value], aq); nx(); });
      if constexpr (first) wait_arr(LD, 1, 1);
#pragma unroll
      for (int qd = 0; qd < 4; ++qd) cs[qd] = *(LAS f32x4*)(LD + csb + 256 + 32 * qd);
#pragma unroll
      for (int r = 0; r < 16; ++r) aq[r] = fmaf(nmean, cs[r >> 2][r & 3], aq[r]);
      f32x16 p2 = {};
      const int wb2 = opqv(L_WPHI + opqv(l16));
      p2 = mfma(lds16(LD, wb2), pack8(aq, 0), p2);
      p2 = mfma(lds16(LD, wb2 + 1024), pack8(aq, 1), p2);
      half_softmax<0>(p2, h, rstd, pq[0], pq[1]);
      half_softmax<1>(p2, h, rstd, pq[2], pq[3]);
    };
    phase1(std::integral_constant<bool, L == 0>{});
    STAMP(3);
    if (w == 0) wait_cnt(ws, C_KV + L * 16 + j, 32);
    BAR();
    STAMP(4);
    kv_reduce(rs, LD, L, j, w, lane);
    BAR();
    STAMP(5);
    {
      f32x16 nm = {};
      const int kb = opqv(L_KVF + opqv(l16));
      v4u kf[4];
#pragma unroll
      for (int s = 0; s < 4; ++s) kf[s] = lds16(LD, kb + s * 1024);
#pragma unroll
      for (int s = 0; s < 4; ++s) nm = mfma(kf[s], pq[s], nm);
      auto r = __builtin_amdgcn_permlane32_swap(__float_as_uint(nm[12]), __float_as_uint(nm[12]), false, false);
      const float den = __uint_as_float(r[1]) + 1e-6f;
      const float rd = __builtin_amdgcn_rcpf(den);
#pragma unroll
      for (int i = 0; i < 16; ++i) nm[i] *= rd;
      const int oo = opq(OFF_O + L * OB + (tt * 32 + 2 * j) * 1024);
      st16(rs, l16, oo, pack8(nm, 0));
      st16(rs, l16, oo + 1024, pack8(nm, 1));
    }
    wave_signal(ws, C_O + (L * 64 + tt) * 2 + (j >> 3), lane);
    STAMP(6);
    wait_cnt(ws, C_O + (L * 64 + tt2) * 2 + kh, 8, true);
    STAMP(7);
    {
      const int xo = opq(OFF_O + L * OB + (tt2 * 32 + 16 * kh) * 1024);
#pragma unroll
      for (int t = 0; t < (XW < 16 ? XW : 16); ++t) x[t] = ld16(rs, l16, xo + t * 1024);
      if (L == 0) RAWBAR();
      f32x16 a0 = {}, a1 = {};
      const int sb2 = opqv(2 * UNIT + kh * 2048 + opqv(l16)), sbx = opqv(L_W2X + kh * 2048 + opqv(l16));
      pipe<32>([&](auto I) AI { constexpr int i = I.value, t = i >> 1; return lds16(LD, (t < 14 ? sb2 + t * 4096 : sbx + (t - 14) * 4096) + (i & 1) * 1024); },
               [&](auto I) AI { if constexpr (I.value == 14) RAWBAR();   },
               [&](auto I, v4u a, auto nx) AI {
                 constexpr int i = I.value;
                 if constexpr (i & 1) a1 = mfma(a, x[i >> 1], a1); else a0 = mfma(a, x[i >> 1], a0);
                 nx();
                 if constexpr (!(i & 1) && (i >> 1) + XW < 16) x[(i >> 1) + XW] = ld16(rs, l16, xo + ((i >> 1) + XW) * 1024);
               });
      const int xb = opqv(L_SCR + w * 4096 + opqv(l16)), xb2 = opqv(L_SCR + (w ^ 2) * 4096 + opqv(l16));
      f32x16 fin;
      if (kh) {
#pragma unroll
        for (int qd = 0; qd < 4; ++qd) *(LAS f32x4*)(LD + xb + qd * 1024) = f32x4{a0[4 * qd], a0[4 * qd + 1], a0[4 * qd + 2], a0[4 * qd + 3]};
        fin = a1;
      } else {
#pragma unroll
        for (int qd = 0; qd < 4; ++qd) *(LAS f32x4*)(LD + xb + qd * 1024) = f32x4{a1[4 * qd], a1[4 * qd + 1], a1[4 * qd + 2], a1[4 * qd + 3]};
        fin = a0;
      }
      BAR();
#pragma unroll
      for (int qd = 0; qd < 4; ++qd) {
        const f32x4 v = *(LAS f32x4*)(LD + xb2 + qd * 1024);
#pragma unroll
        for (int i = 0; i < 4; ++i) fin[4 * qd + i] += v[i];
      }
      finish_tile(fin, res, rs, opq(OFF_X1 + L * XB), opq(OFF_ST1 + L * STB), tt2, cb, kh, lane, false, A.out);
    }
    wave_signal(ws, C_X1 + L * 64 + tt2, lane);
    STAMP(8);
    wait_cnt(ws, C_X1 + L * 64 + tt, 16, true);
    STAMP(9);
    {
      const int xo = opq(OFF_X1 + L * XB + tt * 28 * 1024);
      const int so_ = opq(OFF_ST1 + L * STB + tt * 4096);
      const int sv_ = (opqv(lane) & 31) * 8 + h * 2048;
      constexpr int XW3 = 8;
#pragma unroll
      for (int ks = 0; ks < XW3; ++ks) x[ks] = ld16(rs, l16, xo + ks * 1024);
      const int ho = opq(OFF_H + L * HB + (tt * 112 + 7 * j) * 1024);
      const int lvo = opq(C_H + (L * 64 + tt) * 8);
      u32* fp = cnt_ptr(ws, C_H + (L * 64 + tt2) * 8);
      auto poll = [&](int lv) AI { return __hip_atomic_load(fp + CW * lv, __ATOMIC_RELAXED, __HIP_MEMORY_SCOPE_AGENT); };
      u32 f;
      float nmean, gs, irs;
      const int csb = opqv(L_CS3 + 16 * (opqv(lane) >> 5));
      const int sbA = opqv(l16), sbB = opqv(opqv(l16) + 2 * UNIT);
      f32x4 Gcg[2], Gcu[2]; float Gt[8], Gyu[8]; u32 Go[4];
      auto glu = [&](const f32x16& a, auto N_, auto S_) AI {
        constexpr int n = N_.value, s = S_.value;
        if constexpr (s == 1) Gcu[0] = *(LAS f32x4*)(LD + csb + n * 128 + 32);
        if constexpr (s == 4) Gcg[1] = *(LAS f32x4*)(LD + csb + n * 128 + 64);
        if constexpr (s == 9) Gcu[1] = *(LAS f32x4*)(LD + csb + n * 128 + 96);
        if constexpr (s == 12 && n < 6) Gcg[0] = *(LAS f32x4*)(LD + csb + (n + 1) * 128);
        static_for<8>([&](auto V_) AI {
          constexpr int v = V_.value, k = s - (1 + 4 * (v >> 1) + (v & 1)), hf = v >> 2, i = v & 3;
          if constexpr (k == 0) { Gt[v] = fmaf(nmean, Gcg[hf][i], a[8 * hf + i]); PIN(Gt[v]); }
          if constexpr (k == 1) { Gt[v] = Gt[v] * gs; PIN(Gt[v]); }
          if constexpr (k == 2) { Gt[v] = __builtin_amdgcn_exp2f(Gt[v]); PIN(Gt[v]); }
          if constexpr (k == 3) { Gt[v] = fmaf(Gt[v], irs, irs); PIN(Gt[v]); }
          if constexpr (k == 4) { Gt[v] = __builtin_amdgcn_rcpf(Gt[v]); PIN(Gt[v]); }
          if constexpr (k == 5) { Gyu[v] = fmaf(nmean, Gcu[hf][i], a[8 * hf + 4 + i]); PIN(Gyu[v]); }
          if constexpr (k == 6) { Gt[v] = Gt[v] * Gyu[v]; PIN(Gt[v]); }
          if constexpr (k == 7 && (v & 1)) { Go[v >> 1] = pk2(Gt[v - 1], Gt[v]); PIN(Go[v >> 1]); }
        });
        if constexpr (s == 21) st16(rs, l16, ho + n * 1024, v4u{Go[0], Go[1], Go[2], Go[3]});
      };
      const int ho2 = opq(OFF_H + L * HB + tt2 * 112 * 1024 + kh * 7168);
      v4u hx[28];
      auto ldh = [&](auto T) AI { hx[T.value] = ld16(rs, l16, ho2 + p4_koff(p4_oc(T.value))); };
      f32x16 acc[7];
      constexpr int PB = 7 * PK;
      pipe<196>(
          [&](auto I) AI { constexpr int i = I.value, u = i / 28, sl = u & 3; return lds16(LD, (sl < 2 ? sbA : sbB) + (sl & 1) * UNIT + (i % 28) * 1024); },
          [&](auto I) AI { if constexpr (I.value > 28 && I.value % 28 == 6) { VSTAMP(30 + I.value / 28); RAWBAR(); WSTAMP(22 + I.value / 28); } },
          [&](auto I, v4u a, auto nx) AI {
            constexpr int i = I.value;
            if constexpr (i < PB) {
              constexpr int n = i % 7, ks = i / 7;
              if constexpr (ks == 0) { f32x16 z = {}; acc[n] = mfma(a, x[ks], z); } else acc[n] = mfma(a, x[ks], acc[n]);
              nx();
              constexpr int K2 = 28 - XW3 - PK, fx = ks < K2 ? XW3 + 2 * ks + (n ? 1 : 0) : XW3 + K2 + ks;
              if constexpr (n == 0 || (n == 3 && ks < K2)) x[fx] = ld16(rs, l16, xo + fx * 1024);
              if constexpr (n >= 5 && ks < 4) st[2 * ks + n - 5] = ld8(rs, sv_ + (2 * ks + n - 5) * 256, so_);
              if constexpr (i == 46) {
                ln_stats(st, mean, rstd);
                nmean = -mean; gs = -LOG2E * rstd; irs = __builtin_amdgcn_rcpf(rstd);
                PIN(nmean); PIN(gs); PIN(irs);
              }
            } else {
              constexpr int r = i - PB, n = r / MK, s = r % MK;
              acc[n] = mfma(a, x[PK + s], acc[n]);
              nx();
              if constexpr (r == 8) Gcg[0] = *(LAS f32x4*)(LD + csb);
              if constexpr (n >= 2 && s + MK < 22) glu(acc[n - 2], IC<n - 2>{}, IC<s + MK>{});
              if constexpr (n >= 1) glu(acc[n - 1], IC<n - 1>{}, IC<s>{});
              if constexpr (n >= 3 && s == 20 - MK) { VMCNT(0); signal_cnt(ws, lvo + n - 3); }
            }
          });
      STAMP(10);
      arrive_lds(LD, 2, lane);
      static_for<22>([&](auto S_) AI {
        if constexpr (S_.value + MK < 22) glu(acc[5], IC<5>{}, IC<S_.value + MK>{});
        glu(acc[6], IC<6>{}, S_);
        if constexpr (S_.value == 4) { VMCNT(0); signal_cnt(ws, lvo + 4); }
        if constexpr (S_.value == CP1) f = poll_issue(fp + 3 * CW);
      });
      STAMP(11);
      const int wo_ = opqv(kh * 3072 + opqv(l16)), wp_ = opqv(kh * 1024 + 1024 + opqv(l16));
      const int wo2 = opqv(wo_ + 3 * UNIT), wp2 = opqv(wp_ + 3 * UNIT);
      v4u wq[56];
      auto ldw = [&](auto K) AI {
        constexpr int k = K.value, t = k >> 1, wb = p4_wbase(t);
        if constexpr (wb >= 3 * UNIT) wq[k] = lds16(LD, ((k & 1) ? wp2 : wo2) + wb - 3 * UNIT);
        else wq[k] = lds16(LD, ((k & 1) ? wp_ : wo_) + wb);
      };
      VMCNT(1); signal_cnt(ws, lvo + 5);
      PIN(f);
      const u32 f2 = poll(4);
      if (__all(f >= 16u)) {
        static_for<4>([&](auto T) AI { ldh(T); });
        VMCNT(6); signal_cnt(ws, lvo + 6);
      } else {
        VMCNT(2); signal_cnt(ws, lvo + 6);
        wait_flags(ws, fp + 3 * CW, f);
        static_for<4>([&](auto T) AI { ldh(T); });
      }
      wait_flags(ws, fp + 4 * CW, f2);
      static_for<8>([&](auto T) AI { ldh(IC<4 + T.value>{}); });
      wait_arr(LD, 4, 4 * L + 4);
      static_for<4>([&](auto K) AI { ldw(K); });
      STAMP(12);
      f32x16 fin = {}, oth = {};
      static_for<28>([&](auto T) AI {
        constexpr int t = T.value;
        if constexpr (t == CP5) f = poll(5);
        if constexpr (t == CT5) {
          wait_flags(ws, fp + 5 * CW, f);
          f = poll(6);
          static_for<8>([&](auto K) AI { ldh(IC<12 + K.value>{}); });
          STAMP(13);
        }
        if constexpr (t == 12) {
          wait_flags(ws, fp + 6 * CW, f);
          static_for<8>([&](auto K) AI { ldh(IC<20 + K.value>{}); });
          STAMP(22);
        }
        if constexpr (t == 12) arrive_lds(LD, 3, lane);
        if constexpr (t + 2 < 28) { ldw(IC<2 * t + 4>{}); ldw(IC<2 * t + 5>{}); }
        oth = mfma(wq[2 * t + 1], hx[t], oth);
        fin = mfma(wq[2 * t], hx[t], fin);
        SB();
      });
      STAMP(14);
      wait_arr(LD, 3, 4 * L + 4);
      put16(oth, LD, opqv(L_SCR + w * 4096 + opqv(l16)));
      BAR();
#pragma unroll
      for (int c = 0; c < 4; ++c) add16(fin, LD, opqv(p4_part(w, c) + opqv(l16)));
      add16(fin, LD, opqv(L_SCR + (w ^ 2) * 4096 + opqv(l16)));
      STAMP(15);
      finish_tile(fin, res, rs, OFF_X2, OFF_ST2, tt2, cb, kh, lane, L == 1, A.out);
    }
    if (L == 0) {
      wave_signal(ws, C_X2 + tt2, lane);
      arrive_lds(LD, 0, lane);
      STAMP(16);
      wait_cnt(ws, C_X2 + tt, 16, true);
    }
    STAMP(17);
  };
  clayer(IC<0>{});
  clayer(IC<1>{});
}

extern "C" void kernel_launch(void* const* d_in, const int* in_sizes, int n_in, void* d_out, int out_size, void* d_ws,
                              size_t ws_size, hipStream_t stream) {
  Args a;
  a.x0 = (const u16*)d_in[0]; a.wqkv = (const u16*)d_in[1]; a.wphi = (const u16*)d_in[2]; a.wo = (const u16*)d_in[3];
  a.wg = (const u16*)d_in[5]; a.wu = (const u16*)d_in[6]; a.wd = (const u16*)d_in[7];
  a.ws = (char*)d_ws; a.out = (u16*)d_out;
  (void)hipFuncSetAttribute((const void*)k_main, hipFuncAttributeMaxDynamicSharedMemorySize, LDS_BYTES);
  k_main<<<256, 512, LDS_BYTES, stream>>>(a);
}
```

```cpp
#include <hip/hip_runtime.h>
#include <stdint.h>
#include <utility>
#include <type_traits>

typedef unsigned short u16;
typedef unsigned int u32;
typedef u32 v4u __attribute__((ext_vector_type(4)));
typedef u32 v2u __attribute__((ext_vector_type(2)));
typedef float f32x2 __attribute__((ext_vector_type(2)));
typedef float f32x4 __attribute__((ext_vector_type(4)));
typedef float f32x16 __attribute__((ext_vector_type(16)));
typedef __bf16 bf16x2 __attribute__((ext_vector_type(2)));
typedef __bf16 bf16x8 __attribute__((ext_vector_type(8)));
typedef short s16x4 __attribute__((ext_vector_type(4)));
#define LAS __attribute__((address_space(3)))
#define DI __device__ __forceinline__
#define AI __attribute__((always_inline))
template <int V> using IC = std::integral_constant<int, V>;

constexpr int S = 2048, D = 448, FF = 1792;
constexpr int UNIT = 28672;

constexpr int C_KV = 0;
constexpr int C_W2 = 32;
constexpr int C_W4 = 36;
constexpr int C_W3 = 40;
constexpr int C_ERR = 60;
constexpr int C_X2 = 64;
constexpr int C_X1 = 128;
constexpr int C_O = 256;
constexpr int C_H = 512;
constexpr int C_W1 = 1536;
constexpr int NZ = 25;
constexpr int EPOCH_OFF = NZ * 64 * 128;
constexpr int INIT_OFF = EPOCH_OFF + 256;
constexpr int CTL_BYTES = EPOCH_OFF + 4096;
constexpr int W2B = 65536;
constexpr int OFF_W1 = CTL_BYTES;
constexpr int OFF_W2 = OFF_W1 + 16 * 3 * UNIT;
constexpr int OFF_W3 = OFF_W2 + 8 * W2B;
constexpr int OFF_W4 = OFF_W3 + 16 * 7 * UNIT;
constexpr int OFF_CS3 = OFF_W4 + 8 * 8 * UNIT;
constexpr int XB = S * D * 2;
constexpr int OB = S * 512 * 2;
constexpr int OFF_X2 = OFF_CS3 + 16 * 28 * 896;
constexpr int OFF_X1 = OFF_X2 + XB;
constexpr int OFF_O = OFF_X1 + 2 * XB;
constexpr int STB = S * 16 * 8;
constexpr int OFF_ST2 = OFF_O + 2 * OB;
constexpr int OFF_ST1 = OFF_ST2 + STB;
constexpr int KVB = 16 * 16 * 4096;
constexpr int OFF_KVP = OFF_ST1 + 2 * STB;
constexpr int HB = S * FF * 2;
constexpr int OFF_H = OFF_KVP + 2 * KVB;
constexpr int WS_USED = OFF_H + 2 * HB;

constexpr int L_SCR = 4 * UNIT;
constexpr int PHK_STRIDE = 136, V_STRIDE = 72;
constexpr int L_PHK = L_SCR;
constexpr int L_V = L_SCR + 128 * PHK_STRIDE;
constexpr int L_KVF = L_SCR + 28672;
constexpr int L_W2X = L_KVF + 4096;
constexpr int L_CONST = 155648;
constexpr int L_CS1 = L_CONST, L_CS3 = L_CONST + 384, L_WPHI = L_CONST + 1280, L_ARR = L_CONST + 3328;
constexpr int L_STAGE = 2 * UNIT;
constexpr int STG = 8704;
constexpr int LDS_BYTES = 163840;

constexpr float LOG2E = 1.4426950408889634f;
#define PKV 12
constexpr int PK = PKV, MK = 28 - PK;
constexpr int LAG = 4;

DI u32 pk2(float a, float b) { f32x2 v = {a, b}; return __builtin_bit_cast(u32, __builtin_convertvector(v, bf16x2)); }
DI float bflo(u32 v) { return __uint_as_float(v << 16); }
DI float bfhi(u32 v) { return __uint_as_float(v & 0xffff0000u); }
DI f32x16 mfma(v4u a, v4u b, f32x16 c) {
  f32x16 d = __builtin_amdgcn_mfma_f32_32x32x16_bf16(__builtin_bit_cast(bf16x8, a), __builtin_bit_cast(bf16x8, b), c, 0, 0, 0);
  asm volatile("" : "+v"(d));
  __builtin_amdgcn_sched_barrier(0);
  return d;
}
DI float xsum(float v) {
  auto r = __builtin_amdgcn_permlane32_swap(__float_as_uint(v), __float_as_uint(v), false, false);
  return __uint_as_float(r[0]) + __uint_as_float(r[1]);
}
DI float xmax(float v) {
  auto r = __builtin_amdgcn_permlane32_swap(__float_as_uint(v), __float_as_uint(v), false, false);
  return fmaxf(__uint_as_float(r[0]), __uint_as_float(r[1]));
}
DI float xmin(float v) {
  auto r = __builtin_amdgcn_permlane32_swap(__float_as_uint(v), __float_as_uint(v), false, false);
  return fminf(__uint_as_float(r[0]), __uint_as_float(r[1]));
}
#define BAR() asm volatile("s_waitcnt lgkmcnt(0)\n\ts_barrier" ::: "memory")
#define RAWBAR() asm volatile("s_barrier" ::: "memory")
#define VMCNT(n) asm volatile("s_waitcnt vmcnt(" #n ")" ::: "memory")
#define SB() __builtin_amdgcn_sched_barrier(0)
#define PIN(x) asm volatile("" : "+v"(x))

typedef __amdgpu_buffer_rsrc_t rsrc_t;
DI v4u ld16(rsrc_t rs, int voff, int soff) { return __builtin_amdgcn_raw_buffer_load_b128(rs, voff, soff, 16); }
DI v2u ld8(rsrc_t rs, int voff, int soff) { return __builtin_amdgcn_raw_buffer_load_b64(rs, voff, soff, 16); }
DI u32 ld4(rsrc_t rs, int voff, int soff) { return __builtin_amdgcn_raw_buffer_load_b32(rs, voff, soff, 16); }
DI void st16(rsrc_t rs, int voff, int soff, v4u v) {
  __builtin_amdgcn_raw_buffer_store_b128(v, rs, voff, soff, 16);
  asm volatile("s_nop 1" ::"v"(v) : "memory");
}
DI void st8(rsrc_t rs, int voff, int soff, v2u v) { __builtin_amdgcn_raw_buffer_store_b64(v, rs, voff, soff, 16); }
DI void st4(rsrc_t rs, int voff, int soff, u32 v) { __builtin_amdgcn_raw_buffer_store_b32(v, rs, voff, soff, 16); }
DI int opq(int v) { asm volatile("" : "+s"(v)); return v; }
DI int opqv(int v) { asm volatile("" : "+v"(v)); return v; }
DI v4u lds16(LAS char* l, int off) { return *(LAS v4u*)(l + off); }
DI float dot2(u32 a, u32 b, float c) {
  return __builtin_amdgcn_fdot2_f32_bf16(__builtin_bit_cast(bf16x2, a), __builtin_bit_cast(bf16x2, b), c, false);
}
DI v4u tr_frag(LAS char* L, int base, int step) {
  v2u lo = __builtin_bit_cast(v2u, __builtin_amdgcn_ds_read_tr16_b64_v4i16((LAS s16x4*)(L + base)));
  v2u hi = __builtin_bit_cast(v2u, __builtin_amdgcn_ds_read_tr16_b64_v4i16((LAS s16x4*)(L + base + step)));
  return v4u{lo.x, lo.y, hi.x, hi.y};
}
DI float sum8(v4u o) {
  const u32 one = 0x3f803f80u;
  return dot2(o.x, one, dot2(o.y, one, 0.f)) + dot2(o.z, one, dot2(o.w, one, 0.f));
}

DI u32* cnt_ptr(char* ws, int idx) { return (u32*)(ws + idx * 128); }
#define POLL_D0 16
#define LATE_ARR
#define WOV 0
#define WOS 20
#define H2 6
#define H3 12
#define CP1 12
#define CP5 0
#define CT5 11
#define PT5 8
#define POLL_IV 1
DI bool spin_more(char* ws, u32& spins) {
  u32* e = cnt_ptr(ws, C_ERR);
  __builtin_amdgcn_s_sleep(POLL_IV);
  ++spins;
  if ((spins & 255u) == 0u) {
    if (__builtin_amdgcn_readfirstlane(__hip_atomic_load(e, __ATOMIC_RELAXED, __HIP_MEMORY_SCOPE_AGENT)) != 0) return false;
    if (spins > 400000u) { __hip_atomic_store(e, 1u, __ATOMIC_RELAXED, __HIP_MEMORY_SCOPE_AGENT); return false; }
  }
  return true;
}
DI void wait_cnt(char* ws, int idx, u32 target, bool delay = false) {
  u32* c = cnt_ptr(ws, idx);
  u32 spins = 0;
  if (delay && POLL_D0) __builtin_amdgcn_s_sleep(POLL_D0);
  while ((u32)__builtin_amdgcn_readfirstlane(__hip_atomic_load(c, __ATOMIC_RELAXED, __HIP_MEMORY_SCOPE_AGENT)) < target)
    if (!spin_more(ws, spins)) break;
  __builtin_amdgcn_fence(__ATOMIC_ACQUIRE, "wavefront");
  asm volatile("" ::: "memory");
}
DI void wait_flags(char* ws, u32* fp, u32 f, u32 target = 16u) {
  if (__builtin_expect(!__all(f >= target), 0)) {
    u32 spins = 0;
    while (spin_more(ws, spins))
      if (__all(__hip_atomic_load(fp, __ATOMIC_RELAXED, __HIP_MEMORY_SCOPE_AGENT) >= target)) break;
  }
  __builtin_amdgcn_fence(__ATOMIC_ACQUIRE, "wavefront");
  asm volatile("" ::: "memory");
}
DI u32 poll_issue(const u32* p) { u32 f; asm volatile("global_load_dword %0, %1, off sc1" : "=v"(f) : "v"(p) : "memory"); return f; }
DI void signal_cnt(char* ws, int idx) {
  unsigned long long sv;
  asm volatile("s_mov_b64 %0, exec\n\ts_mov_b64 exec, 1\n\tglobal_atomic_add %1, %2, %3\n\ts_mov_b64 exec, %0"
               : "=&s"(sv) : "v"(idx * 128), "v"(1), "s"(ws) : "memory");
}
DI void wave_signal(char* ws, int idx, int lane) { VMCNT(0); signal_cnt(ws, idx); }
DI void arrive_lds(LAS char* LD, int slot, int lane) {
  if (lane == 0) __hip_atomic_fetch_add((LAS u32*)(LD + L_ARR + slot * 4), 1u, __ATOMIC_RELAXED, __HIP_MEMORY_SCOPE_WORKGROUP);
}
DI void wait_arr(LAS char* LD, int slot, u32 target) {
  while ((u32)__builtin_amdgcn_readfirstlane(__hip_atomic_load((LAS u32*)(LD + L_ARR + slot * 4), __ATOMIC_RELAXED, __HIP_MEMORY_SCOPE_WORKGROUP)) < target)
    __builtin_amdgcn_s_sleep(1);
}

#define STAMP(i)
#define HSTAMP(i)
#define VSTAMP(i)
#define WSTAMP(i)
#define USTAMP(i)

#define DMA_STEP(GSTEP) \
      "s_add_u32 m0, m0, 0x1000\n\t" \
      "v_add_u32 %1, " GSTEP ", %1\n\t" \
      "global_load_lds_dwordx4 %1, %2 sc1\n\t"
#define DMA_FN(NAME, BODY) \
DI void NAME(const char* gsrc, u32 voff, u32 lds_dst) { \
  u32 keep; \
  asm volatile("s_mov_b32 %0, m0\n\ts_mov_b32 m0, %3\n\ts_nop 0\n\tglobal_load_lds_dwordx4 %1, %2 sc1\n\t" BODY "s_mov_b32 m0, %0" \
               : "=&s"(keep), "+v"(voff) : "s"(gsrc), "s"(lds_dst) : "memory", "scc"); }
DMA_FN(dma7, DMA_STEP("0x1000") DMA_STEP("0x1000") DMA_STEP("0x1000") DMA_STEP("0x1000") DMA_STEP("0x1000") DMA_STEP("0x1000"))
DMA_FN(dma2, DMA_STEP("0x1000"))
#define PACE 1
#define STR2(x) #x
#define STR(x) STR2(x)
#define PSTEP "s_sleep " STR(PACE) "\n\t" DMA_STEP("0x1000")
DMA_FN(dma7p, PSTEP PSTEP PSTEP PSTEP PSTEP PSTEP)
DMA_FN(dma7n, DMA_STEP("0x2a000") DMA_STEP("0x2a000") DMA_STEP("0x2a000") DMA_STEP("0x2a000") DMA_STEP("0x2a000") DMA_STEP("0x2a000"))

template <int LO, int GO>
DI void dma1(const char* gsrc, u32 voff, u32 lb) {
  u32 keep, tmp;
  asm volatile("s_mov_b32 %0, m0\n\ts_add_u32 m0, %4, %5\n\tv_add_u32 %1, %6, %2\n\tglobal_load_lds_dwordx4 %1, %3 sc1\n\ts_mov_b32 m0, %0"
               : "=&s"(keep), "=&v"(tmp) : "v"(voff), "s"(gsrc), "s"(lb), "i"(LO), "i"(GO) : "memory", "scc");
}

template <typename F, int... I>
DI void static_for_impl(F&& f, std::integer_sequence<int, I...>) { (f(IC<I>{}), ...); }
template <int N, typename F>
DI void static_for(F&& f) { static_for_impl(f, std::make_integer_sequence<int, N>{}); }

constexpr int PF = 6;
#define WEV 2
template <int N, int RPS = 1, typename RD, typename PRE, typename ST>
DI void pipe(RD rd, PRE pre, ST step) {
  v4u buf[PF];
  static_for<PF>([&](auto I) AI { buf[I.value] = rd(I); });
  SB();
  static_for<N>([&](auto I) AI {
    constexpr int i = I.value;
    pre(I);
    if constexpr (WEV > 1 && i % WEV == 0 && i + PF <= N) __builtin_amdgcn_s_waitcnt(0xC07F | (((PF - WEV) * RPS) << 8));
    const v4u a = buf[i % PF];
    step(I, a, [&]() AI { if constexpr (i + PF < N) buf[i % PF] = rd(IC<i + PF>{}); SB(); });
    SB();
  });
}

DI void ln_finish(float s, float q, float& mean, float& rstd) {
  s = xsum(s); q = xsum(q);
  mean = s * (1.f / 448.f);
  const float var = fmaxf(q * (1.f / 448.f) - mean * mean, 0.f);
  rstd = __builtin_amdgcn_rsqf(var + 1e-5f);
}
DI void ln_stats(const v2u (&st)[8], float& mean, float& rstd) {
  f32x2 a = __builtin_bit_cast(f32x2, st[0]), b = __builtin_bit_cast(f32x2, st[1]);
#pragma unroll
  for (int i = 2; i < 8; i += 2) { a += __builtin_bit_cast(f32x2, st[i]); b += __builtin_bit_cast(f32x2, st[i + 1]); }
  a += b;
  ln_finish(a[0], a[1], mean, rstd);
}
DI v4u pack8(const f32x16& a, int s) {
  v4u r = {pk2(a[8 * s], a[8 * s + 1]), pk2(a[8 * s + 2], a[8 * s + 3]), pk2(a[8 * s + 4], a[8 * s + 5]), pk2(a[8 * s + 6], a[8 * s + 7])};
  return r;
}
template <int SG>
DI void half_softmax(const f32x16& p, int h, float rstd, v4u& o0, v4u& o1) {
  auto mm = [](float a, float b) AI { return SG ? fminf(a, b) : fmaxf(a, b); };
  float m0 = mm(mm(p[0], p[1]), p[2]), m1 = mm(mm(p[3], p[4]), p[5]), m2 = mm(mm(p[6], p[7]), p[8]), m3 = mm(mm(p[9], p[10]), p[11]);
  float m4 = mm(mm(mm(p[12], p[13]), p[14]), p[15]);
  m4 = h ? p[0] : m4;
  float mx = mm(mm(mm(m0, m1), m2), mm(m3, m4));
  mx = SG ? xmin(mx) : xmax(mx);
  const float c = rstd * LOG2E;
  const float cc = SG ? -c : c, off = -mx * cc;
  f32x16 e;
  float s0 = 0.f, s1 = 0.f;
#pragma unroll
  for (int r = 0; r < 16; ++r) {
    float v = __builtin_amdgcn_exp2f(fmaf(p[r], cc, off));
    if (r >= 12) v = h ? 0.f : v;
    e[r] = v;
    if (r & 1) s1 += v; else s0 += v;
  }
  const float rs_ = __builtin_amdgcn_rcpf(xsum(s0 + s1));
#pragma unroll
  for (int r = 0; r < 16; ++r) e[r] *= rs_;
  o0 = pack8(e, 0); o1 = pack8(e, 1);
  PIN(o0); PIN(o1);
  SB();
}

DI void finish_tile(const f32x16& acc, v4u (&res)[2], rsrc_t rs, int xoff, int stoff, int tt, int cb, int nt, int lane, bool last, u16* out) {
  lane = opqv(lane);
  const int h = lane >> 5, tok = lane & 31;
#pragma unroll
  for (int ro = 0; ro < 2; ++ro) {
    v4u r = res[ro];
    v4u o;
    o.x = pk2(bflo(r.x) + acc[8 * ro + 0], bfhi(r.x) + acc[8 * ro + 1]);
    o.y = pk2(bflo(r.y) + acc[8 * ro + 2], bfhi(r.y) + acc[8 * ro + 3]);
    o.z = pk2(bflo(r.z) + acc[8 * ro + 4], bfhi(r.z) + acc[8 * ro + 5]);
    o.w = pk2(bflo(r.w) + acc[8 * ro + 6], bfhi(r.w) + acc[8 * ro + 7]);
    res[ro] = o;
    const int oi = nt * 4 + ro * 2 + h;
    if (oi < 7) {
      const int go = 7 * cb + oi;
      if (last) *(v4u*)(out + (size_t)(tt * 32 + tok) * D + go * 8) = o;
      else st16(rs, ((go >> 1) * 64 + tok + 32 * (go & 1)) * 16, xoff + tt * 28 * 1024, o);
    }
  }
  if (!last) {
    const bool v1 = !(nt == 1 && h == 1);
    const u32 one = 0x3f803f80u;
    float s0 = dot2(res[0].x, one, 0.f), s1 = dot2(res[1].x, one, 0.f), q0 = dot2(res[0].x, res[0].x, 0.f), q1 = dot2(res[1].x, res[1].x, 0.f);
    s0 = dot2(res[0].y, one, s0); s1 = dot2(res[1].y, one, s1); q0 = dot2(res[0].y, res[0].y, q0); q1 = dot2(res[1].y, res[1].y, q1);
    s0 = dot2(res[0].z, one, s0); s1 = dot2(res[1].z, one, s1); q0 = dot2(res[0].z, res[0].z, q0); q1 = dot2(res[1].z, res[1].z, q1);
    s0 = dot2(res[0].w, one, s0); s1 = dot2(res[1].w, one, s1); q0 = dot2(res[0].w, res[0].w, q0); q1 = dot2(res[1].w, res[1].w, q1);
    const float sum = xsum(s0 + (v1 ? s1 : 0.f));
    const float sq = xsum(q0 + (v1 ? q1 : 0.f));
    if (h == 0) st8(rs, tok * 8, stoff + ((tt * 8 + cb) * 2 + nt) * 256, v2u{__float_as_uint(sum), __float_as_uint(sq)});
  }
}

DI void kv_reduce(rsrc_t rs, LAS char* LD, int L, int j, int w, int lane) {
  const int ko = opq(OFF_KVP + L * KVB + j * 16 * 4096 + (w >> 1) * 1024 + (w & 1) * 512);
  const int lz = opqv(lane);
  const int vo = (lz >> 5) * 4096 + (lz & 31) * 16;
  v4u t[8];
#pragma unroll
  for (int i = 0; i < 8; ++i) t[i] = ld16(rs, vo, ko + i * 8192);
  float s[8] = {};
  const u32 lo1 = (u32)opq(0x00003f80), hi1 = (u32)opq(0x3f800000);
#pragma unroll
  for (int i = 0; i < 8; ++i) {
#pragma unroll
    for (int c = 0; c < 4; ++c) { s[2 * c] = dot2(t[i][c], lo1, s[2 * c]); s[2 * c + 1] = dot2(t[i][c], hi1, s[2 * c + 1]); }
  }
#pragma unroll
  for (int c = 0; c < 8; ++c) s[c] = xsum(s[c]);
  if (lz < 32) *(LAS v4u*)(LD + L_KVF + (w >> 1) * 1024 + (w & 1) * 512 + lz * 16) = v4u{pk2(s[0], s[1]), pk2(s[2], s[3]), pk2(s[4], s[5]), pk2(s[6], s[7])};
}

constexpr int p4_oh(int t) { return 4 * t; }
constexpr int p4_oc(int t) { return 56 + 2 * t; }
constexpr int p4_koff(int o) { return (7 * (o & 15) + (o >> 4)) * 1024; }
constexpr int p4_wbase(int t) { return (t < 7 ? 0 : t < 14 ? L_SCR : t < 21 ? 3 * UNIT : UNIT) + (t % 7) * 4096; }
DI int p4_part(int a, int hw) {
  const int i = ((a >> 1) | ((a & 1) << 1)) * 4 + hw;
  return i < 7 ? 2 * UNIT + i * 4096 : i < 10 ? L_KVF + (i - 7) * 4096 : (i - 10) * 4096;
}
DI void put16(const f32x16& acc, LAS char* LD, int off) {
#pragma unroll
  for (int qd = 0; qd < 4; ++qd) *(LAS f32x4*)(LD + off + qd * 1024) = f32x4{acc[4 * qd], acc[4 * qd + 1], acc[4 * qd + 2], acc[4 * qd + 3]};
}
DI void add16(f32x16& acc, LAS char* LD, int off) {
#pragma unroll
  for (int qd = 0; qd < 4; ++qd) {
    const f32x4 v = *(LAS f32x4*)(LD + off + qd * 1024);
#pragma unroll
    for (int i = 0; i < 4; ++i) acc[4 * qd + i] += v[i];
  }
}

struct Args {
  const u16* x0; const u16* wqkv; const u16* wphi; const u16* wo; const u16* wg; const u16* wu; const u16* wd;
  char* ws; u16* out;
};

struct Retile { const v4u* pa[7]; int rd0, rd1, step, o0, o1, ocb, sig; };
DI void retile_prep(const Args& A, int job, int lane, int stage, Retile& R) {
  const int h = lane >> 5, q = (lane & 15) >> 2, c8 = ((lane >> 4) & 1) * 4 + (lane & 3);
  const int hh = c8 & 1, quad = c8 >> 1;
  if (job < 64 || job >= 512) {
    const bool iswo = job < 64;
    const int a0 = iswo ? job >> 2 : (job - 512) >> 2, cg = job & 3;
    const u16* src = (iswo ? A.wo + (size_t)a0 * 28 * D : A.wd + (size_t)a0 * 32 * D) + 112 * cg;
    const int nrow = iswo ? 28 : 32;
#pragma unroll
    for (int i = 0; i < 7; ++i) {
      const int p = lane + 64 * i, r = p / 14, c = p % 14;
      R.pa[i] = (const v4u*)(src + (size_t)(r < nrow ? r : 0) * D + c * 8);
    }
    const int rb = stage + ((iswo ? 4 : 8) * h + q) * 224 + 8 * (quad & 1);
    const int oi = (quad >> 1) * 2 + hh;
    R.rd0 = rb + 16 * oi; R.rd1 = rb + 16 * min(4 + oi, 6); R.step = (iswo ? 8 : 4) * 224;
    int ob[2];
#pragma unroll
    for (int k2 = 0; k2 < 2; ++k2) {
      const int ks = a0 * 2 + k2, o_ = 16 * (ks % 7) + ks / 7;
      int fr;
      if (o_ < 56) fr = 112 + ((o_ & 3) * 14 + (o_ >> 2)) * 2;
      else fr = (o_ - 56) * 2;
      ob[k2] = iswo ? OFF_W2 + cg * 2 * W2B + ((ks & 15) * 2 + (ks >> 4)) * 2048 : OFF_W4 + cg * 16 * UNIT + fr * 1024;
    }
    R.o0 = opq(ob[0]); R.o1 = opq(ob[1]); R.ocb = opq(iswo ? W2B : 8 * UNIT); R.sig = opq((iswo ? C_W2 : C_W4) + cg);
  } else {
    const int a = job - 64, j = a / 28, ks = a % 28;
#pragma unroll
    for (int i = 0; i < 7; ++i) {
      const int p = lane + 64 * i, r = p / 28, c = p % 28;
      R.pa[i] = (const v4u*)((c < 14 ? A.wg : A.wu) + (size_t)(ks * 16 + r) * FF + 112 * j + (c < 14 ? c : c - 14) * 8);
    }
    R.rd0 = stage + (8 * h + q) * 448 + (quad & 1) * 224 + (8 * hh + 4 * (quad >> 1)) * 2; R.rd1 = 0; R.step = 0;
    R.o0 = opq(OFF_W3 + j * 7 * UNIT + (ks < PK ? ks * 7 : 6 * PK + ks) * 1024); R.o1 = opq(ks < PK ? 1024 : MK * 1024);
    R.ocb = opq(OFF_CS3 + (j * 28 + ks) * 896); R.sig = opq(C_W3 + j);
  }
#pragma unroll
  for (int i = 0; i < 7; ++i) PIN(R.pa[i]);
  PIN(R.rd0); PIN(R.rd1);
}
DI void retile_load(const Retile& R, u32 lds_dst) {
#pragma unroll
  for (int i = 0; i < 7; ++i) {
    u32 keep;
    asm volatile("s_mov_b32 %0, m0\n\ts_add_u32 m0, %2, %3\n\ts_nop 0\n\tglobal_load_lds_dwordx4 %1, off\n\ts_mov_b32 m0, %0"
                 : "=&s"(keep) : "v"(R.pa[i]), "s"(lds_dst), "i"(i * 1024) : "memory", "scc");
  }
}
DI void retile_store(int job, const Retile& R, rsrc_t rs, LAS char* LD, int lane) {
  const int h = lane >> 5;
  if (job < 64 || job >= 512) {
    const bool pad = (lane & 0x14) == 0x14;
    const bool kpad = job < 64 && h;
    v4u o[8];
#pragma unroll
    for (int i = 0; i < 8; ++i) {
      const int ad = ((i & 2) ? R.rd1 : R.rd0) + (i >> 2) * 112 + (i & 1) * 16 * 224;
      const v2u lo = __builtin_bit_cast(v2u, __builtin_amdgcn_ds_read_tr16_b64_v4i16((LAS s16x4*)(LD + ad)));
      const v2u hi = __builtin_bit_cast(v2u, __builtin_amdgcn_ds_read_tr16_b64_v4i16((LAS s16x4*)(LD + ad + R.step)));
      o[i] = v4u{lo.x, lo.y, hi.x, hi.y};
    }
#pragma unroll
    for (int i = 0; i < 8; ++i) {
      if ((i & 2) && pad) o[i] = v4u{0u, 0u, 0u, 0u};
      if ((i & 1) && kpad) { o[i].z = 0u; o[i].w = 0u; }
      st16(rs, lane * 16, ((i & 1) ? R.o1 : R.o0) + (i >> 2) * R.ocb + ((i >> 1) & 1) * 1024, o[i]);
    }
  } else {
    v4u o[7];
#pragma unroll
    for (int n = 0; n < 7; ++n) o[n] = tr_frag(LD, R.rd0 + n * 32, 4 * 448);
    float cs[7];
#pragma unroll
    for (int n = 0; n < 7; ++n) {
      st16(rs, lane * 16, R.o0 + n * R.o1, o[n]);
      cs[n] = xsum(sum8(o[n]));
    }
#pragma unroll
    for (int n2 = 0; n2 < 4; ++n2) {
      const float v = h ? cs[n2 < 3 ? 2 * n2 + 1 : 6] : cs[2 * n2];
      if (n2 < 3 || !h) st4(rs, lane * 4, R.ocb + n2 * 256, __float_as_uint(v));
    }
  }
}

__global__ __launch_bounds__(512, 2) void k_main(const Args A) {
  extern __shared__ __attribute__((aligned(16))) char smem[];
  LAS char* LD = (LAS char*)smem;
  char* ws = A.ws;
  const int b = blockIdx.x, xcd = b & 7, bi = b >> 3;
  const int g = (xcd >> 2) * 8 + (bi >> 2), j = (xcd & 3) * 4 + (bi & 3);
  const int rb = 2 * g + (j >> 3), cb = j & 7;
  const int lane = threadIdx.x & 63, w = __builtin_amdgcn_readfirstlane(threadIdx.x >> 6);
  const int h = lane >> 5, tok = lane & 31;
  const rsrc_t rs = __builtin_amdgcn_make_buffer_rsrc((void*)ws, 0, WS_USED, 0x00020000);
  const u32 lbase = (u32)(size_t)smem;
  const int l16 = lane * 16;
  const int tq_ = (lane & 15) >> 2, tc8 = ((lane >> 4) & 1) * 4 + (lane & 3);
  const int w1lane = (8 * h + tq_) * 64 + min(tc8 + (j & 1), 7) * 8;

  const int hw = w - 4;
  const u32 voffn = (hw * 16 + (lane >> 2)) * (3 * D * 2) + (lane & 3) * 16;
  const char* w1 = (const char*)A.wqkv + 56 * j - (j & 1) * 8;
  unsigned long long ep;
  const u32 lb = lbase + hw * 1024;
#define KP(p) dma1<4 * UNIT + (p) * 4096, 896 + (p) * 0x2a000>(w1, voffn, lb)
#define VP(p) dma1<(p) * 4096, 1792 + (p) * 0x2a000>(w1, voffn, lb)
#define QP(p) dma1<UNIT + (p) * 4096, (p) * 0x2a000>(w1, voffn, lb)
  if (opq(w) >= 4) {
    HSTAMP(0);
    if (hw == 1 && b < NZ) {
      asm volatile("global_load_dwordx2 %0, %1, %2 sc1" : "=v"(ep) : "v"(EPOCH_OFF), "s"(ws) : "memory");
      st4(rs, lane * 128, b * 8192, 0u);
    }
    if (hw == 2) {
      dma1<L_KVF, 0>((const char*)A.wphi + j * 1568, l16, lbase);
      dma1<L_KVF + 1024, 0>((const char*)A.wphi + j * 1568, min(1024 + l16, 1552), lbase);
    }
    KP(0); KP(1); VP(0); KP(2); VP(1); KP(3); VP(2);
    if (hw == 3) {
      unsigned long long pcv;
      asm volatile("s_getpc_b64 %0" : "=s"(pcv));
      const char* pc = (const char*)(pcv & ~255ull);
      dma1<L_W2X, 0>(pc, bi * 1024 + l16, lbase);
      dma1<L_W2X + 1024, 32768>(pc, bi * 1024 + l16, lbase);
    }
    HSTAMP(1);
  }

  if (w >= 4) {
    const u32 voff = hw * 1024 + lane * 16;
    const char* w1f = ws + OFF_W1 + j * 3 * UNIT;
    const char* w2 = ws + OFF_W2 + cb * W2B;
    const char* w3 = ws + OFF_W3 + j * 7 * UNIT;
    const char* w4 = ws + OFF_W4 + cb * 8 * UNIT;
#define DMA(src, slot) dma7((src), voff, lbase + (slot) * UNIT + hw * 1024)
#define DMAP(src, slot) dma7p((src), voff, lbase + (slot) * UNIT + hw * 1024)
#define DMAN(which, slot) dma7n(w1 + (which) * 896, voffn, lbase + (slot) * UNIT + hw * 1024)
    v4u wr[28];
    unsigned long long tag = 0;
    auto hlayer = [&](auto LC) AI {
      constexpr int L = LC.value;
      Retile R;
      const int job = hw >= 2 ? b * 2 + hw - 2 : (hw == 0 && b < 224 ? 512 + b : -1);
      float c0 = 0.f, c1 = 0.f, c2 = 0.f, c3 = 0.f;
      auto colpart = [&](int slot, auto K0, auto K1) AI {
        constexpr int k0 = K0.value, n = K1.value - K0.value;
        const int ib = opqv(slot * UNIT + opqv(w1lane));
        v4u fr[n];
#pragma unroll
        for (int ks = 0; ks < n; ++ks) fr[ks] = tr_frag(LD, ib + (k0 + ks) * 1024, 256);
        SB();
        const u32 one = 0x3f803f80u;
#pragma unroll
        for (int ks = 0; ks < n; ++ks) { c0 = dot2(fr[ks].x, one, c0); c1 = dot2(fr[ks].y, one, c1); c2 = dot2(fr[ks].z, one, c2); c3 = dot2(fr[ks].w, one, c3); }
        SB();
      };
      auto colfin = [&](int which) AI {
        const float cs = xsum((c0 + c1) + (c2 + c3));
        if (lane < 32) *(LAS float*)(LD + L_CS1 + which * 128 + lane * 4) = cs;
      };
      v4u csa[7];
      auto export_img = [&](int slot, int which) AI {
        const int ib = opqv(slot * UNIT + opqv(w1lane) + g * 1024);
        const int eo = opq(OFF_W1 + (j * 3 + which) * UNIT + g * 1024);
        st16(rs, l16, eo, tr_frag(LD, ib, 256));
        if (g < 12) st16(rs, l16, eo + 16384, tr_frag(LD, ib + 16384, 256));
      };
      if constexpr (L == 0) {
        if (hw == 1 && lane < 2) *(LAS v4u*)(LD + L_ARR + l16) = v4u{0u, 0u, 0u, 0u};
        HSTAMP(2);
#define VMW(n, n3) if (hw == 3) { VMCNT(n3); } else { VMCNT(n); }
        VMW(4, 6); BAR();
        HSTAMP(3);
        KP(4); VP(3);
        if (hw == 1 && b < NZ) {
          PIN(ep);
          tag = ep + 1ull;
          if (lane == 0) __hip_atomic_store((unsigned long long*)(ws + INIT_OFF + b * 128), tag, __ATOMIC_RELAXED, __HIP_MEMORY_SCOPE_AGENT);
        }
        if (hw == 2) {
          const int wb = opqv(L_KVF + (4 * h + tq_) * 56 + tc8 * 8);
          const v4u f0 = tr_frag(LD, wb, 8 * 56);
          v4u f1 = tr_frag(LD, wb + 16 * 56, 8 * 56);
          if (h) { f1.z = 0u; f1.w = 0u; }
          *(LAS v4u*)(LD + L_WPHI + l16) = f0;
          *(LAS v4u*)(LD + L_WPHI + 1024 + l16) = f1;
        }
        if (hw == 0) colpart(4, IC<0>{}, IC<8>{});
        VMW(4, 6); BAR();
        HSTAMP(4);
        KP(5); VP(4);
        if (job >= 0) retile_prep(A, job, lane, L_STAGE + hw * STG, R);
        if (hw == 1) colpart(0, IC<0>{}, IC<8>{});
        VMW(4, 6); BAR();
        HSTAMP(5);
        KP(6); VP(5); VP(6);
        if (hw == 0) colpart(4, IC<8>{}, IC<16>{});
        if (hw == 1) colpart(0, IC<8>{}, IC<12>{});
        VMCNT(5); BAR();
        HSTAMP(6);
        QP(0); QP(1); QP(2); QP(3);
        if (hw == 0) colpart(4, IC<16>{}, IC<20>{});
        if (hw == 1) colpart(0, IC<12>{}, IC<16>{});
        VMCNT(7); BAR();
        HSTAMP(7);
        QP(4); QP(5); QP(6);
        if (hw == 0) colpart(4, IC<20>{}, IC<24>{});
        if (hw == 1) colpart(0, IC<16>{}, IC<20>{});
        VMCNT(7); BAR();
        HSTAMP(8);
        if (hw == 0) { colpart(4, IC<24>{}, IC<28>{}); colfin(0); }
        if (hw == 1) { colpart(0, IC<20>{}, IC<28>{}); colfin(1); }
        if (hw == 2) export_img(4, 1);
        if (hw == 3) export_img(0, 2);
        HSTAMP(9);
        BAR();
        HSTAMP(10);
        if (job >= 0) retile_load(R, lbase + L_STAGE + hw * STG);
      } else {
        wait_arr(LD, 0, 4);
        DMA(w1f + 2 * UNIT, 0);
        VMCNT(0); BAR();
      }
      {
        const int wb = opqv(L_WPHI + opqv(l16));
        const v4u wf0 = lds16(LD, wb), wf1 = lds16(LD, wb + 1024);
        if (L == 0) HSTAMP(11);
        BAR();
        if (L == 0) HSTAMP(12);
        __builtin_amdgcn_s_setprio(1);
        const int lz = opqv(lane);
        const int xk = opqv(L_W2X + hw * 1024 + lz * 16);
        const v4u kf0 = lds16(LD, xk), kf1 = lds16(LD, xk + 4096);
        const float rstd = *(LAS float*)(LD + L_KVF + hw * 256 + lz * 4);
        f32x16 p = {};
        p = mfma(wf0, kf0, p);
        p = mfma(wf1, kf1, p);
        v4u o0, o1;
        half_softmax<1>(p, h, rstd, o0, o1);
        const int pb = opqv(L_PHK + (hw * 32 + (lz & 31)) * PHK_STRIDE + 8 * (lz >> 5) + 64);
        *(LAS v2u*)(LD + pb) = v2u{o0.x, o0.y}; *(LAS v2u*)(LD + pb + 16) = v2u{o0.z, o0.w};
        *(LAS v2u*)(LD + pb + 32) = v2u{o1.x, o1.y}; *(LAS v2u*)(LD + pb + 48) = v2u{o1.z, o1.w};
        __builtin_amdgcn_s_setprio(0);
      }
      if (L == 0) HSTAMP(13);
      if constexpr (L == 0) {
        if (job >= 0) { VMCNT(7); } else { VMCNT(0); }
      } else VMCNT(0);
      if (L == 0) HSTAMP(14);
      BAR();
      if (hw < 2) {
        __builtin_amdgcn_s_setprio(2);
        f32x16 kp = {};
        const int lz = opqv(lane);
        const int g16 = (lz >> 4) & 1, q = (lz & 15) >> 2, p = lz & 3, hz = lz >> 5;
        const int ab0 = opqv(L_PHK + (8 * hz + q) * PHK_STRIDE + (g16 * 16 + 4 * p) * 2 + hw * 64);
        const int bb0 = opqv(L_V + (8 * hz + q) * V_STRIDE + (g16 * 16 + 4 * p) * 2);
        v4u ap[8], bv[8];
#pragma unroll
        for (int s = 0; s < 8; ++s) { ap[s] = tr_frag(LD, ab0 + 16 * s * PHK_STRIDE, 4 * PHK_STRIDE); bv[s] = tr_frag(LD, bb0 + 16 * s * V_STRIDE, 4 * V_STRIDE); }
        SB();
#pragma unroll
        for (int s = 0; s < 8; ++s) kp = mfma(ap[s], bv[s], kp);
        const int ko = opq(OFF_KVP + L * KVB + (j * 16 + g) * 4096 + hw * 2048);
        st16(rs, l16, ko, pack8(kp, 0));
        st16(rs, l16, ko + 1024, pack8(kp, 1));
        wave_signal(ws, C_KV + L * 16 + j, lane);
        __builtin_amdgcn_s_setprio(0);
      }
      if constexpr (L == 0) {
        HSTAMP(15);
        if (hw == 3) {
          colpart(1, IC<0>{}, IC<28>{}); colfin(2);
          arrive_lds(LD, 1, lane);
        }
        if (hw == 1) { export_img(1, 0); wave_signal(ws, C_W1 + j, lane); }
        if (job >= 0) { VMCNT(0); retile_store(job, R, rs, LD, lane); }
        HSTAMP(16);
        if (job >= 0) wave_signal(ws, R.sig, lane);
        if (hw >= 2) signal_cnt(ws, C_W1 + j);
        HSTAMP(17);
        BAR();
        HSTAMP(18);
        u32* fw = cnt_ptr(ws, (lane & 3) == 0 ? C_W2 + (cb >> 1) : (lane & 3) == 1 ? C_W3 + j : C_W4 + (cb >> 1));
        kv_reduce(rs, LD, L, j, w, lane);
        HSTAMP(19);
        BAR();
        HSTAMP(20);
        wait_flags(ws, fw, __hip_atomic_load(fw, __ATOMIC_RELAXED, __HIP_MEMORY_SCOPE_AGENT), (lane & 3) == 0 ? 16u : (lane & 3) == 1 ? 28u : 56u);
        HSTAMP(21);
        DMA(w2, 2); DMA(w2 + UNIT, 3); dma2(w2 + 2 * UNIT, voff, lbase + L_W2X + hw * 1024);
#pragma unroll
        for (int i = 0; i < 7; ++i) csa[i] = ld16(rs, min(lane, 55) * 16, OFF_CS3 + (j * 28 + hw * 7 + i) * 896);
        DMA(w3, 0);
        HSTAMP(22);
        VMCNT(14); HSTAMP(23); BAR();
        HSTAMP(24);
        DMA(w3 + UNIT, 1);
      } else {
        DMA(w2, 2); DMA(w2 + UNIT, 3); dma2(w2 + 2 * UNIT, voff, lbase + L_W2X + hw * 1024);
        DMA(w3, 0);
        BAR();
        kv_reduce(rs, LD, L, j, w, lane);
        BAR();
        DMA(w3 + UNIT, 1);
      }
      BAR();
      if constexpr (L == 0) {
        f32x4 c4 = __builtin_bit_cast(f32x4, csa[0]);
#pragma unroll
        for (int i = 1; i < 7; ++i) c4 += __builtin_bit_cast(f32x4, csa[i]);
        if (lane < 56) *(LAS f32x4*)(LD + L_KVF + hw * 896 + l16) = c4;
      }
      DMA(w3 + 2 * UNIT, 2);
      VMCNT(7); BAR();
      const int wo4 = opq(OFF_W4 + cb * 8 * UNIT + 4 * UNIT + hw * UNIT);
#define WR(lo, hi) _Pragma("unroll") for (int i = lo; i < hi; ++i) wr[i] = ld16(rs, l16, wo4 + i * 1024)
      if constexpr (L == 0) {
        const int t4 = min(hw * 64 + lane, 223) * 4;
        *(LAS float*)(LD + L_CS3 + t4) = (*(LAS float*)(LD + L_KVF + t4) + *(LAS float*)(LD + L_KVF + 896 + t4)) +
                                         (*(LAS float*)(LD + L_KVF + 1792 + t4) + *(LAS float*)(LD + L_KVF + 2688 + t4));
        WR(0, 28);
      }
      DMA(w3 + 3 * UNIT, 3);
      VMCNT(7); BAR();
      DMAP(w3 + 4 * UNIT, 0);
      DMAP(w4 + UNIT, 4);
      VMCNT(14); BAR();
      DMAP(w3 + 5 * UNIT, 1);
      if (L == 0) wait_cnt(ws, C_W1 + j, 48);
      VMCNT(14); BAR();
      DMAP(w3 + 6 * UNIT, 2);
      VMCNT(7); BAR();
      DMAP(w4 + 2 * UNIT, 3);
      VMCNT(7); BAR();
      {
        u32* fp = cnt_ptr(ws, C_H + (L * 64 + 2 * rb + (lane & 1)) * 8);
        const int ho = opq(OFF_H + L * HB + 2 * rb * 112 * 1024 + hw * 7168);
        v4u hx[28];
        auto ldh = [&](auto I) AI { constexpr int i = I.value; hx[i] = ld16(rs, l16, ho + (i & 1) * 112 * 1024 + p4_koff(p4_oh(i >> 1))); };
        auto poll = [&](int lv) AI { return __hip_atomic_load(fp + 32 * lv, __ATOMIC_RELAXED, __HIP_MEMORY_SCOPE_AGENT); };
        DMAP(w4, 0);
        wait_flags(ws, fp, poll(0));
        static_for<8>([&](auto I) AI { ldh(I); });
        BAR();
        u32 f = poll_issue(fp + 32);
        DMA(w4 + 3 * UNIT, 1);
        VMCNT(7); PIN(f);
        wait_flags(ws, fp + 32, f);
        static_for<6>([&](auto I) AI { ldh(IC<8 + I.value>{}); });
        f32x16 acc[4] = {};
        auto ord = [](int i) constexpr { return i < 20 ? i : (i < 24 ? 20 + 2 * (i - 20) : 21 + 2 * (i - 24)); };
        const int pb0 = opqv(p4_part(0, hw) + opqv(l16)), pb2 = opqv(p4_part(2, hw) + opqv(l16));
        static_for<28>([&](auto I) AI {
          constexpr int i = I.value, k = ord(i), t = k >> 1, tq = k & 1;
          if constexpr (i == 0) f = poll(2);
          if constexpr (i == H2) {
            wait_flags(ws, fp + 64, f);
            VMCNT(2); arrive_lds(LD, 4, lane);
          }
          if constexpr (i == H3 - 3) f = poll(3);
          if constexpr (i == H3) wait_flags(ws, fp + 96, f);
          if constexpr (i == 24) wait_arr(LD, 2, 4 * L + 4);
          acc[tq] = mfma(wr[2 * t], hx[k], acc[tq]);
          acc[2 + tq] = mfma(wr[2 * t + 1], hx[k], acc[2 + tq]);
          if constexpr (i < 2) ldh(IC<14 + i>{});
          if constexpr (i >= H2 && i < H2 + 4) { ldh(IC<16 + 2 * (i - H2)>{}); ldh(IC<17 + 2 * (i - H2)>{}); }
          if constexpr (i >= H3 && i < H3 + 2) { ldh(IC<24 + 2 * (i - H3)>{}); ldh(IC<25 + 2 * (i - H3)>{}); }
          if constexpr (i >= 24) {
            constexpr int qd = i - 24;
            *(LAS f32x4*)(LD + pb0 + qd * 1024) = f32x4{acc[0][4 * qd], acc[0][4 * qd + 1], acc[0][4 * qd + 2], acc[0][4 * qd + 3]};
            *(LAS f32x4*)(LD + pb2 + qd * 1024) = f32x4{acc[2][4 * qd], acc[2][4 * qd + 1], acc[2][4 * qd + 2], acc[2][4 * qd + 3]};
          }
          SB();
        });
        wait_arr(LD, 3, 4 * L + 4);
        put16(acc[1], LD, opqv(p4_part(1, hw) + opqv(l16)));
        put16(acc[3], LD, opqv(p4_part(3, hw) + opqv(l16)));
        BAR();
      }
      if (L == 0) { DMA(w1f + UNIT, 3); DMA(w1f, 1); }
    };
    hlayer(IC<0>{});
    hlayer(IC<1>{});
    if (b == 0 && hw == 1 && lane == 0) __hip_atomic_store((unsigned long long*)(ws + EPOCH_OFF), tag, __ATOMIC_RELAXED, __HIP_MEMORY_SCOPE_AGENT);
    return;
  }

  const int tt = 4 * g + w;
  const int tt2 = 2 * rb + (w & 1);
  const int kh = w >> 1;
  const rsrc_t rx0 = __builtin_amdgcn_make_buffer_rsrc((void*)A.x0, 0, XB, 0x00020000);
  v4u res[2];
#define XWV 14
  constexpr int XW = XWV;

  auto clayer = [&](auto LC) AI {
    constexpr int L = LC.value;
    v4u x[28];
    v2u st[8];
    float mean, rstd;
    v4u pq[4];
    STAMP(0);
    auto phase1 = [&](auto FIRST) AI {
      constexpr bool first = FIRST.value;
      const int xo = opq(first ? tt * 32 * (D * 2) : OFF_X2 + tt * 28 * 1024);
      auto ldx = [&](int ks) AI { return ld16(rs, l16, xo + ks * 1024); };
      const int so_ = opq(OFF_ST2 + tt * 4096);
      const int sv_ = (opqv(lane) & 31) * 8 + h * 2048;
      v4u gx[28];
      const int stg = L_STAGE + w * STG;
      const int gv = (lane >> 4) * (D * 2) + (lane & 15) * 16, gv3 = (lane >> 3) * (D * 2) + (lane & 7) * 16;
      const int gl = stg + (lane >> 4) * 272 + (lane & 15) * 16, gl3 = stg + (lane >> 3) * 272 + (lane & 7) * 16;
      const int srd = opqv(stg + (opqv(lane) & 31) * 272 + h * 16);
      auto ldg = [&](int i) AI {
        return i < 24 ? __builtin_amdgcn_raw_buffer_load_b128(rx0, gv, xo + (i & 7) * 4 * (D * 2) + (i >> 3) * 256, 0)
                      : __builtin_amdgcn_raw_buffer_load_b128(rx0, gv3, xo + (i & 7) * 8 * (D * 2) + 768, 0);
      };
      auto stage = [&](int c) AI {
#pragma unroll
        for (int i = 0; i < (c < 3 ? 8 : 4); ++i) *(LAS v4u*)(LD + (c < 3 ? gl + i * 4 * 272 : gl3 + i * 8 * 272)) = gx[8 * c + i];
#pragma unroll
        for (int i = 0; i < (c < 3 ? 8 : 4); ++i) x[8 * c + i] = lds16(LD, srd + i * 32);
      };
      unsigned long long iv = 0;
      unsigned long long* initp = (unsigned long long*)(ws + (w == 3 && lane < NZ ? INIT_OFF + lane * 128 : EPOCH_OFF));
      auto init_ok = [&](unsigned long long v) AI {
        const unsigned long long e = ((unsigned long long)(u32)__builtin_amdgcn_readlane((int)(v >> 32), 63) << 32) | (u32)__builtin_amdgcn_readlane((int)v, 63);
        return __all(lane >= NZ || v == e + 1ull);
      };
      if constexpr (first) {
#pragma unroll
        for (int i = 0; i < 16; ++i) gx[i] = ldg(i);
        stage(0);
      } else {
#pragma unroll
        for (int ks = 0; ks < XW; ++ks) x[ks] = ldx(ks);
      }
      USTAMP(18);
      BAR();
      USTAMP(19);
      f32x16 ak = {}, av = {}, p = {};
      const int sbk = first ? opqv(opqv(w1lane) + 4 * UNIT) : opqv(opqv(l16) + 3 * UNIT), sbv = first ? opqv(w1lane) : opqv(l16);
      const int csb = opqv(L_CS1 + 16 * (opqv(lane) >> 5)), wb = opqv(L_WPHI + opqv(l16));
      const int xk = opqv(L_W2X + w * 1024 + opqv(l16));
      f32x4 cs[4]; v4u kf[2], wf[2];
      float nmean, s1 = 0.f, s2 = 0.f, q1 = 0.f, q2 = 0.f;
      auto isk = [](int i) constexpr { return i < LAG || (i < 56 - LAG && ((i - LAG) & 1) == 0); };
      auto kst = [](int i) constexpr { return i < LAG ? i : (i < 56 - LAG ? ((((i - LAG) & 1) == 0) ? LAG + (i - LAG) / 2 : (i - LAG) / 2) : i - 28); };
      auto rdw = [&](int base, int ks) AI { if constexpr (first) return tr_frag(LD, base + ks * 1024, 256); else return lds16(LD, base + ks * 1024); };
      pipe<56, first ? 2 : 1>([&](auto I) AI { return rdw(isk(I.value) ? sbk : sbv, kst(I.value)); },
               [&](auto I) AI {
                 constexpr int i = I.value;
                 if constexpr (first && (i == 6 || i == 14 || i == 22 || i == 30 || i == 38 || i == 46)) { USTAMP(40 + i / 8 * 2); RAWBAR(); USTAMP(41 + i / 8 * 2); }
                 if constexpr (i == 54) { USTAMP(20); BAR(); USTAMP(21); }
               },
               [&](auto I, v4u a, auto nx) AI {
                 constexpr int i = I.value, ks = kst(i);
                 if constexpr (i == 54) { p = mfma(wf[0], kf[0], p); p = mfma(wf[1], kf[1], p); }
                 if constexpr (isk(i)) ak = mfma(a, x[ks], ak); else av = mfma(a, x[ks], av);
                 nx();
                 if constexpr (!first && isk(i) && ks + XW < 28) x[ks + XW] = ldx(ks + XW);
                 if constexpr (first) {
                   if constexpr (i < 12) gx[16 + i] = ldg(16 + i);
                   if constexpr (isk(i) && ks % 8 == 6) stage(ks / 8 + 1);
                   if constexpr (isk(i)) {
                     const u32 one = 0x3f803f80u;
                     s1 = dot2(x[ks].x, one, s1); q1 = dot2(x[ks].x, x[ks].x, q1); s2 = dot2(x[ks].y, one, s2); q2 = dot2(x[ks].y, x[ks].y, q2);
                     s1 = dot2(x[ks].z, one, s1); q1 = dot2(x[ks].z, x[ks].z, q1); s2 = dot2(x[ks].w, one, s2); q2 = dot2(x[ks].w, x[ks].w, q2);
                   }
                   if constexpr (i == 45) iv = __hip_atomic_load(initp, __ATOMIC_RELAXED, __HIP_MEMORY_SCOPE_AGENT);
                   if constexpr (i == 51) { ln_finish(s1 + s2, q1 + q2, mean, rstd); nmean = -mean; }
                 } else {
                   if constexpr (!isk(i) && ks >= 4 && ks < 12) st[ks - 4] = ld8(rs, sv_ + (ks - 4) * 256, so_);
                   if constexpr (i == 45) { ln_stats(st, mean, rstd); nmean = -mean; }
                 }
                 if constexpr (i >= 46 && i < 50) cs[i - 46] = *(LAS f32x4*)(LD + csb + 32 * (i - 46));
                 if constexpr (i == 50) wf[0] = lds16(LD, wb);
                 if constexpr (i == 51) wf[1] = lds16(LD, wb + 1024);
                 if constexpr (i == 52 || i == 53) {
#pragma unroll
                   for (int qd = 2 * (i - 52); qd < 2 * (i - 52) + 2; ++qd) {
                     const float y0 = fmaf(nmean, cs[qd][0], ak[4 * qd]), y1 = fmaf(nmean, cs[qd][1], ak[4 * qd + 1]);
                     const float y2 = fmaf(nmean, cs[qd][2], ak[4 * qd + 2]), y3 = fmaf(nmean, cs[qd][3], ak[4 * qd + 3]);
                     kf[qd >> 1][2 * (qd & 1)] = pk2(y0, y1);
                     kf[qd >> 1][2 * (qd & 1) + 1] = pk2(y2, y3);
                   }
                   *(LAS v4u*)(LD + xk + (i - 52) * 4096) = kf[i - 52];
                   if constexpr (i == 52) *(LAS float*)(LD + L_KVF + w * 256 + opqv(lane) * 4) = rstd;
                 }
                 if constexpr (i == 55) {
#pragma unroll
                   for (int qd = 0; qd < 4; ++qd) cs[qd] = *(LAS f32x4*)(LD + csb + 128 + 32 * qd);
                 }
               });
      STAMP(1);
      const int lz = opqv(lane);
      const int trow = w * 32 + (lz & 31);
      const int pb = opqv(L_PHK + trow * PHK_STRIDE + 8 * (lz >> 5)), vb = opqv(L_V + trow * V_STRIDE + 8 * (lz >> 5));
      half_softmax<0>(p, h, rstd, pq[0], pq[1]);
#pragma unroll
      for (int s = 0; s < 2; ++s) {
        *(LAS v2u*)(LD + pb + 32 * s) = v2u{pq[s].x, pq[s].y};
        *(LAS v2u*)(LD + pb + 32 * s + 16) = v2u{pq[s].z, pq[s].w};
      }
      SB();
#pragma unroll
      for (int r = 0; r < 16; ++r) av[r] = fmaf(nmean, cs[r >> 2][r & 3], av[r]) * rstd;
      if (h) { av[12] = 1.f; av[13] = 0.f; av[14] = 0.f; av[15] = 0.f; }
#pragma unroll
      for (int qd = 0; qd < 4; ++qd) *(LAS v2u*)(LD + vb + 16 * qd) = v2u{pk2(av[4 * qd], av[4 * qd + 1]), pk2(av[4 * qd + 2], av[4 * qd + 3])};
      if constexpr (first) {
        if (w == 3 && !init_ok(iv)) {
          u32 spins = 0;
          while (!init_ok(__hip_atomic_load(initp, __ATOMIC_RELAXED, __HIP_MEMORY_SCOPE_AGENT))) {
            __builtin_amdgcn_s_sleep(1);
            if (++spins > 400000u) break;
          }
        }
      }
      STAMP(2);
      BAR();
      if constexpr (first) {
#pragma unroll
        for (int ro = 0; ro < 2; ++ro) {
          const int oi = min(kh * 4 + ro * 2 + h, 6);
          res[ro] = __builtin_amdgcn_raw_buffer_load_b128(rx0, tok * (D * 2) + (7 * cb + oi) * 16, tt2 * 32 * (D * 2), 0);
        }
      }
      f32x16 aq = {};
      const int sbq = first ? opqv(opqv(w1lane) + 1 * UNIT) : opqv(opqv(l16) + 1 * UNIT);
      pipe<28, first ? 2 : 1>([&](auto I) AI { return rdw(sbq, I.value); }, [&](auto) AI {},
               [&](auto I, v4u a, auto nx) AI { aq = mfma(a, x[I.value], aq); nx(); });
      if constexpr (first) wait_arr(LD, 1, 1);
#pragma unroll
      for (int qd = 0; qd < 4; ++qd) cs[qd] = *(LAS f32x4*)(LD + csb + 256 + 32 * qd);
#pragma unroll
      for (int r = 0; r < 16; ++r) aq[r] = fmaf(nmean, cs[r >> 2][r & 3], aq[r]);
      f32x16 p2 = {};
      const int wb2 = opqv(L_WPHI + opqv(l16));
      p2 = mfma(lds16(LD, wb2), pack8(aq, 0), p2);
      p2 = mfma(lds16(LD, wb2 + 1024), pack8(aq, 1), p2);
      half_softmax<0>(p2, h, rstd, pq[0], pq[1]);
      half_softmax<1>(p2, h, rstd, pq[2], pq[3]);
    };
    phase1(std::integral_constant<bool, L == 0>{});
    STAMP(3);
    if (w == 0) wait_cnt(ws, C_KV + L * 16 + j, 32);
    BAR();
    STAMP(4);
    kv_reduce(rs, LD, L, j, w, lane);
    BAR();
    STAMP(5);
    {
      f32x16 nm = {};
      const int kb = opqv(L_KVF + opqv(l16));
      v4u kf[4];
#pragma unroll
      for (int s = 0; s < 4; ++s) kf[s] = lds16(LD, kb + s * 1024);
#pragma unroll
      for (int s = 0; s < 4; ++s) nm = mfma(kf[s], pq[s], nm);
      auto r = __builtin_amdgcn_permlane32_swap(__float_as_uint(nm[12]), __float_as_uint(nm[12]), false, false);
      const float den = __uint_as_float(r[1]) + 1e-6f;
      const float rd = __builtin_amdgcn_rcpf(den);
#pragma unroll
      for (int i = 0; i < 16; ++i) nm[i] *= rd;
      const int oo = opq(OFF_O + L * OB + (tt * 32 + 2 * j) * 1024);
      st16(rs, l16, oo, pack8(nm, 0));
      st16(rs, l16, oo + 1024, pack8(nm, 1));
    }
    wave_signal(ws, C_O + (L * 64 + tt) * 2 + (j >> 3), lane);
    STAMP(6);
    wait_cnt(ws, C_O + (L * 64 + tt2) * 2 + kh, 8, true);
    STAMP(7);
    {
      const int xo = opq(OFF_O + L * OB + (tt2 * 32 + 16 * kh) * 1024);
#pragma unroll
      for (int t = 0; t < (XW < 16 ? XW : 16); ++t) x[t] = ld16(rs, l16, xo + t * 1024);
      if (L == 0) RAWBAR();
      f32x16 a0 = {}, a1 = {};
      const int sb2 = opqv(2 * UNIT + kh * 2048 + opqv(l16)), sbx = opqv(L_W2X + kh * 2048 + opqv(l16));
      pipe<32>([&](auto I) AI { constexpr int i = I.value, t = i >> 1; return lds16(LD, (t < 14 ? sb2 + t * 4096 : sbx + (t - 14) * 4096) + (i & 1) * 1024); },
               [&](auto I) AI { if constexpr (I.value == 14) RAWBAR();   },
               [&](auto I, v4u a, auto nx) AI {
                 constexpr int i = I.value;
                 if constexpr (i & 1) a1 = mfma(a, x[i >> 1], a1); else a0 = mfma(a, x[i >> 1], a0);
                 nx();
                 if constexpr (!(i & 1) && (i >> 1) + XW < 16) x[(i >> 1) + XW] = ld16(rs, l16, xo + ((i >> 1) + XW) * 1024);
               });
      const int xb = opqv(L_SCR + w * 4096 + opqv(l16)), xb2 = opqv(L_SCR + (w ^ 2) * 4096 + opqv(l16));
      f32x16 fin;
      if (kh) {
#pragma unroll
        for (int qd = 0; qd < 4; ++qd) *(LAS f32x4*)(LD + xb + qd * 1024) = f32x4{a0[4 * qd], a0[4 * qd + 1], a0[4 * qd + 2], a0[4 * qd + 3]};
        fin = a1;
      } else {
#pragma unroll
        for (int qd = 0; qd < 4; ++qd) *(LAS f32x4*)(LD + xb + qd * 1024) = f32x4{a1[4 * qd], a1[4 * qd + 1], a1[4 * qd + 2], a1[4 * qd + 3]};
        fin = a0;
      }
      BAR();
#pragma unroll
      for (int qd = 0; qd < 4; ++qd) {
        const f32x4 v = *(LAS f32x4*)(LD + xb2 + qd * 1024);
#pragma unroll
        for (int i = 0; i < 4; ++i) fin[4 * qd + i] += v[i];
      }
      finish_tile(fin, res, rs, opq(OFF_X1 + L * XB), opq(OFF_ST1 + L * STB), tt2, cb, kh, lane, false, A.out);
    }
    wave_signal(ws, C_X1 + L * 64 + tt2, lane);
    STAMP(8);
    wait_cnt(ws, C_X1 + L * 64 + tt, 16, true);
    STAMP(9);
    {
      const int xo = opq(OFF_X1 + L * XB + tt * 28 * 1024);
      const int so_ = opq(OFF_ST1 + L * STB + tt * 4096);
      const int sv_ = (opqv(lane) & 31) * 8 + h * 2048;
      constexpr int XW3 = 8;
#pragma unroll
      for (int ks = 0; ks < XW3; ++ks) x[ks] = ld16(rs, l16, xo + ks * 1024);
      const int ho = opq(OFF_H + L * HB + (tt * 112 + 7 * j) * 1024);
      const int lvo = opq(C_H + (L * 64 + tt) * 8);
      u32* fp = cnt_ptr(ws, C_H + (L * 64 + tt2) * 8);
      auto poll = [&](int lv) AI { return __hip_atomic_load(fp + 32 * lv, __ATOMIC_RELAXED, __HIP_MEMORY_SCOPE_AGENT); };
      u32 f;
      float nmean, gs, irs;
      const int csb = opqv(L_CS3 + 16 * (opqv(lane) >> 5));
      const int sbA = opqv(l16), sbB = opqv(opqv(l16) + 2 * UNIT);
      f32x4 Gcg[2], Gcu[2]; float Gt[8], Gyu[8]; u32 Go[4];
      auto glu = [&](const f32x16& a, auto N_, auto S_) AI {
        constexpr int n = N_.value, s = S_.value;
        if constexpr (s == 1) Gcu[0] = *(LAS f32x4*)(LD + csb + n * 128 + 32);
        if constexpr (s == 4) Gcg[1] = *(LAS f32x4*)(LD + csb + n * 128 + 64);
        if constexpr (s == 9) Gcu[1] = *(LAS f32x4*)(LD + csb + n * 128 + 96);
        if constexpr (s == 12 && n < 6) Gcg[0] = *(LAS f32x4*)(LD + csb + (n + 1) * 128);
        static_for<8>([&](auto V_) AI {
          constexpr int v = V_.value, k = s - (1 + 4 * (v >> 1) + (v & 1)), hf = v >> 2, i = v & 3;
          if constexpr (k == 0) { Gt[v] = fmaf(nmean, Gcg[hf][i], a[8 * hf + i]); PIN(Gt[v]); }
          if constexpr (k == 1) { Gt[v] = Gt[v] * gs; PIN(Gt[v]); }
          if constexpr (k == 2) { Gt[v] = __builtin_amdgcn_exp2f(Gt[v]); PIN(Gt[v]); }
          if constexpr (k == 3) { Gt[v] = fmaf(Gt[v], irs, irs); PIN(Gt[v]); }
          if constexpr (k == 4) { Gt[v] = __builtin_amdgcn_rcpf(Gt[v]); PIN(Gt[v]); }
          if constexpr (k == 5) { Gyu[v] = fmaf(nmean, Gcu[hf][i], a[8 * hf + 4 + i]); PIN(Gyu[v]); }
          if constexpr (k == 6) { Gt[v] = Gt[v] * Gyu[v]; PIN(Gt[v]); }
          if constexpr (k == 7 && (v & 1)) { Go[v >> 1] = pk2(Gt[v - 1], Gt[v]); PIN(Go[v >> 1]); }
        });
        if constexpr (s == 21) st16(rs, l16, ho + n * 1024, v4u{Go[0], Go[1], Go[2], Go[3]});
      };
      const int ho2 = opq(OFF_H + L * HB + tt2 * 112 * 1024 + kh * 7168);
      v4u hx[28];
      auto ldh = [&](auto T) AI { hx[T.value] = ld16(rs, l16, ho2 + p4_koff(p4_oc(T.value))); };
      f32x16 acc[7];
      constexpr int PB = 7 * PK;
      pipe<196>(
          [&](auto I) AI { constexpr int i = I.value, u = i / 28, sl = u & 3; return lds16(LD, (sl < 2 ? sbA : sbB) + (sl & 1) * UNIT + (i % 28) * 1024); },
          [&](auto I) AI { if constexpr (I.value > 28 && I.value % 28 == 6) { VSTAMP(30 + I.value / 28); RAWBAR(); WSTAMP(22 + I.value / 28); } },
          [&](auto I, v4u a, auto nx) AI {
            constexpr int i = I.value;
            if constexpr (i < PB) {
              constexpr int n = i % 7, ks = i / 7;
              if constexpr (ks == 0) { f32x16 z = {}; acc[n] = mfma(a, x[ks], z); } else acc[n] = mfma(a, x[ks], acc[n]);
              nx();
              constexpr int K2 = 28 - XW3 - PK, fx = ks < K2 ? XW3 + 2 * ks + (n ? 1 : 0) : XW3 + K2 + ks;
              if constexpr (n == 0 || (n == 3 && ks < K2)) x[fx] = ld16(rs, l16, xo + fx * 1024);
              if constexpr (n >= 5 && ks < 4) st[2 * ks + n - 5] = ld8(rs, sv_ + (2 * ks + n - 5) * 256, so_);
              if constexpr (i == 46) {
                ln_stats(st, mean, rstd);
                nmean = -mean; gs = -LOG2E * rstd; irs = __builtin_amdgcn_rcpf(rstd);
                PIN(nmean); PIN(gs); PIN(irs);
              }
            } else {
              constexpr int r = i - PB, n = r / MK, s = r % MK;
              acc[n] = mfma(a, x[PK + s], acc[n]);
              nx();
              if constexpr (r == 8) Gcg[0] = *(LAS f32x4*)(LD + csb);
              if constexpr (n >= 2 && s + MK < 22) glu(acc[n - 2], IC<n - 2>{}, IC<s + MK>{});
              if constexpr (n >= 1) glu(acc[n - 1], IC<n - 1>{}, IC<s>{});
              if constexpr (n >= 3 && s == 20 - MK) { VMCNT(0); signal_cnt(ws, lvo + n - 3); }
            }
          });
      STAMP(10);
      arrive_lds(LD, 2, lane);
      static_for<22>([&](auto S_) AI {
        if constexpr (S_.value + MK < 22) glu(acc[5], IC<5>{}, IC<S_.value + MK>{});
        glu(acc[6], IC<6>{}, S_);
        if constexpr (S_.value == 4) { VMCNT(0); signal_cnt(ws, lvo + 4); }
        if constexpr (S_.value == CP1) f = poll_issue(fp + 96);
      });
      STAMP(11);
      const int wo_ = opqv(kh * 3072 + opqv(l16)), wp_ = opqv(kh * 1024 + 1024 + opqv(l16));
      const int wo2 = opqv(wo_ + 3 * UNIT), wp2 = opqv(wp_ + 3 * UNIT);
      v4u wq[56];
      auto ldw = [&](auto K) AI {
        constexpr int k = K.value, t = k >> 1, wb = p4_wbase(t);
        if constexpr (wb >= 3 * UNIT) wq[k] = lds16(LD, ((k & 1) ? wp2 : wo2) + wb - 3 * UNIT);
        else wq[k] = lds16(LD, ((k & 1) ? wp_ : wo_) + wb);
      };
      VMCNT(1); signal_cnt(ws, lvo + 5);
      PIN(f);
      const u32 f2 = poll(4);
      if (__all(f >= 16u)) {
        static_for<4>([&](auto T) AI { ldh(T); });
        VMCNT(6); signal_cnt(ws, lvo + 6);
      } else {
        VMCNT(2); signal_cnt(ws, lvo + 6);
        wait_flags(ws, fp + 96, f);
        static_for<4>([&](auto T) AI { ldh(T); });
      }
      wait_flags(ws, fp + 128, f2);
      static_for<8>([&](auto T) AI { ldh(IC<4 + T.value>{}); });
      wait_arr(LD, 4, 4 * L + 4);
      static_for<4>([&](auto K) AI { ldw(K); });
      STAMP(12);
      f32x16 fin = {}, oth = {};
      static_for<28>([&](auto T) AI {
        constexpr int t = T.value;
        if constexpr (t == CP5) f = poll(5);
        if constexpr (t == CT5) {
          wait_flags(ws, fp + 160, f);
          f = poll(6);
          static_for<8>([&](auto K) AI { ldh(IC<12 + K.value>{}); });
          STAMP(13);
        }
        if constexpr (t == 12) {
          wait_flags(ws, fp + 192, f);
          static_for<8>([&](auto K) AI { ldh(IC<20 + K.value>{}); });
          STAMP(22);
        }
        if constexpr (t == 12) arrive_lds(LD, 3, lane);
        if constexpr (t + 2 < 28) { ldw(IC<2 * t + 4>{}); ldw(IC<2 * t + 5>{}); }
        oth = mfma(wq[2 * t + 1], hx[t], oth);
        fin = mfma(wq[2 * t], hx[t], fin);
        SB();
      });
      STAMP(14);
      wait_arr(LD, 3, 4 * L + 4);
      put16(oth, LD, opqv(L_SCR + w * 4096 + opqv(l16)));
      BAR();
#pragma unroll
      for (int c = 0; c < 4; ++c) add16(fin, LD, opqv(p4_part(w, c) + opqv(l16)));
      add16(fin, LD, opqv(L_SCR + (w ^ 2) * 4096 + opqv(l16)));
      STAMP(15);
      finish_tile(fin, res, rs, OFF_X2, OFF_ST2, tt2, cb, kh, lane, L == 1, A.out);
    }
    if (L == 0) {
      wave_signal(ws, C_X2 + tt2, lane);
      arrive_lds(LD, 0, lane);
      STAMP(16);
      wait_cnt(ws, C_X2 + tt, 16, true);
    }
    STAMP(17);
  };
  clayer(IC<0>{});
  clayer(IC<1>{});
}

extern "C" void kernel_launch(void* const* d_in, const int* in_sizes, int n_in, void* d_out, int out_size, void* d_ws,
                              size_t ws_size, hipStream_t stream) {
  Args a;
  a.x0 = (const u16*)d_in[0]; a.wqkv = (const u16*)d_in[1]; a.wphi = (const u16*)d_in[2]; a.wo = (const u16*)d_in[3];
  a.wg = (const u16*)d_in[5]; a.wu = (const u16*)d_in[6]; a.wd = (const u16*)d_in[7];
  a.ws = (char*)d_ws + 0; a.out = (u16*)d_out;
  (void)hipFuncSetAttribute((const void*)k_main, hipFuncAttributeMaxDynamicSharedMemorySize, LDS_BYTES);
  k_main<<<256, 512, LDS_BYTES, stream>>>(a);
}
```

```cpp
#include <hip/hip_runtime.h>
#include <stdint.h>
#include <utility>
#include <type_traits>

typedef unsigned short u16;
typedef unsigned int u32;
typedef u32 v4u __attribute__((ext_vector_type(4)));
typedef u32 v2u __attribute__((ext_vector_type(2)));
typedef float f32x2 __attribute__((ext_vector_type(2)));
typedef float f32x4 __attribute__((ext_vector_type(4)));
typedef float f32x16 __attribute__((ext_vector_type(16)));
typedef __bf16 bf16x2 __attribute__((ext_vector_type(2)));
typedef __bf16 bf16x8 __attribute__((ext_vector_type(8)));
typedef short s16x4 __attribute__((ext_vector_type(4)));
#define LAS __attribute__((address_space(3)))
#define DI __device__ __forceinline__
#define AI __attribute__((always_inline))
template <int V> using IC = std::integral_constant<int, V>;

constexpr int S = 2048, D = 448, FF = 1792;
constexpr int UNIT = 28672;

constexpr int C_KV = 0;
constexpr int C_W2 = 32;
constexpr int C_W4 = 36;
constexpr int C_W3 = 40;
constexpr int C_ERR = 60;
constexpr int C_X2 = 64;
constexpr int C_X1 = 128;
constexpr int C_O = 256;
constexpr int C_H = 512;
constexpr int C_W1 = 1536;
constexpr int NZ = 25;
constexpr int EPOCH_OFF = NZ * 64 * 128;
constexpr int INIT_OFF = EPOCH_OFF + 256;
constexpr int CTL_BYTES = EPOCH_OFF + 4096;
constexpr int W2B = 65536;
constexpr int OFF_W1 = CTL_BYTES;
constexpr int OFF_W2 = OFF_W1 + 16 * 3 * UNIT;
constexpr int OFF_W3 = OFF_W2 + 8 * W2B;
constexpr int OFF_W4 = OFF_W3 + 16 * 7 * UNIT;
constexpr int OFF_CS3 = OFF_W4 + 8 * 8 * UNIT;
constexpr int XB = S * D * 2;
constexpr int OB = S * 512 * 2;
constexpr int OFF_X2 = OFF_CS3 + 16 * 28 * 896;
constexpr int OFF_X1 = OFF_X2 + XB;
constexpr int OFF_O = OFF_X1 + 2 * XB;
constexpr int STB = S * 16 * 8;
constexpr int OFF_ST2 = OFF_O + 2 * OB;
constexpr int OFF_ST1 = OFF_ST2 + STB;
constexpr int KVB = 16 * 16 * 4096;
constexpr int OFF_KVP = OFF_ST1 + 2 * STB;
constexpr int HB = S * FF * 2;
constexpr int OFF_H = OFF_KVP + 2 * KVB;
constexpr int WS_USED = OFF_H + 2 * HB;

constexpr int L_SCR = 4 * UNIT;
constexpr int PHK_STRIDE = 136, V_STRIDE = 72;
constexpr int L_PHK = L_SCR;
constexpr int L_V = L_SCR + 128 * PHK_STRIDE;
constexpr int L_KVF = L_SCR + 28672;
constexpr int L_W2X = L_KVF + 4096;
constexpr int L_CONST = 155648;
constexpr int L_CS1 = L_CONST, L_CS3 = L_CONST + 384, L_WPHI = L_CONST + 1280, L_ARR = L_CONST + 3328;
constexpr int L_STAGE = 2 * UNIT;
constexpr int STG = 8704;
constexpr int LDS_BYTES = 163840;

constexpr float LOG2E = 1.4426950408889634f;
#define PKV 12
constexpr int PK = PKV, MK = 28 - PK;
constexpr int LAG = 4;

DI u32 pk2(float a, float b) { f32x2 v = {a, b}; return __builtin_bit_cast(u32, __builtin_convertvector(v, bf16x2)); }
DI float bflo(u32 v) { return __uint_as_float(v << 16); }
DI float bfhi(u32 v) { return __uint_as_float(v & 0xffff0000u); }
DI f32x16 mfma(v4u a, v4u b, f32x16 c) {
  f32x16 d = __builtin_amdgcn_mfma_f32_32x32x16_bf16(__builtin_bit_cast(bf16x8, a), __builtin_bit_cast(bf16x8, b), c, 0, 0, 0);
  asm volatile("" : "+v"(d));
  __builtin_amdgcn_sched_barrier(0);
  return d;
}
DI float xsum(float v) {
  auto r = __builtin_amdgcn_permlane32_swap(__float_as_uint(v), __float_as_uint(v), false, false);
  return __uint_as_float(r[0]) + __uint_as_float(r[1]);
}
DI float xmax(float v) {
  auto r = __builtin_amdgcn_permlane32_swap(__float_as_uint(v), __float_as_uint(v), false, false);
  return fmaxf(__uint_as_float(r[0]), __uint_as_float(r[1]));
}
DI float xmin(float v) {
  auto r = __builtin_amdgcn_permlane32_swap(__float_as_uint(v), __float_as_uint(v), false, false);
  return fminf(__uint_as_float(r[0]), __uint_as_float(r[1]));
}
#define BAR() asm volatile("s_waitcnt lgkmcnt(0)\n\ts_barrier" ::: "memory")
#define RAWBAR() asm volatile("s_barrier" ::: "memory")
#define VMCNT(n) asm volatile("s_waitcnt vmcnt(" #n ")" ::: "memory")
#define SB() __builtin_amdgcn_sched_barrier(0)
#define PIN(x) asm volatile("" : "+v"(x))

typedef __amdgpu_buffer_rsrc_t rsrc_t;
DI v4u ld16(rsrc_t rs, int voff, int soff) { return __builtin_amdgcn_raw_buffer_load_b128(rs, voff, soff, 16); }
DI v2u ld8(rsrc_t rs, int voff, int soff) { return __builtin_amdgcn_raw_buffer_load_b64(rs, voff, soff, 16); }
DI u32 ld4(rsrc_t rs, int voff, int soff) { return __builtin_amdgcn_raw_buffer_load_b32(rs, voff, soff, 16); }
DI void st16(rsrc_t rs, int voff, int soff, v4u v) {
  __builtin_amdgcn_raw_buffer_store_b128(v, rs, voff, soff, 16);
  asm volatile("s_nop 1" ::"v"(v) : "memory");
}
DI void st8(rsrc_t rs, int voff, int soff, v2u v) { __builtin_amdgcn_raw_buffer_store_b64(v, rs, voff, soff, 16); }
DI void st4(rsrc_t rs, int voff, int soff, u32 v) { __builtin_amdgcn_raw_buffer_store_b32(v, rs, voff, soff, 16); }
DI int opq(int v) { asm volatile("" : "+s"(v)); return v; }
DI int opqv(int v) { asm volatile("" : "+v"(v)); return v; }
DI v4u lds16(LAS char* l, int off) { return *(LAS v4u*)(l + off); }
DI float dot2(u32 a, u32 b, float c) {
  return __builtin_amdgcn_fdot2_f32_bf16(__builtin_bit_cast(bf16x2, a), __builtin_bit_cast(bf16x2, b), c, false);
}
DI v4u tr_frag(LAS char* L, int base, int step) {
  v2u lo = __builtin_bit_cast(v2u, __builtin_amdgcn_ds_read_tr16_b64_v4i16((LAS s16x4*)(L + base)));
  v2u hi = __builtin_bit_cast(v2u, __builtin_amdgcn_ds_read_tr16_b64_v4i16((LAS s16x4*)(L + base + step)));
  return v4u{lo.x, lo.y, hi.x, hi.y};
}
DI float sum8(v4u o) {
  const u32 one = 0x3f803f80u;
  return dot2(o.x, one, dot2(o.y, one, 0.f)) + dot2(o.z, one, dot2(o.w, one, 0.f));
}

DI u32* cnt_ptr(char* ws, int idx) { return (u32*)(ws + idx * 128); }
#define POLL_D0 16
#define LATE_ARR
#define WOV 0
#define WOS 20
#define H2 6
#define H3 12
#define CP1 12
#define CP5 0
#define CT5 11
#define PT5 8
#define ROT 0
#define POLL_IV 1
DI bool spin_more(char* ws, u32& spins) {
  u32* e = cnt_ptr(ws, C_ERR);
  __builtin_amdgcn_s_sleep(POLL_IV);
  ++spins;
  if ((spins & 255u) == 0u) {
    if (__builtin_amdgcn_readfirstlane(__hip_atomic_load(e, __ATOMIC_RELAXED, __HIP_MEMORY_SCOPE_AGENT)) != 0) return false;
    if (spins > 400000u) { __hip_atomic_store(e, 1u, __ATOMIC_RELAXED, __HIP_MEMORY_SCOPE_AGENT); return false; }
  }
  return true;
}
DI void wait_cnt(char* ws, int idx, u32 target, bool delay = false) {
  u32* c = cnt_ptr(ws, idx);
  u32 spins = 0;
  if (delay && POLL_D0) __builtin_amdgcn_s_sleep(POLL_D0);
  while ((u32)__builtin_amdgcn_readfirstlane(__hip_atomic_load(c, __ATOMIC_RELAXED, __HIP_MEMORY_SCOPE_AGENT)) < target)
    if (!spin_more(ws, spins)) break;
  __builtin_amdgcn_fence(__ATOMIC_ACQUIRE, "wavefront");
  asm volatile("" ::: "memory");
}
DI void wait_flags(char* ws, u32* fp, u32 f, u32 target = 16u) {
  if (__builtin_expect(!__all(f >= target), 0)) {
    u32 spins = 0;
    while (spin_more(ws, spins))
      if (__all(__hip_atomic_load(fp, __ATOMIC_RELAXED, __HIP_MEMORY_SCOPE_AGENT) >= target)) break;
  }
  __builtin_amdgcn_fence(__ATOMIC_ACQUIRE, "wavefront");
  asm volatile("" ::: "memory");
}
DI u32 poll_issue(const u32* p) { u32 f; asm volatile("global_load_dword %0, %1, off sc1" : "=v"(f) : "v"(p) : "memory"); return f; }
DI void signal_cnt(char* ws, int idx) {
  unsigned long long sv;
  asm volatile("s_mov_b64 %0, exec\n\ts_mov_b64 exec, 1\n\tglobal_atomic_add %1, %2, %3\n\ts_mov_b64 exec, %0"
               : "=&s"(sv) : "v"(idx * 128), "v"(1), "s"(ws) : "memory");
}
DI void wave_signal(char* ws, int idx, int lane) { VMCNT(0); signal_cnt(ws, idx); }
DI void arrive_lds(LAS char* LD, int slot, int lane) {
  if (lane == 0) __hip_atomic_fetch_add((LAS u32*)(LD + L_ARR + slot * 4), 1u, __ATOMIC_RELAXED, __HIP_MEMORY_SCOPE_WORKGROUP);
}
DI void wait_arr(LAS char* LD, int slot, u32 target) {
  while ((u32)__builtin_amdgcn_readfirstlane(__hip_atomic_load((LAS u32*)(LD + L_ARR + slot * 4), __ATOMIC_RELAXED, __HIP_MEMORY_SCOPE_WORKGROUP)) < target)
    __builtin_amdgcn_s_sleep(1);
}

#define STAMP(i)
#define HSTAMP(i)
#define VSTAMP(i)
#define WSTAMP(i)
#define USTAMP(i)

#define DMA_STEP(GSTEP) \
      "s_add_u32 m0, m0, 0x1000\n\t" \
      "v_add_u32 %1, " GSTEP ", %1\n\t" \
      "global_load_lds_dwordx4 %1, %2 sc1\n\t"
#define DMA_FN(NAME, BODY) \
DI void NAME(const char* gsrc, u32 voff, u32 lds_dst) { \
  u32 keep; \
  asm volatile("s_mov_b32 %0, m0\n\ts_mov_b32 m0, %3\n\ts_nop 0\n\tglobal_load_lds_dwordx4 %1, %2 sc1\n\t" BODY "s_mov_b32 m0, %0" \
               : "=&s"(keep), "+v"(voff) : "s"(gsrc), "s"(lds_dst) : "memory", "scc"); }
DMA_FN(dma7, DMA_STEP("0x1000") DMA_STEP("0x1000") DMA_STEP("0x1000") DMA_STEP("0x1000") DMA_STEP("0x1000") DMA_STEP("0x1000"))
DMA_FN(dma2, DMA_STEP("0x1000"))
#define PACE 1
#define STR2(x) #x
#define STR(x) STR2(x)
#define PSTEP "s_sleep " STR(PACE) "\n\t" DMA_STEP("0x1000")
DMA_FN(dma7p, PSTEP PSTEP PSTEP PSTEP PSTEP PSTEP)
DMA_FN(dma7n, DMA_STEP("0x2a000") DMA_STEP("0x2a000") DMA_STEP("0x2a000") DMA_STEP("0x2a000") DMA_STEP("0x2a000") DMA_STEP("0x2a000"))

template <int LO, int GO>
DI void dma1(const char* gsrc, u32 voff, u32 lb) {
  u32 keep, tmp;
  asm volatile("s_mov_b32 %0, m0\n\ts_add_u32 m0, %4, %5\n\tv_add_u32 %1, %6, %2\n\tglobal_load_lds_dwordx4 %1, %3 sc1\n\ts_mov_b32 m0, %0"
               : "=&s"(keep), "=&v"(tmp) : "v"(voff), "s"(gsrc), "s"(lb), "i"(LO), "i"(GO) : "memory", "scc");
}

template <typename F, int... I>
DI void static_for_impl(F&& f, std::integer_sequence<int, I...>) { (f(IC<I>{}), ...); }
template <int N, typename F>
DI void static_for(F&& f) { static_for_impl(f, std::make_integer_sequence<int, N>{}); }

constexpr int PF = 6;
#define WEV 2
template <int N, int RPS = 1, typename RD, typename PRE, typename ST>
DI void pipe(RD rd, PRE pre, ST step) {
  v4u buf[PF];
  static_for<PF>([&](auto I) AI { buf[I.value] = rd(I); });
  SB();
  static_for<N>([&](auto I) AI {
    constexpr int i = I.value;
    pre(I);
    if constexpr (WEV > 1 && i % WEV == 0 && i + PF <= N) __builtin_amdgcn_s_waitcnt(0xC07F | (((PF - WEV) * RPS) << 8));
    const v4u a = buf[i % PF];
    step(I, a, [&]() AI { if constexpr (i + PF < N) buf[i % PF] = rd(IC<i + PF>{}); SB(); });
    SB();
  });
}

DI void ln_finish(float s, float q, float& mean, float& rstd) {
  s = xsum(s); q = xsum(q);
  mean = s * (1.f / 448.f);
  const float var = fmaxf(q * (1.f / 448.f) - mean * mean, 0.f);
  rstd = __builtin_amdgcn_rsqf(var + 1e-5f);
}
DI void ln_stats(const v2u (&st)[8], float& mean, float& rstd) {
  f32x2 a = __builtin_bit_cast(f32x2, st[0]), b = __builtin_bit_cast(f32x2, st[1]);
#pragma unroll
  for (int i = 2; i < 8; i += 2) { a += __builtin_bit_cast(f32x2, st[i]); b += __builtin_bit_cast(f32x2, st[i + 1]); }
  a += b;
  ln_finish(a[0], a[1], mean, rstd);
}
DI v4u pack8(const f32x16& a, int s) {
  v4u r = {pk2(a[8 * s], a[8 * s + 1]), pk2(a[8 * s + 2], a[8 * s + 3]), pk2(a[8 * s + 4], a[8 * s + 5]), pk2(a[8 * s + 6], a[8 * s + 7])};
  return r;
}
template <int SG>
DI void half_softmax(const f32x16& p, int h, float rstd, v4u& o0, v4u& o1) {
  auto mm = [](float a, float b) AI { return SG ? fminf(a, b) : fmaxf(a, b); };
  float m0 = mm(mm(p[0], p[1]), p[2]), m1 = mm(mm(p[3], p[4]), p[5]), m2 = mm(mm(p[6], p[7]), p[8]), m3 = mm(mm(p[9], p[10]), p[11]);
  float m4 = mm(mm(mm(p[12], p[13]), p[14]), p[15]);
  m4 = h ? p[0] : m4;
  float mx = mm(mm(mm(m0, m1), m2), mm(m3, m4));
  mx = SG ? xmin(mx) : xmax(mx);
  const float c = rstd * LOG2E;
  const float cc = SG ? -c : c, off = -mx * cc;
  f32x16 e;
  float s0 = 0.f, s1 = 0.f;
#pragma unroll
  for (int r = 0; r < 16; ++r) {
    float v = __builtin_amdgcn_exp2f(fmaf(p[r], cc, off));
    if (r >= 12) v = h ? 0.f : v;
    e[r] = v;
    if (r & 1) s1 += v; else s0 += v;
  }
  const float rs_ = __builtin_amdgcn_rcpf(xsum(s0 + s1));
#pragma unroll
  for (int r = 0; r < 16; ++r) e[r] *= rs_;
  o0 = pack8(e, 0); o1 = pack8(e, 1);
  PIN(o0); PIN(o1);
  SB();
}

DI void finish_tile(const f32x16& acc, v4u (&res)[2], rsrc_t rs, int xoff, int stoff, int tt, int cb, int nt, int lane, bool last, u16* out) {
  lane = opqv(lane);
  const int h = lane >> 5, tok = lane & 31;
#pragma unroll
  for (int ro = 0; ro < 2; ++ro) {
    v4u r = res[ro];
    v4u o;
    o.x = pk2(bflo(r.x) + acc[8 * ro + 0], bfhi(r.x) + acc[8 * ro + 1]);
    o.y = pk2(bflo(r.y) + acc[8 * ro + 2], bfhi(r.y) + acc[8 * ro + 3]);
    o.z = pk2(bflo(r.z) + acc[8 * ro + 4], bfhi(r.z) + acc[8 * ro + 5]);
    o.w = pk2(bflo(r.w) + acc[8 * ro + 6], bfhi(r.w) + acc[8 * ro + 7]);
    res[ro] = o;
    const int oi = nt * 4 + ro * 2 + h;
    if (oi < 7) {
      const int go = 7 * cb + oi;
      if (last) *(v4u*)(out + (size_t)(tt * 32 + tok) * D + go * 8) = o;
      else st16(rs, ((go >> 1) * 64 + tok + 32 * (go & 1)) * 16, xoff + tt * 28 * 1024, o);
    }
  }
  if (!last) {
    const bool v1 = !(nt == 1 && h == 1);
    const u32 one = 0x3f803f80u;
    float s0 = dot2(res[0].x, one, 0.f), s1 = dot2(res[1].x, one, 0.f), q0 = dot2(res[0].x, res[0].x, 0.f), q1 = dot2(res[1].x, res[1].x, 0.f);
    s0 = dot2(res[0].y, one, s0); s1 = dot2(res[1].y, one, s1); q0 = dot2(res[0].y, res[0].y, q0); q1 = dot2(res[1].y, res[1].y, q1);
    s0 = dot2(res[0].z, one, s0); s1 = dot2(res[1].z, one, s1); q0 = dot2(res[0].z, res[0].z, q0); q1 = dot2(res[1].z, res[1].z, q1);
    s0 = dot2(res[0].w, one, s0); s1 = dot2(res[1].w, one, s1); q0 = dot2(res[0].w, res[0].w, q0); q1 = dot2(res[1].w, res[1].w, q1);
    const float sum = xsum(s0 + (v1 ? s1 : 0.f));
    const float sq = xsum(q0 + (v1 ? q1 : 0.f));
    if (h == 0) st8(rs, tok * 8, stoff + ((tt * 8 + cb) * 2 + nt) * 256, v2u{__float_as_uint(sum), __float_as_uint(sq)});
  }
}

DI void kv_reduce(rsrc_t rs, LAS char* LD, int L, int j, int w, int lane) {
  const int ko = opq(OFF_KVP + L * KVB + j * 16 * 4096 + (w >> 1) * 1024 + (w & 1) * 512);
  const int lz = opqv(lane);
  const int vo = (lz >> 5) * 4096 + (lz & 31) * 16;
  v4u t[8];
#pragma unroll
  for (int i = 0; i < 8; ++i) t[i] = ld16(rs, vo, ko + i * 8192);
  float s[8] = {};
  const u32 lo1 = (u32)opq(0x00003f80), hi1 = (u32)opq(0x3f800000);
#pragma unroll
  for (int i = 0; i < 8; ++i) {
#pragma unroll
    for (int c = 0; c < 4; ++c) { s[2 * c] = dot2(t[i][c], lo1, s[2 * c]); s[2 * c + 1] = dot2(t[i][c], hi1, s[2 * c + 1]); }
  }
#pragma unroll
  for (int c = 0; c < 8; ++c) s[c] = xsum(s[c]);
  if (lz < 32) *(LAS v4u*)(LD + L_KVF + (w >> 1) * 1024 + (w & 1) * 512 + lz * 16) = v4u{pk2(s[0], s[1]), pk2(s[2], s[3]), pk2(s[4], s[5]), pk2(s[6], s[7])};
}

constexpr int p4_oh(int t) { return 4 * t; }
constexpr int p4_oc(int t) { return 56 + 2 * t; }
constexpr int p4_koff(int o) { return (7 * (o & 15) + (o >> 4)) * 1024; }
constexpr int p4_wbase(int t) { return (t < 7 ? 0 : t < 14 ? L_SCR : t < 21 ? 3 * UNIT : UNIT) + (t % 7) * 4096; }
DI int p4_part(int a, int hw) {
  const int i = ((a >> 1) | ((a & 1) << 1)) * 4 + hw;
  return i < 7 ? 2 * UNIT + i * 4096 : i < 10 ? L_KVF + (i - 7) * 4096 : (i - 10) * 4096;
}
DI void put16(const f32x16& acc, LAS char* LD, int off) {
#pragma unroll
  for (int qd = 0; qd < 4; ++qd) *(LAS f32x4*)(LD + off + qd * 1024) = f32x4{acc[4 * qd], acc[4 * qd + 1], acc[4 * qd + 2], acc[4 * qd + 3]};
}
DI void add16(f32x16& acc, LAS char* LD, int off) {
#pragma unroll
  for (int qd = 0; qd < 4; ++qd) {
    const f32x4 v = *(LAS f32x4*)(LD + off + qd * 1024);
#pragma unroll
    for (int i = 0; i < 4; ++i) acc[4 * qd + i] += v[i];
  }
}

struct Args {
  const u16* x0; const u16* wqkv; const u16* wphi; const u16* wo; const u16* wg; const u16* wu; const u16* wd;
  char* ws; u16* out;
};

struct Retile { const v4u* pa[7]; int rd0, rd1, step, o0, o1, ocb, sig; };
DI void retile_prep(const Args& A, int job, int lane, int stage, Retile& R) {
  const int h = lane >> 5, q = (lane & 15) >> 2, c8 = ((lane >> 4) & 1) * 4 + (lane & 3);
  const int hh = c8 & 1, quad = c8 >> 1;
  if (job < 64 || job >= 512) {
    const bool iswo = job < 64;
    const int a0 = iswo ? job >> 2 : (job - 512) >> 2, cg = job & 3;
    const u16* src = (iswo ? A.wo + (size_t)a0 * 28 * D : A.wd + (size_t)a0 * 32 * D) + 112 * cg;
    const int nrow = iswo ? 28 : 32;
#pragma unroll
    for (int i = 0; i < 7; ++i) {
      const int p = lane + 64 * i, r = p / 14, c = p % 14;
      R.pa[i] = (const v4u*)(src + (size_t)(r < nrow ? r : 0) * D + c * 8);
    }
    const int rb = stage + ((iswo ? 4 : 8) * h + q) * 224 + 8 * (quad & 1);
    const int oi = (quad >> 1) * 2 + hh;
    R.rd0 = rb + 16 * oi; R.rd1 = rb + 16 * min(4 + oi, 6); R.step = (iswo ? 8 : 4) * 224;
    int ob[2];
#pragma unroll
    for (int k2 = 0; k2 < 2; ++k2) {
      const int ks = a0 * 2 + k2, o_ = 16 * (ks % 7) + ks / 7;
      int fr;
      if (o_ < 56) fr = 112 + ((o_ & 3) * 14 + (o_ >> 2)) * 2;
      else fr = (o_ - 56) * 2;
      ob[k2] = iswo ? OFF_W2 + cg * 2 * W2B + ((ks & 15) * 2 + (ks >> 4)) * 2048 : OFF_W4 + cg * 16 * UNIT + fr * 1024;
    }
    R.o0 = opq(ob[0]); R.o1 = opq(ob[1]); R.ocb = opq(iswo ? W2B : 8 * UNIT); R.sig = opq((iswo ? C_W2 : C_W4) + cg);
  } else {
    const int a = job - 64, j = a / 28, ks = a % 28;
#pragma unroll
    for (int i = 0; i < 7; ++i) {
      const int p = lane + 64 * i, r = p / 28, c = p % 28;
      R.pa[i] = (const v4u*)((c < 14 ? A.wg : A.wu) + (size_t)(ks * 16 + r) * FF + 112 * j + (c < 14 ? c : c - 14) * 8);
    }
    R.rd0 = stage + (8 * h + q) * 448 + (quad & 1) * 224 + (8 * hh + 4 * (quad >> 1)) * 2; R.rd1 = 0; R.step = 0;
    R.o0 = opq(OFF_W3 + j * 7 * UNIT + (ks < PK ? ks * 7 : 6 * PK + ks) * 1024); R.o1 = opq(ks < PK ? 1024 : MK * 1024);
    R.ocb = opq(OFF_CS3 + (j * 28 + ks) * 896); R.sig = opq(C_W3 + j);
  }
#pragma unroll
  for (int i = 0; i < 7; ++i) PIN(R.pa[i]);
  PIN(R.rd0); PIN(R.rd1);
}
DI void retile_load(const Retile& R, u32 lds_dst) {
#pragma unroll
  for (int i = 0; i < 7; ++i) {
    u32 keep;
    asm volatile("s_mov_b32 %0, m0\n\ts_add_u32 m0, %2, %3\n\ts_nop 0\n\tglobal_load_lds_dwordx4 %1, off\n\ts_mov_b32 m0, %0"
                 : "=&s"(keep) : "v"(R.pa[i]), "s"(lds_dst), "i"(i * 1024) : "memory", "scc");
  }
}
DI void retile_store(int job, const Retile& R, rsrc_t rs, LAS char* LD, int lane) {
  const int h = lane >> 5;
  if (job < 64 || job >= 512) {
    const bool pad = (lane & 0x14) == 0x14;
    const bool kpad = job < 64 && h;
    v4u o[8];
#pragma unroll
    for (int i = 0; i < 8; ++i) {
      const int ad = ((i & 2) ? R.rd1 : R.rd0) + (i >> 2) * 112 + (i & 1) * 16 * 224;
      const v2u lo = __builtin_bit_cast(v2u, __builtin_amdgcn_ds_read_tr16_b64_v4i16((LAS s16x4*)(LD + ad)));
      const v2u hi = __builtin_bit_cast(v2u, __builtin_amdgcn_ds_read_tr16_b64_v4i16((LAS s16x4*)(LD + ad + R.step)));
      o[i] = v4u{lo.x, lo.y, hi.x, hi.y};
    }
#pragma unroll
    for (int i = 0; i < 8; ++i) {
      if ((i & 2) && pad) o[i] = v4u{0u, 0u, 0u, 0u};
      if ((i & 1) && kpad) { o[i].z = 0u; o[i].w = 0u; }
      st16(rs, lane * 16, ((i & 1) ? R.o1 : R.o0) + (i >> 2) * R.ocb + ((i >> 1) & 1) * 1024, o[i]);
    }
  } else {
    v4u o[7];
#pragma unroll
    for (int n = 0; n < 7; ++n) o[n] = tr_frag(LD, R.rd0 + n * 32, 4 * 448);
    float cs[7];
#pragma unroll
    for (int n = 0; n < 7; ++n) {
      st16(rs, lane * 16, R.o0 + n * R.o1, o[n]);
      cs[n] = xsum(sum8(o[n]));
    }
#pragma unroll
    for (int n2 = 0; n2 < 4; ++n2) {
      const float v = h ? cs[n2 < 3 ? 2 * n2 + 1 : 6] : cs[2 * n2];
      if (n2 < 3 || !h) st4(rs, lane * 4, R.ocb + n2 * 256, __float_as_uint(v));
    }
  }
}

__global__ __launch_bounds__(512, 2) void k_main(const Args A) {
  extern __shared__ __attribute__((aligned(16))) char smem[];
  LAS char* LD = (LAS char*)smem;
  char* ws = A.ws;
  const int b = blockIdx.x, xcd = b & 7, bi = b >> 3;
  const int g = (xcd >> 2) * 8 + (bi >> 2), j = (xcd & 3) * 4 + (bi & 3);
  const int rb = 2 * g + (j >> 3), cb = j & 7;
  const int lane = threadIdx.x & 63, w = __builtin_amdgcn_readfirstlane(threadIdx.x >> 6);
  const int h = lane >> 5, tok = lane & 31;
  const rsrc_t rs = __builtin_amdgcn_make_buffer_rsrc((void*)ws, 0, WS_USED, 0x00020000);
  const u32 lbase = (u32)(size_t)smem;
  const int l16 = lane * 16;
  const int tq_ = (lane & 15) >> 2, tc8 = ((lane >> 4) & 1) * 4 + (lane & 3);
  const int w1lane = (8 * h + tq_) * 64 + min(tc8 + (j & 1), 7) * 8;

  const int hw = w - 4;
  const u32 voffn = (hw * 16 + (lane >> 2)) * (3 * D * 2) + (lane & 3) * 16;
  const char* w1 = (const char*)A.wqkv + 56 * j - (j & 1) * 8;
  unsigned long long ep;
  const u32 lb = lbase + hw * 1024;
  const int rot = ROT ? (g + 2 * j) % 7 : 0;
  auto blk = [&](int p) AI { const int q = p + rot; return q >= 7 ? q - 7 : q; };
#define KP(p) dma1<4 * UNIT + (p) * 4096, 896>(w1 + blk(p) * 0x2a000, voffn, lb)
#define VP(p) dma1<(p) * 4096, 1792>(w1 + blk(p) * 0x2a000, voffn, lb)
#define QP(p) dma1<UNIT + (p) * 4096, 0>(w1 + blk(p) * 0x2a000, voffn, lb)
  if (opq(w) >= 4) {
    HSTAMP(0);
    if (hw == 1 && b < NZ) {
      asm volatile("global_load_dwordx2 %0, %1, %2 sc1" : "=v"(ep) : "v"(EPOCH_OFF), "s"(ws) : "memory");
      st4(rs, lane * 128, b * 8192, 0u);
    }
    if (hw == 2) {
      dma1<L_KVF, 0>((const char*)A.wphi + j * 1568, l16, lbase);
      dma1<L_KVF + 1024, 0>((const char*)A.wphi + j * 1568, min(1024 + l16, 1552), lbase);
    }
    KP(0); KP(1); VP(0); KP(2); VP(1); KP(3); VP(2);
    if (hw == 3) {
      unsigned long long pcv;
      asm volatile("s_getpc_b64 %0" : "=s"(pcv));
      const char* pc = (const char*)(pcv & ~255ull);
      dma1<L_W2X, 0>(pc, bi * 1024 + l16, lbase);
      dma1<L_W2X + 1024, 32768>(pc, bi * 1024 + l16, lbase);
    }
    HSTAMP(1);
  }

  if (w >= 4) {
    const u32 voff = hw * 1024 + lane * 16;
    const char* w1f = ws + OFF_W1 + j * 3 * UNIT;
    const char* w2 = ws + OFF_W2 + cb * W2B;
    const char* w3 = ws + OFF_W3 + j * 7 * UNIT;
    const char* w4 = ws + OFF_W4 + cb * 8 * UNIT;
#define DMA(src, slot) dma7((src), voff, lbase + (slot) * UNIT + hw * 1024)
#define DMAP(src, slot) dma7p((src), voff, lbase + (slot) * UNIT + hw * 1024)
#define DMAN(which, slot) dma7n(w1 + (which) * 896, voffn, lbase + (slot) * UNIT + hw * 1024)
    v4u wr[28];
    unsigned long long tag = 0;
    auto hlayer = [&](auto LC) AI {
      constexpr int L = LC.value;
      Retile R;
      const int job = hw >= 2 ? b * 2 + hw - 2 : (hw == 0 && b < 224 ? 512 + b : -1);
      float c0 = 0.f, c1 = 0.f, c2 = 0.f, c3 = 0.f;
      auto colpart = [&](int slot, auto K0, auto K1) AI {
        constexpr int k0 = K0.value, n = K1.value - K0.value;
        const int ib = opqv(slot * UNIT + opqv(w1lane));
        v4u fr[n];
#pragma unroll
        for (int ks = 0; ks < n; ++ks) fr[ks] = tr_frag(LD, ib + (k0 + ks) * 1024, 256);
        SB();
        const u32 one = 0x3f803f80u;
#pragma unroll
        for (int ks = 0; ks < n; ++ks) { c0 = dot2(fr[ks].x, one, c0); c1 = dot2(fr[ks].y, one, c1); c2 = dot2(fr[ks].z, one, c2); c3 = dot2(fr[ks].w, one, c3); }
        SB();
      };
      auto colfin = [&](int which) AI {
        const float cs = xsum((c0 + c1) + (c2 + c3));
        if (lane < 32) *(LAS float*)(LD + L_CS1 + which * 128 + lane * 4) = cs;
      };
      v4u csa[7];
      auto export_img = [&](int slot, int which) AI {
        const int ib = opqv(slot * UNIT + opqv(w1lane) + g * 1024);
        const int eo = opq(OFF_W1 + (j * 3 + which) * UNIT + (g & 3) * 1024);
        st16(rs, l16, eo + blk(g >> 2) * 4096, tr_frag(LD, ib, 256));
        if (g < 12) st16(rs, l16, eo + blk(4 + (g >> 2)) * 4096, tr_frag(LD, ib + 16384, 256));
      };
      if constexpr (L == 0) {
        if (hw == 1 && lane < 2) *(LAS v4u*)(LD + L_ARR + l16) = v4u{0u, 0u, 0u, 0u};
        HSTAMP(2);
#define VMW(n, n3) if (hw == 3) { VMCNT(n3); } else { VMCNT(n); }
        VMW(4, 6); BAR();
        HSTAMP(3);
        KP(4); VP(3);
        if (hw == 1 && b < NZ) {
          PIN(ep);
          tag = ep + 1ull;
          if (lane == 0) __hip_atomic_store((unsigned long long*)(ws + INIT_OFF + b * 128), tag, __ATOMIC_RELAXED, __HIP_MEMORY_SCOPE_AGENT);
        }
        if (hw == 2) {
          const int wb = opqv(L_KVF + (4 * h + tq_) * 56 + tc8 * 8);
          const v4u f0 = tr_frag(LD, wb, 8 * 56);
          v4u f1 = tr_frag(LD, wb + 16 * 56, 8 * 56);
          if (h) { f1.z = 0u; f1.w = 0u; }
          *(LAS v4u*)(LD + L_WPHI + l16) = f0;
          *(LAS v4u*)(LD + L_WPHI + 1024 + l16) = f1;
        }
        if (hw == 0) colpart(4, IC<0>{}, IC<8>{});
        VMW(4, 6); BAR();
        HSTAMP(4);
        KP(5); VP(4);
        if (job >= 0) retile_prep(A, job, lane, L_STAGE + hw * STG, R);
        if (hw == 1) colpart(0, IC<0>{}, IC<8>{});
        VMW(4, 6); BAR();
        HSTAMP(5);
        KP(6); VP(5); VP(6);
        if (hw == 0) colpart(4, IC<8>{}, IC<16>{});
        if (hw == 1) colpart(0, IC<8>{}, IC<12>{});
        VMCNT(5); BAR();
        HSTAMP(6);
        QP(0); QP(1); QP(2); QP(3);
        if (hw == 0) colpart(4, IC<16>{}, IC<20>{});
        if (hw == 1) colpart(0, IC<12>{}, IC<16>{});
        VMCNT(7); BAR();
        HSTAMP(7);
        QP(4); QP(5); QP(6);
        if (hw == 0) colpart(4, IC<20>{}, IC<24>{});
        if (hw == 1) colpart(0, IC<16>{}, IC<20>{});
        VMCNT(7); BAR();
        HSTAMP(8);
        if (hw == 0) { colpart(4, IC<24>{}, IC<28>{}); colfin(0); }
        if (hw == 1) { colpart(0, IC<20>{}, IC<28>{}); colfin(1); }
        if (hw == 2) export_img(4, 1);
        if (hw == 3) export_img(0, 2);
        HSTAMP(9);
        BAR();
        HSTAMP(10);
        if (job >= 0) retile_load(R, lbase + L_STAGE + hw * STG);
      } else {
        wait_arr(LD, 0, 4);
        DMA(w1f + 2 * UNIT, 0);
        VMCNT(0); BAR();
      }
      {
        const int wb = opqv(L_WPHI + opqv(l16));
        const v4u wf0 = lds16(LD, wb), wf1 = lds16(LD, wb + 1024);
        if (L == 0) HSTAMP(11);
        BAR();
        if (L == 0) HSTAMP(12);
        __builtin_amdgcn_s_setprio(1);
        const int lz = opqv(lane);
        const int xk = opqv(L_W2X + hw * 1024 + lz * 16);
        const v4u kf0 = lds16(LD, xk), kf1 = lds16(LD, xk + 4096);
        const float rstd = *(LAS float*)(LD + L_KVF + hw * 256 + lz * 4);
        f32x16 p = {};
        p = mfma(wf0, kf0, p);
        p = mfma(wf1, kf1, p);
        v4u o0, o1;
        half_softmax<1>(p, h, rstd, o0, o1);
        const int pb = opqv(L_PHK + (hw * 32 + (lz & 31)) * PHK_STRIDE + 8 * (lz >> 5) + 64);
        *(LAS v2u*)(LD + pb) = v2u{o0.x, o0.y}; *(LAS v2u*)(LD + pb + 16) = v2u{o0.z, o0.w};
        *(LAS v2u*)(LD + pb + 32) = v2u{o1.x, o1.y}; *(LAS v2u*)(LD + pb + 48) = v2u{o1.z, o1.w};
        __builtin_amdgcn_s_setprio(0);
      }
      if (L == 0) HSTAMP(13);
      if constexpr (L == 0) {
        if (job >= 0) { VMCNT(7); } else { VMCNT(0); }
      } else VMCNT(0);
      if (L == 0) HSTAMP(14);
      BAR();
      if (hw < 2) {
        __builtin_amdgcn_s_setprio(2);
        f32x16 kp = {};
        const int lz = opqv(lane);
        const int g16 = (lz >> 4) & 1, q = (lz & 15) >> 2, p = lz & 3, hz = lz >> 5;
        const int ab0 = opqv(L_PHK + (8 * hz + q) * PHK_STRIDE + (g16 * 16 + 4 * p) * 2 + hw * 64);
        const int bb0 = opqv(L_V + (8 * hz + q) * V_STRIDE + (g16 * 16 + 4 * p) * 2);
        v4u ap[8], bv[8];
#pragma unroll
        for (int s = 0; s < 8; ++s) { ap[s] = tr_frag(LD, ab0 + 16 * s * PHK_STRIDE, 4 * PHK_STRIDE); bv[s] = tr_frag(LD, bb0 + 16 * s * V_STRIDE, 4 * V_STRIDE); }
        SB();
#pragma unroll
        for (int s = 0; s < 8; ++s) kp = mfma(ap[s], bv[s], kp);
        const int ko = opq(OFF_KVP + L * KVB + (j * 16 + g) * 4096 + hw * 2048);
        st16(rs, l16, ko, pack8(kp, 0));
        st16(rs, l16, ko + 1024, pack8(kp, 1));
        wave_signal(ws, C_KV + L * 16 + j, lane);
        __builtin_amdgcn_s_setprio(0);
      }
      if constexpr (L == 0) {
        HSTAMP(15);
        if (hw == 3) {
          colpart(1, IC<0>{}, IC<28>{}); colfin(2);
          arrive_lds(LD, 1, lane);
        }
        if (hw == 1) { export_img(1, 0); wave_signal(ws, C_W1 + j, lane); }
        if (job >= 0) { VMCNT(0); retile_store(job, R, rs, LD, lane); }
        HSTAMP(16);
        if (job >= 0) wave_signal(ws, R.sig, lane);
        if (hw >= 2) signal_cnt(ws, C_W1 + j);
        HSTAMP(17);
        BAR();
        HSTAMP(18);
        u32* fw = cnt_ptr(ws, (lane & 3) == 0 ? C_W2 + (cb >> 1) : (lane & 3) == 1 ? C_W3 + j : C_W4 + (cb >> 1));
        kv_reduce(rs, LD, L, j, w, lane);
        HSTAMP(19);
        BAR();
        HSTAMP(20);
        wait_flags(ws, fw, __hip_atomic_load(fw, __ATOMIC_RELAXED, __HIP_MEMORY_SCOPE_AGENT), (lane & 3) == 0 ? 16u : (lane & 3) == 1 ? 28u : 56u);
        HSTAMP(21);
        DMA(w2, 2); DMA(w2 + UNIT, 3); dma2(w2 + 2 * UNIT, voff, lbase + L_W2X + hw * 1024);
#pragma unroll
        for (int i = 0; i < 7; ++i) csa[i] = ld16(rs, min(lane, 55) * 16, OFF_CS3 + (j * 28 + hw * 7 + i) * 896);
        DMA(w3, 0);
        HSTAMP(22);
        VMCNT(14); HSTAMP(23); BAR();
        HSTAMP(24);
        DMA(w3 + UNIT, 1);
      } else {
        DMA(w2, 2); DMA(w2 + UNIT, 3); dma2(w2 + 2 * UNIT, voff, lbase + L_W2X + hw * 1024);
        DMA(w3, 0);
        BAR();
        kv_reduce(rs, LD, L, j, w, lane);
        BAR();
        DMA(w3 + UNIT, 1);
      }
      BAR();
      if constexpr (L == 0) {
        f32x4 c4 = __builtin_bit_cast(f32x4, csa[0]);
#pragma unroll
        for (int i = 1; i < 7; ++i) c4 += __builtin_bit_cast(f32x4, csa[i]);
        if (lane < 56) *(LAS f32x4*)(LD + L_KVF + hw * 896 + l16) = c4;
      }
      DMA(w3 + 2 * UNIT, 2);
      VMCNT(7); BAR();
      const int wo4 = opq(OFF_W4 + cb * 8 * UNIT + 4 * UNIT + hw * UNIT);
#define WR(lo, hi) _Pragma("unroll") for (int i = lo; i < hi; ++i) wr[i] = ld16(rs, l16, wo4 + i * 1024)
      if constexpr (L == 0) {
        const int t4 = min(hw * 64 + lane, 223) * 4;
        *(LAS float*)(LD + L_CS3 + t4) = (*(LAS float*)(LD + L_KVF + t4) + *(LAS float*)(LD + L_KVF + 896 + t4)) +
                                         (*(LAS float*)(LD + L_KVF + 1792 + t4) + *(LAS float*)(LD + L_KVF + 2688 + t4));
        WR(0, 28);
      }
      DMA(w3 + 3 * UNIT, 3);
      VMCNT(7); BAR();
      DMAP(w3 + 4 * UNIT, 0);
      DMAP(w4 + UNIT, 4);
      VMCNT(14); BAR();
      DMAP(w3 + 5 * UNIT, 1);
      if (L == 0) wait_cnt(ws, C_W1 + j, 48);
      VMCNT(14); BAR();
      DMAP(w3 + 6 * UNIT, 2);
      VMCNT(7); BAR();
      DMAP(w4 + 2 * UNIT, 3);
      VMCNT(7); BAR();
      {
        u32* fp = cnt_ptr(ws, C_H + (L * 64 + 2 * rb + (lane & 1)) * 8);
        const int ho = opq(OFF_H + L * HB + 2 * rb * 112 * 1024 + hw * 7168);
        v4u hx[28];
        auto ldh = [&](auto I) AI { constexpr int i = I.value; hx[i] = ld16(rs, l16, ho + (i & 1) * 112 * 1024 + p4_koff(p4_oh(i >> 1))); };
        auto poll = [&](int lv) AI { return __hip_atomic_load(fp + 32 * lv, __ATOMIC_RELAXED, __HIP_MEMORY_SCOPE_AGENT); };
        DMAP(w4, 0);
        wait_flags(ws, fp, poll(0));
        static_for<8>([&](auto I) AI { ldh(I); });
        BAR();
        u32 f = poll_issue(fp + 32);
        DMA(w4 + 3 * UNIT, 1);
        VMCNT(7); PIN(f);
        wait_flags(ws, fp + 32, f);
        static_for<6>([&](auto I) AI { ldh(IC<8 + I.value>{}); });
        f32x16 acc[4] = {};
        auto ord = [](int i) constexpr { return i < 20 ? i : (i < 24 ? 20 + 2 * (i - 20) : 21 + 2 * (i - 24)); };
        const int pb0 = opqv(p4_part(0, hw) + opqv(l16)), pb2 = opqv(p4_part(2, hw) + opqv(l16));
        static_for<28>([&](auto I) AI {
          constexpr int i = I.value, k = ord(i), t = k >> 1, tq = k & 1;
          if constexpr (i == 0) f = poll(2);
          if constexpr (i == H2) {
            wait_flags(ws, fp + 64, f);
            VMCNT(2); arrive_lds(LD, 4, lane);
          }
          if constexpr (i == H3 - 3) f = poll(3);
          if constexpr (i == H3) wait_flags(ws, fp + 96, f);
          if constexpr (i == 24) wait_arr(LD, 2, 4 * L + 4);
          acc[tq] = mfma(wr[2 * t], hx[k], acc[tq]);
          acc[2 + tq] = mfma(wr[2 * t + 1], hx[k], acc[2 + tq]);
          if constexpr (i < 2) ldh(IC<14 + i>{});
          if constexpr (i >= H2 && i < H2 + 4) { ldh(IC<16 + 2 * (i - H2)>{}); ldh(IC<17 + 2 * (i - H2)>{}); }
          if constexpr (i >= H3 && i < H3 + 2) { ldh(IC<24 + 2 * (i - H3)>{}); ldh(IC<25 + 2 * (i - H3)>{}); }
          if constexpr (i >= 24) {
            constexpr int qd = i - 24;
            *(LAS f32x4*)(LD + pb0 + qd * 1024) = f32x4{acc[0][4 * qd], acc[0][4 * qd + 1], acc[0][4 * qd + 2], acc[0][4 * qd + 3]};
            *(LAS f32x4*)(LD + pb2 + qd * 1024) = f32x4{acc[2][4 * qd], acc[2][4 * qd + 1], acc[2][4 * qd + 2], acc[2][4 * qd + 3]};
          }
          SB();
        });
        wait_arr(LD, 3, 4 * L + 4);
        put16(acc[1], LD, opqv(p4_part(1, hw) + opqv(l16)));
        put16(acc[3], LD, opqv(p4_part(3, hw) + opqv(l16)));
        BAR();
      }
      if (L == 0) { DMA(w1f + UNIT, 3); DMA(w1f, 1); }
    };
    hlayer(IC<0>{});
    hlayer(IC<1>{});
    if (b == 0 && hw == 1 && lane == 0) __hip_atomic_store((unsigned long long*)(ws + EPOCH_OFF), tag, __ATOMIC_RELAXED, __HIP_MEMORY_SCOPE_AGENT);
    return;
  }

  const int tt = 4 * g + w;
  const int tt2 = 2 * rb + (w & 1);
  const int kh = w >> 1;
  const rsrc_t rx0 = __builtin_amdgcn_make_buffer_rsrc((void*)A.x0, 0, XB, 0x00020000);
  v4u res[2];
#define XWV 14
  constexpr int XW = XWV;

  auto clayer = [&](auto LC) AI {
    constexpr int L = LC.value;
    v4u x[28];
    v2u st[8];
    float mean, rstd;
    v4u pq[4];
    STAMP(0);
    auto phase1 = [&](auto FIRST) AI {
      constexpr bool first = FIRST.value;
      const int xo = opq(first ? tt * 32 * (D * 2) : OFF_X2 + tt * 28 * 1024);
      auto ldx = [&](int ks) AI { return ld16(rs, l16, xo + ks * 1024); };
      const int so_ = opq(OFF_ST2 + tt * 4096);
      const int sv_ = (opqv(lane) & 31) * 8 + h * 2048;
      v4u gx[28];
      const int stg = L_STAGE + w * STG;
      int gv[3];
#pragma unroll
      for (int c = 0; c < 3; ++c) gv[c] = (lane >> 4) * (D * 2) + (lane & 7) * 16 + ((lane & 8) ? blk(2 * c + 1) : blk(2 * c)) * 128;
      const int gv3 = (lane >> 3) * (D * 2) + (lane & 7) * 16 + blk(6) * 128;
      const int gl = stg + (lane >> 4) * 272 + (lane & 15) * 16, gl3 = stg + (lane >> 3) * 272 + (lane & 7) * 16;
      const int srd = opqv(stg + (opqv(lane) & 31) * 272 + h * 16);
      auto ldg = [&](int i) AI {
        return i < 24 ? __builtin_amdgcn_raw_buffer_load_b128(rx0, gv[i >> 3], xo + (i & 7) * 4 * (D * 2), 0)
                      : __builtin_amdgcn_raw_buffer_load_b128(rx0, gv3, xo + (i & 7) * 8 * (D * 2), 0);
      };
      auto stage = [&](int c) AI {
#pragma unroll
        for (int i = 0; i < (c < 3 ? 8 : 4); ++i) *(LAS v4u*)(LD + (c < 3 ? gl + i * 4 * 272 : gl3 + i * 8 * 272)) = gx[8 * c + i];
#pragma unroll
        for (int i = 0; i < (c < 3 ? 8 : 4); ++i) x[8 * c + i] = lds16(LD, srd + i * 32);
      };
      unsigned long long iv = 0;
      unsigned long long* initp = (unsigned long long*)(ws + (w == 3 && lane < NZ ? INIT_OFF + lane * 128 : EPOCH_OFF));
      auto init_ok = [&](unsigned long long v) AI {
        const unsigned long long e = ((unsigned long long)(u32)__builtin_amdgcn_readlane((int)(v >> 32), 63) << 32) | (u32)__builtin_amdgcn_readlane((int)v, 63);
        return __all(lane >= NZ || v == e + 1ull);
      };
      if constexpr (first) {
#pragma unroll
        for (int i = 0; i < 16; ++i) gx[i] = ldg(i);
        stage(0);
      } else {
#pragma unroll
        for (int ks = 0; ks < XW; ++ks) x[ks] = ldx(ks);
      }
      USTAMP(18);
      BAR();
      USTAMP(19);
      f32x16 ak = {}, av = {}, p = {};
      const int sbk = first ? opqv(opqv(w1lane) + 4 * UNIT) : opqv(opqv(l16) + 3 * UNIT), sbv = first ? opqv(w1lane) : opqv(l16);
      const int csb = opqv(L_CS1 + 16 * (opqv(lane) >> 5)), wb = opqv(L_WPHI + opqv(l16));
      const int xk = opqv(L_W2X + w * 1024 + opqv(l16));
      f32x4 cs[4]; v4u kf[2], wf[2];
      float nmean, s1 = 0.f, s2 = 0.f, q1 = 0.f, q2 = 0.f;
      auto isk = [](int i) constexpr { return i < LAG || (i < 56 - LAG && ((i - LAG) & 1) == 0); };
      auto kst = [](int i) constexpr { return i < LAG ? i : (i < 56 - LAG ? ((((i - LAG) & 1) == 0) ? LAG + (i - LAG) / 2 : (i - LAG) / 2) : i - 28); };
      auto rdw = [&](int base, int ks) AI { if constexpr (first) return tr_frag(LD, base + ks * 1024, 256); else return lds16(LD, base + ks * 1024); };
      pipe<56, first ? 2 : 1>([&](auto I) AI { return rdw(isk(I.value) ? sbk : sbv, kst(I.value)); },
               [&](auto I) AI {
                 constexpr int i = I.value;
                 if constexpr (first && (i == 6 || i == 14 || i == 22 || i == 30 || i == 38 || i == 46)) { USTAMP(40 + i / 8 * 2); RAWBAR(); USTAMP(41 + i / 8 * 2); }
                 if constexpr (i == 54) { USTAMP(20); BAR(); USTAMP(21); }
               },
               [&](auto I, v4u a, auto nx) AI {
                 constexpr int i = I.value, ks = kst(i);
                 if constexpr (i == 54) { p = mfma(wf[0], kf[0], p); p = mfma(wf[1], kf[1], p); }
                 if constexpr (isk(i)) ak = mfma(a, x[ks], ak); else av = mfma(a, x[ks], av);
                 nx();
                 if constexpr (!first && isk(i) && ks + XW < 28) x[ks + XW] = ldx(ks + XW);
                 if constexpr (first) {
                   if constexpr (i < 12) gx[16 + i] = ldg(16 + i);
                   if constexpr (isk(i) && ks % 8 == 6) stage(ks / 8 + 1);
                   if constexpr (isk(i)) {
                     const u32 one = 0x3f803f80u;
                     s1 = dot2(x[ks].x, one, s1); q1 = dot2(x[ks].x, x[ks].x, q1); s2 = dot2(x[ks].y, one, s2); q2 = dot2(x[ks].y, x[ks].y, q2);
                     s1 = dot2(x[ks].z, one, s1); q1 = dot2(x[ks].z, x[ks].z, q1); s2 = dot2(x[ks].w, one, s2); q2 = dot2(x[ks].w, x[ks].w, q2);
                   }
                   if constexpr (i == 45) iv = __hip_atomic_load(initp, __ATOMIC_RELAXED, __HIP_MEMORY_SCOPE_AGENT);
                   if constexpr (i == 51) { ln_finish(s1 + s2, q1 + q2, mean, rstd); nmean = -mean; }
                 } else {
                   if constexpr (!isk(i) && ks >= 4 && ks < 12) st[ks - 4] = ld8(rs, sv_ + (ks - 4) * 256, so_);
                   if constexpr (i == 45) { ln_stats(st, mean, rstd); nmean = -mean; }
                 }
                 if constexpr (i >= 46 && i < 50) cs[i - 46] = *(LAS f32x4*)(LD + csb + 32 * (i - 46));
                 if constexpr (i == 50) wf[0] = lds16(LD, wb);
                 if constexpr (i == 51) wf[1] = lds16(LD, wb + 1024);
                 if constexpr (i == 52 || i == 53) {
#pragma unroll
                   for (int qd = 2 * (i - 52); qd < 2 * (i - 52) + 2; ++qd) {
                     const float y0 = fmaf(nmean, cs[qd][0], ak[4 * qd]), y1 = fmaf(nmean, cs[qd][1], ak[4 * qd + 1]);
                     const float y2 = fmaf(nmean, cs[qd][2], ak[4 * qd + 2]), y3 = fmaf(nmean, cs[qd][3], ak[4 * qd + 3]);
                     kf[qd >> 1][2 * (qd & 1)] = pk2(y0, y1);
                     kf[qd >> 1][2 * (qd & 1) + 1] = pk2(y2, y3);
                   }
                   *(LAS v4u*)(LD + xk + (i - 52) * 4096) = kf[i - 52];
                   if constexpr (i == 52) *(LAS float*)(LD + L_KVF + w * 256 + opqv(lane) * 4) = rstd;
                 }
                 if constexpr (i == 55) {
#pragma unroll
                   for (int qd = 0; qd < 4; ++qd) cs[qd] = *(LAS f32x4*)(LD + csb + 128 + 32 * qd);
                 }
               });
      STAMP(1);
      const int lz = opqv(lane);
      const int trow = w * 32 + (lz & 31);
      const int pb = opqv(L_PHK + trow * PHK_STRIDE + 8 * (lz >> 5)), vb = opqv(L_V + trow * V_STRIDE + 8 * (lz >> 5));
      half_softmax<0>(p, h, rstd, pq[0], pq[1]);
#pragma unroll
      for (int s = 0; s < 2; ++s) {
        *(LAS v2u*)(LD + pb + 32 * s) = v2u{pq[s].x, pq[s].y};
        *(LAS v2u*)(LD + pb + 32 * s + 16) = v2u{pq[s].z, pq[s].w};
      }
      SB();
#pragma unroll
      for (int r = 0; r < 16; ++r) av[r] = fmaf(nmean, cs[r >> 2][r & 3], av[r]) * rstd;
      if (h) { av[12] = 1.f; av[13] = 0.f; av[14] = 0.f; av[15] = 0.f; }
#pragma unroll
      for (int qd = 0; qd < 4; ++qd) *(LAS v2u*)(LD + vb + 16 * qd) = v2u{pk2(av[4 * qd], av[4 * qd + 1]), pk2(av[4 * qd + 2], av[4 * qd + 3])};
      if constexpr (first) {
        if (w == 3 && !init_ok(iv)) {
          u32 spins = 0;
          while (!init_ok(__hip_atomic_load(initp, __ATOMIC_RELAXED, __HIP_MEMORY_SCOPE_AGENT))) {
            __builtin_amdgcn_s_sleep(1);
            if (++spins > 400000u) break;
          }
        }
      }
      STAMP(2);
      BAR();
      if constexpr (first) {
#pragma unroll
        for (int ro = 0; ro < 2; ++ro) {
          const int oi = min(kh * 4 + ro * 2 + h, 6);
          res[ro] = __builtin_amdgcn_raw_buffer_load_b128(rx0, tok * (D * 2) + (7 * cb + oi) * 16, tt2 * 32 * (D * 2), 0);
        }
      }
      f32x16 aq = {};
      const int sbq = first ? opqv(opqv(w1lane) + 1 * UNIT) : opqv(opqv(l16) + 1 * UNIT);
      pipe<28, first ? 2 : 1>([&](auto I) AI { return rdw(sbq, I.value); }, [&](auto) AI {},
               [&](auto I, v4u a, auto nx) AI { aq = mfma(a, x[I.value], aq); nx(); });
      if constexpr (first) wait_arr(LD, 1, 1);
#pragma unroll
      for (int qd = 0; qd < 4; ++qd) cs[qd] = *(LAS f32x4*)(LD + csb + 256 + 32 * qd);
#pragma unroll
      for (int r = 0; r < 16; ++r) aq[r] = fmaf(nmean, cs[r >> 2][r & 3], aq[r]);
      f32x16 p2 = {};
      const int wb2 = opqv(L_WPHI + opqv(l16));
      p2 = mfma(lds16(LD, wb2), pack8(aq, 0), p2);
      p2 = mfma(lds16(LD, wb2 + 1024), pack8(aq, 1), p2);
      half_softmax<0>(p2, h, rstd, pq[0], pq[1]);
      half_softmax<1>(p2, h, rstd, pq[2], pq[3]);
    };
    phase1(std::integral_constant<bool, L == 0>{});
    STAMP(3);
    if (w == 0) wait_cnt(ws, C_KV + L * 16 + j, 32);
    BAR();
    STAMP(4);
    kv_reduce(rs, LD, L, j, w, lane);
    BAR();
    STAMP(5);
    {
      f32x16 nm = {};
      const int kb = opqv(L_KVF + opqv(l16));
      v4u kf[4];
#pragma unroll
      for (int s = 0; s < 4; ++s) kf[s] = lds16(LD, kb + s * 1024);
#pragma unroll
      for (int s = 0; s < 4; ++s) nm = mfma(kf[s], pq[s], nm);
      auto r = __builtin_amdgcn_permlane32_swap(__float_as_uint(nm[12]), __float_as_uint(nm[12]), false, false);
      const float den = __uint_as_float(r[1]) + 1e-6f;
      const float rd = __builtin_amdgcn_rcpf(den);
#pragma unroll
      for (int i = 0; i < 16; ++i) nm[i] *= rd;
      const int oo = opq(OFF_O + L * OB + (tt * 32 + 2 * j) * 1024);
      st16(rs, l16, oo, pack8(nm, 0));
      st16(rs, l16, oo + 1024, pack8(nm, 1));
    }
    wave_signal(ws, C_O + (L * 64 + tt) * 2 + (j >> 3), lane);
    STAMP(6);
    wait_cnt(ws, C_O + (L * 64 + tt2) * 2 + kh, 8, true);
    STAMP(7);
    {
      const int xo = opq(OFF_O + L * OB + (tt2 * 32 + 16 * kh) * 1024);
#pragma unroll
      for (int t = 0; t < (XW < 16 ? XW : 16); ++t) x[t] = ld16(rs, l16, xo + t * 1024);
      if (L == 0) RAWBAR();
      f32x16 a0 = {}, a1 = {};
      const int sb2 = opqv(2 * UNIT + kh * 2048 + opqv(l16)), sbx = opqv(L_W2X + kh * 2048 + opqv(l16));
      pipe<32>([&](auto I) AI { constexpr int i = I.value, t = i >> 1; return lds16(LD, (t < 14 ? sb2 + t * 4096 : sbx + (t - 14) * 4096) + (i & 1) * 1024); },
               [&](auto I) AI { if constexpr (I.value == 14) RAWBAR();   },
               [&](auto I, v4u a, auto nx) AI {
                 constexpr int i = I.value;
                 if constexpr (i & 1) a1 = mfma(a, x[i >> 1], a1); else a0 = mfma(a, x[i >> 1], a0);
                 nx();
                 if constexpr (!(i & 1) && (i >> 1) + XW < 16) x[(i >> 1) + XW] = ld16(rs, l16, xo + ((i >> 1) + XW) * 1024);
               });
      const int xb = opqv(L_SCR + w * 4096 + opqv(l16)), xb2 = opqv(L_SCR + (w ^ 2) * 4096 + opqv(l16));
      f32x16 fin;
      if (kh) {
#pragma unroll
        for (int qd = 0; qd < 4; ++qd) *(LAS f32x4*)(LD + xb + qd * 1024) = f32x4{a0[4 * qd], a0[4 * qd + 1], a0[4 * qd + 2], a0[4 * qd + 3]};
        fin = a1;
      } else {
#pragma unroll
        for (int qd = 0; qd < 4; ++qd) *(LAS f32x4*)(LD + xb + qd * 1024) = f32x4{a1[4 * qd], a1[4 * qd + 1], a1[4 * qd + 2], a1[4 * qd + 3]};
        fin = a0;
      }
      BAR();
#pragma unroll
      for (int qd = 0; qd < 4; ++qd) {
        const f32x4 v = *(LAS f32x4*)(LD + xb2 + qd * 1024);
#pragma unroll
        for (int i = 0; i < 4; ++i) fin[4 * qd + i] += v[i];
      }
      finish_tile(fin, res, rs, opq(OFF_X1 + L * XB), opq(OFF_ST1 + L * STB), tt2, cb, kh, lane, false, A.out);
    }
    wave_signal(ws, C_X1 + L * 64 + tt2, lane);
    STAMP(8);
    wait_cnt(ws, C_X1 + L * 64 + tt, 16, true);
    STAMP(9);
    {
      const int xo = opq(OFF_X1 + L * XB + tt * 28 * 1024);
      const int so_ = opq(OFF_ST1 + L * STB + tt * 4096);
      const int sv_ = (opqv(lane) & 31) * 8 + h * 2048;
      constexpr int XW3 = 8;
#pragma unroll
      for (int ks = 0; ks < XW3; ++ks) x[ks] = ld16(rs, l16, xo + ks * 1024);
      const int ho = opq(OFF_H + L * HB + (tt * 112 + 7 * j) * 1024);
      const int lvo = opq(C_H + (L * 64 + tt) * 8);
      u32* fp = cnt_ptr(ws, C_H + (L * 64 + tt2) * 8);
      auto poll = [&](int lv) AI { return __hip_atomic_load(fp + 32 * lv, __ATOMIC_RELAXED, __HIP_MEMORY_SCOPE_AGENT); };
      u32 f;
      float nmean, gs, irs;
      const int csb = opqv(L_CS3 + 16 * (opqv(lane) >> 5));
      const int sbA = opqv(l16), sbB = opqv(opqv(l16) + 2 * UNIT);
      f32x4 Gcg[2], Gcu[2]; float Gt[8], Gyu[8]; u32 Go[4];
      auto glu = [&](const f32x16& a, auto N_, auto S_) AI {
        constexpr int n = N_.value, s = S_.value;
        if constexpr (s == 1) Gcu[0] = *(LAS f32x4*)(LD + csb + n * 128 + 32);
        if constexpr (s == 4) Gcg[1] = *(LAS f32x4*)(LD + csb + n * 128 + 64);
        if constexpr (s == 9) Gcu[1] = *(LAS f32x4*)(LD + csb + n * 128 + 96);
        if constexpr (s == 12 && n < 6) Gcg[0] = *(LAS f32x4*)(LD + csb + (n + 1) * 128);
        static_for<8>([&](auto V_) AI {
          constexpr int v = V_.value, k = s - (1 + 4 * (v >> 1) + (v & 1)), hf = v >> 2, i = v & 3;
          if constexpr (k == 0) { Gt[v] = fmaf(nmean, Gcg[hf][i], a[8 * hf + i]); PIN(Gt[v]); }
          if constexpr (k == 1) { Gt[v] = Gt[v] * gs; PIN(Gt[v]); }
          if constexpr (k == 2) { Gt[v] = __builtin_amdgcn_exp2f(Gt[v]); PIN(Gt[v]); }
          if constexpr (k == 3) { Gt[v] = fmaf(Gt[v], irs, irs); PIN(Gt[v]); }
          if constexpr (k == 4) { Gt[v] = __builtin_amdgcn_rcpf(Gt[v]); PIN(Gt[v]); }
          if constexpr (k == 5) { Gyu[v] = fmaf(nmean, Gcu[hf][i], a[8 * hf + 4 + i]); PIN(Gyu[v]); }
          if constexpr (k == 6) { Gt[v] = Gt[v] * Gyu[v]; PIN(Gt[v]); }
          if constexpr (k == 7 && (v & 1)) { Go[v >> 1] = pk2(Gt[v - 1], Gt[v]); PIN(Go[v >> 1]); }
        });
        if constexpr (s == 21) st16(rs, l16, ho + n * 1024, v4u{Go[0], Go[1], Go[2], Go[3]});
      };
      const int ho2 = opq(OFF_H + L * HB + tt2 * 112 * 1024 + kh * 7168);
      v4u hx[28];
      auto ldh = [&](auto T) AI { hx[T.value] = ld16(rs, l16, ho2 + p4_koff(p4_oc(T.value))); };
      f32x16 acc[7];
      constexpr int PB = 7 * PK;
      pipe<196>(
          [&](auto I) AI { constexpr int i = I.value, u = i / 28, sl = u & 3; return lds16(LD, (sl < 2 ? sbA : sbB) + (sl & 1) * UNIT + (i % 28) * 1024); },
          [&](auto I) AI { if constexpr (I.value > 28 && I.value % 28 == 6) { VSTAMP(30 + I.value / 28); RAWBAR(); WSTAMP(22 + I.value / 28); } },
          [&](auto I, v4u a, auto nx) AI {
            constexpr int i = I.value;
            if constexpr (i < PB) {
              constexpr int n = i % 7, ks = i / 7;
              if constexpr (ks == 0) { f32x16 z = {}; acc[n] = mfma(a, x[ks], z); } else acc[n] = mfma(a, x[ks], acc[n]);
              nx();
              constexpr int K2 = 28 - XW3 - PK, fx = ks < K2 ? XW3 + 2 * ks + (n ? 1 : 0) : XW3 + K2 + ks;
              if constexpr (n == 0 || (n == 3 && ks < K2)) x[fx] = ld16(rs, l16, xo + fx * 1024);
              if constexpr (n >= 5 && ks < 4) st[2 * ks + n - 5] = ld8(rs, sv_ + (2 * ks + n - 5) * 256, so_);
              if constexpr (i == 46) {
                ln_stats(st, mean, rstd);
                nmean = -mean; gs = -LOG2E * rstd; irs = __builtin_amdgcn_rcpf(rstd);
                PIN(nmean); PIN(gs); PIN(irs);
              }
            } else {
              constexpr int r = i - PB, n = r / MK, s = r % MK;
              acc[n] = mfma(a, x[PK + s], acc[n]);
              nx();
              if constexpr (r == 8) Gcg[0] = *(LAS f32x4*)(LD + csb);
              if constexpr (n >= 2 && s + MK < 22) glu(acc[n - 2], IC<n - 2>{}, IC<s + MK>{});
              if constexpr (n >= 1) glu(acc[n - 1], IC<n - 1>{}, IC<s>{});
              if constexpr (n >= 3 && s == 20 - MK) { VMCNT(0); signal_cnt(ws, lvo + n - 3); }
            }
          });
      STAMP(10);
      arrive_lds(LD, 2, lane);
      static_for<22>([&](auto S_) AI {
        if constexpr (S_.value + MK < 22) glu(acc[5], IC<5>{}, IC<S_.value + MK>{});
        glu(acc[6], IC<6>{}, S_);
        if constexpr (S_.value == 4) { VMCNT(0); signal_cnt(ws, lvo + 4); }
        if constexpr (S_.value == CP1) f = poll_issue(fp + 96);
      });
      STAMP(11);
      const int wo_ = opqv(kh * 3072 + opqv(l16)), wp_ = opqv(kh * 1024 + 1024 + opqv(l16));
      const int wo2 = opqv(wo_ + 3 * UNIT), wp2 = opqv(wp_ + 3 * UNIT);
      v4u wq[56];
      auto ldw = [&](auto K) AI {
        constexpr int k = K.value, t = k >> 1, wb = p4_wbase(t);
        if constexpr (wb >= 3 * UNIT) wq[k] = lds16(LD, ((k & 1) ? wp2 : wo2) + wb - 3 * UNIT);
        else wq[k] = lds16(LD, ((k & 1) ? wp_ : wo_) + wb);
      };
      VMCNT(1); signal_cnt(ws, lvo + 5);
      PIN(f);
      const u32 f2 = poll(4);
      if (__all(f >= 16u)) {
        static_for<4>([&](auto T) AI { ldh(T); });
        VMCNT(6); signal_cnt(ws, lvo + 6);
      } else {
        VMCNT(2); signal_cnt(ws, lvo + 6);
        wait_flags(ws, fp + 96, f);
        static_for<4>([&](auto T) AI { ldh(T); });
      }
      wait_flags(ws, fp + 128, f2);
      static_for<8>([&](auto T) AI { ldh(IC<4 + T.value>{}); });
      wait_arr(LD, 4, 4 * L + 4);
      static_for<4>([&](auto K) AI { ldw(K); });
      STAMP(12);
      f32x16 fin = {}, oth = {};
      static_for<28>([&](auto T) AI {
        constexpr int t = T.value;
        if constexpr (t == CP5) f = poll(5);
        if constexpr (t == CT5) {
          wait_flags(ws, fp + 160, f);
          f = poll(6);
          static_for<8>([&](auto K) AI { ldh(IC<12 + K.value>{}); });
          STAMP(13);
        }
        if constexpr (t == 12) {
          wait_flags(ws, fp + 192, f);
          static_for<8>([&](auto K) AI { ldh(IC<20 + K.value>{}); });
          STAMP(22);
        }
        if constexpr (t == 12) arrive_lds(LD, 3, lane);
        if constexpr (t + 2 < 28) { ldw(IC<2 * t + 4>{}); ldw(IC<2 * t + 5>{}); }
        oth = mfma(wq[2 * t + 1], hx[t], oth);
        fin = mfma(wq[2 * t], hx[t], fin);
        SB();
      });
      STAMP(14);
      wait_arr(LD, 3, 4 * L + 4);
      put16(oth, LD, opqv(L_SCR + w * 4096 + opqv(l16)));
      BAR();
#pragma unroll
      for (int c = 0; c < 4; ++c) add16(fin, LD, opqv(p4_part(w, c) + opqv(l16)));
      add16(fin, LD, opqv(L_SCR + (w ^ 2) * 4096 + opqv(l16)));
      STAMP(15);
      finish_tile(fin, res, rs, OFF_X2, OFF_ST2, tt2, cb, kh, lane, L == 1, A.out);
    }
    if (L == 0) {
      wave_signal(ws, C_X2 + tt2, lane);
      arrive_lds(LD, 0, lane);
      STAMP(16);
      wait_cnt(ws, C_X2 + tt, 16, true);
    }
    STAMP(17);
  };
  clayer(IC<0>{});
  clayer(IC<1>{});
}

extern "C" void kernel_launch(void* const* d_in, const int* in_sizes, int n_in, void* d_out, int out_size, void* d_ws,
                              size_t ws_size, hipStream_t stream) {
  Args a;
  a.x0 = (const u16*)d_in[0]; a.wqkv = (const u16*)d_in[1]; a.wphi = (const u16*)d_in[2]; a.wo = (const u16*)d_in[3];
  a.wg = (const u16*)d_in[5]; a.wu = (const u16*)d_in[6]; a.wd = (const u16*)d_in[7];
  a.ws = (char*)d_ws; a.out = (u16*)d_out;
  (void)hipFuncSetAttribute((const void*)k_main, hipFuncAttributeMaxDynamicSharedMemorySize, LDS_BYTES);
  k_main<<<256, 512, LDS_BYTES, stream>>>(a);
}
```

```cpp
#include <hip/hip_runtime.h>
#include <stdint.h>
#include <utility>
#include <type_traits>

typedef unsigned short u16;
typedef unsigned int u32;
typedef u32 v4u __attribute__((ext_vector_type(4)));
typedef u32 v2u __attribute__((ext_vector_type(2)));
typedef float f32x2 __attribute__((ext_vector_type(2)));
typedef float f32x4 __attribute__((ext_vector_type(4)));
typedef float f32x16 __attribute__((ext_vector_type(16)));
typedef __bf16 bf16x2 __attribute__((ext_vector_type(2)));
typedef __bf16 bf16x8 __attribute__((ext_vector_type(8)));
typedef short s16x4 __attribute__((ext_vector_type(4)));
#define LAS __attribute__((address_space(3)))
#define DI __device__ __forceinline__
#define AI __attribute__((always_inline))
template <int V> using IC = std::integral_constant<int, V>;

constexpr int S = 2048, D = 448, FF = 1792;
constexpr int UNIT = 28672;

constexpr int C_KV = 0;
constexpr int C_W2 = 32;
constexpr int C_W4 = 36;
constexpr int C_W3 = 40;
constexpr int C_ERR = 60;
constexpr int C_X2 = 64;
constexpr int C_X1 = 128;
constexpr int C_O = 256;
constexpr int C_H = 512;
constexpr int C_W1 = 1536;
constexpr int NZ = 25;
constexpr int EPOCH_OFF = NZ * 64 * 128;
constexpr int INIT_OFF = EPOCH_OFF + 256;
constexpr int CTL_BYTES = EPOCH_OFF + 4096;
constexpr int W2B = 65536;
constexpr int OFF_W1 = CTL_BYTES;
constexpr int OFF_W2 = OFF_W1 + 16 * 3 * UNIT;
constexpr int OFF_W3 = OFF_W2 + 8 * W2B;
constexpr int OFF_W4 = OFF_W3 + 16 * 7 * UNIT;
constexpr int OFF_CS3 = OFF_W4 + 8 * 8 * UNIT;
constexpr int XB = S * D * 2;
constexpr int OB = S * 512 * 2;
constexpr int OFF_X2 = OFF_CS3 + 16 * 28 * 896;
constexpr int OFF_X1 = OFF_X2 + XB;
constexpr int OFF_O = OFF_X1 + 2 * XB;
constexpr int STB = S * 16 * 8;
constexpr int OFF_ST2 = OFF_O + 2 * OB;
constexpr int OFF_ST1 = OFF_ST2 + STB;
constexpr int KVB = 16 * 16 * 4096;
constexpr int OFF_KVP = OFF_ST1 + 2 * STB;
constexpr int HB = S * FF * 2;
constexpr int OFF_H = OFF_KVP + 2 * KVB;
constexpr int WS_USED = OFF_H + 2 * HB;

constexpr int L_SCR = 4 * UNIT;
constexpr int PHK_STRIDE = 136, V_STRIDE = 72;
constexpr int L_PHK = L_SCR;
constexpr int L_V = L_SCR + 128 * PHK_STRIDE;
constexpr int L_KVF = L_SCR + 28672;
constexpr int L_W2X = L_KVF + 4096;
constexpr int L_CONST = 155648;
constexpr int L_CS1 = L_CONST, L_CS3 = L_CONST + 384, L_WPHI = L_CONST + 1280, L_ARR = L_CONST + 3328;
constexpr int L_STAGE = 2 * UNIT;
constexpr int STG = 8704;
constexpr int LDS_BYTES = 163840;

constexpr float LOG2E = 1.4426950408889634f;
#define PKV 12
constexpr int PK = PKV, MK = 28 - PK;
constexpr int LAG = 4;

DI u32 pk2(float a, float b) { f32x2 v = {a, b}; return __builtin_bit_cast(u32, __builtin_convertvector(v, bf16x2)); }
DI float bflo(u32 v) { return __uint_as_float(v << 16); }
DI float bfhi(u32 v) { return __uint_as_float(v & 0xffff0000u); }
DI f32x16 mfma(v4u a, v4u b, f32x16 c) {
  f32x16 d = __builtin_amdgcn_mfma_f32_32x32x16_bf16(__builtin_bit_cast(bf16x8, a), __builtin_bit_cast(bf16x8, b), c, 0, 0, 0);
  asm volatile("" : "+v"(d));
  __builtin_amdgcn_sched_barrier(0);
  return d;
}
DI float xsum(float v) {
  auto r = __builtin_amdgcn_permlane32_swap(__float_as_uint(v), __float_as_uint(v), false, false);
  return __uint_as_float(r[0]) + __uint_as_float(r[1]);
}
DI float xmax(float v) {
  auto r = __builtin_amdgcn_permlane32_swap(__float_as_uint(v), __float_as_uint(v), false, false);
  return fmaxf(__uint_as_float(r[0]), __uint_as_float(r[1]));
}
DI float xmin(float v) {
  auto r = __builtin_amdgcn_permlane32_swap(__float_as_uint(v), __float_as_uint(v), false, false);
  return fminf(__uint_as_float(r[0]), __uint_as_float(r[1]));
}
#define BAR() asm volatile("s_waitcnt lgkmcnt(0)\n\ts_barrier" ::: "memory")
#define RAWBAR() asm volatile("s_barrier" ::: "memory")
#define VMCNT(n) asm volatile("s_waitcnt vmcnt(" #n ")" ::: "memory")
#define SB() __builtin_amdgcn_sched_barrier(0)
#define PIN(x) asm volatile("" : "+v"(x))

typedef __amdgpu_buffer_rsrc_t rsrc_t;
DI v4u ld16(rsrc_t rs, int voff, int soff) { return __builtin_amdgcn_raw_buffer_load_b128(rs, voff, soff, 16); }
DI v2u ld8(rsrc_t rs, int voff, int soff) { return __builtin_amdgcn_raw_buffer_load_b64(rs, voff, soff, 16); }
DI u32 ld4(rsrc_t rs, int voff, int soff) { return __builtin_amdgcn_raw_buffer_load_b32(rs, voff, soff, 16); }
DI void st16(rsrc_t rs, int voff, int soff, v4u v) {
  __builtin_amdgcn_raw_buffer_store_b128(v, rs, voff, soff, 16);
  asm volatile("s_nop 1" ::"v"(v) : "memory");
}
DI void st8(rsrc_t rs, int voff, int soff, v2u v) { __builtin_amdgcn_raw_buffer_store_b64(v, rs, voff, soff, 16); }
DI void st4(rsrc_t rs, int voff, int soff, u32 v) { __builtin_amdgcn_raw_buffer_store_b32(v, rs, voff, soff, 16); }
DI int opq(int v) { asm volatile("" : "+s"(v)); return v; }
DI int opqv(int v) { asm volatile("" : "+v"(v)); return v; }
DI v4u lds16(LAS char* l, int off) { return *(LAS v4u*)(l + off); }
DI float dot2(u32 a, u32 b, float c) {
  return __builtin_amdgcn_fdot2_f32_bf16(__builtin_bit_cast(bf16x2, a), __builtin_bit_cast(bf16x2, b), c, false);
}
DI v4u tr_frag(LAS char* L, int base, int step) {
  v2u lo = __builtin_bit_cast(v2u, __builtin_amdgcn_ds_read_tr16_b64_v4i16((LAS s16x4*)(L + base)));
  v2u hi = __builtin_bit_cast(v2u, __builtin_amdgcn_ds_read_tr16_b64_v4i16((LAS s16x4*)(L + base + step)));
  return v4u{lo.x, lo.y, hi.x, hi.y};
}
DI float sum8(v4u o) {
  const u32 one = 0x3f803f80u;
  return dot2(o.x, one, dot2(o.y, one, 0.f)) + dot2(o.z, one, dot2(o.w, one, 0.f));
}

DI u32* cnt_ptr(char* ws, int idx) { return (u32*)(ws + idx * 128); }
#define POLL_D0 16
#define LATE_ARR
#define WOV 0
#define WOS 20
#define H2 6
#define H3 12
#define CP1 12
#define CP5 0
#define CT5 11
#define PT5 8
#define ROT 1
#define POLL_IV 1
DI bool spin_more(char* ws, u32& spins) {
  u32* e = cnt_ptr(ws, C_ERR);
  __builtin_amdgcn_s_sleep(POLL_IV);
  ++spins;
  if ((spins & 255u) == 0u) {
    if (__builtin_amdgcn_readfirstlane(__hip_atomic_load(e, __ATOMIC_RELAXED, __HIP_MEMORY_SCOPE_AGENT)) != 0) return false;
    if (spins > 400000u) { __hip_atomic_store(e, 1u, __ATOMIC_RELAXED, __HIP_MEMORY_SCOPE_AGENT); return false; }
  }
  return true;
}
DI void wait_cnt(char* ws, int idx, u32 target, bool delay = false) {
  u32* c = cnt_ptr(ws, idx);
  u32 spins = 0;
  if (delay && POLL_D0) __builtin_amdgcn_s_sleep(POLL_D0);
  while ((u32)__builtin_amdgcn_readfirstlane(__hip_atomic_load(c, __ATOMIC_RELAXED, __HIP_MEMORY_SCOPE_AGENT)) < target)
    if (!spin_more(ws, spins)) break;
  __builtin_amdgcn_fence(__ATOMIC_ACQUIRE, "wavefront");
  asm volatile("" ::: "memory");
}
DI void wait_flags(char* ws, u32* fp, u32 f, u32 target = 16u) {
  if (__builtin_expect(!__all(f >= target), 0)) {
    u32 spins = 0;
    while (spin_more(ws, spins))
      if (__all(__hip_atomic_load(fp, __ATOMIC_RELAXED, __HIP_MEMORY_SCOPE_AGENT) >= target)) break;
  }
  __builtin_amdgcn_fence(__ATOMIC_ACQUIRE, "wavefront");
  asm volatile("" ::: "memory");
}
DI u32 poll_issue(const u32* p) { u32 f; asm volatile("global_load_dword %0, %1, off sc1" : "=v"(f) : "v"(p) : "memory"); return f; }
DI void signal_cnt(char* ws, int idx) {
  unsigned long long sv;
  asm volatile("s_mov_b64 %0, exec\n\ts_mov_b64 exec, 1\n\tglobal_atomic_add %1, %2, %3\n\ts_mov_b64 exec, %0"
               : "=&s"(sv) : "v"(idx * 128), "v"(1), "s"(ws) : "memory");
}
DI void wave_signal(char* ws, int idx, int lane) { VMCNT(0); signal_cnt(ws, idx); }
DI void arrive_lds(LAS char* LD, int slot, int lane) {
  if (lane == 0) __hip_atomic_fetch_add((LAS u32*)(LD + L_ARR + slot * 4), 1u, __ATOMIC_RELAXED, __HIP_MEMORY_SCOPE_WORKGROUP);
}
DI void wait_arr(LAS char* LD, int slot, u32 target) {
  while ((u32)__builtin_amdgcn_readfirstlane(__hip_atomic_load((LAS u32*)(LD + L_ARR + slot * 4), __ATOMIC_RELAXED, __HIP_MEMORY_SCOPE_WORKGROUP)) < target)
    __builtin_amdgcn_s_sleep(1);
}

#define STAMP(i)
#define HSTAMP(i)
#define VSTAMP(i)
#define WSTAMP(i)
#define USTAMP(i)

#define DMA_STEP(GSTEP) \
      "s_add_u32 m0, m0, 0x1000\n\t" \
      "v_add_u32 %1, " GSTEP ", %1\n\t" \
      "global_load_lds_dwordx4 %1, %2 sc1\n\t"
#define DMA_FN(NAME, BODY) \
DI void NAME(const char* gsrc, u32 voff, u32 lds_dst) { \
  u32 keep; \
  asm volatile("s_mov_b32 %0, m0\n\ts_mov_b32 m0, %3\n\ts_nop 0\n\tglobal_load_lds_dwordx4 %1, %2 sc1\n\t" BODY "s_mov_b32 m0, %0" \
               : "=&s"(keep), "+v"(voff) : "s"(gsrc), "s"(lds_dst) : "memory", "scc"); }
DMA_FN(dma7, DMA_STEP("0x1000") DMA_STEP("0x1000") DMA_STEP("0x1000") DMA_STEP("0x1000") DMA_STEP("0x1000") DMA_STEP("0x1000"))
DMA_FN(dma2, DMA_STEP("0x1000"))
#define PACE 1
#define STR2(x) #x
#define STR(x) STR2(x)
#define PSTEP "s_sleep " STR(PACE) "\n\t" DMA_STEP("0x1000")
DMA_FN(dma7p, PSTEP PSTEP PSTEP PSTEP PSTEP PSTEP)
DMA_FN(dma7n, DMA_STEP("0x2a000") DMA_STEP("0x2a000") DMA_STEP("0x2a000") DMA_STEP("0x2a000") DMA_STEP("0x2a000") DMA_STEP("0x2a000"))

template <int LO, int GO>
DI void dma1(const char* gsrc, u32 voff, u32 lb) {
  u32 keep, tmp;
  asm volatile("s_mov_b32 %0, m0\n\ts_add_u32 m0, %4, %5\n\tv_add_u32 %1, %6, %2\n\tglobal_load_lds_dwordx4 %1, %3 sc1\n\ts_mov_b32 m0, %0"
               : "=&s"(keep), "=&v"(tmp) : "v"(voff), "s"(gsrc), "s"(lb), "i"(LO), "i"(GO) : "memory", "scc");
}

template <typename F, int... I>
DI void static_for_impl(F&& f, std::integer_sequence<int, I...>) { (f(IC<I>{}), ...); }
template <int N, typename F>
DI void static_for(F&& f) { static_for_impl(f, std::make_integer_sequence<int, N>{}); }

constexpr int PF = 6;
#define WEV 2
template <int N, int RPS = 1, typename RD, typename PRE, typename ST>
DI void pipe(RD rd, PRE pre, ST step) {
  v4u buf[PF];
  static_for<PF>([&](auto I) AI { buf[I.value] = rd(I); });
  SB();
  static_for<N>([&](auto I) AI {
    constexpr int i = I.value;
    pre(I);
    if constexpr (WEV > 1 && i % WEV == 0 && i + PF <= N) __builtin_amdgcn_s_waitcnt(0xC07F | (((PF - WEV) * RPS) << 8));
    const v4u a = buf[i % PF];
    step(I, a, [&]() AI { if constexpr (i + PF < N) buf[i % PF] = rd(IC<i + PF>{}); SB(); });
    SB();
  });
}

DI void ln_finish(float s, float q, float& mean, float& rstd) {
  s = xsum(s); q = xsum(q);
  mean = s * (1.f / 448.f);
  const float var = fmaxf(q * (1.f / 448.f) - mean * mean, 0.f);
  rstd = __builtin_amdgcn_rsqf(var + 1e-5f);
}
DI void ln_stats(const v2u (&st)[8], float& mean, float& rstd) {
  f32x2 a = __builtin_bit_cast(f32x2, st[0]), b = __builtin_bit_cast(f32x2, st[1]);
#pragma unroll
  for (int i = 2; i < 8; i += 2) { a += __builtin_bit_cast(f32x2, st[i]); b += __builtin_bit_cast(f32x2, st[i + 1]); }
  a += b;
  ln_finish(a[0], a[1], mean, rstd);
}
DI v4u pack8(const f32x16& a, int s) {
  v4u r = {pk2(a[8 * s], a[8 * s + 1]), pk2(a[8 * s + 2], a[8 * s + 3]), pk2(a[8 * s + 4], a[8 * s + 5]), pk2(a[8 * s + 6], a[8 * s + 7])};
  return r;
}
template <int SG>
DI void half_softmax(const f32x16& p, int h, float rstd, v4u& o0, v4u& o1) {
  auto mm = [](float a, float b) AI { return SG ? fminf(a, b) : fmaxf(a, b); };
  float m0 = mm(mm(p[0], p[1]), p[2]), m1 = mm(mm(p[3], p[4]), p[5]), m2 = mm(mm(p[6], p[7]), p[8]), m3 = mm(mm(p[9], p[10]), p[11]);
  float m4 = mm(mm(mm(p[12], p[13]), p[14]), p[15]);
  m4 = h ? p[0] : m4;
  float mx = mm(mm(mm(m0, m1), m2), mm(m3, m4));
  mx = SG ? xmin(mx) : xmax(mx);
  const float c = rstd * LOG2E;
  const float cc = SG ? -c : c, off = -mx * cc;
  f32x16 e;
  float s0 = 0.f, s1 = 0.f;
#pragma unroll
  for (int r = 0; r < 16; ++r) {
    float v = __builtin_amdgcn_exp2f(fmaf(p[r], cc, off));
    if (r >= 12) v = h ? 0.f : v;
    e[r] = v;
    if (r & 1) s1 += v; else s0 += v;
  }
  const float rs_ = __builtin_amdgcn_rcpf(xsum(s0 + s1));
#pragma unroll
  for (int r = 0; r < 16; ++r) e[r] *= rs_;
  o0 = pack8(e, 0); o1 = pack8(e, 1);
  PIN(o0); PIN(o1);
  SB();
}

DI void finish_tile(const f32x16& acc, v4u (&res)[2], rsrc_t rs, int xoff, int stoff, int tt, int cb, int nt, int lane, bool last, u16* out) {
  lane = opqv(lane);
  const int h = lane >> 5, tok = lane & 31;
#pragma unroll
  for (int ro = 0; ro < 2; ++ro) {
    v4u r = res[ro];
    v4u o;
    o.x = pk2(bflo(r.x) + acc[8 * ro + 0], bfhi(r.x) + acc[8 * ro + 1]);
    o.y = pk2(bflo(r.y) + acc[8 * ro + 2], bfhi(r.y) + acc[8 * ro + 3]);
    o.z = pk2(bflo(r.z) + acc[8 * ro + 4], bfhi(r.z) + acc[8 * ro + 5]);
    o.w = pk2(bflo(r.w) + acc[8 * ro + 6], bfhi(r.w) + acc[8 * ro + 7]);
    res[ro] = o;
    const int oi = nt * 4 + ro * 2 + h;
    if (oi < 7) {
      const int go = 7 * cb + oi;
      if (last) *(v4u*)(out + (size_t)(tt * 32 + tok) * D + go * 8) = o;
      else st16(rs, ((go >> 1) * 64 + tok + 32 * (go & 1)) * 16, xoff + tt * 28 * 1024, o);
    }
  }
  if (!last) {
    const bool v1 = !(nt == 1 && h == 1);
    const u32 one = 0x3f803f80u;
    float s0 = dot2(res[0].x, one, 0.f), s1 = dot2(res[1].x, one, 0.f), q0 = dot2(res[0].x, res[0].x, 0.f), q1 = dot2(res[1].x, res[1].x, 0.f);
    s0 = dot2(res[0].y, one, s0); s1 = dot2(res[1].y, one, s1); q0 = dot2(res[0].y, res[0].y, q0); q1 = dot2(res[1].y, res[1].y, q1);
    s0 = dot2(res[0].z, one, s0); s1 = dot2(res[1].z, one, s1); q0 = dot2(res[0].z, res[0].z, q0); q1 = dot2(res[1].z, res[1].z, q1);
    s0 = dot2(res[0].w, one, s0); s1 = dot2(res[1].w, one, s1); q0 = dot2(res[0].w, res[0].w, q0); q1 = dot2(res[1].w, res[1].w, q1);
    const float sum = xsum(s0 + (v1 ? s1 : 0.f));
    const float sq = xsum(q0 + (v1 ? q1 : 0.f));
    if (h == 0) st8(rs, tok * 8, stoff + ((tt * 8 + cb) * 2 + nt) * 256, v2u{__float_as_uint(sum), __float_as_uint(sq)});
  }
}

DI void kv_reduce(rsrc_t rs, LAS char* LD, int L, int j, int w, int lane) {
  const int ko = opq(OFF_KVP + L * KVB + j * 16 * 4096 + (w >> 1) * 1024 + (w & 1) * 512);
  const int lz = opqv(lane);
  const int vo = (lz >> 5) * 4096 + (lz & 31) * 16;
  v4u t[8];
#pragma unroll
  for (int i = 0; i < 8; ++i) t[i] = ld16(rs, vo, ko + i * 8192);
  float s[8] = {};
  const u32 lo1 = (u32)opq(0x00003f80), hi1 = (u32)opq(0x3f800000);
#pragma unroll
  for (int i = 0; i < 8; ++i) {
#pragma unroll
    for (int c = 0; c < 4; ++c) { s[2 * c] = dot2(t[i][c], lo1, s[2 * c]); s[2 * c + 1] = dot2(t[i][c], hi1, s[2 * c + 1]); }
  }
#pragma unroll
  for (int c = 0; c < 8; ++c) s[c] = xsum(s[c]);
  if (lz < 32) *(LAS v4u*)(LD + L_KVF + (w >> 1) * 1024 + (w & 1) * 512 + lz * 16) = v4u{pk2(s[0], s[1]), pk2(s[2], s[3]), pk2(s[4], s[5]), pk2(s[6], s[7])};
}

constexpr int p4_oh(int t) { return 4 * t; }
constexpr int p4_oc(int t) { return 56 + 2 * t; }
constexpr int p4_koff(int o) { return (7 * (o & 15) + (o >> 4)) * 1024; }
constexpr int p4_wbase(int t) { return (t < 7 ? 0 : t < 14 ? L_SCR : t < 21 ? 3 * UNIT : UNIT) + (t % 7) * 4096; }
DI int p4_part(int a, int hw) {
  const int i = ((a >> 1) | ((a & 1) << 1)) * 4 + hw;
  return i < 7 ? 2 * UNIT + i * 4096 : i < 10 ? L_KVF + (i - 7) * 4096 : (i - 10) * 4096;
}
DI void put16(const f32x16& acc, LAS char* LD, int off) {
#pragma unroll
  for (int qd = 0; qd < 4; ++qd) *(LAS f32x4*)(LD + off + qd * 1024) = f32x4{acc[4 * qd], acc[4 * qd + 1], acc[4 * qd + 2], acc[4 * qd + 3]};
}
DI void add16(f32x16& acc, LAS char* LD, int off) {
#pragma unroll
  for (int qd = 0; qd < 4; ++qd) {
    const f32x4 v = *(LAS f32x4*)(LD + off + qd * 1024);
#pragma unroll
    for (int i = 0; i < 4; ++i) acc[4 * qd + i] += v[i];
  }
}

struct Args {
  const u16* x0; const u16* wqkv; const u16* wphi; const u16* wo; const u16* wg; const u16* wu; const u16* wd;
  char* ws; u16* out;
};

struct Retile { const v4u* pa[7]; int rd0, rd1, step, o0, o1, ocb, sig; };
DI void retile_prep(const Args& A, int job, int lane, int stage, Retile& R) {
  const int h = lane >> 5, q = (lane & 15) >> 2, c8 = ((lane >> 4) & 1) * 4 + (lane & 3);
  const int hh = c8 & 1, quad = c8 >> 1;
  if (job < 64 || job >= 512) {
    const bool iswo = job < 64;
    const int a0 = iswo ? job >> 2 : (job - 512) >> 2, cg = job & 3;
    const u16* src = (iswo ? A.wo + (size_t)a0 * 28 * D : A.wd + (size_t)a0 * 32 * D) + 112 * cg;
    const int nrow = iswo ? 28 : 32;
#pragma unroll
    for (int i = 0; i < 7; ++i) {
      const int p = lane + 64 * i, r = p / 14, c = p % 14;
      R.pa[i] = (const v4u*)(src + (size_t)(r < nrow ? r : 0) * D + c * 8);
    }
    const int rb = stage + ((iswo ? 4 : 8) * h + q) * 224 + 8 * (quad & 1);
    const int oi = (quad >> 1) * 2 + hh;
    R.rd0 = rb + 16 * oi; R.rd1 = rb + 16 * min(4 + oi, 6); R.step = (iswo ? 8 : 4) * 224;
    int ob[2];
#pragma unroll
    for (int k2 = 0; k2 < 2; ++k2) {
      const int ks = a0 * 2 + k2, o_ = 16 * (ks % 7) + ks / 7;
      int fr;
      if (o_ < 56) fr = 112 + ((o_ & 3) * 14 + (o_ >> 2)) * 2;
      else fr = (o_ - 56) * 2;
      ob[k2] = iswo ? OFF_W2 + cg * 2 * W2B + ((ks & 15) * 2 + (ks >> 4)) * 2048 : OFF_W4 + cg * 16 * UNIT + fr * 1024;
    }
    R.o0 = opq(ob[0]); R.o1 = opq(ob[1]); R.ocb = opq(iswo ? W2B : 8 * UNIT); R.sig = opq((iswo ? C_W2 : C_W4) + cg);
  } else {
    const int a = job - 64, j = a / 28, ks = a % 28;
#pragma unroll
    for (int i = 0; i < 7; ++i) {
      const int p = lane + 64 * i, r = p / 28, c = p % 28;
      R.pa[i] = (const v4u*)((c < 14 ? A.wg : A.wu) + (size_t)(ks * 16 + r) * FF + 112 * j + (c < 14 ? c : c - 14) * 8);
    }
    R.rd0 = stage + (8 * h + q) * 448 + (quad & 1) * 224 + (8 * hh + 4 * (quad >> 1)) * 2; R.rd1 = 0; R.step = 0;
    R.o0 = opq(OFF_W3 + j * 7 * UNIT + (ks < PK ? ks * 7 : 6 * PK + ks) * 1024); R.o1 = opq(ks < PK ? 1024 : MK * 1024);
    R.ocb = opq(OFF_CS3 + (j * 28 + ks) * 896); R.sig = opq(C_W3 + j);
  }
#pragma unroll
  for (int i = 0; i < 7; ++i) PIN(R.pa[i]);
  PIN(R.rd0); PIN(R.rd1);
}
DI void retile_load(const Retile& R, u32 lds_dst) {
#pragma unroll
  for (int i = 0; i < 7; ++i) {
    u32 keep;
    asm volatile("s_mov_b32 %0, m0\n\ts_add_u32 m0, %2, %3\n\ts_nop 0\n\tglobal_load_lds_dwordx4 %1, off\n\ts_mov_b32 m0, %0"
                 : "=&s"(keep) : "v"(R.pa[i]), "s"(lds_dst), "i"(i * 1024) : "memory", "scc");
  }
}
DI void retile_store(int job, const Retile& R, rsrc_t rs, LAS char* LD, int lane) {
  const int h = lane >> 5;
  if (job < 64 || job >= 512) {
    const bool pad = (lane & 0x14) == 0x14;
    const bool kpad = job < 64 && h;
    v4u o[8];
#pragma unroll
    for (int i = 0; i < 8; ++i) {
      const int ad = ((i & 2) ? R.rd1 : R.rd0) + (i >> 2) * 112 + (i & 1) * 16 * 224;
      const v2u lo = __builtin_bit_cast(v2u, __builtin_amdgcn_ds_read_tr16_b64_v4i16((LAS s16x4*)(LD + ad)));
      const v2u hi = __builtin_bit_cast(v2u, __builtin_amdgcn_ds_read_tr16_b64_v4i16((LAS s16x4*)(LD + ad + R.step)));
      o[i] = v4u{lo.x, lo.y, hi.x, hi.y};
    }
#pragma unroll
    for (int i = 0; i < 8; ++i) {
      if ((i & 2) && pad) o[i] = v4u{0u, 0u, 0u, 0u};
      if ((i & 1) && kpad) { o[i].z = 0u; o[i].w = 0u; }
      st16(rs, lane * 16, ((i & 1) ? R.o1 : R.o0) + (i >> 2) * R.ocb + ((i >> 1) & 1) * 1024, o[i]);
    }
  } else {
    v4u o[7];
#pragma unroll
    for (int n = 0; n < 7; ++n) o[n] = tr_frag(LD, R.rd0 + n * 32, 4 * 448);
    float cs[7];
#pragma unroll
    for (int n = 0; n < 7; ++n) {
      st16(rs, lane * 16, R.o0 + n * R.o1, o[n]);
      cs[n] = xsum(sum8(o[n]));
    }
#pragma unroll
    for (int n2 = 0; n2 < 4; ++n2) {
      const float v = h ? cs[n2 < 3 ? 2 * n2 + 1 : 6] : cs[2 * n2];
      if (n2 < 3 || !h) st4(rs, lane * 4, R.ocb + n2 * 256, __float_as_uint(v));
    }
  }
}

__global__ __launch_bounds__(512, 2) void k_main(const Args A) {
  extern __shared__ __attribute__((aligned(16))) char smem[];
  LAS char* LD = (LAS char*)smem;
  char* ws = A.ws;
  const int b = blockIdx.x, xcd = b & 7, bi = b >> 3;
  const int g = (xcd >> 2) * 8 + (bi >> 2), j = (xcd & 3) * 4 + (bi & 3);
  const int rb = 2 * g + (j >> 3), cb = j & 7;
  const int lane = threadIdx.x & 63, w = __builtin_amdgcn_readfirstlane(threadIdx.x >> 6);
  const int h = lane >> 5, tok = lane & 31;
  const rsrc_t rs = __builtin_amdgcn_make_buffer_rsrc((void*)ws, 0, WS_USED, 0x00020000);
  const u32 lbase = (u32)(size_t)smem;
  const int l16 = lane * 16;
  const int tq_ = (lane & 15) >> 2, tc8 = ((lane >> 4) & 1) * 4 + (lane & 3);
  const int w1lane = (8 * h + tq_) * 64 + min(tc8 + (j & 1), 7) * 8;

  const int hw = w - 4;
  const u32 voffn = (hw * 16 + (lane >> 2)) * (3 * D * 2) + (lane & 3) * 16;
  const char* w1 = (const char*)A.wqkv + 56 * j - (j & 1) * 8;
  unsigned long long ep;
  const u32 lb = lbase + hw * 1024;
  const int rot = ROT ? (g + 2 * j) % 7 : 0;
  auto blk = [&](int p) AI { const int q = p + rot; return q >= 7 ? q - 7 : q; };
#define KP(p) dma1<4 * UNIT + (p) * 4096, 896>(w1 + blk(p) * 0x2a000, voffn, lb)
#define VP(p) dma1<(p) * 4096, 1792>(w1 + blk(p) * 0x2a000, voffn, lb)
#define QP(p) dma1<UNIT + (p) * 4096, 0>(w1 + blk(p) * 0x2a000, voffn, lb)
  if (opq(w) >= 4) {
    HSTAMP(0);
    if (hw == 1 && b < NZ) {
      asm volatile("global_load_dwordx2 %0, %1, %2 sc1" : "=v"(ep) : "v"(EPOCH_OFF), "s"(ws) : "memory");
      st4(rs, lane * 128, b * 8192, 0u);
    }
    if (hw == 2) {
      dma1<L_KVF, 0>((const char*)A.wphi + j * 1568, l16, lbase);
      dma1<L_KVF + 1024, 0>((const char*)A.wphi + j * 1568, min(1024 + l16, 1552), lbase);
    }
    KP(0); KP(1); VP(0); KP(2); VP(1); KP(3); VP(2);
    if (hw == 3) {
      unsigned long long pcv;
      asm volatile("s_getpc_b64 %0" : "=s"(pcv));
      const char* pc = (const char*)(pcv & ~255ull);
      dma1<L_W2X, 0>(pc, bi * 1024 + l16, lbase);
      dma1<L_W2X + 1024, 32768>(pc, bi * 1024 + l16, lbase);
    }
    HSTAMP(1);
  }

  if (w >= 4) {
    const u32 voff = hw * 1024 + lane * 16;
    const char* w1f = ws + OFF_W1 + j * 3 * UNIT;
    const char* w2 = ws + OFF_W2 + cb * W2B;
    const char* w3 = ws + OFF_W3 + j * 7 * UNIT;
    const char* w4 = ws + OFF_W4 + cb * 8 * UNIT;
#define DMA(src, slot) dma7((src), voff, lbase + (slot) * UNIT + hw * 1024)
#define DMAP(src, slot) dma7p((src), voff, lbase + (slot) * UNIT + hw * 1024)
#define DMAN(which, slot) dma7n(w1 + (which) * 896, voffn, lbase + (slot) * UNIT + hw * 1024)
    v4u wr[28];
    unsigned long long tag = 0;
    auto hlayer = [&](auto LC) AI {
      constexpr int L = LC.value;
      Retile R;
      const int job = hw >= 2 ? b * 2 + hw - 2 : (hw == 0 && b < 224 ? 512 + b : -1);
      float c0 = 0.f, c1 = 0.f, c2 = 0.f, c3 = 0.f;
      auto colpart = [&](int slot, auto K0, auto K1) AI {
        constexpr int k0 = K0.value, n = K1.value - K0.value;
        const int ib = opqv(slot * UNIT + opqv(w1lane));
        v4u fr[n];
#pragma unroll
        for (int ks = 0; ks < n; ++ks) fr[ks] = tr_frag(LD, ib + (k0 + ks) * 1024, 256);
        SB();
        const u32 one = 0x3f803f80u;
#pragma unroll
        for (int ks = 0; ks < n; ++ks) { c0 = dot2(fr[ks].x, one, c0); c1 = dot2(fr[ks].y, one, c1); c2 = dot2(fr[ks].z, one, c2); c3 = dot2(fr[ks].w, one, c3); }
        SB();
      };
      auto colfin = [&](int which) AI {
        const float cs = xsum((c0 + c1) + (c2 + c3));
        if (lane < 32) *(LAS float*)(LD + L_CS1 + which * 128 + lane * 4) = cs;
      };
      v4u csa[7];
      auto export_img = [&](int slot, int which) AI {
        auto place = [&](int q) AI { const int p = q - rot; return p < 0 ? p + 7 : p; };
        const int ib = opqv(slot * UNIT + opqv(w1lane) + (g & 3) * 1024);
        const int eo = opq(OFF_W1 + (j * 3 + which) * UNIT + g * 1024);
        st16(rs, l16, eo, tr_frag(LD, ib + place(g >> 2) * 4096, 256));
        if (g < 12) st16(rs, l16, eo + 16384, tr_frag(LD, ib + place(4 + (g >> 2)) * 4096, 256));
      };
      if constexpr (L == 0) {
        if (hw == 1 && lane < 2) *(LAS v4u*)(LD + L_ARR + l16) = v4u{0u, 0u, 0u, 0u};
        HSTAMP(2);
#define VMW(n, n3) if (hw == 3) { VMCNT(n3); } else { VMCNT(n); }
        VMW(4, 6); BAR();
        HSTAMP(3);
        KP(4); VP(3);
        if (hw == 1 && b < NZ) {
          PIN(ep);
          tag = ep + 1ull;
          if (lane == 0) __hip_atomic_store((unsigned long long*)(ws + INIT_OFF + b * 128), tag, __ATOMIC_RELAXED, __HIP_MEMORY_SCOPE_AGENT);
        }
        if (hw == 2) {
          const int wb = opqv(L_KVF + (4 * h + tq_) * 56 + tc8 * 8);
          const v4u f0 = tr_frag(LD, wb, 8 * 56);
          v4u f1 = tr_frag(LD, wb + 16 * 56, 8 * 56);
          if (h) { f1.z = 0u; f1.w = 0u; }
          *(LAS v4u*)(LD + L_WPHI + l16) = f0;
          *(LAS v4u*)(LD + L_WPHI + 1024 + l16) = f1;
        }
        if (hw == 0) colpart(4, IC<0>{}, IC<8>{});
        VMW(4, 6); BAR();
        HSTAMP(4);
        KP(5); VP(4);
        if (job >= 0) retile_prep(A, job, lane, L_STAGE + hw * STG, R);
        if (hw == 1) colpart(0, IC<0>{}, IC<8>{});
        VMW(4, 6); BAR();
        HSTAMP(5);
        KP(6); VP(5); VP(6);
        if (hw == 0) colpart(4, IC<8>{}, IC<16>{});
        if (hw == 1) colpart(0, IC<8>{}, IC<12>{});
        VMCNT(5); BAR();
        HSTAMP(6);
        QP(0); QP(1); QP(2); QP(3);
        if (hw == 0) colpart(4, IC<16>{}, IC<20>{});
        if (hw == 1) colpart(0, IC<12>{}, IC<16>{});
        VMCNT(7); BAR();
        HSTAMP(7);
        QP(4); QP(5); QP(6);
        if (hw == 0) colpart(4, IC<20>{}, IC<24>{});
        if (hw == 1) colpart(0, IC<16>{}, IC<20>{});
        VMCNT(7); BAR();
        HSTAMP(8);
        if (hw == 0) { colpart(4, IC<24>{}, IC<28>{}); colfin(0); }
        if (hw == 1) { colpart(0, IC<20>{}, IC<28>{}); colfin(1); }
        if (hw == 2) export_img(4, 1);
        if (hw == 3) export_img(0, 2);
        HSTAMP(9);
        BAR();
        HSTAMP(10);
        if (job >= 0) retile_load(R, lbase + L_STAGE + hw * STG);
      } else {
        wait_arr(LD, 0, 4);
        DMA(w1f + 2 * UNIT, 0);
        VMCNT(0); BAR();
      }
      {
        const int wb = opqv(L_WPHI + opqv(l16));
        const v4u wf0 = lds16(LD, wb), wf1 = lds16(LD, wb + 1024);
        if (L == 0) HSTAMP(11);
        BAR();
        if (L == 0) HSTAMP(12);
        __builtin_amdgcn_s_setprio(1);
        const int lz = opqv(lane);
        const int xk = opqv(L_W2X + hw * 1024 + lz * 16);
        const v4u kf0 = lds16(LD, xk), kf1 = lds16(LD, xk + 4096);
        const float rstd = *(LAS float*)(LD + L_KVF + hw * 256 + lz * 4);
        f32x16 p = {};
        p = mfma(wf0, kf0, p);
        p = mfma(wf1, kf1, p);
        v4u o0, o1;
        half_softmax<1>(p, h, rstd, o0, o1);
        const int pb = opqv(L_PHK + (hw * 32 + (lz & 31)) * PHK_STRIDE + 8 * (lz >> 5) + 64);
        *(LAS v2u*)(LD + pb) = v2u{o0.x, o0.y}; *(LAS v2u*)(LD + pb + 16) = v2u{o0.z, o0.w};
        *(LAS v2u*)(LD + pb + 32) = v2u{o1.x, o1.y}; *(LAS v2u*)(LD + pb + 48) = v2u{o1.z, o1.w};
        __builtin_amdgcn_s_setprio(0);
      }
      if (L == 0) HSTAMP(13);
      if constexpr (L == 0) {
        if (job >= 0) { VMCNT(7); } else { VMCNT(0); }
      } else VMCNT(0);
      if (L == 0) HSTAMP(14);
      BAR();
      if (hw < 2) {
        __builtin_amdgcn_s_setprio(2);
        f32x16 kp = {};
        const int lz = opqv(lane);
        const int g16 = (lz >> 4) & 1, q = (lz & 15) >> 2, p = lz & 3, hz = lz >> 5;
        const int ab0 = opqv(L_PHK + (8 * hz + q) * PHK_STRIDE + (g16 * 16 + 4 * p) * 2 + hw * 64);
        const int bb0 = opqv(L_V + (8 * hz + q) * V_STRIDE + (g16 * 16 + 4 * p) * 2);
        v4u ap[8], bv[8];
#pragma unroll
        for (int s = 0; s < 8; ++s) { ap[s] = tr_frag(LD, ab0 + 16 * s * PHK_STRIDE, 4 * PHK_STRIDE); bv[s] = tr_frag(LD, bb0 + 16 * s * V_STRIDE, 4 * V_STRIDE); }
        SB();
#pragma unroll
        for (int s = 0; s < 8; ++s) kp = mfma(ap[s], bv[s], kp);
        const int ko = opq(OFF_KVP + L * KVB + (j * 16 + g) * 4096 + hw * 2048);
        st16(rs, l16, ko, pack8(kp, 0));
        st16(rs, l16, ko + 1024, pack8(kp, 1));
        wave_signal(ws, C_KV + L * 16 + j, lane);
        __builtin_amdgcn_s_setprio(0);
      }
      if constexpr (L == 0) {
        HSTAMP(15);
        if (hw == 3) {
          colpart(1, IC<0>{}, IC<28>{}); colfin(2);
          arrive_lds(LD, 1, lane);
        }
        if (hw == 1) { export_img(1, 0); wave_signal(ws, C_W1 + j, lane); }
        if (job >= 0) { VMCNT(0); retile_store(job, R, rs, LD, lane); }
        HSTAMP(16);
        if (job >= 0) wave_signal(ws, R.sig, lane);
        if (hw >= 2) signal_cnt(ws, C_W1 + j);
        HSTAMP(17);
        BAR();
        HSTAMP(18);
        u32* fw = cnt_ptr(ws, (lane & 3) == 0 ? C_W2 + (cb >> 1) : (lane & 3) == 1 ? C_W3 + j : C_W4 + (cb >> 1));
        kv_reduce(rs, LD, L, j, w, lane);
        HSTAMP(19);
        BAR();
        HSTAMP(20);
        wait_flags(ws, fw, __hip_atomic_load(fw, __ATOMIC_RELAXED, __HIP_MEMORY_SCOPE_AGENT), (lane & 3) == 0 ? 16u : (lane & 3) == 1 ? 28u : 56u);
        HSTAMP(21);
        DMA(w2, 2); DMA(w2 + UNIT, 3); dma2(w2 + 2 * UNIT, voff, lbase + L_W2X + hw * 1024);
#pragma unroll
        for (int i = 0; i < 7; ++i) csa[i] = ld16(rs, min(lane, 55) * 16, OFF_CS3 + (j * 28 + hw * 7 + i) * 896);
        DMA(w3, 0);
        HSTAMP(22);
        VMCNT(14); HSTAMP(23); BAR();
        HSTAMP(24);
        DMA(w3 + UNIT, 1);
      } else {
        DMA(w2, 2); DMA(w2 + UNIT, 3); dma2(w2 + 2 * UNIT, voff, lbase + L_W2X + hw * 1024);
        DMA(w3, 0);
        BAR();
        kv_reduce(rs, LD, L, j, w, lane);
        BAR();
        DMA(w3 + UNIT, 1);
      }
      BAR();
      if constexpr (L == 0) {
        f32x4 c4 = __builtin_bit_cast(f32x4, csa[0]);
#pragma unroll
        for (int i = 1; i < 7; ++i) c4 += __builtin_bit_cast(f32x4, csa[i]);
        if (lane < 56) *(LAS f32x4*)(LD + L_KVF + hw * 896 + l16) = c4;
      }
      DMA(w3 + 2 * UNIT, 2);
      VMCNT(7); BAR();
      const int wo4 = opq(OFF_W4 + cb * 8 * UNIT + 4 * UNIT + hw * UNIT);
#define WR(lo, hi) _Pragma("unroll") for (int i = lo; i < hi; ++i) wr[i] = ld16(rs, l16, wo4 + i * 1024)
      if constexpr (L == 0) {
        const int t4 = min(hw * 64 + lane, 223) * 4;
        *(LAS float*)(LD + L_CS3 + t4) = (*(LAS float*)(LD + L_KVF + t4) + *(LAS float*)(LD + L_KVF + 896 + t4)) +
                                         (*(LAS float*)(LD + L_KVF + 1792 + t4) + *(LAS float*)(LD + L_KVF + 2688 + t4));
        WR(0, 28);
      }
      DMA(w3 + 3 * UNIT, 3);
      VMCNT(7); BAR();
      DMAP(w3 + 4 * UNIT, 0);
      DMAP(w4 + UNIT, 4);
      VMCNT(14); BAR();
      DMAP(w3 + 5 * UNIT, 1);
      if (L == 0) wait_cnt(ws, C_W1 + j, 48);
      VMCNT(14); BAR();
      DMAP(w3 + 6 * UNIT, 2);
      VMCNT(7); BAR();
      DMAP(w4 + 2 * UNIT, 3);
      VMCNT(7); BAR();
      {
        u32* fp = cnt_ptr(ws, C_H + (L * 64 + 2 * rb + (lane & 1)) * 8);
        const int ho = opq(OFF_H + L * HB + 2 * rb * 112 * 1024 + hw * 7168);
        v4u hx[28];
        auto ldh = [&](auto I) AI { constexpr int i = I.value; hx[i] = ld16(rs, l16, ho + (i & 1) * 112 * 1024 + p4_koff(p4_oh(i >> 1))); };
        auto poll = [&](int lv) AI { return __hip_atomic_load(fp + 32 * lv, __ATOMIC_RELAXED, __HIP_MEMORY_SCOPE_AGENT); };
        DMAP(w4, 0);
        wait_flags(ws, fp, poll(0));
        static_for<8>([&](auto I) AI { ldh(I); });
        BAR();
        u32 f = poll_issue(fp + 32);
        DMA(w4 + 3 * UNIT, 1);
        VMCNT(7); PIN(f);
        wait_flags(ws, fp + 32, f);
        static_for<6>([&](auto I) AI { ldh(IC<8 + I.value>{}); });
        f32x16 acc[4] = {};
        auto ord = [](int i) constexpr { return i < 20 ? i : (i < 24 ? 20 + 2 * (i - 20) : 21 + 2 * (i - 24)); };
        const int pb0 = opqv(p4_part(0, hw) + opqv(l16)), pb2 = opqv(p4_part(2, hw) + opqv(l16));
        static_for<28>([&](auto I) AI {
          constexpr int i = I.value, k = ord(i), t = k >> 1, tq = k & 1;
          if constexpr (i == 0) f = poll(2);
          if constexpr (i == H2) {
            wait_flags(ws, fp + 64, f);
            VMCNT(2); arrive_lds(LD, 4, lane);
          }
          if constexpr (i == H3 - 3) f = poll(3);
          if constexpr (i == H3) wait_flags(ws, fp + 96, f);
          if constexpr (i == 24) wait_arr(LD, 2, 4 * L + 4);
          acc[tq] = mfma(wr[2 * t], hx[k], acc[tq]);
          acc[2 + tq] = mfma(wr[2 * t + 1], hx[k], acc[2 + tq]);
          if constexpr (i < 2) ldh(IC<14 + i>{});
          if constexpr (i >= H2 && i < H2 + 4) { ldh(IC<16 + 2 * (i - H2)>{}); ldh(IC<17 + 2 * (i - H2)>{}); }
          if constexpr (i >= H3 && i < H3 + 2) { ldh(IC<24 + 2 * (i - H3)>{}); ldh(IC<25 + 2 * (i - H3)>{}); }
          if constexpr (i >= 24) {
            constexpr int qd = i - 24;
            *(LAS f32x4*)(LD + pb0 + qd * 1024) = f32x4{acc[0][4 * qd], acc[0][4 * qd + 1], acc[0][4 * qd + 2], acc[0][4 * qd + 3]};
            *(LAS f32x4*)(LD + pb2 + qd * 1024) = f32x4{acc[2][4 * qd], acc[2][4 * qd + 1], acc[2][4 * qd + 2], acc[2][4 * qd + 3]};
          }
          SB();
        });
        wait_arr(LD, 3, 4 * L + 4);
        put16(acc[1], LD, opqv(p4_part(1, hw) + opqv(l16)));
        put16(acc[3], LD, opqv(p4_part(3, hw) + opqv(l16)));
        BAR();
      }
      if (L == 0) { DMA(w1f + UNIT, 3); DMA(w1f, 1); }
    };
    hlayer(IC<0>{});
    hlayer(IC<1>{});
    if (b == 0 && hw == 1 && lane == 0) __hip_atomic_store((unsigned long long*)(ws + EPOCH_OFF), tag, __ATOMIC_RELAXED, __HIP_MEMORY_SCOPE_AGENT);
    return;
  }

  const int tt = 4 * g + w;
  const int tt2 = 2 * rb + (w & 1);
  const int kh = w >> 1;
  const rsrc_t rx0 = __builtin_amdgcn_make_buffer_rsrc((void*)A.x0, 0, XB, 0x00020000);
  v4u res[2];
#define XWV 14
  constexpr int XW = XWV;

  auto clayer = [&](auto LC) AI {
    constexpr int L = LC.value;
    v4u x[28];
    v2u st[8];
    float mean, rstd;
    v4u pq[4];
    STAMP(0);
    auto phase1 = [&](auto FIRST) AI {
      constexpr bool first = FIRST.value;
      const int xo = opq(first ? tt * 32 * (D * 2) : OFF_X2 + tt * 28 * 1024);
      auto ldx = [&](int ks) AI { return ld16(rs, l16, xo + ks * 1024); };
      const int so_ = opq(OFF_ST2 + tt * 4096);
      const int sv_ = (opqv(lane) & 31) * 8 + h * 2048;
      v4u gx[28];
      const int stg = L_STAGE + w * STG;
      int gv[3];
#pragma unroll
      for (int c = 0; c < 3; ++c) gv[c] = (lane >> 4) * (D * 2) + (lane & 7) * 16 + ((lane & 8) ? blk(2 * c + 1) : blk(2 * c)) * 128;
      const int gv3 = (lane >> 3) * (D * 2) + (lane & 7) * 16 + blk(6) * 128;
      const int gl = stg + (lane >> 4) * 272 + (lane & 15) * 16, gl3 = stg + (lane >> 3) * 272 + (lane & 7) * 16;
      const int srd = opqv(stg + (opqv(lane) & 31) * 272 + h * 16);
      auto ldg = [&](int i) AI {
        return i < 24 ? __builtin_amdgcn_raw_buffer_load_b128(rx0, gv[i >> 3], xo + (i & 7) * 4 * (D * 2), 0)
                      : __builtin_amdgcn_raw_buffer_load_b128(rx0, gv3, xo + (i & 7) * 8 * (D * 2), 0);
      };
      auto stage = [&](int c) AI {
#pragma unroll
        for (int i = 0; i < (c < 3 ? 8 : 4); ++i) *(LAS v4u*)(LD + (c < 3 ? gl + i * 4 * 272 : gl3 + i * 8 * 272)) = gx[8 * c + i];
#pragma unroll
        for (int i = 0; i < (c < 3 ? 8 : 4); ++i) x[8 * c + i] = lds16(LD, srd + i * 32);
      };
      unsigned long long iv = 0;
      unsigned long long* initp = (unsigned long long*)(ws + (w == 3 && lane < NZ ? INIT_OFF + lane * 128 : EPOCH_OFF));
      auto init_ok = [&](unsigned long long v) AI {
        const unsigned long long e = ((unsigned long long)(u32)__builtin_amdgcn_readlane((int)(v >> 32), 63) << 32) | (u32)__builtin_amdgcn_readlane((int)v, 63);
        return __all(lane >= NZ || v == e + 1ull);
      };
      if constexpr (first) {
#pragma unroll
        for (int i = 0; i < 16; ++i) gx[i] = ldg(i);
        stage(0);
      } else {
#pragma unroll
        for (int ks = 0; ks < XW; ++ks) x[ks] = ldx(ks);
      }
      USTAMP(18);
      BAR();
      USTAMP(19);
      f32x16 ak = {}, av = {}, p = {};
      const int sbk = first ? opqv(opqv(w1lane) + 4 * UNIT) : opqv(opqv(l16) + 3 * UNIT), sbv = first ? opqv(w1lane) : opqv(l16);
      const int csb = opqv(L_CS1 + 16 * (opqv(lane) >> 5)), wb = opqv(L_WPHI + opqv(l16));
      const int xk = opqv(L_W2X + w * 1024 + opqv(l16));
      f32x4 cs[4]; v4u kf[2], wf[2];
      float nmean, s1 = 0.f, s2 = 0.f, q1 = 0.f, q2 = 0.f;
      auto isk = [](int i) constexpr { return i < LAG || (i < 56 - LAG && ((i - LAG) & 1) == 0); };
      auto kst = [](int i) constexpr { return i < LAG ? i : (i < 56 - LAG ? ((((i - LAG) & 1) == 0) ? LAG + (i - LAG) / 2 : (i - LAG) / 2) : i - 28); };
      auto rdw = [&](int base, int ks) AI { if constexpr (first) return tr_frag(LD, base + ks * 1024, 256); else return lds16(LD, base + ks * 1024); };
      pipe<56, first ? 2 : 1>([&](auto I) AI { return rdw(isk(I.value) ? sbk : sbv, kst(I.value)); },
               [&](auto I) AI {
                 constexpr int i = I.value;
                 if constexpr (first && (i == 6 || i == 14 || i == 22 || i == 30 || i == 38 || i == 46)) { USTAMP(40 + i / 8 * 2); RAWBAR(); USTAMP(41 + i / 8 * 2); }
                 if constexpr (i == 54) { USTAMP(20); BAR(); USTAMP(21); }
               },
               [&](auto I, v4u a, auto nx) AI {
                 constexpr int i = I.value, ks = kst(i);
                 if constexpr (i == 54) { p = mfma(wf[0], kf[0], p); p = mfma(wf[1], kf[1], p); }
                 if constexpr (isk(i)) ak = mfma(a, x[ks], ak); else av = mfma(a, x[ks], av);
                 nx();
                 if constexpr (!first && isk(i) && ks + XW < 28) x[ks + XW] = ldx(ks + XW);
                 if constexpr (first) {
                   if constexpr (i < 12) gx[16 + i] = ldg(16 + i);
                   if constexpr (isk(i) && ks % 8 == 6) stage(ks / 8 + 1);
                   if constexpr (isk(i)) {
                     const u32 one = 0x3f803f80u;
                     s1 = dot2(x[ks].x, one, s1); q1 = dot2(x[ks].x, x[ks].x, q1); s2 = dot2(x[ks].y, one, s2); q2 = dot2(x[ks].y, x[ks].y, q2);
                     s1 = dot2(x[ks].z, one, s1); q1 = dot2(x[ks].z, x[ks].z, q1); s2 = dot2(x[ks].w, one, s2); q2 = dot2(x[ks].w, x[ks].w, q2);
                   }
                   if constexpr (i == 45) iv = __hip_atomic_load(initp, __ATOMIC_RELAXED, __HIP_MEMORY_SCOPE_AGENT);
                   if constexpr (i == 51) { ln_finish(s1 + s2, q1 + q2, mean, rstd); nmean = -mean; }
                 } else {
                   if constexpr (!isk(i) && ks >= 4 && ks < 12) st[ks - 4] = ld8(rs, sv_ + (ks - 4) * 256, so_);
                   if constexpr (i == 45) { ln_stats(st, mean, rstd); nmean = -mean; }
                 }
                 if constexpr (i >= 46 && i < 50) cs[i - 46] = *(LAS f32x4*)(LD + csb + 32 * (i - 46));
                 if constexpr (i == 50) wf[0] = lds16(LD, wb);
                 if constexpr (i == 51) wf[1] = lds16(LD, wb + 1024);
                 if constexpr (i == 52 || i == 53) {
#pragma unroll
                   for (int qd = 2 * (i - 52); qd < 2 * (i - 52) + 2; ++qd) {
                     const float y0 = fmaf(nmean, cs[qd][0], ak[4 * qd]), y1 = fmaf(nmean, cs[qd][1], ak[4 * qd + 1]);
                     const float y2 = fmaf(nmean, cs[qd][2], ak[4 * qd + 2]), y3 = fmaf(nmean, cs[qd][3], ak[4 * qd + 3]);
                     kf[qd >> 1][2 * (qd & 1)] = pk2(y0, y1);
                     kf[qd >> 1][2 * (qd & 1) + 1] = pk2(y2, y3);
                   }
                   *(LAS v4u*)(LD + xk + (i - 52) * 4096) = kf[i - 52];
                   if constexpr (i == 52) *(LAS float*)(LD + L_KVF + w * 256 + opqv(lane) * 4) = rstd;
                 }
                 if constexpr (i == 55) {
#pragma unroll
                   for (int qd = 0; qd < 4; ++qd) cs[qd] = *(LAS f32x4*)(LD + csb + 128 + 32 * qd);
                 }
               });
      STAMP(1);
      const int lz = opqv(lane);
      const int trow = w * 32 + (lz & 31);
      const int pb = opqv(L_PHK + trow * PHK_STRIDE + 8 * (lz >> 5)), vb = opqv(L_V + trow * V_STRIDE + 8 * (lz >> 5));
      half_softmax<0>(p, h, rstd, pq[0], pq[1]);
#pragma unroll
      for (int s = 0; s < 2; ++s) {
        *(LAS v2u*)(LD + pb + 32 * s) = v2u{pq[s].x, pq[s].y};
        *(LAS v2u*)(LD + pb + 32 * s + 16) = v2u{pq[s].z, pq[s].w};
      }
      SB();
#pragma unroll
      for (int r = 0; r < 16; ++r) av[r] = fmaf(nmean, cs[r >> 2][r & 3], av[r]) * rstd;
      if (h) { av[12] = 1.f; av[13] = 0.f; av[14] = 0.f; av[15] = 0.f; }
#pragma unroll
      for (int qd = 0; qd < 4; ++qd) *(LAS v2u*)(LD + vb + 16 * qd) = v2u{pk2(av[4 * qd], av[4 * qd + 1]), pk2(av[4 * qd + 2], av[4 * qd + 3])};
      if constexpr (first) {
        if (w == 3 && !init_ok(iv)) {
          u32 spins = 0;
          while (!init_ok(__hip_atomic_load(initp, __ATOMIC_RELAXED, __HIP_MEMORY_SCOPE_AGENT))) {
            __builtin_amdgcn_s_sleep(1);
            if (++spins > 400000u) break;
          }
        }
      }
      STAMP(2);
      BAR();
      if constexpr (first) {
#pragma unroll
        for (int ro = 0; ro < 2; ++ro) {
          const int oi = min(kh * 4 + ro * 2 + h, 6);
          res[ro] = __builtin_amdgcn_raw_buffer_load_b128(rx0, tok * (D * 2) + (7 * cb + oi) * 16, tt2 * 32 * (D * 2), 0);
        }
      }
      f32x16 aq = {};
      const int sbq = first ? opqv(opqv(w1lane) + 1 * UNIT) : opqv(opqv(l16) + 1 * UNIT);
      pipe<28, first ? 2 : 1>([&](auto I) AI { return rdw(sbq, I.value); }, [&](auto) AI {},
               [&](auto I, v4u a, auto nx) AI { aq = mfma(a, x[I.value], aq); nx(); });
      if constexpr (first) wait_arr(LD, 1, 1);
#pragma unroll
      for (int qd = 0; qd < 4; ++qd) cs[qd] = *(LAS f32x4*)(LD + csb + 256 + 32 * qd);
#pragma unroll
      for (int r = 0; r < 16; ++r) aq[r] = fmaf(nmean, cs[r >> 2][r & 3], aq[r]);
      f32x16 p2 = {};
      const int wb2 = opqv(L_WPHI + opqv(l16));
      p2 = mfma(lds16(LD, wb2), pack8(aq, 0), p2);
      p2 = mfma(lds16(LD, wb2 + 1024), pack8(aq, 1), p2);
      half_softmax<0>(p2, h, rstd, pq[0], pq[1]);
      half_softmax<1>(p2, h, rstd, pq[2], pq[3]);
    };
    phase1(std::integral_constant<bool, L == 0>{});
    STAMP(3);
    if (w == 0) wait_cnt(ws, C_KV + L * 16 + j, 32);
    BAR();
    STAMP(4);
    kv_reduce(rs, LD, L, j, w, lane);
    BAR();
    STAMP(5);
    {
      f32x16 nm = {};
      const int kb = opqv(L_KVF + opqv(l16));
      v4u kf[4];
#pragma unroll
      for (int s = 0; s < 4; ++s) kf[s] = lds16(LD, kb + s * 1024);
#pragma unroll
      for (int s = 0; s < 4; ++s) nm = mfma(kf[s], pq[s], nm);
      auto r = __builtin_amdgcn_permlane32_swap(__float_as_uint(nm[12]), __float_as_uint(nm[12]), false, false);
      const float den = __uint_as_float(r[1]) + 1e-6f;
      const float rd = __builtin_amdgcn_rcpf(den);
#pragma unroll
      for (int i = 0; i < 16; ++i) nm[i] *= rd;
      const int oo = opq(OFF_O + L * OB + (tt * 32 + 2 * j) * 1024);
      st16(rs, l16, oo, pack8(nm, 0));
      st16(rs, l16, oo + 1024, pack8(nm, 1));
    }
    wave_signal(ws, C_O + (L * 64 + tt) * 2 + (j >> 3), lane);
    STAMP(6);
    wait_cnt(ws, C_O + (L * 64 + tt2) * 2 + kh, 8, true);
    STAMP(7);
    {
      const int xo = opq(OFF_O + L * OB + (tt2 * 32 + 16 * kh) * 1024);
#pragma unroll
      for (int t = 0; t < (XW < 16 ? XW : 16); ++t) x[t] = ld16(rs, l16, xo + t * 1024);
      if (L == 0) RAWBAR();
      f32x16 a0 = {}, a1 = {};
      const int sb2 = opqv(2 * UNIT + kh * 2048 + opqv(l16)), sbx = opqv(L_W2X + kh * 2048 + opqv(l16));
      pipe<32>([&](auto I) AI { constexpr int i = I.value, t = i >> 1; return lds16(LD, (t < 14 ? sb2 + t * 4096 : sbx + (t - 14) * 4096) + (i & 1) * 1024); },
               [&](auto I) AI { if constexpr (I.value == 14) RAWBAR();   },
               [&](auto I, v4u a, auto nx) AI {
                 constexpr int i = I.value;
                 if constexpr (i & 1) a1 = mfma(a, x[i >> 1], a1); else a0 = mfma(a, x[i >> 1], a0);
                 nx();
                 if constexpr (!(i & 1) && (i >> 1) + XW < 16) x[(i >> 1) + XW] = ld16(rs, l16, xo + ((i >> 1) + XW) * 1024);
               });
      const int xb = opqv(L_SCR + w * 4096 + opqv(l16)), xb2 = opqv(L_SCR + (w ^ 2) * 4096 + opqv(l16));
      f32x16 fin;
      if (kh) {
#pragma unroll
        for (int qd = 0; qd < 4; ++qd) *(LAS f32x4*)(LD + xb + qd * 1024) = f32x4{a0[4 * qd], a0[4 * qd + 1], a0[4 * qd + 2], a0[4 * qd + 3]};
        fin = a1;
      } else {
#pragma unroll
        for (int qd = 0; qd < 4; ++qd) *(LAS f32x4*)(LD + xb + qd * 1024) = f32x4{a1[4 * qd], a1[4 * qd + 1], a1[4 * qd + 2], a1[4 * qd + 3]};
        fin = a0;
      }
      BAR();
#pragma unroll
      for (int qd = 0; qd < 4; ++qd) {
        const f32x4 v = *(LAS f32x4*)(LD + xb2 + qd * 1024);
#pragma unroll
        for (int i = 0; i < 4; ++i) fin[4 * qd + i] += v[i];
      }
      finish_tile(fin, res, rs, opq(OFF_X1 + L * XB), opq(OFF_ST1 + L * STB), tt2, cb, kh, lane, false, A.out);
    }
    wave_signal(ws, C_X1 + L * 64 + tt2, lane);
    STAMP(8);
    wait_cnt(ws, C_X1 + L * 64 + tt, 16, true);
    STAMP(9);
    {
      const int xo = opq(OFF_X1 + L * XB + tt * 28 * 1024);
      const int so_ = opq(OFF_ST1 + L * STB + tt * 4096);
      const int sv_ = (opqv(lane) & 31) * 8 + h * 2048;
      constexpr int XW3 = 8;
#pragma unroll
      for (int ks = 0; ks < XW3; ++ks) x[ks] = ld16(rs, l16, xo + ks * 1024);
      const int ho = opq(OFF_H + L * HB + (tt * 112 + 7 * j) * 1024);
      const int lvo = opq(C_H + (L * 64 + tt) * 8);
      u32* fp = cnt_ptr(ws, C_H + (L * 64 + tt2) * 8);
      auto poll = [&](int lv) AI { return __hip_atomic_load(fp + 32 * lv, __ATOMIC_RELAXED, __HIP_MEMORY_SCOPE_AGENT); };
      u32 f;
      float nmean, gs, irs;
      const int csb = opqv(L_CS3 + 16 * (opqv(lane) >> 5));
      const int sbA = opqv(l16), sbB = opqv(opqv(l16) + 2 * UNIT);
      f32x4 Gcg[2], Gcu[2]; float Gt[8], Gyu[8]; u32 Go[4];
      auto glu = [&](const f32x16& a, auto N_, auto S_) AI {
        constexpr int n = N_.value, s = S_.value;
        if constexpr (s == 1) Gcu[0] = *(LAS f32x4*)(LD + csb + n * 128 + 32);
        if constexpr (s == 4) Gcg[1] = *(LAS f32x4*)(LD + csb + n * 128 + 64);
        if constexpr (s == 9) Gcu[1] = *(LAS f32x4*)(LD + csb + n * 128 + 96);
        if constexpr (s == 12 && n < 6) Gcg[0] = *(LAS f32x4*)(LD + csb + (n + 1) * 128);
        static_for<8>([&](auto V_) AI {
          constexpr int v = V_.value, k = s - (1 + 4 * (v >> 1) + (v & 1)), hf = v >> 2, i = v & 3;
          if constexpr (k == 0) { Gt[v] = fmaf(nmean, Gcg[hf][i], a[8 * hf + i]); PIN(Gt[v]); }
          if constexpr (k == 1) { Gt[v] = Gt[v] * gs; PIN(Gt[v]); }
          if constexpr (k == 2) { Gt[v] = __builtin_amdgcn_exp2f(Gt[v]); PIN(Gt[v]); }
          if constexpr (k == 3) { Gt[v] = fmaf(Gt[v], irs, irs); PIN(Gt[v]); }
          if constexpr (k == 4) { Gt[v] = __builtin_amdgcn_rcpf(Gt[v]); PIN(Gt[v]); }
          if constexpr (k == 5) { Gyu[v] = fmaf(nmean, Gcu[hf][i], a[8 * hf + 4 + i]); PIN(Gyu[v]); }
          if constexpr (k == 6) { Gt[v] = Gt[v] * Gyu[v]; PIN(Gt[v]); }
          if constexpr (k == 7 && (v & 1)) { Go[v >> 1] = pk2(Gt[v - 1], Gt[v]); PIN(Go[v >> 1]); }
        });
        if constexpr (s == 21) st16(rs, l16, ho + n * 1024, v4u{Go[0], Go[1], Go[2], Go[3]});
      };
      const int ho2 = opq(OFF_H + L * HB + tt2 * 112 * 1024 + kh * 7168);
      v4u hx[28];
      auto ldh = [&](auto T) AI { hx[T.value] = ld16(rs, l16, ho2 + p4_koff(p4_oc(T.value))); };
      f32x16 acc[7];
      constexpr int PB = 7 * PK;
      pipe<196>(
          [&](auto I) AI { constexpr int i = I.value, u = i / 28, sl = u & 3; return lds16(LD, (sl < 2 ? sbA : sbB) + (sl & 1) * UNIT + (i % 28) * 1024); },
          [&](auto I) AI { if constexpr (I.value > 28 && I.value % 28 == 6) { VSTAMP(30 + I.value / 28); RAWBAR(); WSTAMP(22 + I.value / 28); } },
          [&](auto I, v4u a, auto nx) AI {
            constexpr int i = I.value;
            if constexpr (i < PB) {
              constexpr int n = i % 7, ks = i / 7;
              if constexpr (ks == 0) { f32x16 z = {}; acc[n] = mfma(a, x[ks], z); } else acc[n] = mfma(a, x[ks], acc[n]);
              nx();
              constexpr int K2 = 28 - XW3 - PK, fx = ks < K2 ? XW3 + 2 * ks + (n ? 1 : 0) : XW3 + K2 + ks;
              if constexpr (n == 0 || (n == 3 && ks < K2)) x[fx] = ld16(rs, l16, xo + fx * 1024);
              if constexpr (n >= 5 && ks < 4) st[2 * ks + n - 5] = ld8(rs, sv_ + (2 * ks + n - 5) * 256, so_);
              if constexpr (i == 46) {
                ln_stats(st, mean, rstd);
                nmean = -mean; gs = -LOG2E * rstd; irs = __builtin_amdgcn_rcpf(rstd);
                PIN(nmean); PIN(gs); PIN(irs);
              }
            } else {
              constexpr int r = i - PB, n = r / MK, s = r % MK;
              acc[n] = mfma(a, x[PK + s], acc[n]);
              nx();
              if constexpr (r == 8) Gcg[0] = *(LAS f32x4*)(LD + csb);
              if constexpr (n >= 2 && s + MK < 22) glu(acc[n - 2], IC<n - 2>{}, IC<s + MK>{});
              if constexpr (n >= 1) glu(acc[n - 1], IC<n - 1>{}, IC<s>{});
              if constexpr (n >= 3 && s == 20 - MK) { VMCNT(0); signal_cnt(ws, lvo + n - 3); }
            }
          });
      STAMP(10);
      arrive_lds(LD, 2, lane);
      static_for<22>([&](auto S_) AI {
        if constexpr (S_.value + MK < 22) glu(acc[5], IC<5>{}, IC<S_.value + MK>{});
        glu(acc[6], IC<6>{}, S_);
        if constexpr (S_.value == 4) { VMCNT(0); signal_cnt(ws, lvo + 4); }
        if constexpr (S_.value == CP1) f = poll_issue(fp + 96);
      });
      STAMP(11);
      const int wo_ = opqv(kh * 3072 + opqv(l16)), wp_ = opqv(kh * 1024 + 1024 + opqv(l16));
      const int wo2 = opqv(wo_ + 3 * UNIT), wp2 = opqv(wp_ + 3 * UNIT);
      v4u wq[56];
      auto ldw = [&](auto K) AI {
        constexpr int k = K.value, t = k >> 1, wb = p4_wbase(t);
        if constexpr (wb >= 3 * UNIT) wq[k] = lds16(LD, ((k & 1) ? wp2 : wo2) + wb - 3 * UNIT);
        else wq[k] = lds16(LD, ((k & 1) ? wp_ : wo_) + wb);
      };
      VMCNT(1); signal_cnt(ws, lvo + 5);
      PIN(f);
      const u32 f2 = poll(4);
      if (__all(f >= 16u)) {
        static_for<4>([&](auto T) AI { ldh(T); });
        VMCNT(6); signal_cnt(ws, lvo + 6);
      } else {
        VMCNT(2); signal_cnt(ws, lvo + 6);
        wait_flags(ws, fp + 96, f);
        static_for<4>([&](auto T) AI { ldh(T); });
      }
      wait_flags(ws, fp + 128, f2);
      static_for<8>([&](auto T) AI { ldh(IC<4 + T.value>{}); });
      wait_arr(LD, 4, 4 * L + 4);
      static_for<4>([&](auto K) AI { ldw(K); });
      STAMP(12);
      f32x16 fin = {}, oth = {};
      static_for<28>([&](auto T) AI {
        constexpr int t = T.value;
        if constexpr (t == CP5) f = poll(5);
        if constexpr (t == CT5) {
          wait_flags(ws, fp + 160, f);
          f = poll(6);
          static_for<8>([&](auto K) AI { ldh(IC<12 + K.value>{}); });
          STAMP(13);
        }
        if constexpr (t == 12) {
          wait_flags(ws, fp + 192, f);
          static_for<8>([&](auto K) AI { ldh(IC<20 + K.value>{}); });
          STAMP(22);
        }
        if constexpr (t == 12) arrive_lds(LD, 3, lane);
        if constexpr (t + 2 < 28) { ldw(IC<2 * t + 4>{}); ldw(IC<2 * t + 5>{}); }
        oth = mfma(wq[2 * t + 1], hx[t], oth);
        fin = mfma(wq[2 * t], hx[t], fin);
        SB();
      });
      STAMP(14);
      wait_arr(LD, 3, 4 * L + 4);
      put16(oth, LD, opqv(L_SCR + w * 4096 + opqv(l16)));
      BAR();
#pragma unroll
      for (int c = 0; c < 4; ++c) add16(fin, LD, opqv(p4_part(w, c) + opqv(l16)));
      add16(fin, LD, opqv(L_SCR + (w ^ 2) * 4096 + opqv(l16)));
      STAMP(15);
      finish_tile(fin, res, rs, OFF_X2, OFF_ST2, tt2, cb, kh, lane, L == 1, A.out);
    }
    if (L == 0) {
      wave_signal(ws, C_X2 + tt2, lane);
      arrive_lds(LD, 0, lane);
      STAMP(16);
      wait_cnt(ws, C_X2 + tt, 16, true);
    }
    STAMP(17);
  };
  clayer(IC<0>{});
  clayer(IC<1>{});
}

extern "C" void kernel_launch(void* const* d_in, const int* in_sizes, int n_in, void* d_out, int out_size, void* d_ws,
                              size_t ws_size, hipStream_t stream) {
  Args a;
  a.x0 = (const u16*)d_in[0]; a.wqkv = (const u16*)d_in[1]; a.wphi = (const u16*)d_in[2]; a.wo = (const u16*)d_in[3];
  a.wg = (const u16*)d_in[5]; a.wu = (const u16*)d_in[6]; a.wd = (const u16*)d_in[7];
  a.ws = (char*)d_ws; a.out = (u16*)d_out;
  (void)hipFuncSetAttribute((const void*)k_main, hipFuncAttributeMaxDynamicSharedMemorySize, LDS_BYTES);
  k_main<<<256, 512, LDS_BYTES, stream>>>(a);
}
```

```cpp
#include <hip/hip_runtime.h>
#include <stdint.h>
#include <utility>
#include <type_traits>

typedef unsigned short u16;
typedef unsigned int u32;
typedef u32 v4u __attribute__((ext_vector_type(4)));
typedef u32 v2u __attribute__((ext_vector_type(2)));
typedef float f32x2 __attribute__((ext_vector_type(2)));
typedef float f32x4 __attribute__((ext_vector_type(4)));
typedef float f32x16 __attribute__((ext_vector_type(16)));
typedef __bf16 bf16x2 __attribute__((ext_vector_type(2)));
typedef __bf16 bf16x8 __attribute__((ext_vector_type(8)));
typedef short s16x4 __attribute__((ext_vector_type(4)));
#define LAS __attribute__((address_space(3)))
#define DI __device__ __forceinline__
#define AI __attribute__((always_inline))
template <int V> using IC = std::integral_constant<int, V>;

constexpr int S = 2048, D = 448, FF = 1792;
constexpr int UNIT = 28672;

constexpr int C_KV = 0;
constexpr int C_W2 = 32;
constexpr int C_W4 = 36;
constexpr int C_W3 = 40;
constexpr int C_ERR = 60;
constexpr int C_X2 = 64;
constexpr int C_X1 = 128;
constexpr int C_O = 256;
constexpr int C_H = 512;
constexpr int C_W1 = 1536;
constexpr int NZ = 25;
constexpr int EPOCH_OFF = NZ * 64 * 128;
constexpr int INIT_OFF = EPOCH_OFF + 256;
constexpr int CTL_BYTES = EPOCH_OFF + 4096;
constexpr int W2B = 65536;
constexpr int OFF_W1 = CTL_BYTES;
constexpr int OFF_W2 = OFF_W1 + 16 * 3 * UNIT;
constexpr int OFF_W3 = OFF_W2 + 8 * W2B;
constexpr int OFF_W4 = OFF_W3 + 16 * 7 * UNIT;
constexpr int OFF_CS3 = OFF_W4 + 8 * 8 * UNIT;
constexpr int XB = S * D * 2;
constexpr int OB = S * 512 * 2;
constexpr int OFF_X2 = OFF_CS3 + 16 * 28 * 896;
constexpr int OFF_X1 = OFF_X2 + XB;
constexpr int OFF_O = OFF_X1 + 2 * XB;
constexpr int STB = S * 16 * 8;
constexpr int OFF_ST2 = OFF_O + 2 * OB;
constexpr int OFF_ST1 = OFF_ST2 + STB;
constexpr int KVB = 16 * 16 * 4096;
constexpr int OFF_KVP = OFF_ST1 + 2 * STB;
constexpr int HB = S * FF * 2;
constexpr int OFF_H = OFF_KVP + 2 * KVB;
constexpr int WS_USED = OFF_H + 2 * HB;

constexpr int L_SCR = 4 * UNIT;
constexpr int PHK_STRIDE = 136, V_STRIDE = 72;
constexpr int L_PHK = L_SCR;
constexpr int L_V = L_SCR + 128 * PHK_STRIDE;
constexpr int L_KVF = L_SCR + 28672;
constexpr int L_W2X = L_KVF + 4096;
constexpr int L_CONST = 155648;
constexpr int L_CS1 = L_CONST, L_CS3 = L_CONST + 384, L_WPHI = L_CONST + 1280, L_ARR = L_CONST + 3328;
constexpr int L_STAGE = 2 * UNIT;
constexpr int STG = 8704;
constexpr int LDS_BYTES = 163840;

constexpr float LOG2E = 1.4426950408889634f;
constexpr int PKV = 12;
constexpr int PK = PKV, MK = 28 - PK;
constexpr int LAG = 4;

DI u32 pk2(float a, float b) { f32x2 v = {a, b}; return __builtin_bit_cast(u32, __builtin_convertvector(v, bf16x2)); }
DI float bflo(u32 v) { return __uint_as_float(v << 16); }
DI float bfhi(u32 v) { return __uint_as_float(v & 0xffff0000u); }
DI f32x16 mfma(v4u a, v4u b, f32x16 c) {
  f32x16 d = __builtin_amdgcn_mfma_f32_32x32x16_bf16(__builtin_bit_cast(bf16x8, a), __builtin_bit_cast(bf16x8, b), c, 0, 0, 0);
  asm volatile("" : "+v"(d));
  __builtin_amdgcn_sched_barrier(0);
  return d;
}
DI float xsum(float v) {
  auto r = __builtin_amdgcn_permlane32_swap(__float_as_uint(v), __float_as_uint(v), false, false);
  return __uint_as_float(r[0]) + __uint_as_float(r[1]);
}
DI float xmax(float v) {
  auto r = __builtin_amdgcn_permlane32_swap(__float_as_uint(v), __float_as_uint(v), false, false);
  return fmaxf(__uint_as_float(r[0]), __uint_as_float(r[1]));
}
DI float xmin(float v) {
  auto r = __builtin_amdgcn_permlane32_swap(__float_as_uint(v), __float_as_uint(v), false, false);
  return fminf(__uint_as_float(r[0]), __uint_as_float(r[1]));
}
#define BAR() asm volatile("s_waitcnt lgkmcnt(0)\n\ts_barrier" ::: "memory")
#define RAWBAR() asm volatile("s_barrier" ::: "memory")
#define VMCNT(n) asm volatile("s_waitcnt vmcnt(" #n ")" ::: "memory")
#define SB() __builtin_amdgcn_sched_barrier(0)
#define PIN(x) asm volatile("" : "+v"(x))

typedef __amdgpu_buffer_rsrc_t rsrc_t;
DI v4u ld16(rsrc_t rs, int voff, int soff) { return __builtin_amdgcn_raw_buffer_load_b128(rs, voff, soff, 16); }
DI v2u ld8(rsrc_t rs, int voff, int soff) { return __builtin_amdgcn_raw_buffer_load_b64(rs, voff, soff, 16); }
DI u32 ld4(rsrc_t rs, int voff, int soff) { return __builtin_amdgcn_raw_buffer_load_b32(rs, voff, soff, 16); }
DI void st16(rsrc_t rs, int voff, int soff, v4u v) {
  __builtin_amdgcn_raw_buffer_store_b128(v, rs, voff, soff, 16);
  asm volatile("s_nop 1" ::"v"(v) : "memory");
}
DI void st8(rsrc_t rs, int voff, int soff, v2u v) { __builtin_amdgcn_raw_buffer_store_b64(v, rs, voff, soff, 16); }
DI void st4(rsrc_t rs, int voff, int soff, u32 v) { __builtin_amdgcn_raw_buffer_store_b32(v, rs, voff, soff, 16); }
DI int opq(int v) { asm volatile("" : "+s"(v)); return v; }
DI int opqv(int v) { asm volatile("" : "+v"(v)); return v; }
DI v4u lds16(LAS char* l, int off) { return *(LAS v4u*)(l + off); }
DI float dot2(u32 a, u32 b, float c) {
  return __builtin_amdgcn_fdot2_f32_bf16(__builtin_bit_cast(bf16x2, a), __builtin_bit_cast(bf16x2, b), c, false);
}
DI v4u tr_frag(LAS char* L, int base, int step) {
  v2u lo = __builtin_bit_cast(v2u, __builtin_amdgcn_ds_read_tr16_b64_v4i16((LAS s16x4*)(L + base)));
  v2u hi = __builtin_bit_cast(v2u, __builtin_amdgcn_ds_read_tr16_b64_v4i16((LAS s16x4*)(L + base + step)));
  return v4u{lo.x, lo.y, hi.x, hi.y};
}
DI float sum8(v4u o) {
  const u32 one = 0x3f803f80u;
  return dot2(o.x, one, dot2(o.y, one, 0.f)) + dot2(o.z, one, dot2(o.w, one, 0.f));
}

DI u32* cnt_ptr(char* ws, int idx) { return (u32*)(ws + idx * 128); }
constexpr int POLL_D0 = 16;
constexpr int H2 = 6;
constexpr int H3 = 12;
constexpr int CP1 = 12;
constexpr int CP5 = 0;
constexpr int CT5 = 11;
constexpr int POLL_IV = 1;
DI bool spin_more(char* ws, u32& spins) {
  u32* e = cnt_ptr(ws, C_ERR);
  __builtin_amdgcn_s_sleep(POLL_IV);
  ++spins;
  if ((spins & 255u) == 0u) {
    if (__builtin_amdgcn_readfirstlane(__hip_atomic_load(e, __ATOMIC_RELAXED, __HIP_MEMORY_SCOPE_AGENT)) != 0) return false;
    if (spins > 400000u) { __hip_atomic_store(e, 1u, __ATOMIC_RELAXED, __HIP_MEMORY_SCOPE_AGENT); return false; }
  }
  return true;
}
DI void wait_cnt(char* ws, int idx, u32 target, bool delay = false) {
  u32* c = cnt_ptr(ws, idx);
  u32 spins = 0;
  if (delay && POLL_D0) __builtin_amdgcn_s_sleep(POLL_D0);
  while ((u32)__builtin_amdgcn_readfirstlane(__hip_atomic_load(c, __ATOMIC_RELAXED, __HIP_MEMORY_SCOPE_AGENT)) < target)
    if (!spin_more(ws, spins)) break;
  __builtin_amdgcn_fence(__ATOMIC_ACQUIRE, "wavefront");
  asm volatile("" ::: "memory");
}
DI void wait_flags(char* ws, u32* fp, u32 f, u32 target = 16u) {
  if (__builtin_expect(!__all(f >= target), 0)) {
    u32 spins = 0;
    while (spin_more(ws, spins))
      if (__all(__hip_atomic_load(fp, __ATOMIC_RELAXED, __HIP_MEMORY_SCOPE_AGENT) >= target)) break;
  }
  __builtin_amdgcn_fence(__ATOMIC_ACQUIRE, "wavefront");
  asm volatile("" ::: "memory");
}
DI u32 poll_issue(const u32* p) { u32 f; asm volatile("global_load_dword %0, %1, off sc1" : "=v"(f) : "v"(p) : "memory"); return f; }
DI void signal_cnt(char* ws, int idx) {
  unsigned long long sv;
  asm volatile("s_mov_b64 %0, exec\n\ts_mov_b64 exec, 1\n\tglobal_atomic_add %1, %2, %3\n\ts_mov_b64 exec, %0"
               : "=&s"(sv) : "v"(idx * 128), "v"(1), "s"(ws) : "memory");
}
DI void wave_signal(char* ws, int idx, int lane) { VMCNT(0); signal_cnt(ws, idx); }
DI void arrive_lds(LAS char* LD, int slot, int lane) {
  if (lane == 0) __hip_atomic_fetch_add((LAS u32*)(LD + L_ARR + slot * 4), 1u, __ATOMIC_RELAXED, __HIP_MEMORY_SCOPE_WORKGROUP);
}
DI void wait_arr(LAS char* LD, int slot, u32 target) {
  while ((u32)__builtin_amdgcn_readfirstlane(__hip_atomic_load((LAS u32*)(LD + L_ARR + slot * 4), __ATOMIC_RELAXED, __HIP_MEMORY_SCOPE_WORKGROUP)) < target)
    __builtin_amdgcn_s_sleep(1);
}


#define DMA_STEP(GSTEP) \
      "s_add_u32 m0, m0, 0x1000\n\t" \
      "v_add_u32 %1, " GSTEP ", %1\n\t" \
      "global_load_lds_dwordx4 %1, %2 sc1\n\t"
#define DMA_FN(NAME, BODY) \
DI void NAME(const char* gsrc, u32 voff, u32 lds_dst) { \
  u32 keep; \
  asm volatile("s_mov_b32 %0, m0\n\ts_mov_b32 m0, %3\n\ts_nop 0\n\tglobal_load_lds_dwordx4 %1, %2 sc1\n\t" BODY "s_mov_b32 m0, %0" \
               : "=&s"(keep), "+v"(voff) : "s"(gsrc), "s"(lds_dst) : "memory", "scc"); }
DMA_FN(dma7, DMA_STEP("0x1000") DMA_STEP("0x1000") DMA_STEP("0x1000") DMA_STEP("0x1000") DMA_STEP("0x1000") DMA_STEP("0x1000"))
DMA_FN(dma2, DMA_STEP("0x1000"))
#define PACE 1
#define STR2(x) #x
#define STR(x) STR2(x)
#define PSTEP "s_sleep " STR(PACE) "\n\t" DMA_STEP("0x1000")
DMA_FN(dma7p, PSTEP PSTEP PSTEP PSTEP PSTEP PSTEP)
DMA_FN(dma7n, DMA_STEP("0x2a000") DMA_STEP("0x2a000") DMA_STEP("0x2a000") DMA_STEP("0x2a000") DMA_STEP("0x2a000") DMA_STEP("0x2a000"))

template <int LO, int GO>
DI void dma1(const char* gsrc, u32 voff, u32 lb) {
  u32 keep, tmp;
  asm volatile("s_mov_b32 %0, m0\n\ts_add_u32 m0, %4, %5\n\tv_add_u32 %1, %6, %2\n\tglobal_load_lds_dwordx4 %1, %3 sc1\n\ts_mov_b32 m0, %0"
               : "=&s"(keep), "=&v"(tmp) : "v"(voff), "s"(gsrc), "s"(lb), "i"(LO), "i"(GO) : "memory", "scc");
}

template <typename F, int... I>
DI void static_for_impl(F&& f, std::integer_sequence<int, I...>) { (f(IC<I>{}), ...); }
template <int N, typename F>
DI void static_for(F&& f) { static_for_impl(f, std::make_integer_sequence<int, N>{}); }

constexpr int PF = 6;
constexpr int WEV = 2;
template <int N, int RPS = 1, typename RD, typename PRE, typename ST>
DI void pipe(RD rd, PRE pre, ST step) {
  v4u buf[PF];
  static_for<PF>([&](auto I) AI { buf[I.value] = rd(I); });
  SB();
  static_for<N>([&](auto I) AI {
    constexpr int i = I.value;
    pre(I);
    if constexpr (WEV > 1 && i % WEV == 0 && i + PF <= N) __builtin_amdgcn_s_waitcnt(0xC07F | (((PF - WEV) * RPS) << 8));
    const v4u a = buf[i % PF];
    step(I, a, [&]() AI { if constexpr (i + PF < N) buf[i % PF] = rd(IC<i + PF>{}); SB(); });
    SB();
  });
}

DI void ln_finish(float s, float q, float& mean, float& rstd) {
  s = xsum(s); q = xsum(q);
  mean = s * (1.f / 448.f);
  const float var = fmaxf(q * (1.f / 448.f) - mean * mean, 0.f);
  rstd = __builtin_amdgcn_rsqf(var + 1e-5f);
}
DI void ln_stats(const v2u (&st)[8], float& mean, float& rstd) {
  f32x2 a = __builtin_bit_cast(f32x2, st[0]), b = __builtin_bit_cast(f32x2, st[1]);
#pragma unroll
  for (int i = 2; i < 8; i += 2) { a += __builtin_bit_cast(f32x2, st[i]); b += __builtin_bit_cast(f32x2, st[i + 1]); }
  a += b;
  ln_finish(a[0], a[1], mean, rstd);
}
DI v4u pack8(const f32x16& a, int s) {
  v4u r = {pk2(a[8 * s], a[8 * s + 1]), pk2(a[8 * s + 2], a[8 * s + 3]), pk2(a[8 * s + 4], a[8 * s + 5]), pk2(a[8 * s + 6], a[8 * s + 7])};
  return r;
}
template <int SG>
DI void half_softmax(const f32x16& p, int h, float rstd, v4u& o0, v4u& o1) {
  auto mm = [](float a, float b) AI { return SG ? fminf(a, b) : fmaxf(a, b); };
  float m0 = mm(mm(p[0], p[1]), p[2]), m1 = mm(mm(p[3], p[4]), p[5]), m2 = mm(mm(p[6], p[7]), p[8]), m3 = mm(mm(p[9], p[10]), p[11]);
  float m4 = mm(mm(mm(p[12], p[13]), p[14]), p[15]);
  m4 = h ? p[0] : m4;
  float mx = mm(mm(mm(m0, m1), m2), mm(m3, m4));
  mx = SG ? xmin(mx) : xmax(mx);
  const float c = rstd * LOG2E;
  const float cc = SG ? -c : c, off = -mx * cc;
  f32x16 e;
  float s0 = 0.f, s1 = 0.f;
#pragma unroll
  for (int r = 0; r < 16; ++r) {
    float v = __builtin_amdgcn_exp2f(fmaf(p[r], cc, off));
    if (r >= 12) v = h ? 0.f : v;
    e[r] = v;
    if (r & 1) s1 += v; else s0 += v;
  }
  const float rs_ = __builtin_amdgcn_rcpf(xsum(s0 + s1));
#pragma unroll
  for (int r = 0; r < 16; ++r) e[r] *= rs_;
  o0 = pack8(e, 0); o1 = pack8(e, 1);
  PIN(o0); PIN(o1);
  SB();
}

DI void finish_tile(const f32x16& acc, v4u (&res)[2], rsrc_t rs, int xoff, int stoff, int tt, int cb, int nt, int lane, bool last, u16* out) {
  lane = opqv(lane);
  const int h = lane >> 5, tok = lane & 31;
#pragma unroll
  for (int ro = 0; ro < 2; ++ro) {
    v4u r = res[ro];
    v4u o;
    o.x = pk2(bflo(r.x) + acc[8 * ro + 0], bfhi(r.x) + acc[8 * ro + 1]);
    o.y = pk2(bflo(r.y) + acc[8 * ro + 2], bfhi(r.y) + acc[8 * ro + 3]);
    o.z = pk2(bflo(r.z) + acc[8 * ro + 4], bfhi(r.z) + acc[8 * ro + 5]);
    o.w = pk2(bflo(r.w) + acc[8 * ro + 6], bfhi(r.w) + acc[8 * ro + 7]);
    res[ro] = o;
    const int oi = nt * 4 + ro * 2 + h;
    if (oi < 7) {
      const int go = 7 * cb + oi;
      if (last) *(v4u*)(out + (size_t)(tt * 32 + tok) * D + go * 8) = o;
      else st16(rs, ((go >> 1) * 64 + tok + 32 * (go & 1)) * 16, xoff + tt * 28 * 1024, o);
    }
  }
  if (!last) {
    const bool v1 = !(nt == 1 && h == 1);
    const u32 one = 0x3f803f80u;
    float s0 = dot2(res[0].x, one, 0.f), s1 = dot2(res[1].x, one, 0.f), q0 = dot2(res[0].x, res[0].x, 0.f), q1 = dot2(res[1].x, res[1].x, 0.f);
    s0 = dot2(res[0].y, one, s0); s1 = dot2(res[1].y, one, s1); q0 = dot2(res[0].y, res[0].y, q0); q1 = dot2(res[1].y, res[1].y, q1);
    s0 = dot2(res[0].z, one, s0); s1 = dot2(res[1].z, one, s1); q0 = dot2(res[0].z, res[0].z, q0); q1 = dot2(res[1].z, res[1].z, q1);
    s0 = dot2(res[0].w, one, s0); s1 = dot2(res[1].w, one, s1); q0 = dot2(res[0].w, res[0].w, q0); q1 = dot2(res[1].w, res[1].w, q1);
    const float sum = xsum(s0 + (v1 ? s1 : 0.f));
    const float sq = xsum(q0 + (v1 ? q1 : 0.f));
    if (h == 0) st8(rs, tok * 8, stoff + ((tt * 8 + cb) * 2 + nt) * 256, v2u{__float_as_uint(sum), __float_as_uint(sq)});
  }
}

DI void kv_reduce(rsrc_t rs, LAS char* LD, int L, int j, int w, int lane) {
  const int ko = opq(OFF_KVP + L * KVB + j * 16 * 4096 + (w >> 1) * 1024 + (w & 1) * 512);
  const int lz = opqv(lane);
  const int vo = (lz >> 5) * 4096 + (lz & 31) * 16;
  v4u t[8];
#pragma unroll
  for (int i = 0; i < 8; ++i) t[i] = ld16(rs, vo, ko + i * 8192);
  float s[8] = {};
  const u32 lo1 = (u32)opq(0x00003f80), hi1 = (u32)opq(0x3f800000);
#pragma unroll
  for (int i = 0; i < 8; ++i) {
#pragma unroll
    for (int c = 0; c < 4; ++c) { s[2 * c] = dot2(t[i][c], lo1, s[2 * c]); s[2 * c + 1] = dot2(t[i][c], hi1, s[2 * c + 1]); }
  }
#pragma unroll
  for (int c = 0; c < 8; ++c) s[c] = xsum(s[c]);
  if (lz < 32) *(LAS v4u*)(LD + L_KVF + (w >> 1) * 1024 + (w & 1) * 512 + lz * 16) = v4u{pk2(s[0], s[1]), pk2(s[2], s[3]), pk2(s[4], s[5]), pk2(s[6], s[7])};
}

constexpr int p4_oh(int t) { return 4 * t; }
constexpr int p4_oc(int t) { return 56 + 2 * t; }
constexpr int p4_koff(int o) { return (7 * (o & 15) + (o >> 4)) * 1024; }
constexpr int p4_wbase(int t) { return (t < 7 ? 0 : t < 14 ? L_SCR : t < 21 ? 3 * UNIT : UNIT) + (t % 7) * 4096; }
DI int p4_part(int a, int hw) {
  const int i = ((a >> 1) | ((a & 1) << 1)) * 4 + hw;
  return i < 7 ? 2 * UNIT + i * 4096 : i < 10 ? L_KVF + (i - 7) * 4096 : (i - 10) * 4096;
}
DI void put16(const f32x16& acc, LAS char* LD, int off) {
#pragma unroll
  for (int qd = 0; qd < 4; ++qd) *(LAS f32x4*)(LD + off + qd * 1024) = f32x4{acc[4 * qd], acc[4 * qd + 1], acc[4 * qd + 2], acc[4 * qd + 3]};
}
DI void add16(f32x16& acc, LAS char* LD, int off) {
#pragma unroll
  for (int qd = 0; qd < 4; ++qd) {
    const f32x4 v = *(LAS f32x4*)(LD + off + qd * 1024);
#pragma unroll
    for (int i = 0; i < 4; ++i) acc[4 * qd + i] += v[i];
  }
}

struct Args {
  const u16* x0; const u16* wqkv; const u16* wphi; const u16* wo; const u16* wg; const u16* wu; const u16* wd;
  char* ws; u16* out;
};

struct Retile { const v4u* pa[7]; int rd0, rd1, step, o0, o1, ocb, sig; };
DI void retile_prep(const Args& A, int job, int lane, int stage, Retile& R) {
  const int h = lane >> 5, q = (lane & 15) >> 2, c8 = ((lane >> 4) & 1) * 4 + (lane & 3);
  const int hh = c8 & 1, quad = c8 >> 1;
  if (job < 64 || job >= 512) {
    const bool iswo = job < 64;
    const int a0 = iswo ? job >> 2 : (job - 512) >> 2, cg = job & 3;
    const u16* src = (iswo ? A.wo + (size_t)a0 * 28 * D : A.wd + (size_t)a0 * 32 * D) + 112 * cg;
    const int nrow = iswo ? 28 : 32;
#pragma unroll
    for (int i = 0; i < 7; ++i) {
      const int p = lane + 64 * i, r = p / 14, c = p % 14;
      R.pa[i] = (const v4u*)(src + (size_t)(r < nrow ? r : 0) * D + c * 8);
    }
    const int rb = stage + ((iswo ? 4 : 8) * h + q) * 224 + 8 * (quad & 1);
    const int oi = (quad >> 1) * 2 + hh;
    R.rd0 = rb + 16 * oi; R.rd1 = rb + 16 * min(4 + oi, 6); R.step = (iswo ? 8 : 4) * 224;
    int ob[2];
#pragma unroll
    for (int k2 = 0; k2 < 2; ++k2) {
      const int ks = a0 * 2 + k2, o_ = 16 * (ks % 7) + ks / 7;
      int fr;
      if (o_ < 56) fr = 112 + ((o_ & 3) * 14 + (o_ >> 2)) * 2;
      else fr = (o_ - 56) * 2;
      ob[k2] = iswo ? OFF_W2 + cg * 2 * W2B + ((ks & 15) * 2 + (ks >> 4)) * 2048 : OFF_W4 + cg * 16 * UNIT + fr * 1024;
    }
    R.o0 = opq(ob[0]); R.o1 = opq(ob[1]); R.ocb = opq(iswo ? W2B : 8 * UNIT); R.sig = opq((iswo ? C_W2 : C_W4) + cg);
  } else {
    const int a = job - 64, j = a / 28, ks = a % 28;
#pragma unroll
    for (int i = 0; i < 7; ++i) {
      const int p = lane + 64 * i, r = p / 28, c = p % 28;
      R.pa[i] = (const v4u*)((c < 14 ? A.wg : A.wu) + (size_t)(ks * 16 + r) * FF + 112 * j + (c < 14 ? c : c - 14) * 8);
    }
    R.rd0 = stage + (8 * h + q) * 448 + (quad & 1) * 224 + (8 * hh + 4 * (quad >> 1)) * 2; R.rd1 = 0; R.step = 0;
    R.o0 = opq(OFF_W3 + j * 7 * UNIT + (ks < PK ? ks * 7 : 6 * PK + ks) * 1024); R.o1 = opq(ks < PK ? 1024 : MK * 1024);
    R.ocb = opq(OFF_CS3 + (j * 28 + ks) * 896); R.sig = opq(C_W3 + j);
  }
#pragma unroll
  for (int i = 0; i < 7; ++i) PIN(R.pa[i]);
  PIN(R.rd0); PIN(R.rd1);
}
DI void retile_load(const Retile& R, u32 lds_dst) {
#pragma unroll
  for (int i = 0; i < 7; ++i) {
    u32 keep;
    asm volatile("s_mov_b32 %0, m0\n\ts_add_u32 m0, %2, %3\n\ts_nop 0\n\tglobal_load_lds_dwordx4 %1, off\n\ts_mov_b32 m0, %0"
                 : "=&s"(keep) : "v"(R.pa[i]), "s"(lds_dst), "i"(i * 1024) : "memory", "scc");
  }
}
DI void retile_store(int job, const Retile& R, rsrc_t rs, LAS char* LD, int lane) {
  const int h = lane >> 5;
  if (job < 64 || job >= 512) {
    const bool pad = (lane & 0x14) == 0x14;
    const bool kpad = job < 64 && h;
    v4u o[8];
#pragma unroll
    for (int i = 0; i < 8; ++i) {
      const int ad = ((i & 2) ? R.rd1 : R.rd0) + (i >> 2) * 112 + (i & 1) * 16 * 224;
      const v2u lo = __builtin_bit_cast(v2u, __builtin_amdgcn_ds_read_tr16_b64_v4i16((LAS s16x4*)(LD + ad)));
      const v2u hi = __builtin_bit_cast(v2u, __builtin_amdgcn_ds_read_tr16_b64_v4i16((LAS s16x4*)(LD + ad + R.step)));
      o[i] = v4u{lo.x, lo.y, hi.x, hi.y};
    }
#pragma unroll
    for (int i = 0; i < 8; ++i) {
      if ((i & 2) && pad) o[i] = v4u{0u, 0u, 0u, 0u};
      if ((i & 1) && kpad) { o[i].z = 0u; o[i].w = 0u; }
      st16(rs, lane * 16, ((i & 1) ? R.o1 : R.o0) + (i >> 2) * R.ocb + ((i >> 1) & 1) * 1024, o[i]);
    }
  } else {
    v4u o[7];
#pragma unroll
    for (int n = 0; n < 7; ++n) o[n] = tr_frag(LD, R.rd0 + n * 32, 4 * 448);
    float cs[7];
#pragma unroll
    for (int n = 0; n < 7; ++n) {
      st16(rs, lane * 16, R.o0 + n * R.o1, o[n]);
      cs[n] = xsum(sum8(o[n]));
    }
#pragma unroll
    for (int n2 = 0; n2 < 4; ++n2) {
      const float v = h ? cs[n2 < 3 ? 2 * n2 + 1 : 6] : cs[2 * n2];
      if (n2 < 3 || !h) st4(rs, lane * 4, R.ocb + n2 * 256, __float_as_uint(v));
    }
  }
}

__global__ __launch_bounds__(512, 2) void k_main(const Args A) {
  extern __shared__ __attribute__((aligned(16))) char smem[];
  LAS char* LD = (LAS char*)smem;
  char* ws = A.ws;
  const int b = blockIdx.x, xcd = b & 7, bi = b >> 3;
  const int g = (xcd >> 2) * 8 + (bi >> 2), j = (xcd & 3) * 4 + (bi & 3);
  const int rb = 2 * g + (j >> 3), cb = j & 7;
  const int lane = threadIdx.x & 63, w = __builtin_amdgcn_readfirstlane(threadIdx.x >> 6);
  const int h = lane >> 5, tok = lane & 31;
  const rsrc_t rs = __builtin_amdgcn_make_buffer_rsrc((void*)ws, 0, WS_USED, 0x00020000);
  const u32 lbase = (u32)(size_t)smem;
  const int l16 = lane * 16;
  const int tq_ = (lane & 15) >> 2, tc8 = ((lane >> 4) & 1) * 4 + (lane & 3);
  const int w1lane = (8 * h + tq_) * 64 + min(tc8 + (j & 1), 7) * 8;

  const int hw = w - 4;
  const u32 voffn = (hw * 16 + (lane >> 2)) * (3 * D * 2) + (lane & 3) * 16;
  const char* w1 = (const char*)A.wqkv + 56 * j - (j & 1) * 8;
  unsigned long long ep;
  const u32 lb = lbase + hw * 1024;
#define KP(p) dma1<4 * UNIT + (p) * 4096, 896 + (p) * 0x2a000>(w1, voffn, lb)
#define VP(p) dma1<(p) * 4096, 1792 + (p) * 0x2a000>(w1, voffn, lb)
#define QP(p) dma1<UNIT + (p) * 4096, (p) * 0x2a000>(w1, voffn, lb)
  if (opq(w) >= 4) {
    if (hw == 1 && b < NZ) {
      asm volatile("global_load_dwordx2 %0, %1, %2 sc1" : "=v"(ep) : "v"(EPOCH_OFF), "s"(ws) : "memory");
      st4(rs, lane * 128, b * 8192, 0u);
    }
    if (hw == 2) {
      dma1<L_KVF, 0>((const char*)A.wphi + j * 1568, l16, lbase);
      dma1<L_KVF + 1024, 0>((const char*)A.wphi + j * 1568, min(1024 + l16, 1552), lbase);
    }
    KP(0); KP(1); VP(0); KP(2); VP(1); KP(3); VP(2);
    if (hw == 3) {
      unsigned long long pcv;
      asm volatile("s_getpc_b64 %0" : "=s"(pcv));
      const char* pc = (const char*)(pcv & ~255ull);
      dma1<L_W2X, 0>(pc, bi * 1024 + l16, lbase);
      dma1<L_W2X + 1024, 32768>(pc, bi * 1024 + l16, lbase);
    }
  }

  if (w >= 4) {
    const u32 voff = hw * 1024 + lane * 16;
    const char* w1f = ws + OFF_W1 + j * 3 * UNIT;
    const char* w2 = ws + OFF_W2 + cb * W2B;
    const char* w3 = ws + OFF_W3 + j * 7 * UNIT;
    const char* w4 = ws + OFF_W4 + cb * 8 * UNIT;
#define DMA(src, slot) dma7((src), voff, lbase + (slot) * UNIT + hw * 1024)
#define DMAP(src, slot) dma7p((src), voff, lbase + (slot) * UNIT + hw * 1024)
#define DMAN(which, slot) dma7n(w1 + (which) * 896, voffn, lbase + (slot) * UNIT + hw * 1024)
    v4u wr[28];
    unsigned long long tag = 0;
    auto hlayer = [&](auto LC) AI {
      constexpr int L = LC.value;
      Retile R;
      const int job = hw >= 2 ? b * 2 + hw - 2 : (hw == 0 && b < 224 ? 512 + b : -1);
      float c0 = 0.f, c1 = 0.f, c2 = 0.f, c3 = 0.f;
      auto colpart = [&](int slot, auto K0, auto K1) AI {
        constexpr int k0 = K0.value, n = K1.value - K0.value;
        const int ib = opqv(slot * UNIT + opqv(w1lane));
        v4u fr[n];
#pragma unroll
        for (int ks = 0; ks < n; ++ks) fr[ks] = tr_frag(LD, ib + (k0 + ks) * 1024, 256);
        SB();
        const u32 one = 0x3f803f80u;
#pragma unroll
        for (int ks = 0; ks < n; ++ks) { c0 = dot2(fr[ks].x, one, c0); c1 = dot2(fr[ks].y, one, c1); c2 = dot2(fr[ks].z, one, c2); c3 = dot2(fr[ks].w, one, c3); }
        SB();
      };
      auto colfin = [&](int which) AI {
        const float cs = xsum((c0 + c1) + (c2 + c3));
        if (lane < 32) *(LAS float*)(LD + L_CS1 + which * 128 + lane * 4) = cs;
      };
      v4u csa[7];
      auto export_img = [&](int slot, int which) AI {
        const int ib = opqv(slot * UNIT + opqv(w1lane) + g * 1024);
        const int eo = opq(OFF_W1 + (j * 3 + which) * UNIT + g * 1024);
        st16(rs, l16, eo, tr_frag(LD, ib, 256));
        if (g < 12) st16(rs, l16, eo + 16384, tr_frag(LD, ib + 16384, 256));
      };
      if constexpr (L == 0) {
        if (hw == 1 && lane < 2) *(LAS v4u*)(LD + L_ARR + l16) = v4u{0u, 0u, 0u, 0u};
#define VMW(n, n3) if (hw == 3) { VMCNT(n3); } else { VMCNT(n); }
        VMW(4, 6); BAR();
        KP(4); VP(3);
        if (hw == 1 && b < NZ) {
          PIN(ep);
          tag = ep + 1ull;
          if (lane == 0) __hip_atomic_store((unsigned long long*)(ws + INIT_OFF + b * 128), tag, __ATOMIC_RELAXED, __HIP_MEMORY_SCOPE_AGENT);
        }
        if (hw == 2) {
          const int wb = opqv(L_KVF + (4 * h + tq_) * 56 + tc8 * 8);
          const v4u f0 = tr_frag(LD, wb, 8 * 56);
          v4u f1 = tr_frag(LD, wb + 16 * 56, 8 * 56);
          if (h) { f1.z = 0u; f1.w = 0u; }
          *(LAS v4u*)(LD + L_WPHI + l16) = f0;
          *(LAS v4u*)(LD + L_WPHI + 1024 + l16) = f1;
        }
        if (hw == 0) colpart(4, IC<0>{}, IC<8>{});
        VMW(4, 6); BAR();
        KP(5); VP(4);
        if (job >= 0) retile_prep(A, job, lane, L_STAGE + hw * STG, R);
        if (hw == 1) colpart(0, IC<0>{}, IC<8>{});
        VMW(4, 6); BAR();
        KP(6); VP(5); VP(6);
        if (hw == 0) colpart(4, IC<8>{}, IC<16>{});
        if (hw == 1) colpart(0, IC<8>{}, IC<12>{});
        VMCNT(5); BAR();
        QP(0); QP(1); QP(2); QP(3);
        if (hw == 0) colpart(4, IC<16>{}, IC<20>{});
        if (hw == 1) colpart(0, IC<12>{}, IC<16>{});
        VMCNT(7); BAR();
        QP(4); QP(5); QP(6);
        if (hw == 0) colpart(4, IC<20>{}, IC<24>{});
        if (hw == 1) colpart(0, IC<16>{}, IC<20>{});
        VMCNT(7); BAR();
        if (hw == 0) { colpart(4, IC<24>{}, IC<28>{}); colfin(0); }
        if (hw == 1) { colpart(0, IC<20>{}, IC<28>{}); colfin(1); }
        if (hw == 2) export_img(4, 1);
        if (hw == 3) export_img(0, 2);
        BAR();
        if (job >= 0) retile_load(R, lbase + L_STAGE + hw * STG);
      } else {
        wait_arr(LD, 0, 4);
        DMA(w1f + 2 * UNIT, 0);
        VMCNT(0); BAR();
      }
      {
        const int wb = opqv(L_WPHI + opqv(l16));
        const v4u wf0 = lds16(LD, wb), wf1 = lds16(LD, wb + 1024);
        BAR();
        __builtin_amdgcn_s_setprio(1);
        const int lz = opqv(lane);
        const int xk = opqv(L_W2X + hw * 1024 + lz * 16);
        const v4u kf0 = lds16(LD, xk), kf1 = lds16(LD, xk + 4096);
        const float rstd = *(LAS float*)(LD + L_KVF + hw * 256 + lz * 4);
        f32x16 p = {};
        p = mfma(wf0, kf0, p);
        p = mfma(wf1, kf1, p);
        v4u o0, o1;
        half_softmax<1>(p, h, rstd, o0, o1);
        const int pb = opqv(L_PHK + (hw * 32 + (lz & 31)) * PHK_STRIDE + 8 * (lz >> 5) + 64);
        *(LAS v2u*)(LD + pb) = v2u{o0.x, o0.y}; *(LAS v2u*)(LD + pb + 16) = v2u{o0.z, o0.w};
        *(LAS v2u*)(LD + pb + 32) = v2u{o1.x, o1.y}; *(LAS v2u*)(LD + pb + 48) = v2u{o1.z, o1.w};
        __builtin_amdgcn_s_setprio(0);
      }
      if constexpr (L == 0) {
        if (job >= 0) { VMCNT(7); } else { VMCNT(0); }
      } else VMCNT(0);
      BAR();
      if (hw < 2) {
        __builtin_amdgcn_s_setprio(2);
        f32x16 kp = {};
        const int lz = opqv(lane);
        const int g16 = (lz >> 4) & 1, q = (lz & 15) >> 2, p = lz & 3, hz = lz >> 5;
        const int ab0 = opqv(L_PHK + (8 * hz + q) * PHK_STRIDE + (g16 * 16 + 4 * p) * 2 + hw * 64);
        const int bb0 = opqv(L_V + (8 * hz + q) * V_STRIDE + (g16 * 16 + 4 * p) * 2);
        v4u ap[8], bv[8];
#pragma unroll
        for (int s = 0; s < 8; ++s) { ap[s] = tr_frag(LD, ab0 + 16 * s * PHK_STRIDE, 4 * PHK_STRIDE); bv[s] = tr_frag(LD, bb0 + 16 * s * V_STRIDE, 4 * V_STRIDE); }
        SB();
#pragma unroll
        for (int s = 0; s < 8; ++s) kp = mfma(ap[s], bv[s], kp);
        const int ko = opq(OFF_KVP + L * KVB + (j * 16 + g) * 4096 + hw * 2048);
        st16(rs, l16, ko, pack8(kp, 0));
        st16(rs, l16, ko + 1024, pack8(kp, 1));
        wave_signal(ws, C_KV + L * 16 + j, lane);
        __builtin_amdgcn_s_setprio(0);
      }
      if constexpr (L == 0) {
        if (hw == 3) {
          colpart(1, IC<0>{}, IC<28>{}); colfin(2);
          arrive_lds(LD, 1, lane);
        }
        if (hw == 1) { export_img(1, 0); wave_signal(ws, C_W1 + j, lane); }
        if (job >= 0) { VMCNT(0); retile_store(job, R, rs, LD, lane); }
        if (job >= 0) wave_signal(ws, R.sig, lane);
        if (hw >= 2) signal_cnt(ws, C_W1 + j);
        BAR();
        u32* fw = cnt_ptr(ws, (lane & 3) == 0 ? C_W2 + (cb >> 1) : (lane & 3) == 1 ? C_W3 + j : C_W4 + (cb >> 1));
        kv_reduce(rs, LD, L, j, w, lane);
        BAR();
        wait_flags(ws, fw, __hip_atomic_load(fw, __ATOMIC_RELAXED, __HIP_MEMORY_SCOPE_AGENT), (lane & 3) == 0 ? 16u : (lane & 3) == 1 ? 28u : 56u);
        DMA(w2, 2); DMA(w2 + UNIT, 3); dma2(w2 + 2 * UNIT, voff, lbase + L_W2X + hw * 1024);
#pragma unroll
        for (int i = 0; i < 7; ++i) csa[i] = ld16(rs, min(lane, 55) * 16, OFF_CS3 + (j * 28 + hw * 7 + i) * 896);
        DMA(w3, 0);
        VMCNT(14); BAR();
        DMA(w3 + UNIT, 1);
      } else {
        DMA(w2, 2); DMA(w2 + UNIT, 3); dma2(w2 + 2 * UNIT, voff, lbase + L_W2X + hw * 1024);
        DMA(w3, 0);
        BAR();
        kv_reduce(rs, LD, L, j, w, lane);
        BAR();
        DMA(w3 + UNIT, 1);
      }
      BAR();
      if constexpr (L == 0) {
        f32x4 c4 = __builtin_bit_cast(f32x4, csa[0]);
#pragma unroll
        for (int i = 1; i < 7; ++i) c4 += __builtin_bit_cast(f32x4, csa[i]);
        if (lane < 56) *(LAS f32x4*)(LD + L_KVF + hw * 896 + l16) = c4;
      }
      DMA(w3 + 2 * UNIT, 2);
      VMCNT(7); BAR();
      const int wo4 = opq(OFF_W4 + cb * 8 * UNIT + 4 * UNIT + hw * UNIT);
#define WR(lo, hi) _Pragma("unroll") for (int i = lo; i < hi; ++i) wr[i] = ld16(rs, l16, wo4 + i * 1024)
      if constexpr (L == 0) {
        const int t4 = min(hw * 64 + lane, 223) * 4;
        *(LAS float*)(LD + L_CS3 + t4) = (*(LAS float*)(LD + L_KVF + t4) + *(LAS float*)(LD + L_KVF + 896 + t4)) +
                                         (*(LAS float*)(LD + L_KVF + 1792 + t4) + *(LAS float*)(LD + L_KVF + 2688 + t4));
        WR(0, 28);
      }
      DMA(w3 + 3 * UNIT, 3);
      VMCNT(7); BAR();
      DMAP(w3 + 4 * UNIT, 0);
      DMAP(w4 + UNIT, 4);
      VMCNT(14); BAR();
      DMAP(w3 + 5 * UNIT, 1);
      if (L == 0) wait_cnt(ws, C_W1 + j, 48);
      VMCNT(14); BAR();
      DMAP(w3 + 6 * UNIT, 2);
      VMCNT(7); BAR();
      DMAP(w4 + 2 * UNIT, 3);
      VMCNT(7); BAR();
      {
        u32* fp = cnt_ptr(ws, C_H + (L * 64 + 2 * rb + (lane & 1)) * 8);
        const int ho = opq(OFF_H + L * HB + 2 * rb * 112 * 1024 + hw * 7168);
        v4u hx[28];
        auto ldh = [&](auto I) AI { constexpr int i = I.value; hx[i] = ld16(rs, l16, ho + (i & 1) * 112 * 1024 + p4_koff(p4_oh(i >> 1))); };
        auto poll = [&](int lv) AI { return __hip_atomic_load(fp + 32 * lv, __ATOMIC_RELAXED, __HIP_MEMORY_SCOPE_AGENT); };
        DMAP(w4, 0);
        wait_flags(ws, fp, poll(0));
        static_for<8>([&](auto I) AI { ldh(I); });
        BAR();
        u32 f = poll_issue(fp + 32);
        DMA(w4 + 3 * UNIT, 1);
        VMCNT(7); PIN(f);
        wait_flags(ws, fp + 32, f);
        static_for<6>([&](auto I) AI { ldh(IC<8 + I.value>{}); });
        f32x16 acc[4] = {};
        auto ord = [](int i) constexpr { return i < 20 ? i : (i < 24 ? 20 + 2 * (i - 20) : 21 + 2 * (i - 24)); };
        const int pb0 = opqv(p4_part(0, hw) + opqv(l16)), pb2 = opqv(p4_part(2, hw) + opqv(l16));
        static_for<28>([&](auto I) AI {
          constexpr int i = I.value, k = ord(i), t = k >> 1, tq = k & 1;
          if constexpr (i == 0) f = poll(2);
          if constexpr (i == H2) {
            wait_flags(ws, fp + 64, f);
            VMCNT(2); arrive_lds(LD, 4, lane);
          }
          if constexpr (i == H3 - 3) f = poll(3);
          if constexpr (i == H3) wait_flags(ws, fp + 96, f);
          if constexpr (i == 24) wait_arr(LD, 2, 4 * L + 4);
          acc[tq] = mfma(wr[2 * t], hx[k], acc[tq]);
          acc[2 + tq] = mfma(wr[2 * t + 1], hx[k], acc[2 + tq]);
          if constexpr (i < 2) ldh(IC<14 + i>{});
          if constexpr (i >= H2 && i < H2 + 4) { ldh(IC<16 + 2 * (i - H2)>{}); ldh(IC<17 + 2 * (i - H2)>{}); }
          if constexpr (i >= H3 && i < H3 + 2) { ldh(IC<24 + 2 * (i - H3)>{}); ldh(IC<25 + 2 * (i - H3)>{}); }
          if constexpr (i >= 24) {
            constexpr int qd = i - 24;
            *(LAS f32x4*)(LD + pb0 + qd * 1024) = f32x4{acc[0][4 * qd], acc[0][4 * qd + 1], acc[0][4 * qd + 2], acc[0][4 * qd + 3]};
            *(LAS f32x4*)(LD + pb2 + qd * 1024) = f32x4{acc[2][4 * qd], acc[2][4 * qd + 1], acc[2][4 * qd + 2], acc[2][4 * qd + 3]};
          }
          SB();
        });
        wait_arr(LD, 3, 4 * L + 4);
        put16(acc[1], LD, opqv(p4_part(1, hw) + opqv(l16)));
        put16(acc[3], LD, opqv(p4_part(3, hw) + opqv(l16)));
        BAR();
      }
      if (L == 0) { DMA(w1f + UNIT, 3); DMA(w1f, 1); }
    };
    hlayer(IC<0>{});
    hlayer(IC<1>{});
    if (b == 0 && hw == 1 && lane == 0) __hip_atomic_store((unsigned long long*)(ws + EPOCH_OFF), tag, __ATOMIC_RELAXED, __HIP_MEMORY_SCOPE_AGENT);
    return;
  }

  const int tt = 4 * g + w;
  const int tt2 = 2 * rb + (w & 1);
  const int kh = w >> 1;
  const rsrc_t rx0 = __builtin_amdgcn_make_buffer_rsrc((void*)A.x0, 0, XB, 0x00020000);
  v4u res[2];
constexpr int XWV = 14;
  constexpr int XW = XWV;

  auto clayer = [&](auto LC) AI {
    constexpr int L = LC.value;
    v4u x[28];
    v2u st[8];
    float mean, rstd;
    v4u pq[4];
    auto phase1 = [&](auto FIRST) AI {
      constexpr bool first = FIRST.value;
      const int xo = opq(first ? tt * 32 * (D * 2) : OFF_X2 + tt * 28 * 1024);
      auto ldx = [&](int ks) AI { return ld16(rs, l16, xo + ks * 1024); };
      const int so_ = opq(OFF_ST2 + tt * 4096);
      const int sv_ = (opqv(lane) & 31) * 8 + h * 2048;
      v4u gx[28];
      const int stg = L_STAGE + w * STG;
      const int gv = (lane >> 4) * (D * 2) + (lane & 15) * 16, gv3 = (lane >> 3) * (D * 2) + (lane & 7) * 16;
      const int gl = stg + (lane >> 4) * 272 + (lane & 15) * 16, gl3 = stg + (lane >> 3) * 272 + (lane & 7) * 16;
      const int srd = opqv(stg + (opqv(lane) & 31) * 272 + h * 16);
      auto ldg = [&](int i) AI {
        return i < 24 ? __builtin_amdgcn_raw_buffer_load_b128(rx0, gv, xo + (i & 7) * 4 * (D * 2) + (i >> 3) * 256, 0)
                      : __builtin_amdgcn_raw_buffer_load_b128(rx0, gv3, xo + (i & 7) * 8 * (D * 2) + 768, 0);
      };
      auto stage = [&](int c) AI {
#pragma unroll
        for (int i = 0; i < (c < 3 ? 8 : 4); ++i) *(LAS v4u*)(LD + (c < 3 ? gl + i * 4 * 272 : gl3 + i * 8 * 272)) = gx[8 * c + i];
#pragma unroll
        for (int i = 0; i < (c < 3 ? 8 : 4); ++i) x[8 * c + i] = lds16(LD, srd + i * 32);
      };
      unsigned long long iv = 0;
      unsigned long long* initp = (unsigned long long*)(ws + (w == 3 && lane < NZ ? INIT_OFF + lane * 128 : EPOCH_OFF));
      auto init_ok = [&](unsigned long long v) AI {
        const unsigned long long e = ((unsigned long long)(u32)__builtin_amdgcn_readlane((int)(v >> 32), 63) << 32) | (u32)__builtin_amdgcn_readlane((int)v, 63);
        return __all(lane >= NZ || v == e + 1ull);
      };
      if constexpr (first) {
#pragma unroll
        for (int i = 0; i < 16; ++i) gx[i] = ldg(i);
        stage(0);
      } else {
#pragma unroll
        for (int ks = 0; ks < XW; ++ks) x[ks] = ldx(ks);
      }
      BAR();
      f32x16 ak = {}, av = {}, p = {};
      const int sbk = first ? opqv(opqv(w1lane) + 4 * UNIT) : opqv(opqv(l16) + 3 * UNIT), sbv = first ? opqv(w1lane) : opqv(l16);
      const int csb = opqv(L_CS1 + 16 * (opqv(lane) >> 5)), wb = opqv(L_WPHI + opqv(l16));
      const int xk = opqv(L_W2X + w * 1024 + opqv(l16));
      f32x4 cs[4]; v4u kf[2], wf[2];
      float nmean, s1 = 0.f, s2 = 0.f, q1 = 0.f, q2 = 0.f;
      auto isk = [](int i) constexpr { return i < LAG || (i < 56 - LAG && ((i - LAG) & 1) == 0); };
      auto kst = [](int i) constexpr { return i < LAG ? i : (i < 56 - LAG ? ((((i - LAG) & 1) == 0) ? LAG + (i - LAG) / 2 : (i - LAG) / 2) : i - 28); };
      auto rdw = [&](int base, int ks) AI { if constexpr (first) return tr_frag(LD, base + ks * 1024, 256); else return lds16(LD, base + ks * 1024); };
      pipe<56, first ? 2 : 1>([&](auto I) AI { return rdw(isk(I.value) ? sbk : sbv, kst(I.value)); },
               [&](auto I) AI {
                 constexpr int i = I.value;
                 if constexpr (first && (i == 6 || i == 14 || i == 22 || i == 30 || i == 38 || i == 46)) { RAWBAR(); }
                 if constexpr (i == 54) { BAR(); }
               },
               [&](auto I, v4u a, auto nx) AI {
                 constexpr int i = I.value, ks = kst(i);
                 if constexpr (i == 54) { p = mfma(wf[0], kf[0], p); p = mfma(wf[1], kf[1], p); }
                 if constexpr (isk(i)) ak = mfma(a, x[ks], ak); else av = mfma(a, x[ks], av);
                 nx();
                 if constexpr (!first && isk(i) && ks + XW < 28) x[ks + XW] = ldx(ks + XW);
                 if constexpr (first) {
                   if constexpr (i < 12) gx[16 + i] = ldg(16 + i);
                   if constexpr (isk(i) && ks % 8 == 6) stage(ks / 8 + 1);
                   if constexpr (isk(i)) {
                     const u32 one = 0x3f803f80u;
                     s1 = dot2(x[ks].x, one, s1); q1 = dot2(x[ks].x, x[ks].x, q1); s2 = dot2(x[ks].y, one, s2); q2 = dot2(x[ks].y, x[ks].y, q2);
                     s1 = dot2(x[ks].z, one, s1); q1 = dot2(x[ks].z, x[ks].z, q1); s2 = dot2(x[ks].w, one, s2); q2 = dot2(x[ks].w, x[ks].w, q2);
                   }
                   if constexpr (i == 45) iv = __hip_atomic_load(initp, __ATOMIC_RELAXED, __HIP_MEMORY_SCOPE_AGENT);
                   if constexpr (i == 51) { ln_finish(s1 + s2, q1 + q2, mean, rstd); nmean = -mean; }
                 } else {
                   if constexpr (!isk(i) && ks >= 4 && ks < 12) st[ks - 4] = ld8(rs, sv_ + (ks - 4) * 256, so_);
                   if constexpr (i == 45) { ln_stats(st, mean, rstd); nmean = -mean; }
                 }
                 if constexpr (i >= 46 && i < 50) cs[i - 46] = *(LAS f32x4*)(LD + csb + 32 * (i - 46));
                 if constexpr (i == 50) wf[0] = lds16(LD, wb);
                 if constexpr (i == 51) wf[1] = lds16(LD, wb + 1024);
                 if constexpr (i == 52 || i == 53) {
#pragma unroll
                   for (int qd = 2 * (i - 52); qd < 2 * (i - 52) + 2; ++qd) {
                     const float y0 = fmaf(nmean, cs[qd][0], ak[4 * qd]), y1 = fmaf(nmean, cs[qd][1], ak[4 * qd + 1]);
                     const float y2 = fmaf(nmean, cs[qd][2], ak[4 * qd + 2]), y3 = fmaf(nmean, cs[qd][3], ak[4 * qd + 3]);
                     kf[qd >> 1][2 * (qd & 1)] = pk2(y0, y1);
                     kf[qd >> 1][2 * (qd & 1) + 1] = pk2(y2, y3);
                   }
                   *(LAS v4u*)(LD + xk + (i - 52) * 4096) = kf[i - 52];
                   if constexpr (i == 52) *(LAS float*)(LD + L_KVF + w * 256 + opqv(lane) * 4) = rstd;
                 }
                 if constexpr (i == 55) {
#pragma unroll
                   for (int qd = 0; qd < 4; ++qd) cs[qd] = *(LAS f32x4*)(LD + csb + 128 + 32 * qd);
                 }
               });
      const int lz = opqv(lane);
      const int trow = w * 32 + (lz & 31);
      const int pb = opqv(L_PHK + trow * PHK_STRIDE + 8 * (lz >> 5)), vb = opqv(L_V + trow * V_STRIDE + 8 * (lz >> 5));
      half_softmax<0>(p, h, rstd, pq[0], pq[1]);
#pragma unroll
      for (int s = 0; s < 2; ++s) {
        *(LAS v2u*)(LD + pb + 32 * s) = v2u{pq[s].x, pq[s].y};
        *(LAS v2u*)(LD + pb + 32 * s + 16) = v2u{pq[s].z, pq[s].w};
      }
      SB();
#pragma unroll
      for (int r = 0; r < 16; ++r) av[r] = fmaf(nmean, cs[r >> 2][r & 3], av[r]) * rstd;
      if (h) { av[12] = 1.f; av[13] = 0.f; av[14] = 0.f; av[15] = 0.f; }
#pragma unroll
      for (int qd = 0; qd < 4; ++qd) *(LAS v2u*)(LD + vb + 16 * qd) = v2u{pk2(av[4 * qd], av[4 * qd + 1]), pk2(av[4 * qd + 2], av[4 * qd + 3])};
      if constexpr (first) {
        if (w == 3 && !init_ok(iv)) {
          u32 spins = 0;
          while (!init_ok(__hip_atomic_load(initp, __ATOMIC_RELAXED, __HIP_MEMORY_SCOPE_AGENT))) {
            __builtin_amdgcn_s_sleep(1);
            if (++spins > 400000u) break;
          }
        }
      }
      BAR();
      if constexpr (first) {
#pragma unroll
        for (int ro = 0; ro < 2; ++ro) {
          const int oi = min(kh * 4 + ro * 2 + h, 6);
          res[ro] = __builtin_amdgcn_raw_buffer_load_b128(rx0, tok * (D * 2) + (7 * cb + oi) * 16, tt2 * 32 * (D * 2), 0);
        }
      }
      f32x16 aq = {};
      const int sbq = first ? opqv(opqv(w1lane) + 1 * UNIT) : opqv(opqv(l16) + 1 * UNIT);
      pipe<28, first ? 2 : 1>([&](auto I) AI { return rdw(sbq, I.value); }, [&](auto) AI {},
               [&](auto I, v4u a, auto nx) AI { aq = mfma(a, x[I.value], aq); nx(); });
      if constexpr (first) wait_arr(LD, 1, 1);
#pragma unroll
      for (int qd = 0; qd < 4; ++qd) cs[qd] = *(LAS f32x4*)(LD + csb + 256 + 32 * qd);
#pragma unroll
      for (int r = 0; r < 16; ++r) aq[r] = fmaf(nmean, cs[r >> 2][r & 3], aq[r]);
      f32x16 p2 = {};
      const int wb2 = opqv(L_WPHI + opqv(l16));
      p2 = mfma(lds16(LD, wb2), pack8(aq, 0), p2);
      p2 = mfma(lds16(LD, wb2 + 1024), pack8(aq, 1), p2);
      half_softmax<0>(p2, h, rstd, pq[0], pq[1]);
      half_softmax<1>(p2, h, rstd, pq[2], pq[3]);
    };
    phase1(std::integral_constant<bool, L == 0>{});
    if (w == 0) wait_cnt(ws, C_KV + L * 16 + j, 32);
    BAR();
    kv_reduce(rs, LD, L, j, w, lane);
    BAR();
    {
      f32x16 nm = {};
      const int kb = opqv(L_KVF + opqv(l16));
      v4u kf[4];
#pragma unroll
      for (int s = 0; s < 4; ++s) kf[s] = lds16(LD, kb + s * 1024);
#pragma unroll
      for (int s = 0; s < 4; ++s) nm = mfma(kf[s], pq[s], nm);
      auto r = __builtin_amdgcn_permlane32_swap(__float_as_uint(nm[12]), __float_as_uint(nm[12]), false, false);
      const float den = __uint_as_float(r[1]) + 1e-6f;
      const float rd = __builtin_amdgcn_rcpf(den);
#pragma unroll
      for (int i = 0; i < 16; ++i) nm[i] *= rd;
      const int oo = opq(OFF_O + L * OB + (tt * 32 + 2 * j) * 1024);
      st16(rs, l16, oo, pack8(nm, 0));
      st16(rs, l16, oo + 1024, pack8(nm, 1));
    }
    wave_signal(ws, C_O + (L * 64 + tt) * 2 + (j >> 3), lane);
    wait_cnt(ws, C_O + (L * 64 + tt2) * 2 + kh, 8, true);
    {
      const int xo = opq(OFF_O + L * OB + (tt2 * 32 + 16 * kh) * 1024);
#pragma unroll
      for (int t = 0; t < (XW < 16 ? XW : 16); ++t) x[t] = ld16(rs, l16, xo + t * 1024);
      if (L == 0) RAWBAR();
      f32x16 a0 = {}, a1 = {};
      const int sb2 = opqv(2 * UNIT + kh * 2048 + opqv(l16)), sbx = opqv(L_W2X + kh * 2048 + opqv(l16));
      pipe<32>([&](auto I) AI { constexpr int i = I.value, t = i >> 1; return lds16(LD, (t < 14 ? sb2 + t * 4096 : sbx + (t - 14) * 4096) + (i & 1) * 1024); },
               [&](auto I) AI { if constexpr (I.value == 14) RAWBAR();   },
               [&](auto I, v4u a, auto nx) AI {
                 constexpr int i = I.value;
                 if constexpr (i & 1) a1 = mfma(a, x[i >> 1], a1); else a0 = mfma(a, x[i >> 1], a0);
                 nx();
                 if constexpr (!(i & 1) && (i >> 1) + XW < 16) x[(i >> 1) + XW] = ld16(rs, l16, xo + ((i >> 1) + XW) * 1024);
               });
      const int xb = opqv(L_SCR + w * 4096 + opqv(l16)), xb2 = opqv(L_SCR + (w ^ 2) * 4096 + opqv(l16));
      f32x16 fin;
      if (kh) {
#pragma unroll
        for (int qd = 0; qd < 4; ++qd) *(LAS f32x4*)(LD + xb + qd * 1024) = f32x4{a0[4 * qd], a0[4 * qd + 1], a0[4 * qd + 2], a0[4 * qd + 3]};
        fin = a1;
      } else {
#pragma unroll
        for (int qd = 0; qd < 4; ++qd) *(LAS f32x4*)(LD + xb + qd * 1024) = f32x4{a1[4 * qd], a1[4 * qd + 1], a1[4 * qd + 2], a1[4 * qd + 3]};
        fin = a0;
      }
      BAR();
#pragma unroll
      for (int qd = 0; qd < 4; ++qd) {
        const f32x4 v = *(LAS f32x4*)(LD + xb2 + qd * 1024);
#pragma unroll
        for (int i = 0; i < 4; ++i) fin[4 * qd + i] += v[i];
      }
      finish_tile(fin, res, rs, opq(OFF_X1 + L * XB), opq(OFF_ST1 + L * STB), tt2, cb, kh, lane, false, A.out);
    }
    wave_signal(ws, C_X1 + L * 64 + tt2, lane);
    wait_cnt(ws, C_X1 + L * 64 + tt, 16, true);
    {
      const int xo = opq(OFF_X1 + L * XB + tt * 28 * 1024);
      const int so_ = opq(OFF_ST1 + L * STB + tt * 4096);
      const int sv_ = (opqv(lane) & 31) * 8 + h * 2048;
      constexpr int XW3 = 8;
#pragma unroll
      for (int ks = 0; ks < XW3; ++ks) x[ks] = ld16(rs, l16, xo + ks * 1024);
      const int ho = opq(OFF_H + L * HB + (tt * 112 + 7 * j) * 1024);
      const int lvo = opq(C_H + (L * 64 + tt) * 8);
      u32* fp = cnt_ptr(ws, C_H + (L * 64 + tt2) * 8);
      auto poll = [&](int lv) AI { return __hip_atomic_load(fp + 32 * lv, __ATOMIC_RELAXED, __HIP_MEMORY_SCOPE_AGENT); };
      u32 f;
      float nmean, gs, irs;
      const int csb = opqv(L_CS3 + 16 * (opqv(lane) >> 5));
      const int sbA = opqv(l16), sbB = opqv(opqv(l16) + 2 * UNIT);
      f32x4 Gcg[2], Gcu[2]; float Gt[8], Gyu[8]; u32 Go[4];
      auto glu = [&](const f32x16& a, auto N_, auto S_) AI {
        constexpr int n = N_.value, s = S_.value;
        if constexpr (s == 1) Gcu[0] = *(LAS f32x4*)(LD + csb + n * 128 + 32);
        if constexpr (s == 4) Gcg[1] = *(LAS f32x4*)(LD + csb + n * 128 + 64);
        if constexpr (s == 9) Gcu[1] = *(LAS f32x4*)(LD + csb + n * 128 + 96);
        if constexpr (s == 12 && n < 6) Gcg[0] = *(LAS f32x4*)(LD + csb + (n + 1) * 128);
        static_for<8>([&](auto V_) AI {
          constexpr int v = V_.value, k = s - (1 + 4 * (v >> 1) + (v & 1)), hf = v >> 2, i = v & 3;
          if constexpr (k == 0) { Gt[v] = fmaf(nmean, Gcg[hf][i], a[8 * hf + i]); PIN(Gt[v]); }
          if constexpr (k == 1) { Gt[v] = Gt[v] * gs; PIN(Gt[v]); }
          if constexpr (k == 2) { Gt[v] = __builtin_amdgcn_exp2f(Gt[v]); PIN(Gt[v]); }
          if constexpr (k == 3) { Gt[v] = fmaf(Gt[v], irs, irs); PIN(Gt[v]); }
          if constexpr (k == 4) { Gt[v] = __builtin_amdgcn_rcpf(Gt[v]); PIN(Gt[v]); }
          if constexpr (k == 5) { Gyu[v] = fmaf(nmean, Gcu[hf][i], a[8 * hf + 4 + i]); PIN(Gyu[v]); }
          if constexpr (k == 6) { Gt[v] = Gt[v] * Gyu[v]; PIN(Gt[v]); }
          if constexpr (k == 7 && (v & 1)) { Go[v >> 1] = pk2(Gt[v - 1], Gt[v]); PIN(Go[v >> 1]); }
        });
        if constexpr (s == 21) st16(rs, l16, ho + n * 1024, v4u{Go[0], Go[1], Go[2], Go[3]});
      };
      const int ho2 = opq(OFF_H + L * HB + tt2 * 112 * 1024 + kh * 7168);
      v4u hx[28];
      auto ldh = [&](auto T) AI { hx[T.value] = ld16(rs, l16, ho2 + p4_koff(p4_oc(T.value))); };
      f32x16 acc[7];
      constexpr int PB = 7 * PK;
      pipe<196>(
          [&](auto I) AI { constexpr int i = I.value, u = i / 28, sl = u & 3; return lds16(LD, (sl < 2 ? sbA : sbB) + (sl & 1) * UNIT + (i % 28) * 1024); },
          [&](auto I) AI { if constexpr (I.value > 28 && I.value % 28 == 6) { RAWBAR(); } },
          [&](auto I, v4u a, auto nx) AI {
            constexpr int i = I.value;
            if constexpr (i < PB) {
              constexpr int n = i % 7, ks = i / 7;
              if constexpr (ks == 0) { f32x16 z = {}; acc[n] = mfma(a, x[ks], z); } else acc[n] = mfma(a, x[ks], acc[n]);
              nx();
              constexpr int K2 = 28 - XW3 - PK, fx = ks < K2 ? XW3 + 2 * ks + (n ? 1 : 0) : XW3 + K2 + ks;
              if constexpr (n == 0 || (n == 3 && ks < K2)) x[fx] = ld16(rs, l16, xo + fx * 1024);
              if constexpr (n >= 5 && ks < 4) st[2 * ks + n - 5] = ld8(rs, sv_ + (2 * ks + n - 5) * 256, so_);
              if constexpr (i == 46) {
                ln_stats(st, mean, rstd);
                nmean = -mean; gs = -LOG2E * rstd; irs = __builtin_amdgcn_rcpf(rstd);
                PIN(nmean); PIN(gs); PIN(irs);
              }
            } else {
              constexpr int r = i - PB, n = r / MK, s = r % MK;
              acc[n] = mfma(a, x[PK + s], acc[n]);
              nx();
              if constexpr (r == 8) Gcg[0] = *(LAS f32x4*)(LD + csb);
              if constexpr (n >= 2 && s + MK < 22) glu(acc[n - 2], IC<n - 2>{}, IC<s + MK>{});
              if constexpr (n >= 1) glu(acc[n - 1], IC<n - 1>{}, IC<s>{});
              if constexpr (n >= 3 && s == 20 - MK) { VMCNT(0); signal_cnt(ws, lvo + n - 3); }
            }
          });
      arrive_lds(LD, 2, lane);
      static_for<22>([&](auto S_) AI {
        if constexpr (S_.value + MK < 22) glu(acc[5], IC<5>{}, IC<S_.value + MK>{});
        glu(acc[6], IC<6>{}, S_);
        if constexpr (S_.value == 4) { VMCNT(0); signal_cnt(ws, lvo + 4); }
        if constexpr (S_.value == CP1) f = poll_issue(fp + 96);
      });
      const int wo_ = opqv(kh * 3072 + opqv(l16)), wp_ = opqv(kh * 1024 + 1024 + opqv(l16));
      const int wo2 = opqv(wo_ + 3 * UNIT), wp2 = opqv(wp_ + 3 * UNIT);
      v4u wq[56];
      auto ldw = [&](auto K) AI {
        constexpr int k = K.value, t = k >> 1, wb = p4_wbase(t);
        if constexpr (wb >= 3 * UNIT) wq[k] = lds16(LD, ((k & 1) ? wp2 : wo2) + wb - 3 * UNIT);
        else wq[k] = lds16(LD, ((k & 1) ? wp_ : wo_) + wb);
      };
      VMCNT(1); signal_cnt(ws, lvo + 5);
      PIN(f);
      const u32 f2 = poll(4);
      if (__all(f >= 16u)) {
        static_for<4>([&](auto T) AI { ldh(T); });
        VMCNT(6); signal_cnt(ws, lvo + 6);
      } else {
        VMCNT(2); signal_cnt(ws, lvo + 6);
        wait_flags(ws, fp + 96, f);
        static_for<4>([&](auto T) AI { ldh(T); });
      }
      wait_flags(ws, fp + 128, f2);
      static_for<8>([&](auto T) AI { ldh(IC<4 + T.value>{}); });
      wait_arr(LD, 4, 4 * L + 4);
      static_for<4>([&](auto K) AI { ldw(K); });
      f32x16 fin = {}, oth = {};
      static_for<28>([&](auto T) AI {
        constexpr int t = T.value;
        if constexpr (t == CP5) f = poll(5);
        if constexpr (t == CT5) {
          wait_flags(ws, fp + 160, f);
          f = poll(6);
          static_for<8>([&](auto K) AI { ldh(IC<12 + K.value>{}); });
        }
        if constexpr (t == 12) {
          wait_flags(ws, fp + 192, f);
          static_for<8>([&](auto K) AI { ldh(IC<20 + K.value>{}); });
        }
        if constexpr (t == 12) arrive_lds(LD, 3, lane);
        if constexpr (t + 2 < 28) { ldw(IC<2 * t + 4>{}); ldw(IC<2 * t + 5>{}); }
        oth = mfma(wq[2 * t + 1], hx[t], oth);
        fin = mfma(wq[2 * t], hx[t], fin);
        SB();
      });
      wait_arr(LD, 3, 4 * L + 4);
      put16(oth, LD, opqv(L_SCR + w * 4096 + opqv(l16)));
      BAR();
#pragma unroll
      for (int c = 0; c < 4; ++c) add16(fin, LD, opqv(p4_part(w, c) + opqv(l16)));
      add16(fin, LD, opqv(L_SCR + (w ^ 2) * 4096 + opqv(l16)));
      finish_tile(fin, res, rs, OFF_X2, OFF_ST2, tt2, cb, kh, lane, L == 1, A.out);
    }
    if (L == 0) {
      wave_signal(ws, C_X2 + tt2, lane);
      arrive_lds(LD, 0, lane);
      wait_cnt(ws, C_X2 + tt, 16, true);
    }
  };
  clayer(IC<0>{});
  clayer(IC<1>{});
}

extern "C" void kernel_launch(void* const* d_in, const int* in_sizes, int n_in, void* d_out, int out_size, void* d_ws,
                              size_t ws_size, hipStream_t stream) {
  Args a;
  a.x0 = (const u16*)d_in[0]; a.wqkv = (const u16*)d_in[1]; a.wphi = (const u16*)d_in[2]; a.wo = (const u16*)d_in[3];
  a.wg = (const u16*)d_in[5]; a.wu = (const u16*)d_in[6]; a.wd = (const u16*)d_in[7];
  a.ws = (char*)d_ws; a.out = (u16*)d_out;
  (void)hipFuncSetAttribute((const void*)k_main, hipFuncAttributeMaxDynamicSharedMemorySize, LDS_BYTES);
  k_main<<<256, 512, LDS_BYTES, stream>>>(a);
}
```
